```python
import torch

def _softmax(x, axis=-1):
    return torch.softmax(x, dim=axis)
def _gelu(x):
    return torch.nn.functional.gelu(x, approximate="tanh")
def _silu(x):
    return torch.nn.functional.silu(x)
def _sigmoid(x):
    return torch.sigmoid(x)
def _layernorm(x, axis=-1, eps=1e-5):
    m = x.mean(dim=axis, keepdim=True)
    v = x.var(dim=axis, keepdim=True, unbiased=False)
    return (x - m) / torch.sqrt(v + eps)
def _scatter_add(tgt, idx, src, axis=0):
    return tgt.clone().index_add_(axis, idx, src)
def _segment_sum(src, seg, n):
    out = torch.zeros((n,) + src.shape[1:], dtype=src.dtype)
    return out.index_add_(0, seg, src)
def _pad(x, pw):
    flat = tuple(v for pair in reversed(pw) for v in pair)
    return torch.nn.functional.pad(x, flat)

kernel_name = 'recurrent_scan__mha_block_cached__mixer_s_mlp__prompt'

def setup_inputs():

    _w = (lambda a: (lambda t: t.to(torch.bfloat16) if t.is_floating_point() else t)(torch.as_tensor(a)))
    def _A_setup():
        B, Sq, D = 1, 2048, 576
        H, Sk, Dh = 32, 2048, 128
        return {
            "x": _w((lambda *s: torch.randn(*s, dtype=torch.bfloat16) * 0.05)(B, Sq, D)),
            "k_cache": _w((lambda *s: torch.randn(*s, dtype=torch.bfloat16) * 0.8)(B, H, Sk, Dh)),
            "v_cache": _w((lambda *s: torch.randn(*s, dtype=torch.bfloat16) * 0.8)(B, H, Sk, Dh)),
            "wq": _w((lambda *s: torch.randn(*s, dtype=torch.bfloat16) * 0.8)(D, H * Dh) * (D ** -0.5)),
            "wo": _w((lambda *s: torch.randn(*s, dtype=torch.bfloat16) * 0.8)(H * Dh, D) * ((H * Dh) ** -0.5)),
        }
    _A_in = _A_setup()

    def _B_setup():
        B, S, D, F = 1, 2048, 576, 4608
        return {
            "x":  _w((lambda *s: torch.randn(*s, dtype=torch.bfloat16) * 0.05)(B, S, D)),
            "w1": _w((lambda *s: torch.randn(*s, dtype=torch.bfloat16) * 3.2)(D, D) * (D ** -0.5)),
            "w2": _w((lambda *s: torch.randn(*s, dtype=torch.bfloat16) * 3.2)(D, D) * (D ** -0.5)),
            "w3": _w((lambda *s: torch.randn(*s, dtype=torch.bfloat16) * 3.2)(D, F) * (D ** -0.5)),
            "w4": _w((lambda *s: torch.randn(*s, dtype=torch.bfloat16) * 3.2)(F, D) * (F ** -0.5)),
        }
    _B_in = _B_setup()

    return {
        "A_x": _A_in["x"],
        "A_k_cache": _A_in["k_cache"],
        "A_v_cache": _A_in["v_cache"],
        "A_wq": _A_in["wq"],
        "A_wo": _A_in["wo"],
        "B_x": _B_in["x"],
        "B_w1": _B_in["w1"],
        "B_w2": _B_in["w2"],
        "B_w3": _B_in["w3"],
        "B_w4": _B_in["w4"],
    }

def reference(A_x, A_k_cache, A_v_cache, A_wq, A_wo, B_x, B_w1, B_w2, B_w3, B_w4):
    def _A_ref(x, k_cache, v_cache, wq, wo):
        B, Sq, D = x.shape
        H, Dh = 32, 128
        q = (lambda x, *d: x.permute(*d))((x @ wq).reshape(B, Sq, H, Dh), 0, 2, 1, 3)
        scores = torch.einsum("bhqd,bhkd->bhqk", q, k_cache) * (Dh ** -0.5)
        p = _softmax(scores, axis=-1)
        o = torch.einsum("bhqk,bhkd->bhqd", p, v_cache)
        return (lambda x, *d: x.permute(*d))(o, 0, 2, 1, 3).reshape(B, Sq, H * Dh) @ wo

    def _B_ref(x, w1, w2, w3, w4):
        B, S, D = x.shape
        h = _gelu(_layernorm(x, axis=-1) @ w1)
        c = D // 4
        g0 = _pad(h[:, :, 0*c:1*c], ((0, 0), (1, 0), (0, 0)))[:, :S, :]
        g1 = _pad(h[:, :, 1*c:2*c], ((0, 0), (0, 1), (0, 0)))[:, 1:, :]
        g2 = _pad(h[:, :, 2*c:3*c], ((0, 0), (2, 0), (0, 0)))[:, :S, :]
        g3 = _pad(h[:, :, 3*c:   ], ((0, 0), (0, 2), (0, 0)))[:, 2:, :]
        hs = torch.concatenate([g0, g1, g2, g3], axis=-1) @ w2
        x1 = x + hs
        h2 = _gelu(_layernorm(x1, axis=-1) @ w3) @ w4
        return x1 + h2

    x = A_x
    xn = _layernorm(x, axis=-1)
    s = _A_ref(xn, A_k_cache, A_v_cache, A_wq, A_wo)
    g = _silu(_B_ref(xn, B_w1, B_w2, B_w3, B_w4))
    return x + s * g
```

```cpp
#include <hip/hip_runtime.h>
#include <hip/hip_bf16.h>
#include <cstdio>

typedef unsigned short u16;
typedef float    f32x16 __attribute__((ext_vector_type(16)));
typedef float    f32x4  __attribute__((ext_vector_type(4)));
typedef float    f32x2  __attribute__((ext_vector_type(2)));
typedef __bf16   bf16x8 __attribute__((ext_vector_type(8)));
typedef __bf16   bf16x4 __attribute__((ext_vector_type(4)));
typedef __bf16   bf16x2 __attribute__((ext_vector_type(2)));
typedef unsigned u32x4  __attribute__((ext_vector_type(4)));
typedef unsigned u32x2  __attribute__((ext_vector_type(2)));
#define DI __device__ __forceinline__
#define LAS __attribute__((address_space(3)))

constexpr int S = 2048, D = 576, H = 32, DH = 128, F = 4608, HD = H * DH;

DI unsigned cvtpk(float lo, float hi) { f32x2 v = {lo, hi}; bf16x2 b = __builtin_convertvector(v, bf16x2); return __builtin_bit_cast(unsigned, b); }
DI float bflo(unsigned w) { return __uint_as_float(w << 16); }
DI float bfhi(unsigned w) { return __uint_as_float(w & 0xffff0000u); }
DI float gelu_t(float x) {
  const float u = x * (0.7978845608028654f + 0.035677408136300125f * x * x);
  const float e = __builtin_amdgcn_exp2f(u * -2.885390081777927f);
  return x * __builtin_amdgcn_rcpf(1.f + e);
}
DI float silu(float x) { return x * __builtin_amdgcn_rcpf(1.f + __builtin_amdgcn_exp2f(x * -1.4426950408889634f)); }
DI float wave_sum(float v) {
  for (int o = 32; o > 0; o >>= 1) v += __shfl_xor(v, o);
  return v;
}

__global__ __launch_bounds__(256) void ln_kernel(const u16* __restrict__ x, u16* __restrict__ y1, u16* __restrict__ y2) {
  const int row = blockIdx.x * 4 + (threadIdx.x >> 6), lane = threadIdx.x & 63;
  float v[9];
  for (int i = 0; i < 9; ++i) v[i] = bflo(x[row * D + lane + 64 * i]);
  for (int pass = 0; pass < 2; ++pass) {
    float s = 0; for (int i = 0; i < 9; ++i) s += v[i];
    const float m = wave_sum(s) * (1.f / D);
    float q = 0; for (int i = 0; i < 9; ++i) { v[i] -= m; q += v[i] * v[i]; }
    const float r = rsqrtf(wave_sum(q) * (1.f / D) + 1e-5f);
    u16* y = pass ? y2 : y1;
    if (!y) break;
    for (int i = 0; i < 9; ++i) { unsigned w = cvtpk(v[i] * r, 0.f); y[row * D + lane + 64 * i] = (u16)w; v[i] = bflo(w); }
  }
}

template <int BM, int BN, int WM, int WN, class Epi>
DI void gemm_tile(const u16* __restrict__ A, int lda, const u16* __restrict__ W, int ldw, int K, int m0, int n0, char* lds, f32x16 (&acc)[BN / WN / 32][BM / WM / 32]) {
  constexpr int TM = BM / WM / 32, TN = BN / WN / 32;
  constexpr int ABYTES = BM * 128, WBYTES = BN * 128, STAGE = ABYTES + WBYTES;
  constexpr int APIECES = BM / 8, WPIECES = BN / 8;
  const int tid = threadIdx.x, lane = tid & 63, wid = __builtin_amdgcn_readfirstlane(tid >> 6);
  const int wm = wid % WM, wn = wid / WM;
  const int l32 = lane & 31, hi = lane >> 5;
  auto stage = [&](int buf, int kt) {
    char* base = lds + buf * STAGE;
#pragma unroll
    for (int p = wid; p < APIECES; p += 4) {
      const int row = 8 * p + ((lane >> 2) & 7), ch = 4 * hi + ((lane & 3) ^ ((row >> 2) & 3));
      __builtin_amdgcn_global_load_lds((const unsigned*)(A + (long)(m0 + row) * lda + kt * 64 + ch * 8), (LAS void*)(base + p * 1024), 16, 0, 0);
    }
#pragma unroll
    for (int p = wid; p < WPIECES; p += 4) {
      const int st = 2 * p + hi, rg = st / (BN / 32), cb = st % (BN / 32);
      const int k = 8 * rg + ((lane >> 2) & 7), c = 32 * cb + 8 * (lane & 3);
      __builtin_amdgcn_global_load_lds((const unsigned*)(W + (long)(kt * 64 + k) * ldw + n0 + c), (LAS void*)(base + ABYTES + p * 1024), 16, 0, 0);
    }
  };
  const int nt = K / 64;
  stage(0, 0);
  for (int kt = 0; kt < nt; ++kt) {
    __syncthreads();
    if (kt + 1 < nt) stage((kt + 1) & 1, kt + 1);
    char* base = lds + (kt & 1) * STAGE;
#pragma unroll
    for (int ks = 0; ks < 4; ++ks) {
      bf16x8 xa[TM], wf[TN];
#pragma unroll
      for (int i = 0; i < TM; ++i) {
        const int row = (wm * TM + i) * 32 + l32, ch = 2 * ks + hi;
        xa[i] = *(const bf16x8*)(base + 1024 * (row >> 3) + 512 * (ch >> 2) + 64 * (row & 7) + 16 * ((ch & 3) ^ ((row >> 2) & 3)));
      }
#pragma unroll
      for (int j = 0; j < TN; ++j) {
        char* a = base + ABYTES + (BN / 32 * 512) * (2 * ks + hi) + 512 * (wn * TN + j) + 64 * ((lane >> 2) & 3) + 32 * ((lane >> 4) & 1) + 8 * (lane & 3);
        bf16x4 lo = __builtin_amdgcn_ds_read_tr16_b64_v4bf16((LAS bf16x4*)a);
        bf16x4 up = __builtin_amdgcn_ds_read_tr16_b64_v4bf16((LAS bf16x4*)(a + 256));
        wf[j] = __builtin_shufflevector(lo, up, 0, 1, 2, 3, 4, 5, 6, 7);
      }
#pragma unroll
      for (int j = 0; j < TN; ++j)
#pragma unroll
        for (int i = 0; i < TM; ++i) acc[j][i] = __builtin_amdgcn_mfma_f32_32x32x16_bf16(wf[j], xa[i], acc[j][i], 0, 0, 0);
    }
  }
  __syncthreads();
}

struct Params {
  const u16 *A, *W, *A2, *W2, *R1, *R2; u16* out;
  int lda, ldw, K, K2, ldo, mode;
};
template <int BM, int BN, int WM, int WN, int MODE>
__global__ __launch_bounds__(256) void gemm_kernel(Params p) {
  extern __shared__ __attribute__((aligned(16))) char lds[];
  constexpr int TM = BM / WM / 32, TN = BN / WN / 32;
  const int m0 = blockIdx.y * BM, n0 = blockIdx.x * BN;
  f32x16 acc[TN][TM] = {}, acc2[TN][TM] = {};
  gemm_tile<BM, BN, WM, WN, void>(p.A, p.lda, p.W, p.ldw, p.K, m0, n0, lds, acc);
  if (MODE == 4) gemm_tile<BM, BN, WM, WN, void>(p.A2, p.K2, p.W2, p.ldw, p.K2, m0, n0, lds, acc2);
  const int tid = threadIdx.x, lane = tid & 63, wid = tid >> 6, wm = wid % WM, wn = wid / WM, l32 = lane & 31, hi = lane >> 5;
#pragma unroll
  for (int j = 0; j < TN; ++j)
#pragma unroll
    for (int i = 0; i < TM; ++i)
#pragma unroll
      for (int g = 0; g < 4; ++g) {
        const int m = m0 + (wm * TM + i) * 32 + l32, n = n0 + (wn * TN + j) * 32 + 8 * g + 4 * hi;
        float v[4];
        for (int e = 0; e < 4; ++e) v[e] = acc[j][i][4 * g + e];
        int mo = m;
        if (MODE == 0) { for (int e = 0; e < 4; ++e) v[e] *= 0.12751743082f; }
        if (MODE == 1 || MODE == 3) { for (int e = 0; e < 4; ++e) v[e] = gelu_t(v[e]); }
        if (MODE == 1) {
          const int grp = n / 144, sh = grp == 0 ? 1 : grp == 1 ? -1 : grp == 2 ? 2 : -2;
          mo = m + sh;
          if (mo < 0 || mo >= S) { mo = (mo + S) & (S - 1); for (int e = 0; e < 4; ++e) v[e] = 0.f; }
        }
        if (MODE == 2) { const u32x2 r = *(const u32x2*)(p.R1 + (long)m * D + n); v[0] += bflo(r[0]); v[1] += bfhi(r[0]); v[2] += bflo(r[1]); v[3] += bfhi(r[1]); }
        if (MODE == 4) {
          const u32x2 r = *(const u32x2*)(p.R1 + (long)m * D + n), q = *(const u32x2*)(p.R2 + (long)m * D + n);
          const float x1[4] = {bflo(q[0]), bfhi(q[0]), bflo(q[1]), bfhi(q[1])}, x[4] = {bflo(r[0]), bfhi(r[0]), bflo(r[1]), bfhi(r[1])};
          for (int e = 0; e < 4; ++e) v[e] = x[e] + v[e] * silu(x1[e] + acc2[j][i][4 * g + e]);
        }
        u32x2 w = {cvtpk(v[0], v[1]), cvtpk(v[2], v[3])};
        *(u32x2*)(p.out + (long)mo * p.ldo + n) = w;
      }
}

using s16x8 = __attribute__((ext_vector_type(8))) short;
using s16x4 = __attribute__((ext_vector_type(4))) short;
constexpr int NW = 8, QBLK = 32, KVBLK = 64;
constexpr float THR = 8.f;
constexpr size_t SHM_V = KVBLK * DH * 2, SHM_K = KVBLK * DH * 2, SHM_ATTN = 2 * SHM_V + 2 * SHM_K + NW * 64 * 4;
#define KSWZ(row, colB) ((row) * 256 + ((colB) ^ (((row) & 7) << 4)))
#define SBAR() __builtin_amdgcn_sched_barrier(0)
DI int crow(int r, int hi) { return (r & 3) + 8 * (r >> 2) + 4 * hi; }
DI unsigned cvtpk_a(float lo, float hi) { unsigned r; asm volatile("v_cvt_pk_bf16_f32 %0, %1, %2" : "=v"(r) : "v"(lo), "v"(hi)); return r; }
DI void partialSM(f32x16& p0, f32x16& p1, float& m_reg, float& mn, float& alpha) {
  float pmax = p0[0]; for (int r = 1; r < 16; ++r) pmax = fmaxf(pmax, p0[r]); for (int r = 0; r < 16; ++r) pmax = fmaxf(pmax, p1[r]);
  { auto rr = __builtin_amdgcn_permlane32_swap(__float_as_uint(pmax), __float_as_uint(pmax), false, false);
    pmax = fmaxf(__uint_as_float(rr[0]), __uint_as_float(rr[1])); }
  if (__builtin_expect(__all(pmax - m_reg <= THR), 1)) { mn = m_reg; alpha = 1.f; }
  else { mn = fmaxf(m_reg, pmax); alpha = __builtin_amdgcn_exp2f(m_reg - mn); m_reg = mn; }
  for (int r = 0; r < 16; ++r) p0[r] = p0[r] - mn; for (int r = 0; r < 16; ++r) p1[r] = p1[r] - mn;
  for (int r = 0; r < 16; ++r) p0[r] = __builtin_amdgcn_exp2f(p0[r]);
}
DI void finishSM(f32x16& p0, f32x16& p1, float alpha, float& l_reg, s16x8& pa0, s16x8& pa1, s16x8& pa2, s16x8& pa3) {
  for (int r = 0; r < 16; ++r) p1[r] = __builtin_amdgcn_exp2f(p1[r]);
  float ps = 0; for (int r = 0; r < 16; ++r) ps += p0[r]; for (int r = 0; r < 16; ++r) ps += p1[r];
  { auto rr = __builtin_amdgcn_permlane32_swap(__float_as_uint(ps), __float_as_uint(ps), false, false);
    ps = __uint_as_float(rr[0]) + __uint_as_float(rr[1]); }
  l_reg = l_reg * alpha + ps;
#define PK4(P, BASE, OUT) do { unsigned a0 = cvtpk_a(P[BASE + 0], P[BASE + 1]), a1 = cvtpk_a(P[BASE + 2], P[BASE + 3]);   \
    unsigned b0 = cvtpk_a(P[BASE + 4], P[BASE + 5]), b1 = cvtpk_a(P[BASE + 6], P[BASE + 7]);                              \
    auto r0 = __builtin_amdgcn_permlane32_swap(a0, b0, false, false); auto r1 = __builtin_amdgcn_permlane32_swap(a1, b1, false, false); \
    u32x4 w = {r0[0], r1[0], r0[1], r1[1]}; OUT = *reinterpret_cast<s16x8*>(&w); } while (0)
  PK4(p0, 0, pa0); PK4(p0, 8, pa1); PK4(p1, 0, pa2); PK4(p1, 8, pa3);
#undef PK4
}
#define MF(a, b, c) __builtin_amdgcn_mfma_f32_32x32x16_bf16(__builtin_bit_cast(bf16x8, a), __builtin_bit_cast(bf16x8, b), c, 0, 0, 0)
DI void qkt(f32x16& p0, f32x16& p1, const u16* Ks, const s16x8* qr, int r32, int hi) {
  p0 = f32x16{}; p1 = f32x16{};
  for (int d0 = 0; d0 < 8; ++d0) { int cb = (d0 * 16 + hi * 8) * 2;
    s16x8 b0 = *reinterpret_cast<const s16x8*>((const char*)Ks + KSWZ(r32, cb));
    s16x8 b1 = *reinterpret_cast<const s16x8*>((const char*)Ks + KSWZ(32 + r32, cb));
    p0 = MF(b0, qr[d0], p0);
    p1 = MF(b1, qr[d0], p1); }
}
DI int v_st(int k, int c) { const int kk = (k & ~0xC) | ((k & 4) << 1) | ((k & 8) >> 1); return ((kk >> 3) * 4 + (c >> 5)) * 512 + ((kk & 7) * 32 + (c & 31)) * 2; }
DI int v_rd_base(int lane) { return ((lane & 3) << 3) | (((lane >> 2) & 3) << 6) | (((lane >> 4) & 1) << 5) | (((lane >> 5) & 1) << 8); }
constexpr int v_rd_off(int d0, int ks, int half) { return d0 * 512 + ks * 4096 + half * 2048; }
template <int OFF> DI s16x4 tr_read(int vb) {
  s16x4 r; asm volatile("ds_read_b64_tr_b16 %0, %1 offset:%2" : "=&v"(r) : "v"(vb), "i"(OFF) : "memory"); return r;
}
template <int D0> DI void pv_one(f32x16& od, int vb, s16x8 pa0, s16x8 pa1, s16x8 pa2, s16x8 pa3) {
  const s16x4 l0 = tr_read<v_rd_off(D0, 0, 0)>(vb), h0 = tr_read<v_rd_off(D0, 0, 1)>(vb), l1 = tr_read<v_rd_off(D0, 1, 0)>(vb), h1 = tr_read<v_rd_off(D0, 1, 1)>(vb);
  const s16x4 l2 = tr_read<v_rd_off(D0, 2, 0)>(vb), h2 = tr_read<v_rd_off(D0, 2, 1)>(vb), l3 = tr_read<v_rd_off(D0, 3, 0)>(vb), h3 = tr_read<v_rd_off(D0, 3, 1)>(vb);
  asm volatile("s_waitcnt lgkmcnt(0)" ::: "memory"); SBAR();
#define PK(L, H) (s16x8){L[0], L[1], L[2], L[3], H[0], H[1], H[2], H[3]}
  od = MF(pa0, PK(l0, h0), od);
  od = MF(pa1, PK(l1, h1), od);
  od = MF(pa2, PK(l2, h2), od);
  od = MF(pa3, PK(l3, h3), od);
#undef PK
}
DI void pv_d0(f32x16* o, int vb, s16x8 pa0, s16x8 pa1, s16x8 pa2, s16x8 pa3) {
  pv_one<0>(o[0], vb, pa0, pa1, pa2, pa3); pv_one<1>(o[1], vb, pa0, pa1, pa2, pa3); pv_one<2>(o[2], vb, pa0, pa1, pa2, pa3); pv_one<3>(o[3], vb, pa0, pa1, pa2, pa3);
}
__global__ __launch_bounds__(512, 1) void attn_kernel(const u16* __restrict__ Q, const u16* __restrict__ Kc, const u16* __restrict__ Vc, u16* __restrict__ O) {
  extern __shared__ __attribute__((aligned(16))) char lds[];
  const int bid = blockIdx.x, xcd = bid & 7, idx = bid >> 3, h = xcd * 4 + (idx & 3), qb = idx >> 2;
  const u16* Qb = Q + (long)qb * 256 * HD + h * DH;
  const u16* Kh = Kc + (long)h * S * DH; const u16* Vh = Vc + (long)h * S * DH;
  u16* Ob = O + (long)qb * 256 * HD + h * DH;
  constexpr int LDQ = HD, LDK = DH, LDO = HD, seq = S;
  const int tid = threadIdx.x, wid = tid >> 6, lane = tid & 63, r32 = lane & 31, hi = lane >> 5;
  u16* V_lds = (u16*)lds; u16* K_lds = (u16*)(lds + 2 * SHM_V);
  float* ws = (float*)(lds + 2 * SHM_V + 2 * SHM_K) + wid * 64; float* li_l = ws; float* al_l = ws + 32;
  float m_reg = -1e30f, l_reg = 0; f32x16 o[4] = {}; s16x8 qr[8];
  const u16* Qw = Qb + (long)(wid * QBLK + r32) * LDQ + hi * 8;
#pragma unroll
  for (int d0 = 0; d0 < 8; ++d0) qr[d0] = *(const s16x8*)(Qw + d0 * 16);
  const int sr = tid >> 4, sc = (tid & 15) * 8, vst0 = v_st(sr, sc), vst1 = v_st(32 + sr, sc);
  const int vb0 = (int)(uintptr_t)V_lds + v_rd_base(lane);
  struct { s16x8 vs0, vs1, ks0, ks1; } sr_[2];
#define LD8(p) (*(const s16x8*)(p))
#define SLOAD(i, k0) do { sr_[i].vs0 = LD8(&Vh[(long)((k0) + sr) * LDK + sc]); sr_[i].vs1 = LD8(&Vh[(long)((k0) + 32 + sr) * LDK + sc]); \
    sr_[i].ks0 = LD8(&Kh[(long)((k0) + sr) * LDK + sc]); sr_[i].ks1 = LD8(&Kh[(long)((k0) + 32 + sr) * LDK + sc]); } while (0)
#define SWRITE(b, i) do { *(s16x8*)((char*)V_lds + (b) * SHM_V + vst0) = sr_[i].vs0;          \
    *(s16x8*)((char*)V_lds + (b) * SHM_V + vst1) = sr_[i].vs1; int kc = sc * 2;               \
    *(s16x8*)((char*)K_lds + (b) * SHM_K + KSWZ(sr, kc)) = sr_[i].ks0;                       \
    *(s16x8*)((char*)K_lds + (b) * SHM_K + KSWZ(32 + sr, kc)) = sr_[i].ks1; } while (0)
#define SWAIT() asm volatile("s_waitcnt vmcnt(4)" ::: "memory")
#define RESC(a) do { if (__any((a) < 1.f)) { if (hi == 0) al_l[r32] = (a); asm volatile("s_waitcnt lgkmcnt(0)" ::: "memory"); \
    for (int d = 0; d < 4; ++d) for (int r = 0; r < 16; ++r) o[d][r] *= al_l[crow(r, hi)]; } } while (0)
  f32x16 pA0, pA1, pB0, pB1; float mnA, mnB, alA, alB; s16x8 pa0, pa1, pa2, pa3; const int NT = seq / KVBLK;
  constexpr int SE = 0, SO = 1;
  SLOAD(SE, 0); asm volatile("s_waitcnt vmcnt(0)" ::: "memory"); SWRITE(0, SE); __syncthreads();
  qkt(pA0, pA1, K_lds, qr, r32, hi); partialSM(pA0, pA1, m_reg, mnA, alA);
  SLOAD(SO, KVBLK); SLOAD(SE, 2 * KVBLK);
  SWAIT(); SWRITE(1, SO); __syncthreads();
  for (int j = 1; j + 1 < NT; j += 2) {
    SBAR(); qkt(pB0, pB1, (u16*)((char*)K_lds + SHM_K), qr, r32, hi);
    finishSM(pA0, pA1, alA, l_reg, pa0, pa1, pa2, pa3); SBAR();
    SLOAD(SO, (j + 2) * KVBLK); SBAR();
    pv_d0(o, vb0, pa0, pa1, pa2, pa3); partialSM(pB0, pB1, m_reg, mnB, alB);
    __syncthreads(); SWAIT(); SWRITE(0, SE);
    RESC(alB); __syncthreads();
    SBAR(); qkt(pA0, pA1, K_lds, qr, r32, hi);
    finishSM(pB0, pB1, alB, l_reg, pa0, pa1, pa2, pa3); SBAR();
    if (j + 3 < NT) SLOAD(SE, (j + 3) * KVBLK); SBAR();
    pv_d0(o, vb0 + (int)SHM_V, pa0, pa1, pa2, pa3); partialSM(pA0, pA1, m_reg, mnA, alA);
    __syncthreads(); SWAIT(); SWRITE(1, SO);
    RESC(alA); __syncthreads();
  }
  SBAR(); qkt(pB0, pB1, (u16*)((char*)K_lds + SHM_K), qr, r32, hi);
  finishSM(pA0, pA1, alA, l_reg, pa0, pa1, pa2, pa3); SBAR();
  pv_d0(o, vb0, pa0, pa1, pa2, pa3); partialSM(pB0, pB1, m_reg, mnB, alB);
  __syncthreads(); RESC(alB);
  finishSM(pB0, pB1, alB, l_reg, pa0, pa1, pa2, pa3); SBAR();
  pv_d0(o, vb0 + (int)SHM_V, pa0, pa1, pa2, pa3);
  if (hi == 0) li_l[r32] = l_reg; asm volatile("s_waitcnt lgkmcnt(0)" ::: "memory");
  float rli[16];
#pragma unroll
  for (int r = 0; r < 16; ++r) rli[r] = __builtin_amdgcn_rcpf(li_l[crow(r, hi)]);
  u16* Ow = Ob + (long)(wid * QBLK) * LDO;
#pragma unroll
  for (int r = 0; r < 16; ++r) { int orow = crow(r, hi);
    for (int d0 = 0; d0 < 4; ++d0) Ow[(long)orow * LDO + d0 * 32 + r32] = (u16)cvtpk(o[d0][r] * rli[r], 0.f); }
}

template <int BM, int BN, int WM, int WN, int MODE>
static void launch_gemm(const Params& p, int N, hipStream_t st) {
  constexpr int shm = 2 * (BM + BN) * 128;
  hipFuncSetAttribute((const void*)gemm_kernel<BM, BN, WM, WN, MODE>, hipFuncAttributeMaxDynamicSharedMemorySize, shm);
  gemm_kernel<BM, BN, WM, WN, MODE><<<dim3(N / BN, S / BM), 256, shm, st>>>(p);
}

extern "C" void kernel_launch(void* const* d_in, const int* in_sizes, int n_in, void* d_out, int out_size, void* d_ws, size_t ws_size, hipStream_t stream) {
  const u16* x = (const u16*)d_in[0]; const u16* kc = (const u16*)d_in[1]; const u16* vc = (const u16*)d_in[2];
  const u16* wq = (const u16*)d_in[3]; const u16* wo = (const u16*)d_in[4];
  const u16* w1 = (const u16*)d_in[6]; const u16* w2 = (const u16*)d_in[7]; const u16* w3 = (const u16*)d_in[8]; const u16* w4 = (const u16*)d_in[9];
  char* ws = (char*)d_ws;
  const size_t SD = (size_t)S * D * 2;
  u16* xn = (u16*)ws; u16* xnn = (u16*)(ws + SD); u16* hs = (u16*)(ws + 2 * SD); u16* x1 = (u16*)(ws + 3 * SD); u16* x1n = (u16*)(ws + 4 * SD);
  u16* q = (u16*)(ws + 5 * SD); u16* o = q + (size_t)S * HD; u16* h3 = o + (size_t)S * HD;
  ln_kernel<<<S / 4, 256, 0, stream>>>(x, xn, xnn);
  Params p{};
  p = Params{xn, wq, nullptr, nullptr, nullptr, nullptr, q, D, HD, D, 0, HD, 0};   launch_gemm<128, 128, 2, 2, 0>(p, HD, stream);
  p = Params{xnn, w1, nullptr, nullptr, nullptr, nullptr, hs, D, D, D, 0, D, 1};   launch_gemm<64, 64, 2, 2, 1>(p, D, stream);
  p = Params{hs, w2, nullptr, nullptr, xn, nullptr, x1, D, D, D, 0, D, 2};         launch_gemm<64, 64, 2, 2, 2>(p, D, stream);
  ln_kernel<<<S / 4, 256, 0, stream>>>(x1, x1n, nullptr);
  p = Params{x1n, w3, nullptr, nullptr, nullptr, nullptr, h3, D, F, D, 0, F, 3};   launch_gemm<128, 128, 2, 2, 3>(p, F, stream);
  hipFuncSetAttribute((const void*)attn_kernel, hipFuncAttributeMaxDynamicSharedMemorySize, (int)SHM_ATTN);
  attn_kernel<<<256, 512, SHM_ATTN, stream>>>(q, kc, vc, o);
  p = Params{o, wo, h3, w4, x, x1, (u16*)d_out, HD, D, HD, F, D, 4};               launch_gemm<64, 64, 2, 2, 4>(p, D, stream);
}
```

```cpp
#include <hip/hip_runtime.h>
#include <hip/hip_bf16.h>

typedef unsigned short u16;
typedef float    f32x16 __attribute__((ext_vector_type(16)));
typedef float    f32x8  __attribute__((ext_vector_type(8)));
typedef float    f32x4  __attribute__((ext_vector_type(4)));
typedef float    f32x2  __attribute__((ext_vector_type(2)));
typedef __bf16   bf16x8 __attribute__((ext_vector_type(8)));
typedef __bf16   bf16x2 __attribute__((ext_vector_type(2)));
typedef unsigned u32x4  __attribute__((ext_vector_type(4)));
typedef unsigned u32x2  __attribute__((ext_vector_type(2)));
typedef __amdgpu_buffer_rsrc_t srd_t;
#define DI __device__ __forceinline__
#define LAS __attribute__((address_space(3)))

constexpr int S = 2048, D = 576, H = 32, DH = 128, F = 4608, HD = H * DH;

DI unsigned cvtpk(float lo, float hi) { f32x2 v = {lo, hi}; bf16x2 b = __builtin_convertvector(v, bf16x2); return __builtin_bit_cast(unsigned, b); }
DI float bflo(unsigned w) { return __uint_as_float(w << 16); }
DI float bfhi(unsigned w) { return __uint_as_float(w & 0xffff0000u); }
constexpr float GELU_A = -0.4686686933567191f;
DI float silu(float x) { return x * __builtin_amdgcn_rcpf(1.f + __builtin_amdgcn_exp2f(x * -1.4426950408889634f)); }
DI unsigned rfl(unsigned x) { return __builtin_amdgcn_readfirstlane(x); }
DI srd_t mk_srd(const void* p, unsigned bytes) {
  const unsigned long long a = (unsigned long long)p, u = ((unsigned long long)rfl((unsigned)(a >> 32)) << 32) | rfl((unsigned)a);
  return __builtin_amdgcn_make_buffer_rsrc((void*)u, (short)0, (int)rfl(bytes), 0x00020000);
}
DI float sum_g(float v) {
  auto a = __builtin_amdgcn_permlane32_swap(__float_as_uint(v), __float_as_uint(v), false, false);
  v = __uint_as_float(a[0]) + __uint_as_float(a[1]);
  auto b = __builtin_amdgcn_permlane16_swap(__float_as_uint(v), __float_as_uint(v), false, false);
  return __uint_as_float(b[0]) + __uint_as_float(b[1]);
}

constexpr unsigned MB = 1u << 20;
constexpr unsigned WS_XQ = 0, WS_RS = 3 * MB, WS_REQ = WS_RS + 16384, WS_ECHO = WS_RS + 32768, WS_FLAGS = WS_RS + 65536, WS_X1 = 9 * MB, WS_X1N = 12 * MB,
                   WS_EX = 16 * MB, WS_A = 42 * MB, WS_END = 76 * MB, OOB = 0x70000000u;
constexpr float QSCALE = 0.12751743082f;
constexpr unsigned FL_X = 0, FL_O = 64, FL_H = 320, FL_R = 512, FL_A = 768, FL_L = 1024, NFLAGS = 1280, FSTR = 16, INBOX = 73728, NYO = 6;

DI void set_flag(char* ws, const unsigned i) { __hip_atomic_store((unsigned*)(ws + WS_FLAGS + FSTR * i), 1u, __ATOMIC_RELAXED, __HIP_MEMORY_SCOPE_AGENT); }
DI void wait_flag(char* ws, const unsigned i) {
  for (unsigned spin = 0; spin < (1u << 18); ++spin) {
    const unsigned f = __hip_atomic_load((unsigned*)(ws + WS_FLAGS + FSTR * i), __ATOMIC_RELAXED, __HIP_MEMORY_SCOPE_AGENT);
    if (__all(f == 1u)) break;
    __builtin_amdgcn_s_sleep(4);
  }
  __builtin_amdgcn_fence(__ATOMIC_ACQUIRE, "wavefront");
}

DI unsigned head_ks(const unsigned h) { return 4u * (h < 24u ? (NYO + 11u) * (h / NYO) + h % NYO : (NYO + 11u) * ((h - 24u) >> 1) + NYO + 9u + (h & 1u)); }
DI unsigned h3_ks(const unsigned k) { return 4u * ((NYO + 11u) * (k / 36u) + NYO) + k % 36u; }

DI void touch(unsigned& t, const srd_t srd, const unsigned voff, const unsigned soff) {
  asm volatile("s_nop 4\n\tbuffer_load_dword %0, %1, %2, %3 offen" : "+v"(t) : "v"(voff), "s"(srd), "s"(soff) : "memory");
}
DI srd_t code_srd() {
  unsigned long long base; int delta; unsigned len;
  asm volatile("s_getpc_b64 %0\nLcp_%=:\n\ts_mov_b32 %1, k1_kernel-Lcp_%=\n\ts_mov_b32 %2, k1_code_end-k1_kernel" : "=s"(base), "=s"(delta), "=s"(len));
  return mk_srd((const void*)(base + (long long)delta), len);
}

#define FLAG(i) (WS_FLAGS + (i) * FSTR)
DI void early_loads(const u16* __restrict__ wq, const unsigned h, const unsigned p, const unsigned w, char* lds) {
  const unsigned l = threadIdx.x & 63u;
  asm volatile(
    "s_nop 4\n\t"
    "s_add_u32 m0, %[m0w], 0\n\t"
    "s_add_u32 s41, %[ws], 0\n\t"
    "buffer_load_dwordx4 %[wv], %[srdw], s41 offen lds\n\t"
    "s_add_u32 m0, %[m0w], 1024\n\t"
    "s_add_u32 s41, %[ws], 128\n\t"
    "buffer_load_dwordx4 %[wv], %[srdw], s41 offen lds\n\t"
    "s_add_u32 m0, %[m0w], 2048\n\t"
    "s_add_u32 s41, %[ws], 65536\n\t"
    "buffer_load_dwordx4 %[wv], %[srdw], s41 offen lds\n\t"
    "s_add_u32 m0, %[m0w], 3072\n\t"
    "s_add_u32 s41, %[ws], 65664\n\t"
    "buffer_load_dwordx4 %[wv], %[srdw], s41 offen lds\n\t"
    "s_add_u32 m0, %[m0w], 4096\n\t"
    "s_add_u32 s41, %[ws], 131072\n\t"
    "buffer_load_dwordx4 %[wv], %[srdw], s41 offen lds\n\t"
    "s_add_u32 m0, %[m0w], 5120\n\t"
    "s_add_u32 s41, %[ws], 131200\n\t"
    "buffer_load_dwordx4 %[wv], %[srdw], s41 offen lds\n\t"
    :: [wv] "v"(8192u * ((l >> 2) & 7u) + 64u * (l >> 5) + 16u * ((l & 3u) ^ (2u * ((l >> 4) & 1u)))),
       [srdw] "s"(mk_srd((const char*)wq + h * 256u, 576u * 8192u - h * 256u)),
       [m0w] "s"(rfl((unsigned)(size_t)lds) + 16384u + 6144u * w + 24576u * p), [ws] "s"((24u * w + 96u * p) * 8192u)
    : "s41", "scc", "memory");
}

DI unsigned ld_dev(char* ws, const unsigned off) { return __hip_atomic_load((unsigned*)(ws + off), __ATOMIC_RELAXED, __HIP_MEMORY_SCOPE_AGENT); }
DI void st_dev(char* ws, const unsigned off, const unsigned v) { __hip_atomic_store((unsigned*)(ws + off), v, __ATOMIC_RELAXED, __HIP_MEMORY_SCOPE_AGENT); }
DI float sum8(float v) {
  v += __uint_as_float(__builtin_amdgcn_update_dpp(0u, __float_as_uint(v), 0x128, 0xf, 0xf, false));
  return sum_g(v);
}

DI void start_unit(char* __restrict__ ws, const u16* __restrict__ x, const u16* __restrict__ wq, const u16* __restrict__ w1,
                   const bool y, const unsigned h, const unsigned qblk, char* lds) {
  const unsigned tid = threadIdx.x, l = tid & 63u, w = rfl(tid >> 6), bid = blockIdx.x;
  f32x2* red = (f32x2*)(lds + 147456);
  LAS unsigned* cnt = (LAS unsigned*)(lds + 147456 + 256);
  if (w == 3) {
    if (bid == 0) {
      const srd_t srd = mk_srd(ws, WS_END);
#pragma unroll
      for (unsigned i = 0; i < NFLAGS * FSTR / 1024u; ++i)
        __builtin_amdgcn_raw_buffer_store_b128((u32x4){0u, 0u, 0u, 0u}, srd, WS_FLAGS + 1024u * i + 16u * l, 0, 16);
      asm volatile("s_waitcnt vmcnt(0)" ::: "memory");
      for (unsigned spin = 0; spin < (1u << 18); ++spin) {
        asm volatile("" ::: "memory");
        const u32x4 r = __builtin_amdgcn_raw_buffer_load_b128(srd, WS_REQ + 16u * l, 0, 16);
        __builtin_amdgcn_raw_buffer_store_b128(r, srd, WS_ECHO + 16u * l, 0, 16);
        if (spin < 2) asm volatile("s_barrier" ::: "memory");
        if (spin == 0) continue;
        if (spin == 1 && l < 2) set_flag(ws, l ? FL_A : FL_L);
        unsigned a = 1u;
#pragma unroll
        for (unsigned i = 0; i < 4; ++i) a &= ld_dev(ws, FLAG(FL_A + 64u * i + l));
        if (__all(a == 1u)) break;
      }
    } else {
      unsigned nonce = 0u, t = 0u;
      asm volatile("s_nop 4\n\tbuffer_load_dword %0, %1, %2, 0 offen sc1" : "+v"(nonce) : "v"(WS_ECHO + 4u * bid), "s"(mk_srd(ws, WS_END)) : "memory");
#pragma unroll
      for (unsigned i = 0; i < 3; ++i)
        touch(t, mk_srd((const char*)wq + (y ? h : 0u) * 256u, y ? 576u * 8192u - h * 256u : 0u), l < 48u ? (l >> 1) * 8192u + 128u * (l & 1u) : OOB, (qblk + 8u * i) * 24u * 8192u);
      touch(t, code_srd(), 128u * l, (31u - (bid >> 3)) * 8192u);
      asm volatile("s_waitcnt vmcnt(4)" : "+v"(nonce) :: "memory");
      st_dev(ws, WS_REQ + 4u * bid, ++nonce);
      asm volatile("s_barrier" ::: "memory");
      for (unsigned spin = 0; spin < (1u << 18); ++spin) {
        if (ld_dev(ws, WS_ECHO + 4u * bid) == nonce) break;
        __builtin_amdgcn_s_sleep(2);
      }
      asm volatile("s_barrier" :: "v"(t) : "memory");
      if (l < 2) set_flag(ws, (l ? FL_A : FL_L) + bid);
    }
  } else {
    const unsigned g = (l >> 3) & 3u, hf = l >> 5, m = 8u * bid + (l & 7u);
    u32x2 a[6];
#pragma unroll
    for (int j = 0; j < 6; ++j) a[j] = *(const u32x2*)(x + (size_t)m * D + 192u * w + 96u * hf + 16 * j + 4u * g);
    f32x2 v[12], s = {0.f, 0.f}, q = {0.f, 0.f};
#pragma unroll
    for (int i = 0; i < 12; ++i) { const unsigned d = a[i >> 1][i & 1]; v[i] = (f32x2){bflo(d), bfhi(d)}; s += v[i]; }
    const float mw = sum8(s.x + s.y) * (1.f / 192.f);
#pragma unroll
    for (int i = 0; i < 12; ++i) { v[i] -= mw; q += v[i] * v[i]; }
    const float qq = sum8(q.x + q.y);
    if (l < 8) red[w * 8u + l] = (f32x2){mw, qq};
    if (tid == 0) *cnt = 0u;
    asm volatile("s_waitcnt lgkmcnt(0)\n\ts_barrier" ::: "memory");
    f32x2 p[3]; float mean = 0.f, m2 = 0.f;
#pragma unroll
    for (int j = 0; j < 3; ++j) { p[j] = red[j * 8 + (l & 7u)]; mean += p[j].x; }
    mean *= (1.f / 3.f);
#pragma unroll
    for (int j = 0; j < 3; ++j) { const float d = p[j].x - mean; m2 += p[j].y + 192.f * d * d; }
    const float var = m2 * (1.f / 576.f), r1 = __builtin_amdgcn_rsqf(var + 1e-5f);
    const float rs = r1 * QSCALE, os = (mw - mean) * rs;
    const srd_t srd = mk_srd(ws, WS_END);
#pragma unroll
    for (int k = 0; k < 3; ++k) {
      u32x4 o;
#pragma unroll
      for (int e = 0; e < 4; ++e) { const f32x2 z = v[4 * k + e] * rs + os; o[e] = cvtpk(z.x, z.y); }
      __builtin_amdgcn_raw_buffer_store_b128(o, srd, WS_XQ + (m >> 4) * 18432u + (6u * w + 3u * hf + k) * 1024u + (16u * g + (m & 15u)) * 16u, 0, 16);
    }
    if (tid < 8) st_dev(ws, WS_RS + 4u * m, __float_as_uint(__builtin_amdgcn_rsqf(var * r1 * r1 + 1e-5f) * (GELU_A / QSCALE)));
    unsigned t = 0u;
    if (y) {
      early_loads(wq, h, 0, w, lds);
      asm volatile("s_waitcnt vmcnt(6)" ::: "memory");
    } else {
#pragma unroll
      for (unsigned i = 0; i < 4; ++i) touch(t, mk_srd(w1, D * D * 2), 128u * l, ((qblk * 3u + w) * 4u + i) * 8192u);
      asm volatile("s_waitcnt vmcnt(4)" ::: "memory");
    }
    if (l == 0 && __hip_atomic_fetch_add(cnt, 1u, __ATOMIC_RELAXED, __HIP_MEMORY_SCOPE_WORKGROUP) == 2u) set_flag(ws, FL_L + bid);
    if (y) {
      for (unsigned i = 1; i < 4; ++i) early_loads(wq, h, i < 3 ? i : w, i < 3 ? w : 3u, lds);
      asm volatile("s_barrier" ::: "memory");
    } else {
      asm volatile("s_barrier\n\ts_waitcnt vmcnt(0)" :: "v"(t) : "memory");
    }
  }
}

DI void mix_unit(char* __restrict__ ws, const u16* __restrict__ w1, const u16* __restrict__ w2, const unsigned j, const unsigned rot,
                 const u16* __restrict__ wq, char* lds) {
  constexpr unsigned ROFF = 147456;
  char* x1f = ws + WS_X1; char* x1nf = ws + WS_X1N;
  const unsigned tid = threadIdx.x, l = tid & 63u, w = rfl(tid >> 6), g = l >> 4, i16 = l & 15u, q4 = (l >> 2) & 3u, p4 = l & 3u;
  const unsigned ldsb = rfl((unsigned)(size_t)lds);
  const srd_t srd1 = mk_srd(w1, D * D * 2), srd2 = mk_srd(w2, D * D * 2), srdn = mk_srd(ws + WS_XQ, S * D * 2), srds = mk_srd(ws + WS_RS, S * 4);
  const unsigned xb = 2u * j - 1u + w;
  const srd_t srdx = mk_srd(ws + WS_XQ + (xb < 128u ? xb : 0u) * 18432u, xb < 128u ? 18432u : 0u);
  const unsigned rho = i16 + 16u - (w == 0 ? 1u : w == 1 ? -1u : w == 2 ? 2u : -2u);
  const unsigned wv = 1152u * (8u * w + ((l >> 2) & 7u)) + 64u * (l >> 5) + 16u * (p4 ^ (2u * (g & 1u))), xl = 16u * l;
  const unsigned n0 = 9u * w + (w & 1u), n8 = (w & 1u) ? 9u * w : 9u * w + 8u;
  const unsigned c0 = (n0 >> 1) * 1024u, c8 = (n8 >> 1) * 1024u + 8u * (n8 & 1u);
  unsigned ra[3];
#pragma unroll
  for (unsigned e = 0; e < 3; ++e)
    ra[e] = ldsb + 9216u * (g >> 1) + 512u * ((e < 2 ? n0 : n8) >> 1) + 256u * (g & 1u) + 64u * q4 + 16u * ((2u * (e < 2 ? e : n8 & 1u) + (p4 >> 1)) ^ (2u * (g & 1u))) + 8u * (p4 & 1u);
  wait_flag(ws, FL_L + ((l & 8u) ? 32u * (j >> 3) + 8u * w + (l & 7u) : (4u * j - 2u + (l & 7u)) & 255u));
  f32x8 acc[9];
  asm volatile(
    "s_nop 4\n\t"
    "s_mov_b32 s40, %[wrot]\n\t"
    "s_mov_b32 s41, 0\n\t"
    "buffer_load_dwordx4 a[0:3], %[xl], %[srdx], s41 offen sc1\n\t"
    "buffer_load_dwordx4 a[4:7], %[xl], %[srdx], s41 offen offset:1024 sc1\n\t"
    "buffer_load_dwordx4 a[8:11], %[xl], %[srdx], s41 offen offset:2048 sc1\n\t"
    "buffer_load_dwordx4 a[12:15], %[xl], %[srdx], s41 offen offset:3072 sc1\n\t"
    "s_mov_b32 s41, 4096\n\t"
    "buffer_load_dwordx4 a[16:19], %[xl], %[srdx], s41 offen sc1\n\t"
    "buffer_load_dwordx4 a[20:23], %[xl], %[srdx], s41 offen offset:1024 sc1\n\t"
    "buffer_load_dwordx4 a[24:27], %[xl], %[srdx], s41 offen offset:2048 sc1\n\t"
    "buffer_load_dwordx4 a[28:31], %[xl], %[srdx], s41 offen offset:3072 sc1\n\t"
    "s_mov_b32 s41, 8192\n\t"
    "buffer_load_dwordx4 a[32:35], %[xl], %[srdx], s41 offen sc1\n\t"
    "buffer_load_dwordx4 a[36:39], %[xl], %[srdx], s41 offen offset:1024 sc1\n\t"
    "buffer_load_dwordx4 a[40:43], %[xl], %[srdx], s41 offen offset:2048 sc1\n\t"
    "buffer_load_dwordx4 a[44:47], %[xl], %[srdx], s41 offen offset:3072 sc1\n\t"
    "s_mov_b32 s41, 12288\n\t"
    "buffer_load_dwordx4 a[48:51], %[xl], %[srdx], s41 offen sc1\n\t"
    "buffer_load_dwordx4 a[52:55], %[xl], %[srdx], s41 offen offset:1024 sc1\n\t"
    "buffer_load_dwordx4 a[56:59], %[xl], %[srdx], s41 offen offset:2048 sc1\n\t"
    "buffer_load_dwordx4 a[60:63], %[xl], %[srdx], s41 offen offset:3072 sc1\n\t"
    "s_mov_b32 s41, 16384\n\t"
    "buffer_load_dwordx4 a[64:67], %[xl], %[srdx], s41 offen sc1\n\t"
    "buffer_load_dwordx4 a[68:71], %[xl], %[srdx], s41 offen offset:1024 sc1\n\t"
    "s_add_u32 m0, %[m0w], 0\n\t"
    "s_add_u32 s41, s40, 0\n\t"
    "buffer_load_dwordx4 %[wv], %[srd1], s41 offen lds\n\t"
    "s_add_u32 m0, %[m0w], 1024\n\t"
    "s_add_u32 s41, s40, 128\n\t"
    "buffer_load_dwordx4 %[wv], %[srd1], s41 offen lds\n\t"
    "s_add_u32 m0, %[m0w], 2048\n\t"
    "s_add_u32 s41, s40, 256\n\t"
    "buffer_load_dwordx4 %[wv], %[srd1], s41 offen lds\n\t"
    "s_add_u32 m0, %[m0w], 3072\n\t"
    "s_add_u32 s41, s40, 384\n\t"
    "buffer_load_dwordx4 %[wv], %[srd1], s41 offen lds\n\t"
    "s_add_u32 m0, %[m0w], 4096\n\t"
    "s_add_u32 s41, s40, 512\n\t"
    "buffer_load_dwordx4 %[wv], %[srd1], s41 offen lds\n\t"
    "s_add_u32 m0, %[m0w], 5120\n\t"
    "s_add_u32 s41, s40, 640\n\t"
    "buffer_load_dwordx4 %[wv], %[srd1], s41 offen lds\n\t"
    "s_add_u32 m0, %[m0w], 6144\n\t"
    "s_add_u32 s41, s40, 768\n\t"
    "buffer_load_dwordx4 %[wv], %[srd1], s41 offen lds\n\t"
    "s_add_u32 m0, %[m0w], 7168\n\t"
    "s_add_u32 s41, s40, 896\n\t"
    "buffer_load_dwordx4 %[wv], %[srd1], s41 offen lds\n\t"
    "s_add_u32 m0, %[m0w], 8192\n\t"
    "s_add_u32 s41, s40, 1024\n\t"
    "buffer_load_dwordx4 %[wv], %[srd1], s41 offen lds\n\t"
    "s_add_u32 s40, s40, 36864\n\t"
    "s_cmp_eq_u32 s40, 663552\n\t"
    "s_cselect_b32 s40, 0, s40\n\t"
    "s_add_u32 m0, %[m0w], 36864\n\t"
    "s_add_u32 s41, s40, 0\n\t"
    "buffer_load_dwordx4 %[wv], %[srd1], s41 offen lds\n\t"
    "s_add_u32 m0, %[m0w], 37888\n\t"
    "s_add_u32 s41, s40, 128\n\t"
    "buffer_load_dwordx4 %[wv], %[srd1], s41 offen lds\n\t"
    "s_add_u32 m0, %[m0w], 38912\n\t"
    "s_add_u32 s41, s40, 256\n\t"
    "buffer_load_dwordx4 %[wv], %[srd1], s41 offen lds\n\t"
    "s_add_u32 m0, %[m0w], 39936\n\t"
    "s_add_u32 s41, s40, 384\n\t"
    "buffer_load_dwordx4 %[wv], %[srd1], s41 offen lds\n\t"
    "s_add_u32 m0, %[m0w], 40960\n\t"
    "s_add_u32 s41, s40, 512\n\t"
    "buffer_load_dwordx4 %[wv], %[srd1], s41 offen lds\n\t"
    "s_add_u32 m0, %[m0w], 41984\n\t"
    "s_add_u32 s41, s40, 640\n\t"
    "buffer_load_dwordx4 %[wv], %[srd1], s41 offen lds\n\t"
    "s_add_u32 m0, %[m0w], 43008\n\t"
    "s_add_u32 s41, s40, 768\n\t"
    "buffer_load_dwordx4 %[wv], %[srd1], s41 offen lds\n\t"
    "s_add_u32 m0, %[m0w], 44032\n\t"
    "s_add_u32 s41, s40, 896\n\t"
    "buffer_load_dwordx4 %[wv], %[srd1], s41 offen lds\n\t"
    "s_add_u32 m0, %[m0w], 45056\n\t"
    "s_add_u32 s41, s40, 1024\n\t"
    "buffer_load_dwordx4 %[wv], %[srd1], s41 offen lds\n\t"
    "s_add_u32 s40, s40, 36864\n\t"
    "s_cmp_eq_u32 s40, 663552\n\t"
    "s_cselect_b32 s40, 0, s40\n\t"
    "s_add_u32 s41, %[xn0], 0\n\t"
    "buffer_load_dwordx4 v[104:107], %[xl], %[srdn], s41 offen sc1\n\t"
    "buffer_load_dwordx4 v[108:111], %[xl], %[srdn], s41 offen offset:1024 sc1\n\t"
    "buffer_load_dwordx4 v[112:115], %[xl], %[srdn], s41 offen offset:2048 sc1\n\t"
    "buffer_load_dwordx4 v[116:119], %[xl], %[srdn], s41 offen offset:3072 sc1\n\t"
    "s_add_u32 s41, %[xn8], 0\n\t"
    "buffer_load_dwordx2 v[120:121], %[xl], %[srdn], s41 offen sc1\n\t"
    "s_add_u32 s41, %[xn0], 18432\n\t"
    "buffer_load_dwordx4 v[122:125], %[xl], %[srdn], s41 offen sc1\n\t"
    "buffer_load_dwordx4 v[126:129], %[xl], %[srdn], s41 offen offset:1024 sc1\n\t"
    "buffer_load_dwordx4 v[130:133], %[xl], %[srdn], s41 offen offset:2048 sc1\n\t"
    "buffer_load_dwordx4 v[134:137], %[xl], %[srdn], s41 offen offset:3072 sc1\n\t"
    "s_add_u32 s41, %[xn8], 18432\n\t"
    "buffer_load_dwordx2 v[138:139], %[xl], %[srdn], s41 offen sc1\n\t"
    "buffer_load_dword v145, %[sco0], %[srds], 0 offen sc1\n\t"
    "buffer_load_dword v146, %[sco1], %[srds], 0 offen sc1\n\t"
    "v_mov_b32 v144, 0xbeeff558\n\t"
    "s_waitcnt vmcnt(30)\n\t"
    "ds_write_b128 %[xw], a[0:3] offset:0\n\t"
    "ds_write_b128 %[xw], a[4:7] offset:1024\n\t"
    "ds_write_b128 %[xw], a[8:11] offset:2048\n\t"
    "ds_write_b128 %[xw], a[12:15] offset:3072\n\t"
    "ds_write_b128 %[xw], a[16:19] offset:4096\n\t"
    "ds_write_b128 %[xw], a[20:23] offset:5120\n\t"
    "ds_write_b128 %[xw], a[24:27] offset:6144\n\t"
    "ds_write_b128 %[xw], a[28:31] offset:7168\n\t"
    "ds_write_b128 %[xw], a[32:35] offset:8192\n\t"
    "ds_write_b128 %[xw], a[36:39] offset:9216\n\t"
    "ds_write_b128 %[xw], a[40:43] offset:10240\n\t"
    "ds_write_b128 %[xw], a[44:47] offset:11264\n\t"
    "ds_write_b128 %[xw], a[48:51] offset:12288\n\t"
    "ds_write_b128 %[xw], a[52:55] offset:13312\n\t"
    "ds_write_b128 %[xw], a[56:59] offset:14336\n\t"
    "ds_write_b128 %[xw], a[60:63] offset:15360\n\t"
    "ds_write_b128 %[xw], a[64:67] offset:16384\n\t"
    "ds_write_b128 %[xw], a[68:71] offset:17408\n\t"
    "s_waitcnt vmcnt(21) lgkmcnt(0)\n\t"
    "s_barrier\n\t"
    "v_mov_b32 v140, %[xb]\n\t"
    "s_cmp_eq_u32 %[wrapat], 0\n\t"
    "s_cselect_b32 s42, 18432, 0\n\t"
    "v_subrev_u32 v140, s42, v140\n\t"
    "ds_read_b128 a[0:3], v140 offset:0\n\t"
    "ds_read_b128 a[4:7], v140 offset:18432\n\t"
    "s_cmp_eq_u32 %[wrapat], 1\n\t"
    "s_cselect_b32 s42, 18432, 0\n\t"
    "v_subrev_u32 v140, s42, v140\n\t"
    "ds_read_b128 a[8:11], v140 offset:1024\n\t"
    "ds_read_b128 a[12:15], v140 offset:19456\n\t"
    "s_cmp_eq_u32 %[wrapat], 2\n\t"
    "s_cselect_b32 s42, 18432, 0\n\t"
    "v_subrev_u32 v140, s42, v140\n\t"
    "ds_read_b128 a[16:19], v140 offset:2048\n\t"
    "ds_read_b128 a[20:23], v140 offset:20480\n\t"
    "s_cmp_eq_u32 %[wrapat], 3\n\t"
    "s_cselect_b32 s42, 18432, 0\n\t"
    "v_subrev_u32 v140, s42, v140\n\t"
    "ds_read_b128 a[24:27], v140 offset:3072\n\t"
    "ds_read_b128 a[28:31], v140 offset:21504\n\t"
    "s_cmp_eq_u32 %[wrapat], 4\n\t"
    "s_cselect_b32 s42, 18432, 0\n\t"
    "v_subrev_u32 v140, s42, v140\n\t"
    "ds_read_b128 a[32:35], v140 offset:4096\n\t"
    "ds_read_b128 a[36:39], v140 offset:22528\n\t"
    "s_cmp_eq_u32 %[wrapat], 5\n\t"
    "s_cselect_b32 s42, 18432, 0\n\t"
    "v_subrev_u32 v140, s42, v140\n\t"
    "ds_read_b128 a[40:43], v140 offset:5120\n\t"
    "ds_read_b128 a[44:47], v140 offset:23552\n\t"
    "s_cmp_eq_u32 %[wrapat], 6\n\t"
    "s_cselect_b32 s42, 18432, 0\n\t"
    "v_subrev_u32 v140, s42, v140\n\t"
    "ds_read_b128 a[48:51], v140 offset:6144\n\t"
    "ds_read_b128 a[52:55], v140 offset:24576\n\t"
    "s_cmp_eq_u32 %[wrapat], 7\n\t"
    "s_cselect_b32 s42, 18432, 0\n\t"
    "v_subrev_u32 v140, s42, v140\n\t"
    "ds_read_b128 a[56:59], v140 offset:7168\n\t"
    "ds_read_b128 a[60:63], v140 offset:25600\n\t"
    "s_cmp_eq_u32 %[wrapat], 8\n\t"
    "s_cselect_b32 s42, 18432, 0\n\t"
    "v_subrev_u32 v140, s42, v140\n\t"
    "ds_read_b128 a[64:67], v140 offset:8192\n\t"
    "ds_read_b128 a[68:71], v140 offset:26624\n\t"
    "s_cmp_eq_u32 %[wrapat], 9\n\t"
    "s_cselect_b32 s42, 18432, 0\n\t"
    "v_subrev_u32 v140, s42, v140\n\t"
    "ds_read_b128 a[72:75], v140 offset:9216\n\t"
    "ds_read_b128 a[76:79], v140 offset:27648\n\t"
    "s_cmp_eq_u32 %[wrapat], 10\n\t"
    "s_cselect_b32 s42, 18432, 0\n\t"
    "v_subrev_u32 v140, s42, v140\n\t"
    "ds_read_b128 a[80:83], v140 offset:10240\n\t"
    "ds_read_b128 a[84:87], v140 offset:28672\n\t"
    "s_cmp_eq_u32 %[wrapat], 11\n\t"
    "s_cselect_b32 s42, 18432, 0\n\t"
    "v_subrev_u32 v140, s42, v140\n\t"
    "ds_read_b128 a[88:91], v140 offset:11264\n\t"
    "ds_read_b128 a[92:95], v140 offset:29696\n\t"
    "s_cmp_eq_u32 %[wrapat], 12\n\t"
    "s_cselect_b32 s42, 18432, 0\n\t"
    "v_subrev_u32 v140, s42, v140\n\t"
    "ds_read_b128 a[96:99], v140 offset:12288\n\t"
    "ds_read_b128 a[100:103], v140 offset:30720\n\t"
    "s_cmp_eq_u32 %[wrapat], 13\n\t"
    "s_cselect_b32 s42, 18432, 0\n\t"
    "v_subrev_u32 v140, s42, v140\n\t"
    "ds_read_b128 a[104:107], v140 offset:13312\n\t"
    "ds_read_b128 a[108:111], v140 offset:31744\n\t"
    "s_cmp_eq_u32 %[wrapat], 14\n\t"
    "s_cselect_b32 s42, 18432, 0\n\t"
    "v_subrev_u32 v140, s42, v140\n\t"
    "ds_read_b128 a[112:115], v140 offset:14336\n\t"
    "ds_read_b128 a[116:119], v140 offset:32768\n\t"
    "s_cmp_eq_u32 %[wrapat], 15\n\t"
    "s_cselect_b32 s42, 18432, 0\n\t"
    "v_subrev_u32 v140, s42, v140\n\t"
    "ds_read_b128 a[120:123], v140 offset:15360\n\t"
    "ds_read_b128 a[124:127], v140 offset:33792\n\t"
    "s_cmp_eq_u32 %[wrapat], 16\n\t"
    "s_cselect_b32 s42, 18432, 0\n\t"
    "v_subrev_u32 v140, s42, v140\n\t"
    "ds_read_b128 a[128:131], v140 offset:16384\n\t"
    "ds_read_b128 a[132:135], v140 offset:34816\n\t"
    "s_cmp_eq_u32 %[wrapat], 17\n\t"
    "s_cselect_b32 s42, 18432, 0\n\t"
    "v_subrev_u32 v140, s42, v140\n\t"
    "ds_read_b128 a[136:139], v140 offset:17408\n\t"
    "ds_read_b128 a[140:143], v140 offset:35840\n\t"
    "ds_read_b64_tr_b16 a[144:145], %[ra00] offset:0\n\t"
    "ds_read_b64_tr_b16 a[146:147], %[ra00] offset:18432\n\t"
    "ds_read_b64_tr_b16 a[148:149], %[ra01] offset:0\n\t"
    "ds_read_b64_tr_b16 a[150:151], %[ra01] offset:18432\n\t"
    "ds_read_b64_tr_b16 a[152:153], %[ra00] offset:512\n\t"
    "ds_read_b64_tr_b16 a[154:155], %[ra00] offset:18944\n\t"
    "ds_read_b64_tr_b16 a[156:157], %[ra01] offset:512\n\t"
    "ds_read_b64_tr_b16 a[158:159], %[ra01] offset:18944\n\t"
    "ds_read_b64_tr_b16 a[160:161], %[ra00] offset:1024\n\t"
    "ds_read_b64_tr_b16 a[162:163], %[ra00] offset:19456\n\t"
    "ds_read_b64_tr_b16 a[164:165], %[ra01] offset:1024\n\t"
    "ds_read_b64_tr_b16 a[166:167], %[ra01] offset:19456\n\t"
    "ds_read_b64_tr_b16 a[168:169], %[ra00] offset:1536\n\t"
    "ds_read_b64_tr_b16 a[170:171], %[ra00] offset:19968\n\t"
    "ds_read_b64_tr_b16 a[172:173], %[ra01] offset:1536\n\t"
    "ds_read_b64_tr_b16 a[174:175], %[ra01] offset:19968\n\t"
    "ds_read_b64_tr_b16 a[176:177], %[ra02] offset:0\n\t"
    "ds_read_b64_tr_b16 a[178:179], %[ra02] offset:18432\n\t"
    "s_waitcnt vmcnt(12) lgkmcnt(0)\n\t"
    "s_barrier\n\t"
    "s_add_u32 m0, %[m0w], 73728\n\t"
    "s_add_u32 s41, s40, 0\n\t"
    "buffer_load_dwordx4 %[wv], %[srd1], s41 offen lds\n\t"
    "s_add_u32 m0, %[m0w], 74752\n\t"
    "s_add_u32 s41, s40, 128\n\t"
    "buffer_load_dwordx4 %[wv], %[srd1], s41 offen lds\n\t"
    "s_add_u32 m0, %[m0w], 75776\n\t"
    "s_add_u32 s41, s40, 256\n\t"
    "buffer_load_dwordx4 %[wv], %[srd1], s41 offen lds\n\t"
    "s_add_u32 m0, %[m0w], 76800\n\t"
    "s_add_u32 s41, s40, 384\n\t"
    "buffer_load_dwordx4 %[wv], %[srd1], s41 offen lds\n\t"
    "s_add_u32 m0, %[m0w], 77824\n\t"
    "s_add_u32 s41, s40, 512\n\t"
    "buffer_load_dwordx4 %[wv], %[srd1], s41 offen lds\n\t"
    "s_add_u32 m0, %[m0w], 78848\n\t"
    "s_add_u32 s41, s40, 640\n\t"
    "buffer_load_dwordx4 %[wv], %[srd1], s41 offen lds\n\t"
    "s_add_u32 m0, %[m0w], 79872\n\t"
    "s_add_u32 s41, s40, 768\n\t"
    "buffer_load_dwordx4 %[wv], %[srd1], s41 offen lds\n\t"
    "s_add_u32 m0, %[m0w], 80896\n\t"
    "s_add_u32 s41, s40, 896\n\t"
    "buffer_load_dwordx4 %[wv], %[srd1], s41 offen lds\n\t"
    "s_add_u32 m0, %[m0w], 81920\n\t"
    "s_add_u32 s41, s40, 1024\n\t"
    "buffer_load_dwordx4 %[wv], %[srd1], s41 offen lds\n\t"
    "s_add_u32 s40, s40, 36864\n\t"
    "s_cmp_eq_u32 s40, 663552\n\t"
    "s_cselect_b32 s40, 0, s40\n\t"
    "s_add_u32 m0, %[m0w], 110592\n\t"
    "s_add_u32 s41, s40, 0\n\t"
    "buffer_load_dwordx4 %[wv], %[srd1], s41 offen lds\n\t"
    "s_add_u32 m0, %[m0w], 111616\n\t"
    "s_add_u32 s41, s40, 128\n\t"
    "buffer_load_dwordx4 %[wv], %[srd1], s41 offen lds\n\t"
    "s_add_u32 m0, %[m0w], 112640\n\t"
    "s_add_u32 s41, s40, 256\n\t"
    "buffer_load_dwordx4 %[wv], %[srd1], s41 offen lds\n\t"
    "s_add_u32 m0, %[m0w], 113664\n\t"
    "s_add_u32 s41, s40, 384\n\t"
    "buffer_load_dwordx4 %[wv], %[srd1], s41 offen lds\n\t"
    "s_add_u32 m0, %[m0w], 114688\n\t"
    "s_add_u32 s41, s40, 512\n\t"
    "buffer_load_dwordx4 %[wv], %[srd1], s41 offen lds\n\t"
    "s_add_u32 m0, %[m0w], 115712\n\t"
    "s_add_u32 s41, s40, 640\n\t"
    "buffer_load_dwordx4 %[wv], %[srd1], s41 offen lds\n\t"
    "s_add_u32 m0, %[m0w], 116736\n\t"
    "s_add_u32 s41, s40, 768\n\t"
    "buffer_load_dwordx4 %[wv], %[srd1], s41 offen lds\n\t"
    "s_add_u32 m0, %[m0w], 117760\n\t"
    "s_add_u32 s41, s40, 896\n\t"
    "buffer_load_dwordx4 %[wv], %[srd1], s41 offen lds\n\t"
    "s_add_u32 m0, %[m0w], 118784\n\t"
    "s_add_u32 s41, s40, 1024\n\t"
    "buffer_load_dwordx4 %[wv], %[srd1], s41 offen lds\n\t"
    "s_add_u32 s40, s40, 36864\n\t"
    "s_cmp_eq_u32 s40, 663552\n\t"
    "s_cselect_b32 s40, 0, s40\n\t"
    "v_mfma_f32_16x16x32_bf16 v[32:35], a[144:147], a[0:3], 0\n\t"
    "ds_read_b64_tr_b16 a[180:181], %[ra00] offset:36864\n\t"
    "ds_read_b64_tr_b16 a[182:183], %[ra00] offset:55296\n\t"
    "s_add_u32 m0, %[m0w], 0\n\t"
    "s_add_u32 s41, s40, 0\n\t"
    "v_mfma_f32_16x16x32_bf16 v[36:39], a[144:147], a[4:7], 0\n\t"
    "ds_read_b64_tr_b16 a[184:185], %[ra01] offset:36864\n\t"
    "buffer_load_dwordx4 %[wv], %[srd1], s41 offen lds\n\t"
    "v_mfma_f32_16x16x32_bf16 v[44:47], a[148:151], a[4:7], 0\n\t"
    "ds_read_b64_tr_b16 a[186:187], %[ra01] offset:55296\n\t"
    "s_add_u32 m0, %[m0w], 1024\n\t"
    "s_add_u32 s41, s40, 128\n\t"
    "v_mfma_f32_16x16x32_bf16 v[40:43], a[148:151], a[0:3], 0\n\t"
    "ds_read_b64_tr_b16 a[188:189], %[ra00] offset:37376\n\t"
    "ds_read_b64_tr_b16 a[190:191], %[ra00] offset:55808\n\t"
    "buffer_load_dwordx4 %[wv], %[srd1], s41 offen lds\n\t"
    "v_mfma_f32_16x16x32_bf16 v[48:51], a[152:155], a[0:3], 0\n\t"
    "ds_read_b64_tr_b16 a[192:193], %[ra01] offset:37376\n\t"
    "s_add_u32 m0, %[m0w], 2048\n\t"
    "s_add_u32 s41, s40, 256\n\t"
    "v_mfma_f32_16x16x32_bf16 v[52:55], a[152:155], a[4:7], 0\n\t"
    "ds_read_b64_tr_b16 a[194:195], %[ra01] offset:55808\n\t"
    "buffer_load_dwordx4 %[wv], %[srd1], s41 offen lds\n\t"
    "v_mfma_f32_16x16x32_bf16 v[60:63], a[156:159], a[4:7], 0\n\t"
    "ds_read_b64_tr_b16 a[196:197], %[ra00] offset:37888\n\t"
    "s_add_u32 m0, %[m0w], 3072\n\t"
    "s_add_u32 s41, s40, 384\n\t"
    "v_mfma_f32_16x16x32_bf16 v[56:59], a[156:159], a[0:3], 0\n\t"
    "ds_read_b64_tr_b16 a[198:199], %[ra00] offset:56320\n\t"
    "ds_read_b64_tr_b16 a[200:201], %[ra01] offset:37888\n\t"
    "buffer_load_dwordx4 %[wv], %[srd1], s41 offen lds\n\t"
    "v_mfma_f32_16x16x32_bf16 v[64:67], a[160:163], a[0:3], 0\n\t"
    "ds_read_b64_tr_b16 a[202:203], %[ra01] offset:56320\n\t"
    "s_add_u32 m0, %[m0w], 4096\n\t"
    "s_add_u32 s41, s40, 512\n\t"
    "v_mfma_f32_16x16x32_bf16 v[68:71], a[160:163], a[4:7], 0\n\t"
    "ds_read_b64_tr_b16 a[204:205], %[ra00] offset:38400\n\t"
    "buffer_load_dwordx4 %[wv], %[srd1], s41 offen lds\n\t"
    "v_mfma_f32_16x16x32_bf16 v[76:79], a[164:167], a[4:7], 0\n\t"
    "ds_read_b64_tr_b16 a[206:207], %[ra00] offset:56832\n\t"
    "ds_read_b64_tr_b16 a[208:209], %[ra01] offset:38400\n\t"
    "s_add_u32 m0, %[m0w], 5120\n\t"
    "s_add_u32 s41, s40, 640\n\t"
    "v_mfma_f32_16x16x32_bf16 v[72:75], a[164:167], a[0:3], 0\n\t"
    "ds_read_b64_tr_b16 a[210:211], %[ra01] offset:56832\n\t"
    "buffer_load_dwordx4 %[wv], %[srd1], s41 offen lds\n\t"
    "v_mfma_f32_16x16x32_bf16 v[80:83], a[168:171], a[0:3], 0\n\t"
    "ds_read_b64_tr_b16 a[212:213], %[ra02] offset:36864\n\t"
    "s_add_u32 m0, %[m0w], 6144\n\t"
    "s_add_u32 s41, s40, 768\n\t"
    "v_mfma_f32_16x16x32_bf16 v[84:87], a[168:171], a[4:7], 0\n\t"
    "ds_read_b64_tr_b16 a[214:215], %[ra02] offset:55296\n\t"
    "buffer_load_dwordx4 %[wv], %[srd1], s41 offen lds\n\t"
    "v_mfma_f32_16x16x32_bf16 v[92:95], a[172:175], a[4:7], 0\n\t"
    "s_add_u32 m0, %[m0w], 7168\n\t"
    "s_add_u32 s41, s40, 896\n\t"
    "v_mfma_f32_16x16x32_bf16 v[88:91], a[172:175], a[0:3], 0\n\t"
    "buffer_load_dwordx4 %[wv], %[srd1], s41 offen lds\n\t"
    "v_mfma_f32_16x16x32_bf16 v[96:99], a[176:179], a[0:3], 0\n\t"
    "s_add_u32 m0, %[m0w], 8192\n\t"
    "s_add_u32 s41, s40, 1024\n\t"
    "v_mfma_f32_16x16x32_bf16 v[100:103], a[176:179], a[4:7], 0\n\t"
    "buffer_load_dwordx4 %[wv], %[srd1], s41 offen lds\n\t"
    "s_add_u32 s40, s40, 36864\n\t"
    "s_cmp_eq_u32 s40, 663552\n\t"
    "s_cselect_b32 s40, 0, s40\n\t"
    "s_waitcnt vmcnt(18) lgkmcnt(0)\n\t"
    "s_barrier\n\t"
    "v_mfma_f32_16x16x32_bf16 v[32:35], a[180:183], a[8:11], v[32:35]\n\t"
    "ds_read_b64_tr_b16 a[144:145], %[ra10] offset:0\n\t"
    "ds_read_b64_tr_b16 a[146:147], %[ra10] offset:18432\n\t"
    "s_add_u32 m0, %[m0w], 36864\n\t"
    "s_add_u32 s41, s40, 0\n\t"
    "v_mfma_f32_16x16x32_bf16 v[36:39], a[180:183], a[12:15], v[36:39]\n\t"
    "ds_read_b64_tr_b16 a[148:149], %[ra11] offset:0\n\t"
    "buffer_load_dwordx4 %[wv], %[srd1], s41 offen lds\n\t"
    "v_mfma_f32_16x16x32_bf16 v[44:47], a[184:187], a[12:15], v[44:47]\n\t"
    "ds_read_b64_tr_b16 a[150:151], %[ra11] offset:18432\n\t"
    "s_add_u32 m0, %[m0w], 37888\n\t"
    "s_add_u32 s41, s40, 128\n\t"
    "v_mfma_f32_16x16x32_bf16 v[40:43], a[184:187], a[8:11], v[40:43]\n\t"
    "ds_read_b64_tr_b16 a[152:153], %[ra10] offset:512\n\t"
    "ds_read_b64_tr_b16 a[154:155], %[ra10] offset:18944\n\t"
    "buffer_load_dwordx4 %[wv], %[srd1], s41 offen lds\n\t"
    "v_mfma_f32_16x16x32_bf16 v[48:51], a[188:191], a[8:11], v[48:51]\n\t"
    "ds_read_b64_tr_b16 a[156:157], %[ra11] offset:512\n\t"
    "s_add_u32 m0, %[m0w], 38912\n\t"
    "s_add_u32 s41, s40, 256\n\t"
    "v_mfma_f32_16x16x32_bf16 v[52:55], a[188:191], a[12:15], v[52:55]\n\t"
    "ds_read_b64_tr_b16 a[158:159], %[ra11] offset:18944\n\t"
    "buffer_load_dwordx4 %[wv], %[srd1], s41 offen lds\n\t"
    "v_mfma_f32_16x16x32_bf16 v[60:63], a[192:195], a[12:15], v[60:63]\n\t"
    "ds_read_b64_tr_b16 a[160:161], %[ra10] offset:1024\n\t"
    "s_add_u32 m0, %[m0w], 39936\n\t"
    "s_add_u32 s41, s40, 384\n\t"
    "v_mfma_f32_16x16x32_bf16 v[56:59], a[192:195], a[8:11], v[56:59]\n\t"
    "ds_read_b64_tr_b16 a[162:163], %[ra10] offset:19456\n\t"
    "ds_read_b64_tr_b16 a[164:165], %[ra11] offset:1024\n\t"
    "buffer_load_dwordx4 %[wv], %[srd1], s41 offen lds\n\t"
    "v_mfma_f32_16x16x32_bf16 v[64:67], a[196:199], a[8:11], v[64:67]\n\t"
    "ds_read_b64_tr_b16 a[166:167], %[ra11] offset:19456\n\t"
    "s_add_u32 m0, %[m0w], 40960\n\t"
    "s_add_u32 s41, s40, 512\n\t"
    "v_mfma_f32_16x16x32_bf16 v[68:71], a[196:199], a[12:15], v[68:71]\n\t"
    "ds_read_b64_tr_b16 a[168:169], %[ra10] offset:1536\n\t"
    "buffer_load_dwordx4 %[wv], %[srd1], s41 offen lds\n\t"
    "v_mfma_f32_16x16x32_bf16 v[76:79], a[200:203], a[12:15], v[76:79]\n\t"
    "ds_read_b64_tr_b16 a[170:171], %[ra10] offset:19968\n\t"
    "ds_read_b64_tr_b16 a[172:173], %[ra11] offset:1536\n\t"
    "s_add_u32 m0, %[m0w], 41984\n\t"
    "s_add_u32 s41, s40, 640\n\t"
    "v_mfma_f32_16x16x32_bf16 v[72:75], a[200:203], a[8:11], v[72:75]\n\t"
    "ds_read_b64_tr_b16 a[174:175], %[ra11] offset:19968\n\t"
    "buffer_load_dwordx4 %[wv], %[srd1], s41 offen lds\n\t"
    "v_mfma_f32_16x16x32_bf16 v[80:83], a[204:207], a[8:11], v[80:83]\n\t"
    "ds_read_b64_tr_b16 a[176:177], %[ra12] offset:0\n\t"
    "s_add_u32 m0, %[m0w], 43008\n\t"
    "s_add_u32 s41, s40, 768\n\t"
    "v_mfma_f32_16x16x32_bf16 v[84:87], a[204:207], a[12:15], v[84:87]\n\t"
    "ds_read_b64_tr_b16 a[178:179], %[ra12] offset:18432\n\t"
    "buffer_load_dwordx4 %[wv], %[srd1], s41 offen lds\n\t"
    "v_mfma_f32_16x16x32_bf16 v[92:95], a[208:211], a[12:15], v[92:95]\n\t"
    "s_add_u32 m0, %[m0w], 44032\n\t"
    "s_add_u32 s41, s40, 896\n\t"
    "v_mfma_f32_16x16x32_bf16 v[88:91], a[208:211], a[8:11], v[88:91]\n\t"
    "buffer_load_dwordx4 %[wv], %[srd1], s41 offen lds\n\t"
    "s_mov_b32 s43, 0\n\t"
    "v_mfma_f32_16x16x32_bf16 v[96:99], a[212:215], a[8:11], v[96:99]\n\t"
    "buffer_load_dword v141, %[tch], %[srd2], s43 offen\n\t"
    "s_add_u32 m0, %[m0w], 45056\n\t"
    "s_add_u32 s41, s40, 1024\n\t"
    "v_mfma_f32_16x16x32_bf16 v[100:103], a[212:215], a[12:15], v[100:103]\n\t"
    "buffer_load_dwordx4 %[wv], %[srd1], s41 offen lds\n\t"
    "s_add_u32 s40, s40, 36864\n\t"
    "s_cmp_eq_u32 s40, 663552\n\t"
    "s_cselect_b32 s40, 0, s40\n\t"
    "s_waitcnt vmcnt(19) lgkmcnt(0)\n\t"
    "s_barrier\n\t"
    "v_mfma_f32_16x16x32_bf16 v[32:35], a[144:147], a[16:19], v[32:35]\n\t"
    "ds_read_b64_tr_b16 a[180:181], %[ra10] offset:36864\n\t"
    "ds_read_b64_tr_b16 a[182:183], %[ra10] offset:55296\n\t"
    "s_add_u32 m0, %[m0w], 73728\n\t"
    "s_add_u32 s41, s40, 0\n\t"
    "v_mfma_f32_16x16x32_bf16 v[36:39], a[144:147], a[20:23], v[36:39]\n\t"
    "ds_read_b64_tr_b16 a[184:185], %[ra11] offset:36864\n\t"
    "buffer_load_dwordx4 %[wv], %[srd1], s41 offen lds\n\t"
    "v_mfma_f32_16x16x32_bf16 v[44:47], a[148:151], a[20:23], v[44:47]\n\t"
    "ds_read_b64_tr_b16 a[186:187], %[ra11] offset:55296\n\t"
    "s_add_u32 m0, %[m0w], 74752\n\t"
    "s_add_u32 s41, s40, 128\n\t"
    "v_mfma_f32_16x16x32_bf16 v[40:43], a[148:151], a[16:19], v[40:43]\n\t"
    "ds_read_b64_tr_b16 a[188:189], %[ra10] offset:37376\n\t"
    "ds_read_b64_tr_b16 a[190:191], %[ra10] offset:55808\n\t"
    "buffer_load_dwordx4 %[wv], %[srd1], s41 offen lds\n\t"
    "v_mfma_f32_16x16x32_bf16 v[48:51], a[152:155], a[16:19], v[48:51]\n\t"
    "ds_read_b64_tr_b16 a[192:193], %[ra11] offset:37376\n\t"
    "s_add_u32 m0, %[m0w], 75776\n\t"
    "s_add_u32 s41, s40, 256\n\t"
    "v_mfma_f32_16x16x32_bf16 v[52:55], a[152:155], a[20:23], v[52:55]\n\t"
    "ds_read_b64_tr_b16 a[194:195], %[ra11] offset:55808\n\t"
    "buffer_load_dwordx4 %[wv], %[srd1], s41 offen lds\n\t"
    "v_mfma_f32_16x16x32_bf16 v[60:63], a[156:159], a[20:23], v[60:63]\n\t"
    "ds_read_b64_tr_b16 a[196:197], %[ra10] offset:37888\n\t"
    "s_add_u32 m0, %[m0w], 76800\n\t"
    "s_add_u32 s41, s40, 384\n\t"
    "v_mfma_f32_16x16x32_bf16 v[56:59], a[156:159], a[16:19], v[56:59]\n\t"
    "ds_read_b64_tr_b16 a[198:199], %[ra10] offset:56320\n\t"
    "ds_read_b64_tr_b16 a[200:201], %[ra11] offset:37888\n\t"
    "buffer_load_dwordx4 %[wv], %[srd1], s41 offen lds\n\t"
    "v_mfma_f32_16x16x32_bf16 v[64:67], a[160:163], a[16:19], v[64:67]\n\t"
    "ds_read_b64_tr_b16 a[202:203], %[ra11] offset:56320\n\t"
    "s_add_u32 m0, %[m0w], 77824\n\t"
    "s_add_u32 s41, s40, 512\n\t"
    "v_mfma_f32_16x16x32_bf16 v[68:71], a[160:163], a[20:23], v[68:71]\n\t"
    "ds_read_b64_tr_b16 a[204:205], %[ra10] offset:38400\n\t"
    "buffer_load_dwordx4 %[wv], %[srd1], s41 offen lds\n\t"
    "v_mfma_f32_16x16x32_bf16 v[76:79], a[164:167], a[20:23], v[76:79]\n\t"
    "ds_read_b64_tr_b16 a[206:207], %[ra10] offset:56832\n\t"
    "ds_read_b64_tr_b16 a[208:209], %[ra11] offset:38400\n\t"
    "s_add_u32 m0, %[m0w], 78848\n\t"
    "s_add_u32 s41, s40, 640\n\t"
    "v_mfma_f32_16x16x32_bf16 v[72:75], a[164:167], a[16:19], v[72:75]\n\t"
    "ds_read_b64_tr_b16 a[210:211], %[ra11] offset:56832\n\t"
    "buffer_load_dwordx4 %[wv], %[srd1], s41 offen lds\n\t"
    "v_mfma_f32_16x16x32_bf16 v[80:83], a[168:171], a[16:19], v[80:83]\n\t"
    "ds_read_b64_tr_b16 a[212:213], %[ra12] offset:36864\n\t"
    "s_add_u32 m0, %[m0w], 79872\n\t"
    "s_add_u32 s41, s40, 768\n\t"
    "v_mfma_f32_16x16x32_bf16 v[84:87], a[168:171], a[20:23], v[84:87]\n\t"
    "ds_read_b64_tr_b16 a[214:215], %[ra12] offset:55296\n\t"
    "buffer_load_dwordx4 %[wv], %[srd1], s41 offen lds\n\t"
    "v_mfma_f32_16x16x32_bf16 v[92:95], a[172:175], a[20:23], v[92:95]\n\t"
    "s_add_u32 m0, %[m0w], 80896\n\t"
    "s_add_u32 s41, s40, 896\n\t"
    "v_mfma_f32_16x16x32_bf16 v[88:91], a[172:175], a[16:19], v[88:91]\n\t"
    "buffer_load_dwordx4 %[wv], %[srd1], s41 offen lds\n\t"
    "v_mfma_f32_16x16x32_bf16 v[96:99], a[176:179], a[16:19], v[96:99]\n\t"
    "s_add_u32 m0, %[m0w], 81920\n\t"
    "s_add_u32 s41, s40, 1024\n\t"
    "v_mfma_f32_16x16x32_bf16 v[100:103], a[176:179], a[20:23], v[100:103]\n\t"
    "buffer_load_dwordx4 %[wv], %[srd1], s41 offen lds\n\t"
    "s_add_u32 s40, s40, 36864\n\t"
    "s_cmp_eq_u32 s40, 663552\n\t"
    "s_cselect_b32 s40, 0, s40\n\t"
    "s_waitcnt vmcnt(19) lgkmcnt(0)\n\t"
    "s_barrier\n\t"
    "v_mfma_f32_16x16x32_bf16 v[32:35], a[180:183], a[24:27], v[32:35]\n\t"
    "ds_read_b64_tr_b16 a[144:145], %[ra00] offset:0\n\t"
    "ds_read_b64_tr_b16 a[146:147], %[ra00] offset:18432\n\t"
    "s_add_u32 m0, %[m0w], 110592\n\t"
    "s_add_u32 s41, s40, 0\n\t"
    "v_mfma_f32_16x16x32_bf16 v[36:39], a[180:183], a[28:31], v[36:39]\n\t"
    "ds_read_b64_tr_b16 a[148:149], %[ra01] offset:0\n\t"
    "buffer_load_dwordx4 %[wv], %[srd1], s41 offen lds\n\t"
    "v_mfma_f32_16x16x32_bf16 v[44:47], a[184:187], a[28:31], v[44:47]\n\t"
    "ds_read_b64_tr_b16 a[150:151], %[ra01] offset:18432\n\t"
    "s_add_u32 m0, %[m0w], 111616\n\t"
    "s_add_u32 s41, s40, 128\n\t"
    "v_mfma_f32_16x16x32_bf16 v[40:43], a[184:187], a[24:27], v[40:43]\n\t"
    "ds_read_b64_tr_b16 a[152:153], %[ra00] offset:512\n\t"
    "ds_read_b64_tr_b16 a[154:155], %[ra00] offset:18944\n\t"
    "buffer_load_dwordx4 %[wv], %[srd1], s41 offen lds\n\t"
    "v_mfma_f32_16x16x32_bf16 v[48:51], a[188:191], a[24:27], v[48:51]\n\t"
    "ds_read_b64_tr_b16 a[156:157], %[ra01] offset:512\n\t"
    "s_add_u32 m0, %[m0w], 112640\n\t"
    "s_add_u32 s41, s40, 256\n\t"
    "v_mfma_f32_16x16x32_bf16 v[52:55], a[188:191], a[28:31], v[52:55]\n\t"
    "ds_read_b64_tr_b16 a[158:159], %[ra01] offset:18944\n\t"
    "buffer_load_dwordx4 %[wv], %[srd1], s41 offen lds\n\t"
    "v_mfma_f32_16x16x32_bf16 v[60:63], a[192:195], a[28:31], v[60:63]\n\t"
    "ds_read_b64_tr_b16 a[160:161], %[ra00] offset:1024\n\t"
    "s_add_u32 m0, %[m0w], 113664\n\t"
    "s_add_u32 s41, s40, 384\n\t"
    "v_mfma_f32_16x16x32_bf16 v[56:59], a[192:195], a[24:27], v[56:59]\n\t"
    "ds_read_b64_tr_b16 a[162:163], %[ra00] offset:19456\n\t"
    "ds_read_b64_tr_b16 a[164:165], %[ra01] offset:1024\n\t"
    "buffer_load_dwordx4 %[wv], %[srd1], s41 offen lds\n\t"
    "v_mfma_f32_16x16x32_bf16 v[64:67], a[196:199], a[24:27], v[64:67]\n\t"
    "ds_read_b64_tr_b16 a[166:167], %[ra01] offset:19456\n\t"
    "s_add_u32 m0, %[m0w], 114688\n\t"
    "s_add_u32 s41, s40, 512\n\t"
    "v_mfma_f32_16x16x32_bf16 v[68:71], a[196:199], a[28:31], v[68:71]\n\t"
    "ds_read_b64_tr_b16 a[168:169], %[ra00] offset:1536\n\t"
    "buffer_load_dwordx4 %[wv], %[srd1], s41 offen lds\n\t"
    "v_mfma_f32_16x16x32_bf16 v[76:79], a[200:203], a[28:31], v[76:79]\n\t"
    "ds_read_b64_tr_b16 a[170:171], %[ra00] offset:19968\n\t"
    "ds_read_b64_tr_b16 a[172:173], %[ra01] offset:1536\n\t"
    "s_add_u32 m0, %[m0w], 115712\n\t"
    "s_add_u32 s41, s40, 640\n\t"
    "v_mfma_f32_16x16x32_bf16 v[72:75], a[200:203], a[24:27], v[72:75]\n\t"
    "ds_read_b64_tr_b16 a[174:175], %[ra01] offset:19968\n\t"
    "buffer_load_dwordx4 %[wv], %[srd1], s41 offen lds\n\t"
    "v_mfma_f32_16x16x32_bf16 v[80:83], a[204:207], a[24:27], v[80:83]\n\t"
    "ds_read_b64_tr_b16 a[176:177], %[ra02] offset:0\n\t"
    "s_add_u32 m0, %[m0w], 116736\n\t"
    "s_add_u32 s41, s40, 768\n\t"
    "v_mfma_f32_16x16x32_bf16 v[84:87], a[204:207], a[28:31], v[84:87]\n\t"
    "ds_read_b64_tr_b16 a[178:179], %[ra02] offset:18432\n\t"
    "buffer_load_dwordx4 %[wv], %[srd1], s41 offen lds\n\t"
    "v_mfma_f32_16x16x32_bf16 v[92:95], a[208:211], a[28:31], v[92:95]\n\t"
    "s_add_u32 m0, %[m0w], 117760\n\t"
    "s_add_u32 s41, s40, 896\n\t"
    "v_mfma_f32_16x16x32_bf16 v[88:91], a[208:211], a[24:27], v[88:91]\n\t"
    "buffer_load_dwordx4 %[wv], %[srd1], s41 offen lds\n\t"
    "v_mfma_f32_16x16x32_bf16 v[96:99], a[212:215], a[24:27], v[96:99]\n\t"
    "s_add_u32 m0, %[m0w], 118784\n\t"
    "s_add_u32 s41, s40, 1024\n\t"
    "v_mfma_f32_16x16x32_bf16 v[100:103], a[212:215], a[28:31], v[100:103]\n\t"
    "buffer_load_dwordx4 %[wv], %[srd1], s41 offen lds\n\t"
    "s_add_u32 s40, s40, 36864\n\t"
    "s_cmp_eq_u32 s40, 663552\n\t"
    "s_cselect_b32 s40, 0, s40\n\t"
    "s_waitcnt vmcnt(18) lgkmcnt(0)\n\t"
    "s_barrier\n\t"
    "v_mfma_f32_16x16x32_bf16 v[32:35], a[144:147], a[32:35], v[32:35]\n\t"
    "ds_read_b64_tr_b16 a[180:181], %[ra00] offset:36864\n\t"
    "ds_read_b64_tr_b16 a[182:183], %[ra00] offset:55296\n\t"
    "s_add_u32 m0, %[m0w], 0\n\t"
    "s_add_u32 s41, s40, 0\n\t"
    "v_mfma_f32_16x16x32_bf16 v[36:39], a[144:147], a[36:39], v[36:39]\n\t"
    "ds_read_b64_tr_b16 a[184:185], %[ra01] offset:36864\n\t"
    "buffer_load_dwordx4 %[wv], %[srd1], s41 offen lds\n\t"
    "v_mfma_f32_16x16x32_bf16 v[44:47], a[148:151], a[36:39], v[44:47]\n\t"
    "ds_read_b64_tr_b16 a[186:187], %[ra01] offset:55296\n\t"
    "s_add_u32 m0, %[m0w], 1024\n\t"
    "s_add_u32 s41, s40, 128\n\t"
    "v_mfma_f32_16x16x32_bf16 v[40:43], a[148:151], a[32:35], v[40:43]\n\t"
    "ds_read_b64_tr_b16 a[188:189], %[ra00] offset:37376\n\t"
    "ds_read_b64_tr_b16 a[190:191], %[ra00] offset:55808\n\t"
    "buffer_load_dwordx4 %[wv], %[srd1], s41 offen lds\n\t"
    "v_mfma_f32_16x16x32_bf16 v[48:51], a[152:155], a[32:35], v[48:51]\n\t"
    "ds_read_b64_tr_b16 a[192:193], %[ra01] offset:37376\n\t"
    "s_add_u32 m0, %[m0w], 2048\n\t"
    "s_add_u32 s41, s40, 256\n\t"
    "v_mfma_f32_16x16x32_bf16 v[52:55], a[152:155], a[36:39], v[52:55]\n\t"
    "ds_read_b64_tr_b16 a[194:195], %[ra01] offset:55808\n\t"
    "buffer_load_dwordx4 %[wv], %[srd1], s41 offen lds\n\t"
    "v_mfma_f32_16x16x32_bf16 v[60:63], a[156:159], a[36:39], v[60:63]\n\t"
    "ds_read_b64_tr_b16 a[196:197], %[ra00] offset:37888\n\t"
    "s_add_u32 m0, %[m0w], 3072\n\t"
    "s_add_u32 s41, s40, 384\n\t"
    "v_mfma_f32_16x16x32_bf16 v[56:59], a[156:159], a[32:35], v[56:59]\n\t"
    "ds_read_b64_tr_b16 a[198:199], %[ra00] offset:56320\n\t"
    "ds_read_b64_tr_b16 a[200:201], %[ra01] offset:37888\n\t"
    "buffer_load_dwordx4 %[wv], %[srd1], s41 offen lds\n\t"
    "v_mfma_f32_16x16x32_bf16 v[64:67], a[160:163], a[32:35], v[64:67]\n\t"
    "ds_read_b64_tr_b16 a[202:203], %[ra01] offset:56320\n\t"
    "s_add_u32 m0, %[m0w], 4096\n\t"
    "s_add_u32 s41, s40, 512\n\t"
    "v_mfma_f32_16x16x32_bf16 v[68:71], a[160:163], a[36:39], v[68:71]\n\t"
    "ds_read_b64_tr_b16 a[204:205], %[ra00] offset:38400\n\t"
    "buffer_load_dwordx4 %[wv], %[srd1], s41 offen lds\n\t"
    "v_mfma_f32_16x16x32_bf16 v[76:79], a[164:167], a[36:39], v[76:79]\n\t"
    "ds_read_b64_tr_b16 a[206:207], %[ra00] offset:56832\n\t"
    "ds_read_b64_tr_b16 a[208:209], %[ra01] offset:38400\n\t"
    "s_add_u32 m0, %[m0w], 5120\n\t"
    "s_add_u32 s41, s40, 640\n\t"
    "v_mfma_f32_16x16x32_bf16 v[72:75], a[164:167], a[32:35], v[72:75]\n\t"
    "ds_read_b64_tr_b16 a[210:211], %[ra01] offset:56832\n\t"
    "buffer_load_dwordx4 %[wv], %[srd1], s41 offen lds\n\t"
    "v_mfma_f32_16x16x32_bf16 v[80:83], a[168:171], a[32:35], v[80:83]\n\t"
    "ds_read_b64_tr_b16 a[212:213], %[ra02] offset:36864\n\t"
    "s_add_u32 m0, %[m0w], 6144\n\t"
    "s_add_u32 s41, s40, 768\n\t"
    "v_mfma_f32_16x16x32_bf16 v[84:87], a[168:171], a[36:39], v[84:87]\n\t"
    "ds_read_b64_tr_b16 a[214:215], %[ra02] offset:55296\n\t"
    "buffer_load_dwordx4 %[wv], %[srd1], s41 offen lds\n\t"
    "v_mfma_f32_16x16x32_bf16 v[92:95], a[172:175], a[36:39], v[92:95]\n\t"
    "s_add_u32 m0, %[m0w], 7168\n\t"
    "s_add_u32 s41, s40, 896\n\t"
    "v_mfma_f32_16x16x32_bf16 v[88:91], a[172:175], a[32:35], v[88:91]\n\t"
    "buffer_load_dwordx4 %[wv], %[srd1], s41 offen lds\n\t"
    "s_mov_b32 s43, 8192\n\t"
    "v_mfma_f32_16x16x32_bf16 v[96:99], a[176:179], a[32:35], v[96:99]\n\t"
    "buffer_load_dword v141, %[tch], %[srd2], s43 offen\n\t"
    "s_add_u32 m0, %[m0w], 8192\n\t"
    "s_add_u32 s41, s40, 1024\n\t"
    "v_mfma_f32_16x16x32_bf16 v[100:103], a[176:179], a[36:39], v[100:103]\n\t"
    "buffer_load_dwordx4 %[wv], %[srd1], s41 offen lds\n\t"
    "s_add_u32 s40, s40, 36864\n\t"
    "s_cmp_eq_u32 s40, 663552\n\t"
    "s_cselect_b32 s40, 0, s40\n\t"
    "s_waitcnt vmcnt(19) lgkmcnt(0)\n\t"
    "s_barrier\n\t"
    "v_mfma_f32_16x16x32_bf16 v[32:35], a[180:183], a[40:43], v[32:35]\n\t"
    "ds_read_b64_tr_b16 a[144:145], %[ra10] offset:0\n\t"
    "ds_read_b64_tr_b16 a[146:147], %[ra10] offset:18432\n\t"
    "s_add_u32 m0, %[m0w], 36864\n\t"
    "s_add_u32 s41, s40, 0\n\t"
    "v_mfma_f32_16x16x32_bf16 v[36:39], a[180:183], a[44:47], v[36:39]\n\t"
    "ds_read_b64_tr_b16 a[148:149], %[ra11] offset:0\n\t"
    "buffer_load_dwordx4 %[wv], %[srd1], s41 offen lds\n\t"
    "v_mfma_f32_16x16x32_bf16 v[44:47], a[184:187], a[44:47], v[44:47]\n\t"
    "ds_read_b64_tr_b16 a[150:151], %[ra11] offset:18432\n\t"
    "s_add_u32 m0, %[m0w], 37888\n\t"
    "s_add_u32 s41, s40, 128\n\t"
    "v_mfma_f32_16x16x32_bf16 v[40:43], a[184:187], a[40:43], v[40:43]\n\t"
    "ds_read_b64_tr_b16 a[152:153], %[ra10] offset:512\n\t"
    "ds_read_b64_tr_b16 a[154:155], %[ra10] offset:18944\n\t"
    "buffer_load_dwordx4 %[wv], %[srd1], s41 offen lds\n\t"
    "v_mfma_f32_16x16x32_bf16 v[48:51], a[188:191], a[40:43], v[48:51]\n\t"
    "ds_read_b64_tr_b16 a[156:157], %[ra11] offset:512\n\t"
    "s_add_u32 m0, %[m0w], 38912\n\t"
    "s_add_u32 s41, s40, 256\n\t"
    "v_mfma_f32_16x16x32_bf16 v[52:55], a[188:191], a[44:47], v[52:55]\n\t"
    "ds_read_b64_tr_b16 a[158:159], %[ra11] offset:18944\n\t"
    "buffer_load_dwordx4 %[wv], %[srd1], s41 offen lds\n\t"
    "v_mfma_f32_16x16x32_bf16 v[60:63], a[192:195], a[44:47], v[60:63]\n\t"
    "ds_read_b64_tr_b16 a[160:161], %[ra10] offset:1024\n\t"
    "s_add_u32 m0, %[m0w], 39936\n\t"
    "s_add_u32 s41, s40, 384\n\t"
    "v_mfma_f32_16x16x32_bf16 v[56:59], a[192:195], a[40:43], v[56:59]\n\t"
    "ds_read_b64_tr_b16 a[162:163], %[ra10] offset:19456\n\t"
    "ds_read_b64_tr_b16 a[164:165], %[ra11] offset:1024\n\t"
    "buffer_load_dwordx4 %[wv], %[srd1], s41 offen lds\n\t"
    "v_mfma_f32_16x16x32_bf16 v[64:67], a[196:199], a[40:43], v[64:67]\n\t"
    "ds_read_b64_tr_b16 a[166:167], %[ra11] offset:19456\n\t"
    "s_add_u32 m0, %[m0w], 40960\n\t"
    "s_add_u32 s41, s40, 512\n\t"
    "v_mfma_f32_16x16x32_bf16 v[68:71], a[196:199], a[44:47], v[68:71]\n\t"
    "ds_read_b64_tr_b16 a[168:169], %[ra10] offset:1536\n\t"
    "buffer_load_dwordx4 %[wv], %[srd1], s41 offen lds\n\t"
    "v_mfma_f32_16x16x32_bf16 v[76:79], a[200:203], a[44:47], v[76:79]\n\t"
    "ds_read_b64_tr_b16 a[170:171], %[ra10] offset:19968\n\t"
    "ds_read_b64_tr_b16 a[172:173], %[ra11] offset:1536\n\t"
    "s_add_u32 m0, %[m0w], 41984\n\t"
    "s_add_u32 s41, s40, 640\n\t"
    "v_mfma_f32_16x16x32_bf16 v[72:75], a[200:203], a[40:43], v[72:75]\n\t"
    "ds_read_b64_tr_b16 a[174:175], %[ra11] offset:19968\n\t"
    "buffer_load_dwordx4 %[wv], %[srd1], s41 offen lds\n\t"
    "v_mfma_f32_16x16x32_bf16 v[80:83], a[204:207], a[40:43], v[80:83]\n\t"
    "ds_read_b64_tr_b16 a[176:177], %[ra12] offset:0\n\t"
    "s_add_u32 m0, %[m0w], 43008\n\t"
    "s_add_u32 s41, s40, 768\n\t"
    "v_mfma_f32_16x16x32_bf16 v[84:87], a[204:207], a[44:47], v[84:87]\n\t"
    "ds_read_b64_tr_b16 a[178:179], %[ra12] offset:18432\n\t"
    "buffer_load_dwordx4 %[wv], %[srd1], s41 offen lds\n\t"
    "v_mfma_f32_16x16x32_bf16 v[92:95], a[208:211], a[44:47], v[92:95]\n\t"
    "s_add_u32 m0, %[m0w], 44032\n\t"
    "s_add_u32 s41, s40, 896\n\t"
    "v_mfma_f32_16x16x32_bf16 v[88:91], a[208:211], a[40:43], v[88:91]\n\t"
    "buffer_load_dwordx4 %[wv], %[srd1], s41 offen lds\n\t"
    "v_mfma_f32_16x16x32_bf16 v[96:99], a[212:215], a[40:43], v[96:99]\n\t"
    "s_add_u32 m0, %[m0w], 45056\n\t"
    "s_add_u32 s41, s40, 1024\n\t"
    "v_mfma_f32_16x16x32_bf16 v[100:103], a[212:215], a[44:47], v[100:103]\n\t"
    "buffer_load_dwordx4 %[wv], %[srd1], s41 offen lds\n\t"
    "s_add_u32 s40, s40, 36864\n\t"
    "s_cmp_eq_u32 s40, 663552\n\t"
    "s_cselect_b32 s40, 0, s40\n\t"
    "s_waitcnt vmcnt(19) lgkmcnt(0)\n\t"
    "s_barrier\n\t"
    "v_mfma_f32_16x16x32_bf16 v[32:35], a[144:147], a[48:51], v[32:35]\n\t"
    "ds_read_b64_tr_b16 a[180:181], %[ra10] offset:36864\n\t"
    "ds_read_b64_tr_b16 a[182:183], %[ra10] offset:55296\n\t"
    "s_add_u32 m0, %[m0w], 73728\n\t"
    "s_add_u32 s41, s40, 0\n\t"
    "v_mfma_f32_16x16x32_bf16 v[36:39], a[144:147], a[52:55], v[36:39]\n\t"
    "ds_read_b64_tr_b16 a[184:185], %[ra11] offset:36864\n\t"
    "buffer_load_dwordx4 %[wv], %[srd1], s41 offen lds\n\t"
    "v_mfma_f32_16x16x32_bf16 v[44:47], a[148:151], a[52:55], v[44:47]\n\t"
    "ds_read_b64_tr_b16 a[186:187], %[ra11] offset:55296\n\t"
    "s_add_u32 m0, %[m0w], 74752\n\t"
    "s_add_u32 s41, s40, 128\n\t"
    "v_mfma_f32_16x16x32_bf16 v[40:43], a[148:151], a[48:51], v[40:43]\n\t"
    "ds_read_b64_tr_b16 a[188:189], %[ra10] offset:37376\n\t"
    "ds_read_b64_tr_b16 a[190:191], %[ra10] offset:55808\n\t"
    "buffer_load_dwordx4 %[wv], %[srd1], s41 offen lds\n\t"
    "v_mfma_f32_16x16x32_bf16 v[48:51], a[152:155], a[48:51], v[48:51]\n\t"
    "ds_read_b64_tr_b16 a[192:193], %[ra11] offset:37376\n\t"
    "s_add_u32 m0, %[m0w], 75776\n\t"
    "s_add_u32 s41, s40, 256\n\t"
    "v_mfma_f32_16x16x32_bf16 v[52:55], a[152:155], a[52:55], v[52:55]\n\t"
    "ds_read_b64_tr_b16 a[194:195], %[ra11] offset:55808\n\t"
    "buffer_load_dwordx4 %[wv], %[srd1], s41 offen lds\n\t"
    "v_mfma_f32_16x16x32_bf16 v[60:63], a[156:159], a[52:55], v[60:63]\n\t"
    "ds_read_b64_tr_b16 a[196:197], %[ra10] offset:37888\n\t"
    "s_add_u32 m0, %[m0w], 76800\n\t"
    "s_add_u32 s41, s40, 384\n\t"
    "v_mfma_f32_16x16x32_bf16 v[56:59], a[156:159], a[48:51], v[56:59]\n\t"
    "ds_read_b64_tr_b16 a[198:199], %[ra10] offset:56320\n\t"
    "ds_read_b64_tr_b16 a[200:201], %[ra11] offset:37888\n\t"
    "buffer_load_dwordx4 %[wv], %[srd1], s41 offen lds\n\t"
    "v_mfma_f32_16x16x32_bf16 v[64:67], a[160:163], a[48:51], v[64:67]\n\t"
    "ds_read_b64_tr_b16 a[202:203], %[ra11] offset:56320\n\t"
    "s_add_u32 m0, %[m0w], 77824\n\t"
    "s_add_u32 s41, s40, 512\n\t"
    "v_mfma_f32_16x16x32_bf16 v[68:71], a[160:163], a[52:55], v[68:71]\n\t"
    "ds_read_b64_tr_b16 a[204:205], %[ra10] offset:38400\n\t"
    "buffer_load_dwordx4 %[wv], %[srd1], s41 offen lds\n\t"
    "v_mfma_f32_16x16x32_bf16 v[76:79], a[164:167], a[52:55], v[76:79]\n\t"
    "ds_read_b64_tr_b16 a[206:207], %[ra10] offset:56832\n\t"
    "ds_read_b64_tr_b16 a[208:209], %[ra11] offset:38400\n\t"
    "s_add_u32 m0, %[m0w], 78848\n\t"
    "s_add_u32 s41, s40, 640\n\t"
    "v_mfma_f32_16x16x32_bf16 v[72:75], a[164:167], a[48:51], v[72:75]\n\t"
    "ds_read_b64_tr_b16 a[210:211], %[ra11] offset:56832\n\t"
    "buffer_load_dwordx4 %[wv], %[srd1], s41 offen lds\n\t"
    "v_mfma_f32_16x16x32_bf16 v[80:83], a[168:171], a[48:51], v[80:83]\n\t"
    "ds_read_b64_tr_b16 a[212:213], %[ra12] offset:36864\n\t"
    "s_add_u32 m0, %[m0w], 79872\n\t"
    "s_add_u32 s41, s40, 768\n\t"
    "v_mfma_f32_16x16x32_bf16 v[84:87], a[168:171], a[52:55], v[84:87]\n\t"
    "ds_read_b64_tr_b16 a[214:215], %[ra12] offset:55296\n\t"
    "buffer_load_dwordx4 %[wv], %[srd1], s41 offen lds\n\t"
    "v_mfma_f32_16x16x32_bf16 v[92:95], a[172:175], a[52:55], v[92:95]\n\t"
    "s_add_u32 m0, %[m0w], 80896\n\t"
    "s_add_u32 s41, s40, 896\n\t"
    "v_mfma_f32_16x16x32_bf16 v[88:91], a[172:175], a[48:51], v[88:91]\n\t"
    "buffer_load_dwordx4 %[wv], %[srd1], s41 offen lds\n\t"
    "v_mfma_f32_16x16x32_bf16 v[96:99], a[176:179], a[48:51], v[96:99]\n\t"
    "s_add_u32 m0, %[m0w], 81920\n\t"
    "s_add_u32 s41, s40, 1024\n\t"
    "v_mfma_f32_16x16x32_bf16 v[100:103], a[176:179], a[52:55], v[100:103]\n\t"
    "buffer_load_dwordx4 %[wv], %[srd1], s41 offen lds\n\t"
    "s_add_u32 s40, s40, 36864\n\t"
    "s_cmp_eq_u32 s40, 663552\n\t"
    "s_cselect_b32 s40, 0, s40\n\t"
    "s_waitcnt vmcnt(18) lgkmcnt(0)\n\t"
    "s_barrier\n\t"
    "v_mfma_f32_16x16x32_bf16 v[32:35], a[180:183], a[56:59], v[32:35]\n\t"
    "ds_read_b64_tr_b16 a[144:145], %[ra00] offset:0\n\t"
    "ds_read_b64_tr_b16 a[146:147], %[ra00] offset:18432\n\t"
    "s_add_u32 m0, %[m0w], 110592\n\t"
    "s_add_u32 s41, s40, 0\n\t"
    "v_mfma_f32_16x16x32_bf16 v[36:39], a[180:183], a[60:63], v[36:39]\n\t"
    "ds_read_b64_tr_b16 a[148:149], %[ra01] offset:0\n\t"
    "buffer_load_dwordx4 %[wv], %[srd1], s41 offen lds\n\t"
    "v_mfma_f32_16x16x32_bf16 v[44:47], a[184:187], a[60:63], v[44:47]\n\t"
    "ds_read_b64_tr_b16 a[150:151], %[ra01] offset:18432\n\t"
    "s_add_u32 m0, %[m0w], 111616\n\t"
    "s_add_u32 s41, s40, 128\n\t"
    "v_mfma_f32_16x16x32_bf16 v[40:43], a[184:187], a[56:59], v[40:43]\n\t"
    "ds_read_b64_tr_b16 a[152:153], %[ra00] offset:512\n\t"
    "ds_read_b64_tr_b16 a[154:155], %[ra00] offset:18944\n\t"
    "buffer_load_dwordx4 %[wv], %[srd1], s41 offen lds\n\t"
    "v_mfma_f32_16x16x32_bf16 v[48:51], a[188:191], a[56:59], v[48:51]\n\t"
    "ds_read_b64_tr_b16 a[156:157], %[ra01] offset:512\n\t"
    "s_add_u32 m0, %[m0w], 112640\n\t"
    "s_add_u32 s41, s40, 256\n\t"
    "v_mfma_f32_16x16x32_bf16 v[52:55], a[188:191], a[60:63], v[52:55]\n\t"
    "ds_read_b64_tr_b16 a[158:159], %[ra01] offset:18944\n\t"
    "buffer_load_dwordx4 %[wv], %[srd1], s41 offen lds\n\t"
    "v_mfma_f32_16x16x32_bf16 v[60:63], a[192:195], a[60:63], v[60:63]\n\t"
    "ds_read_b64_tr_b16 a[160:161], %[ra00] offset:1024\n\t"
    "s_add_u32 m0, %[m0w], 113664\n\t"
    "s_add_u32 s41, s40, 384\n\t"
    "v_mfma_f32_16x16x32_bf16 v[56:59], a[192:195], a[56:59], v[56:59]\n\t"
    "ds_read_b64_tr_b16 a[162:163], %[ra00] offset:19456\n\t"
    "ds_read_b64_tr_b16 a[164:165], %[ra01] offset:1024\n\t"
    "buffer_load_dwordx4 %[wv], %[srd1], s41 offen lds\n\t"
    "v_mfma_f32_16x16x32_bf16 v[64:67], a[196:199], a[56:59], v[64:67]\n\t"
    "ds_read_b64_tr_b16 a[166:167], %[ra01] offset:19456\n\t"
    "s_add_u32 m0, %[m0w], 114688\n\t"
    "s_add_u32 s41, s40, 512\n\t"
    "v_mfma_f32_16x16x32_bf16 v[68:71], a[196:199], a[60:63], v[68:71]\n\t"
    "ds_read_b64_tr_b16 a[168:169], %[ra00] offset:1536\n\t"
    "buffer_load_dwordx4 %[wv], %[srd1], s41 offen lds\n\t"
    "v_mfma_f32_16x16x32_bf16 v[76:79], a[200:203], a[60:63], v[76:79]\n\t"
    "ds_read_b64_tr_b16 a[170:171], %[ra00] offset:19968\n\t"
    "ds_read_b64_tr_b16 a[172:173], %[ra01] offset:1536\n\t"
    "s_add_u32 m0, %[m0w], 115712\n\t"
    "s_add_u32 s41, s40, 640\n\t"
    "v_mfma_f32_16x16x32_bf16 v[72:75], a[200:203], a[56:59], v[72:75]\n\t"
    "ds_read_b64_tr_b16 a[174:175], %[ra01] offset:19968\n\t"
    "buffer_load_dwordx4 %[wv], %[srd1], s41 offen lds\n\t"
    "v_mfma_f32_16x16x32_bf16 v[80:83], a[204:207], a[56:59], v[80:83]\n\t"
    "ds_read_b64_tr_b16 a[176:177], %[ra02] offset:0\n\t"
    "s_add_u32 m0, %[m0w], 116736\n\t"
    "s_add_u32 s41, s40, 768\n\t"
    "v_mfma_f32_16x16x32_bf16 v[84:87], a[204:207], a[60:63], v[84:87]\n\t"
    "ds_read_b64_tr_b16 a[178:179], %[ra02] offset:18432\n\t"
    "buffer_load_dwordx4 %[wv], %[srd1], s41 offen lds\n\t"
    "v_mfma_f32_16x16x32_bf16 v[92:95], a[208:211], a[60:63], v[92:95]\n\t"
    "s_add_u32 m0, %[m0w], 117760\n\t"
    "s_add_u32 s41, s40, 896\n\t"
    "v_mfma_f32_16x16x32_bf16 v[88:91], a[208:211], a[56:59], v[88:91]\n\t"
    "buffer_load_dwordx4 %[wv], %[srd1], s41 offen lds\n\t"
    "s_mov_b32 s43, 16384\n\t"
    "v_mfma_f32_16x16x32_bf16 v[96:99], a[212:215], a[56:59], v[96:99]\n\t"
    "buffer_load_dword v141, %[tch], %[srd2], s43 offen\n\t"
    "s_add_u32 m0, %[m0w], 118784\n\t"
    "s_add_u32 s41, s40, 1024\n\t"
    "v_mfma_f32_16x16x32_bf16 v[100:103], a[212:215], a[60:63], v[100:103]\n\t"
    "buffer_load_dwordx4 %[wv], %[srd1], s41 offen lds\n\t"
    "s_add_u32 s40, s40, 36864\n\t"
    "s_cmp_eq_u32 s40, 663552\n\t"
    "s_cselect_b32 s40, 0, s40\n\t"
    "s_waitcnt vmcnt(19) lgkmcnt(0)\n\t"
    "s_barrier\n\t"
    "v_mfma_f32_16x16x32_bf16 v[32:35], a[144:147], a[64:67], v[32:35]\n\t"
    "ds_read_b64_tr_b16 a[180:181], %[ra00] offset:36864\n\t"
    "ds_read_b64_tr_b16 a[182:183], %[ra00] offset:55296\n\t"
    "s_add_u32 m0, %[m0w], 0\n\t"
    "s_add_u32 s41, s40, 0\n\t"
    "v_mfma_f32_16x16x32_bf16 v[36:39], a[144:147], a[68:71], v[36:39]\n\t"
    "ds_read_b64_tr_b16 a[184:185], %[ra01] offset:36864\n\t"
    "buffer_load_dwordx4 %[wv], %[srd1], s41 offen lds\n\t"
    "v_mfma_f32_16x16x32_bf16 v[44:47], a[148:151], a[68:71], v[44:47]\n\t"
    "ds_read_b64_tr_b16 a[186:187], %[ra01] offset:55296\n\t"
    "s_add_u32 m0, %[m0w], 1024\n\t"
    "s_add_u32 s41, s40, 128\n\t"
    "v_mfma_f32_16x16x32_bf16 v[40:43], a[148:151], a[64:67], v[40:43]\n\t"
    "ds_read_b64_tr_b16 a[188:189], %[ra00] offset:37376\n\t"
    "ds_read_b64_tr_b16 a[190:191], %[ra00] offset:55808\n\t"
    "buffer_load_dwordx4 %[wv], %[srd1], s41 offen lds\n\t"
    "v_mfma_f32_16x16x32_bf16 v[48:51], a[152:155], a[64:67], v[48:51]\n\t"
    "ds_read_b64_tr_b16 a[192:193], %[ra01] offset:37376\n\t"
    "s_add_u32 m0, %[m0w], 2048\n\t"
    "s_add_u32 s41, s40, 256\n\t"
    "v_mfma_f32_16x16x32_bf16 v[52:55], a[152:155], a[68:71], v[52:55]\n\t"
    "ds_read_b64_tr_b16 a[194:195], %[ra01] offset:55808\n\t"
    "buffer_load_dwordx4 %[wv], %[srd1], s41 offen lds\n\t"
    "v_mfma_f32_16x16x32_bf16 v[60:63], a[156:159], a[68:71], v[60:63]\n\t"
    "ds_read_b64_tr_b16 a[196:197], %[ra00] offset:37888\n\t"
    "s_add_u32 m0, %[m0w], 3072\n\t"
    "s_add_u32 s41, s40, 384\n\t"
    "v_mfma_f32_16x16x32_bf16 v[56:59], a[156:159], a[64:67], v[56:59]\n\t"
    "ds_read_b64_tr_b16 a[198:199], %[ra00] offset:56320\n\t"
    "ds_read_b64_tr_b16 a[200:201], %[ra01] offset:37888\n\t"
    "buffer_load_dwordx4 %[wv], %[srd1], s41 offen lds\n\t"
    "v_mfma_f32_16x16x32_bf16 v[64:67], a[160:163], a[64:67], v[64:67]\n\t"
    "ds_read_b64_tr_b16 a[202:203], %[ra01] offset:56320\n\t"
    "s_add_u32 m0, %[m0w], 4096\n\t"
    "s_add_u32 s41, s40, 512\n\t"
    "v_mfma_f32_16x16x32_bf16 v[68:71], a[160:163], a[68:71], v[68:71]\n\t"
    "ds_read_b64_tr_b16 a[204:205], %[ra00] offset:38400\n\t"
    "buffer_load_dwordx4 %[wv], %[srd1], s41 offen lds\n\t"
    "v_mfma_f32_16x16x32_bf16 v[76:79], a[164:167], a[68:71], v[76:79]\n\t"
    "ds_read_b64_tr_b16 a[206:207], %[ra00] offset:56832\n\t"
    "ds_read_b64_tr_b16 a[208:209], %[ra01] offset:38400\n\t"
    "s_add_u32 m0, %[m0w], 5120\n\t"
    "s_add_u32 s41, s40, 640\n\t"
    "v_mfma_f32_16x16x32_bf16 v[72:75], a[164:167], a[64:67], v[72:75]\n\t"
    "ds_read_b64_tr_b16 a[210:211], %[ra01] offset:56832\n\t"
    "buffer_load_dwordx4 %[wv], %[srd1], s41 offen lds\n\t"
    "v_mfma_f32_16x16x32_bf16 v[80:83], a[168:171], a[64:67], v[80:83]\n\t"
    "ds_read_b64_tr_b16 a[212:213], %[ra02] offset:36864\n\t"
    "s_add_u32 m0, %[m0w], 6144\n\t"
    "s_add_u32 s41, s40, 768\n\t"
    "v_mfma_f32_16x16x32_bf16 v[84:87], a[168:171], a[68:71], v[84:87]\n\t"
    "ds_read_b64_tr_b16 a[214:215], %[ra02] offset:55296\n\t"
    "buffer_load_dwordx4 %[wv], %[srd1], s41 offen lds\n\t"
    "v_mfma_f32_16x16x32_bf16 v[92:95], a[172:175], a[68:71], v[92:95]\n\t"
    "s_add_u32 m0, %[m0w], 7168\n\t"
    "s_add_u32 s41, s40, 896\n\t"
    "v_mfma_f32_16x16x32_bf16 v[88:91], a[172:175], a[64:67], v[88:91]\n\t"
    "buffer_load_dwordx4 %[wv], %[srd1], s41 offen lds\n\t"
    "v_mfma_f32_16x16x32_bf16 v[96:99], a[176:179], a[64:67], v[96:99]\n\t"
    "s_add_u32 m0, %[m0w], 8192\n\t"
    "s_add_u32 s41, s40, 1024\n\t"
    "v_mfma_f32_16x16x32_bf16 v[100:103], a[176:179], a[68:71], v[100:103]\n\t"
    "buffer_load_dwordx4 %[wv], %[srd1], s41 offen lds\n\t"
    "s_add_u32 s40, s40, 36864\n\t"
    "s_cmp_eq_u32 s40, 663552\n\t"
    "s_cselect_b32 s40, 0, s40\n\t"
    "s_waitcnt vmcnt(19) lgkmcnt(0)\n\t"
    "s_barrier\n\t"
    "v_mfma_f32_16x16x32_bf16 v[32:35], a[180:183], a[72:75], v[32:35]\n\t"
    "ds_read_b64_tr_b16 a[144:145], %[ra10] offset:0\n\t"
    "ds_read_b64_tr_b16 a[146:147], %[ra10] offset:18432\n\t"
    "s_add_u32 m0, %[m0w], 36864\n\t"
    "s_add_u32 s41, s40, 0\n\t"
    "v_mfma_f32_16x16x32_bf16 v[36:39], a[180:183], a[76:79], v[36:39]\n\t"
    "ds_read_b64_tr_b16 a[148:149], %[ra11] offset:0\n\t"
    "buffer_load_dwordx4 %[wv], %[srd1], s41 offen lds\n\t"
    "v_mfma_f32_16x16x32_bf16 v[44:47], a[184:187], a[76:79], v[44:47]\n\t"
    "ds_read_b64_tr_b16 a[150:151], %[ra11] offset:18432\n\t"
    "s_add_u32 m0, %[m0w], 37888\n\t"
    "s_add_u32 s41, s40, 128\n\t"
    "v_mfma_f32_16x16x32_bf16 v[40:43], a[184:187], a[72:75], v[40:43]\n\t"
    "ds_read_b64_tr_b16 a[152:153], %[ra10] offset:512\n\t"
    "ds_read_b64_tr_b16 a[154:155], %[ra10] offset:18944\n\t"
    "buffer_load_dwordx4 %[wv], %[srd1], s41 offen lds\n\t"
    "v_mfma_f32_16x16x32_bf16 v[48:51], a[188:191], a[72:75], v[48:51]\n\t"
    "ds_read_b64_tr_b16 a[156:157], %[ra11] offset:512\n\t"
    "s_add_u32 m0, %[m0w], 38912\n\t"
    "s_add_u32 s41, s40, 256\n\t"
    "v_mfma_f32_16x16x32_bf16 v[52:55], a[188:191], a[76:79], v[52:55]\n\t"
    "ds_read_b64_tr_b16 a[158:159], %[ra11] offset:18944\n\t"
    "buffer_load_dwordx4 %[wv], %[srd1], s41 offen lds\n\t"
    "v_mfma_f32_16x16x32_bf16 v[60:63], a[192:195], a[76:79], v[60:63]\n\t"
    "ds_read_b64_tr_b16 a[160:161], %[ra10] offset:1024\n\t"
    "s_add_u32 m0, %[m0w], 39936\n\t"
    "s_add_u32 s41, s40, 384\n\t"
    "v_mfma_f32_16x16x32_bf16 v[56:59], a[192:195], a[72:75], v[56:59]\n\t"
    "ds_read_b64_tr_b16 a[162:163], %[ra10] offset:19456\n\t"
    "ds_read_b64_tr_b16 a[164:165], %[ra11] offset:1024\n\t"
    "buffer_load_dwordx4 %[wv], %[srd1], s41 offen lds\n\t"
    "v_mfma_f32_16x16x32_bf16 v[64:67], a[196:199], a[72:75], v[64:67]\n\t"
    "ds_read_b64_tr_b16 a[166:167], %[ra11] offset:19456\n\t"
    "s_add_u32 m0, %[m0w], 40960\n\t"
    "s_add_u32 s41, s40, 512\n\t"
    "v_mfma_f32_16x16x32_bf16 v[68:71], a[196:199], a[76:79], v[68:71]\n\t"
    "ds_read_b64_tr_b16 a[168:169], %[ra10] offset:1536\n\t"
    "buffer_load_dwordx4 %[wv], %[srd1], s41 offen lds\n\t"
    "v_mfma_f32_16x16x32_bf16 v[76:79], a[200:203], a[76:79], v[76:79]\n\t"
    "ds_read_b64_tr_b16 a[170:171], %[ra10] offset:19968\n\t"
    "ds_read_b64_tr_b16 a[172:173], %[ra11] offset:1536\n\t"
    "s_add_u32 m0, %[m0w], 41984\n\t"
    "s_add_u32 s41, s40, 640\n\t"
    "v_mfma_f32_16x16x32_bf16 v[72:75], a[200:203], a[72:75], v[72:75]\n\t"
    "ds_read_b64_tr_b16 a[174:175], %[ra11] offset:19968\n\t"
    "buffer_load_dwordx4 %[wv], %[srd1], s41 offen lds\n\t"
    "v_mfma_f32_16x16x32_bf16 v[80:83], a[204:207], a[72:75], v[80:83]\n\t"
    "ds_read_b64_tr_b16 a[176:177], %[ra12] offset:0\n\t"
    "s_add_u32 m0, %[m0w], 43008\n\t"
    "s_add_u32 s41, s40, 768\n\t"
    "v_mfma_f32_16x16x32_bf16 v[84:87], a[204:207], a[76:79], v[84:87]\n\t"
    "ds_read_b64_tr_b16 a[178:179], %[ra12] offset:18432\n\t"
    "buffer_load_dwordx4 %[wv], %[srd1], s41 offen lds\n\t"
    "v_mfma_f32_16x16x32_bf16 v[92:95], a[208:211], a[76:79], v[92:95]\n\t"
    "s_add_u32 m0, %[m0w], 44032\n\t"
    "s_add_u32 s41, s40, 896\n\t"
    "v_mfma_f32_16x16x32_bf16 v[88:91], a[208:211], a[72:75], v[88:91]\n\t"
    "buffer_load_dwordx4 %[wv], %[srd1], s41 offen lds\n\t"
    "v_mfma_f32_16x16x32_bf16 v[96:99], a[212:215], a[72:75], v[96:99]\n\t"
    "s_add_u32 m0, %[m0w], 45056\n\t"
    "s_add_u32 s41, s40, 1024\n\t"
    "v_mfma_f32_16x16x32_bf16 v[100:103], a[212:215], a[76:79], v[100:103]\n\t"
    "buffer_load_dwordx4 %[wv], %[srd1], s41 offen lds\n\t"
    "s_add_u32 s40, s40, 36864\n\t"
    "s_cmp_eq_u32 s40, 663552\n\t"
    "s_cselect_b32 s40, 0, s40\n\t"
    "s_waitcnt vmcnt(18) lgkmcnt(0)\n\t"
    "s_barrier\n\t"
    "v_mfma_f32_16x16x32_bf16 v[32:35], a[144:147], a[80:83], v[32:35]\n\t"
    "ds_read_b64_tr_b16 a[180:181], %[ra10] offset:36864\n\t"
    "ds_read_b64_tr_b16 a[182:183], %[ra10] offset:55296\n\t"
    "s_add_u32 m0, %[m0w], 73728\n\t"
    "s_add_u32 s41, s40, 0\n\t"
    "v_mfma_f32_16x16x32_bf16 v[36:39], a[144:147], a[84:87], v[36:39]\n\t"
    "ds_read_b64_tr_b16 a[184:185], %[ra11] offset:36864\n\t"
    "buffer_load_dwordx4 %[wv], %[srd1], s41 offen lds\n\t"
    "v_mfma_f32_16x16x32_bf16 v[44:47], a[148:151], a[84:87], v[44:47]\n\t"
    "ds_read_b64_tr_b16 a[186:187], %[ra11] offset:55296\n\t"
    "s_add_u32 m0, %[m0w], 74752\n\t"
    "s_add_u32 s41, s40, 128\n\t"
    "v_mfma_f32_16x16x32_bf16 v[40:43], a[148:151], a[80:83], v[40:43]\n\t"
    "ds_read_b64_tr_b16 a[188:189], %[ra10] offset:37376\n\t"
    "ds_read_b64_tr_b16 a[190:191], %[ra10] offset:55808\n\t"
    "buffer_load_dwordx4 %[wv], %[srd1], s41 offen lds\n\t"
    "v_mfma_f32_16x16x32_bf16 v[48:51], a[152:155], a[80:83], v[48:51]\n\t"
    "ds_read_b64_tr_b16 a[192:193], %[ra11] offset:37376\n\t"
    "s_add_u32 m0, %[m0w], 75776\n\t"
    "s_add_u32 s41, s40, 256\n\t"
    "v_mfma_f32_16x16x32_bf16 v[52:55], a[152:155], a[84:87], v[52:55]\n\t"
    "ds_read_b64_tr_b16 a[194:195], %[ra11] offset:55808\n\t"
    "buffer_load_dwordx4 %[wv], %[srd1], s41 offen lds\n\t"
    "v_mfma_f32_16x16x32_bf16 v[60:63], a[156:159], a[84:87], v[60:63]\n\t"
    "ds_read_b64_tr_b16 a[196:197], %[ra10] offset:37888\n\t"
    "s_add_u32 m0, %[m0w], 76800\n\t"
    "s_add_u32 s41, s40, 384\n\t"
    "v_mfma_f32_16x16x32_bf16 v[56:59], a[156:159], a[80:83], v[56:59]\n\t"
    "ds_read_b64_tr_b16 a[198:199], %[ra10] offset:56320\n\t"
    "ds_read_b64_tr_b16 a[200:201], %[ra11] offset:37888\n\t"
    "buffer_load_dwordx4 %[wv], %[srd1], s41 offen lds\n\t"
    "v_mfma_f32_16x16x32_bf16 v[64:67], a[160:163], a[80:83], v[64:67]\n\t"
    "ds_read_b64_tr_b16 a[202:203], %[ra11] offset:56320\n\t"
    "s_add_u32 m0, %[m0w], 77824\n\t"
    "s_add_u32 s41, s40, 512\n\t"
    "v_mfma_f32_16x16x32_bf16 v[68:71], a[160:163], a[84:87], v[68:71]\n\t"
    "ds_read_b64_tr_b16 a[204:205], %[ra10] offset:38400\n\t"
    "buffer_load_dwordx4 %[wv], %[srd1], s41 offen lds\n\t"
    "v_mfma_f32_16x16x32_bf16 v[76:79], a[164:167], a[84:87], v[76:79]\n\t"
    "ds_read_b64_tr_b16 a[206:207], %[ra10] offset:56832\n\t"
    "ds_read_b64_tr_b16 a[208:209], %[ra11] offset:38400\n\t"
    "s_add_u32 m0, %[m0w], 78848\n\t"
    "s_add_u32 s41, s40, 640\n\t"
    "v_mfma_f32_16x16x32_bf16 v[72:75], a[164:167], a[80:83], v[72:75]\n\t"
    "ds_read_b64_tr_b16 a[210:211], %[ra11] offset:56832\n\t"
    "buffer_load_dwordx4 %[wv], %[srd1], s41 offen lds\n\t"
    "v_mfma_f32_16x16x32_bf16 v[80:83], a[168:171], a[80:83], v[80:83]\n\t"
    "ds_read_b64_tr_b16 a[212:213], %[ra12] offset:36864\n\t"
    "s_add_u32 m0, %[m0w], 79872\n\t"
    "s_add_u32 s41, s40, 768\n\t"
    "v_mfma_f32_16x16x32_bf16 v[84:87], a[168:171], a[84:87], v[84:87]\n\t"
    "ds_read_b64_tr_b16 a[214:215], %[ra12] offset:55296\n\t"
    "buffer_load_dwordx4 %[wv], %[srd1], s41 offen lds\n\t"
    "v_mfma_f32_16x16x32_bf16 v[92:95], a[172:175], a[84:87], v[92:95]\n\t"
    "s_add_u32 m0, %[m0w], 80896\n\t"
    "s_add_u32 s41, s40, 896\n\t"
    "v_mfma_f32_16x16x32_bf16 v[88:91], a[172:175], a[80:83], v[88:91]\n\t"
    "buffer_load_dwordx4 %[wv], %[srd1], s41 offen lds\n\t"
    "v_mfma_f32_16x16x32_bf16 v[96:99], a[176:179], a[80:83], v[96:99]\n\t"
    "s_add_u32 m0, %[m0w], 81920\n\t"
    "s_add_u32 s41, s40, 1024\n\t"
    "v_mfma_f32_16x16x32_bf16 v[100:103], a[176:179], a[84:87], v[100:103]\n\t"
    "buffer_load_dwordx4 %[wv], %[srd1], s41 offen lds\n\t"
    "s_add_u32 s40, s40, 36864\n\t"
    "s_cmp_eq_u32 s40, 663552\n\t"
    "s_cselect_b32 s40, 0, s40\n\t"
    "s_waitcnt vmcnt(18) lgkmcnt(0)\n\t"
    "s_barrier\n\t"
    "v_mfma_f32_16x16x32_bf16 v[32:35], a[180:183], a[88:91], v[32:35]\n\t"
    "ds_read_b64_tr_b16 a[144:145], %[ra00] offset:0\n\t"
    "ds_read_b64_tr_b16 a[146:147], %[ra00] offset:18432\n\t"
    "s_add_u32 m0, %[m0w], 110592\n\t"
    "s_add_u32 s41, s40, 0\n\t"
    "v_mfma_f32_16x16x32_bf16 v[36:39], a[180:183], a[92:95], v[36:39]\n\t"
    "ds_read_b64_tr_b16 a[148:149], %[ra01] offset:0\n\t"
    "buffer_load_dwordx4 %[wv], %[srd1], s41 offen lds\n\t"
    "v_mfma_f32_16x16x32_bf16 v[44:47], a[184:187], a[92:95], v[44:47]\n\t"
    "ds_read_b64_tr_b16 a[150:151], %[ra01] offset:18432\n\t"
    "s_add_u32 m0, %[m0w], 111616\n\t"
    "s_add_u32 s41, s40, 128\n\t"
    "v_mfma_f32_16x16x32_bf16 v[40:43], a[184:187], a[88:91], v[40:43]\n\t"
    "ds_read_b64_tr_b16 a[152:153], %[ra00] offset:512\n\t"
    "ds_read_b64_tr_b16 a[154:155], %[ra00] offset:18944\n\t"
    "buffer_load_dwordx4 %[wv], %[srd1], s41 offen lds\n\t"
    "v_mfma_f32_16x16x32_bf16 v[48:51], a[188:191], a[88:91], v[48:51]\n\t"
    "ds_read_b64_tr_b16 a[156:157], %[ra01] offset:512\n\t"
    "s_add_u32 m0, %[m0w], 112640\n\t"
    "s_add_u32 s41, s40, 256\n\t"
    "v_mfma_f32_16x16x32_bf16 v[52:55], a[188:191], a[92:95], v[52:55]\n\t"
    "ds_read_b64_tr_b16 a[158:159], %[ra01] offset:18944\n\t"
    "buffer_load_dwordx4 %[wv], %[srd1], s41 offen lds\n\t"
    "v_mfma_f32_16x16x32_bf16 v[60:63], a[192:195], a[92:95], v[60:63]\n\t"
    "ds_read_b64_tr_b16 a[160:161], %[ra00] offset:1024\n\t"
    "s_add_u32 m0, %[m0w], 113664\n\t"
    "s_add_u32 s41, s40, 384\n\t"
    "v_mfma_f32_16x16x32_bf16 v[56:59], a[192:195], a[88:91], v[56:59]\n\t"
    "ds_read_b64_tr_b16 a[162:163], %[ra00] offset:19456\n\t"
    "ds_read_b64_tr_b16 a[164:165], %[ra01] offset:1024\n\t"
    "buffer_load_dwordx4 %[wv], %[srd1], s41 offen lds\n\t"
    "v_mfma_f32_16x16x32_bf16 v[64:67], a[196:199], a[88:91], v[64:67]\n\t"
    "ds_read_b64_tr_b16 a[166:167], %[ra01] offset:19456\n\t"
    "s_add_u32 m0, %[m0w], 114688\n\t"
    "s_add_u32 s41, s40, 512\n\t"
    "v_mfma_f32_16x16x32_bf16 v[68:71], a[196:199], a[92:95], v[68:71]\n\t"
    "ds_read_b64_tr_b16 a[168:169], %[ra00] offset:1536\n\t"
    "buffer_load_dwordx4 %[wv], %[srd1], s41 offen lds\n\t"
    "v_mfma_f32_16x16x32_bf16 v[76:79], a[200:203], a[92:95], v[76:79]\n\t"
    "ds_read_b64_tr_b16 a[170:171], %[ra00] offset:19968\n\t"
    "ds_read_b64_tr_b16 a[172:173], %[ra01] offset:1536\n\t"
    "s_add_u32 m0, %[m0w], 115712\n\t"
    "s_add_u32 s41, s40, 640\n\t"
    "v_mfma_f32_16x16x32_bf16 v[72:75], a[200:203], a[88:91], v[72:75]\n\t"
    "ds_read_b64_tr_b16 a[174:175], %[ra01] offset:19968\n\t"
    "buffer_load_dwordx4 %[wv], %[srd1], s41 offen lds\n\t"
    "v_mfma_f32_16x16x32_bf16 v[80:83], a[204:207], a[88:91], v[80:83]\n\t"
    "ds_read_b64_tr_b16 a[176:177], %[ra02] offset:0\n\t"
    "s_add_u32 m0, %[m0w], 116736\n\t"
    "s_add_u32 s41, s40, 768\n\t"
    "v_mfma_f32_16x16x32_bf16 v[84:87], a[204:207], a[92:95], v[84:87]\n\t"
    "ds_read_b64_tr_b16 a[178:179], %[ra02] offset:18432\n\t"
    "buffer_load_dwordx4 %[wv], %[srd1], s41 offen lds\n\t"
    "v_mfma_f32_16x16x32_bf16 v[92:95], a[208:211], a[92:95], v[92:95]\n\t"
    "s_add_u32 m0, %[m0w], 117760\n\t"
    "s_add_u32 s41, s40, 896\n\t"
    "v_mfma_f32_16x16x32_bf16 v[88:91], a[208:211], a[88:91], v[88:91]\n\t"
    "buffer_load_dwordx4 %[wv], %[srd1], s41 offen lds\n\t"
    "v_mfma_f32_16x16x32_bf16 v[96:99], a[212:215], a[88:91], v[96:99]\n\t"
    "s_add_u32 m0, %[m0w], 118784\n\t"
    "s_add_u32 s41, s40, 1024\n\t"
    "v_mfma_f32_16x16x32_bf16 v[100:103], a[212:215], a[92:95], v[100:103]\n\t"
    "buffer_load_dwordx4 %[wv], %[srd1], s41 offen lds\n\t"
    "s_add_u32 s40, s40, 36864\n\t"
    "s_cmp_eq_u32 s40, 663552\n\t"
    "s_cselect_b32 s40, 0, s40\n\t"
    "s_waitcnt vmcnt(18) lgkmcnt(0)\n\t"
    "s_barrier\n\t"
    "v_mfma_f32_16x16x32_bf16 v[32:35], a[144:147], a[96:99], v[32:35]\n\t"
    "ds_read_b64_tr_b16 a[180:181], %[ra00] offset:36864\n\t"
    "ds_read_b64_tr_b16 a[182:183], %[ra00] offset:55296\n\t"
    "s_add_u32 m0, %[m0w], 0\n\t"
    "s_add_u32 s41, s40, 0\n\t"
    "v_mfma_f32_16x16x32_bf16 v[36:39], a[144:147], a[100:103], v[36:39]\n\t"
    "ds_read_b64_tr_b16 a[184:185], %[ra01] offset:36864\n\t"
    "buffer_load_dwordx4 %[wv], %[srd1], s41 offen lds\n\t"
    "v_mfma_f32_16x16x32_bf16 v[44:47], a[148:151], a[100:103], v[44:47]\n\t"
    "ds_read_b64_tr_b16 a[186:187], %[ra01] offset:55296\n\t"
    "s_add_u32 m0, %[m0w], 1024\n\t"
    "s_add_u32 s41, s40, 128\n\t"
    "v_mfma_f32_16x16x32_bf16 v[40:43], a[148:151], a[96:99], v[40:43]\n\t"
    "ds_read_b64_tr_b16 a[188:189], %[ra00] offset:37376\n\t"
    "ds_read_b64_tr_b16 a[190:191], %[ra00] offset:55808\n\t"
    "buffer_load_dwordx4 %[wv], %[srd1], s41 offen lds\n\t"
    "v_mfma_f32_16x16x32_bf16 v[48:51], a[152:155], a[96:99], v[48:51]\n\t"
    "ds_read_b64_tr_b16 a[192:193], %[ra01] offset:37376\n\t"
    "s_add_u32 m0, %[m0w], 2048\n\t"
    "s_add_u32 s41, s40, 256\n\t"
    "v_mfma_f32_16x16x32_bf16 v[52:55], a[152:155], a[100:103], v[52:55]\n\t"
    "ds_read_b64_tr_b16 a[194:195], %[ra01] offset:55808\n\t"
    "buffer_load_dwordx4 %[wv], %[srd1], s41 offen lds\n\t"
    "v_mfma_f32_16x16x32_bf16 v[60:63], a[156:159], a[100:103], v[60:63]\n\t"
    "ds_read_b64_tr_b16 a[196:197], %[ra00] offset:37888\n\t"
    "s_add_u32 m0, %[m0w], 3072\n\t"
    "s_add_u32 s41, s40, 384\n\t"
    "v_mfma_f32_16x16x32_bf16 v[56:59], a[156:159], a[96:99], v[56:59]\n\t"
    "ds_read_b64_tr_b16 a[198:199], %[ra00] offset:56320\n\t"
    "ds_read_b64_tr_b16 a[200:201], %[ra01] offset:37888\n\t"
    "buffer_load_dwordx4 %[wv], %[srd1], s41 offen lds\n\t"
    "v_mfma_f32_16x16x32_bf16 v[64:67], a[160:163], a[96:99], v[64:67]\n\t"
    "ds_read_b64_tr_b16 a[202:203], %[ra01] offset:56320\n\t"
    "s_add_u32 m0, %[m0w], 4096\n\t"
    "s_add_u32 s41, s40, 512\n\t"
    "v_mfma_f32_16x16x32_bf16 v[68:71], a[160:163], a[100:103], v[68:71]\n\t"
    "ds_read_b64_tr_b16 a[204:205], %[ra00] offset:38400\n\t"
    "buffer_load_dwordx4 %[wv], %[srd1], s41 offen lds\n\t"
    "v_mfma_f32_16x16x32_bf16 v[76:79], a[164:167], a[100:103], v[76:79]\n\t"
    "ds_read_b64_tr_b16 a[206:207], %[ra00] offset:56832\n\t"
    "ds_read_b64_tr_b16 a[208:209], %[ra01] offset:38400\n\t"
    "s_add_u32 m0, %[m0w], 5120\n\t"
    "s_add_u32 s41, s40, 640\n\t"
    "v_mfma_f32_16x16x32_bf16 v[72:75], a[164:167], a[96:99], v[72:75]\n\t"
    "ds_read_b64_tr_b16 a[210:211], %[ra01] offset:56832\n\t"
    "buffer_load_dwordx4 %[wv], %[srd1], s41 offen lds\n\t"
    "v_mfma_f32_16x16x32_bf16 v[80:83], a[168:171], a[96:99], v[80:83]\n\t"
    "ds_read_b64_tr_b16 a[212:213], %[ra02] offset:36864\n\t"
    "s_add_u32 m0, %[m0w], 6144\n\t"
    "s_add_u32 s41, s40, 768\n\t"
    "v_mfma_f32_16x16x32_bf16 v[84:87], a[168:171], a[100:103], v[84:87]\n\t"
    "ds_read_b64_tr_b16 a[214:215], %[ra02] offset:55296\n\t"
    "buffer_load_dwordx4 %[wv], %[srd1], s41 offen lds\n\t"
    "v_mfma_f32_16x16x32_bf16 v[92:95], a[172:175], a[100:103], v[92:95]\n\t"
    "s_add_u32 m0, %[m0w], 7168\n\t"
    "s_add_u32 s41, s40, 896\n\t"
    "v_mfma_f32_16x16x32_bf16 v[88:91], a[172:175], a[96:99], v[88:91]\n\t"
    "buffer_load_dwordx4 %[wv], %[srd1], s41 offen lds\n\t"
    "v_mfma_f32_16x16x32_bf16 v[96:99], a[176:179], a[96:99], v[96:99]\n\t"
    "s_add_u32 m0, %[m0w], 8192\n\t"
    "s_add_u32 s41, s40, 1024\n\t"
    "v_mfma_f32_16x16x32_bf16 v[100:103], a[176:179], a[100:103], v[100:103]\n\t"
    "buffer_load_dwordx4 %[wv], %[srd1], s41 offen lds\n\t"
    "s_add_u32 s40, s40, 36864\n\t"
    "s_cmp_eq_u32 s40, 663552\n\t"
    "s_cselect_b32 s40, 0, s40\n\t"
    "s_waitcnt vmcnt(18) lgkmcnt(0)\n\t"
    "s_barrier\n\t"
    "v_mfma_f32_16x16x32_bf16 v[32:35], a[180:183], a[104:107], v[32:35]\n\t"
    "ds_read_b64_tr_b16 a[144:145], %[ra10] offset:0\n\t"
    "ds_read_b64_tr_b16 a[146:147], %[ra10] offset:18432\n\t"
    "s_add_u32 m0, %[m0w], 36864\n\t"
    "s_add_u32 s41, s40, 0\n\t"
    "v_mfma_f32_16x16x32_bf16 v[36:39], a[180:183], a[108:111], v[36:39]\n\t"
    "ds_read_b64_tr_b16 a[148:149], %[ra11] offset:0\n\t"
    "buffer_load_dwordx4 %[wv], %[srd1], s41 offen lds\n\t"
    "v_mfma_f32_16x16x32_bf16 v[44:47], a[184:187], a[108:111], v[44:47]\n\t"
    "ds_read_b64_tr_b16 a[150:151], %[ra11] offset:18432\n\t"
    "s_add_u32 m0, %[m0w], 37888\n\t"
    "s_add_u32 s41, s40, 128\n\t"
    "v_mfma_f32_16x16x32_bf16 v[40:43], a[184:187], a[104:107], v[40:43]\n\t"
    "ds_read_b64_tr_b16 a[152:153], %[ra10] offset:512\n\t"
    "ds_read_b64_tr_b16 a[154:155], %[ra10] offset:18944\n\t"
    "buffer_load_dwordx4 %[wv], %[srd1], s41 offen lds\n\t"
    "v_mfma_f32_16x16x32_bf16 v[48:51], a[188:191], a[104:107], v[48:51]\n\t"
    "ds_read_b64_tr_b16 a[156:157], %[ra11] offset:512\n\t"
    "s_add_u32 m0, %[m0w], 38912\n\t"
    "s_add_u32 s41, s40, 256\n\t"
    "v_mfma_f32_16x16x32_bf16 v[52:55], a[188:191], a[108:111], v[52:55]\n\t"
    "ds_read_b64_tr_b16 a[158:159], %[ra11] offset:18944\n\t"
    "buffer_load_dwordx4 %[wv], %[srd1], s41 offen lds\n\t"
    "v_mfma_f32_16x16x32_bf16 v[60:63], a[192:195], a[108:111], v[60:63]\n\t"
    "ds_read_b64_tr_b16 a[160:161], %[ra10] offset:1024\n\t"
    "s_add_u32 m0, %[m0w], 39936\n\t"
    "s_add_u32 s41, s40, 384\n\t"
    "v_mfma_f32_16x16x32_bf16 v[56:59], a[192:195], a[104:107], v[56:59]\n\t"
    "ds_read_b64_tr_b16 a[162:163], %[ra10] offset:19456\n\t"
    "ds_read_b64_tr_b16 a[164:165], %[ra11] offset:1024\n\t"
    "buffer_load_dwordx4 %[wv], %[srd1], s41 offen lds\n\t"
    "v_mfma_f32_16x16x32_bf16 v[64:67], a[196:199], a[104:107], v[64:67]\n\t"
    "ds_read_b64_tr_b16 a[166:167], %[ra11] offset:19456\n\t"
    "s_add_u32 m0, %[m0w], 40960\n\t"
    "s_add_u32 s41, s40, 512\n\t"
    "v_mfma_f32_16x16x32_bf16 v[68:71], a[196:199], a[108:111], v[68:71]\n\t"
    "ds_read_b64_tr_b16 a[168:169], %[ra10] offset:1536\n\t"
    "buffer_load_dwordx4 %[wv], %[srd1], s41 offen lds\n\t"
    "v_mfma_f32_16x16x32_bf16 v[76:79], a[200:203], a[108:111], v[76:79]\n\t"
    "ds_read_b64_tr_b16 a[170:171], %[ra10] offset:19968\n\t"
    "ds_read_b64_tr_b16 a[172:173], %[ra11] offset:1536\n\t"
    "s_add_u32 m0, %[m0w], 41984\n\t"
    "s_add_u32 s41, s40, 640\n\t"
    "v_mfma_f32_16x16x32_bf16 v[72:75], a[200:203], a[104:107], v[72:75]\n\t"
    "ds_read_b64_tr_b16 a[174:175], %[ra11] offset:19968\n\t"
    "buffer_load_dwordx4 %[wv], %[srd1], s41 offen lds\n\t"
    "v_mfma_f32_16x16x32_bf16 v[80:83], a[204:207], a[104:107], v[80:83]\n\t"
    "ds_read_b64_tr_b16 a[176:177], %[ra12] offset:0\n\t"
    "s_add_u32 m0, %[m0w], 43008\n\t"
    "s_add_u32 s41, s40, 768\n\t"
    "v_mfma_f32_16x16x32_bf16 v[84:87], a[204:207], a[108:111], v[84:87]\n\t"
    "ds_read_b64_tr_b16 a[178:179], %[ra12] offset:18432\n\t"
    "buffer_load_dwordx4 %[wv], %[srd1], s41 offen lds\n\t"
    "v_mfma_f32_16x16x32_bf16 v[92:95], a[208:211], a[108:111], v[92:95]\n\t"
    "s_add_u32 m0, %[m0w], 44032\n\t"
    "s_add_u32 s41, s40, 896\n\t"
    "v_mfma_f32_16x16x32_bf16 v[88:91], a[208:211], a[104:107], v[88:91]\n\t"
    "buffer_load_dwordx4 %[wv], %[srd1], s41 offen lds\n\t"
    "v_mfma_f32_16x16x32_bf16 v[96:99], a[212:215], a[104:107], v[96:99]\n\t"
    "s_add_u32 m0, %[m0w], 45056\n\t"
    "s_add_u32 s41, s40, 1024\n\t"
    "v_mfma_f32_16x16x32_bf16 v[100:103], a[212:215], a[108:111], v[100:103]\n\t"
    "buffer_load_dwordx4 %[wv], %[srd1], s41 offen lds\n\t"
    "s_add_u32 s40, s40, 36864\n\t"
    "s_cmp_eq_u32 s40, 663552\n\t"
    "s_cselect_b32 s40, 0, s40\n\t"
    "s_waitcnt vmcnt(18) lgkmcnt(0)\n\t"
    "s_barrier\n\t"
    "v_mfma_f32_16x16x32_bf16 v[32:35], a[144:147], a[112:115], v[32:35]\n\t"
    "ds_read_b64_tr_b16 a[180:181], %[ra10] offset:36864\n\t"
    "ds_read_b64_tr_b16 a[182:183], %[ra10] offset:55296\n\t"
    "s_add_u32 m0, %[m0w], 73728\n\t"
    "s_add_u32 s41, s40, 0\n\t"
    "v_mfma_f32_16x16x32_bf16 v[36:39], a[144:147], a[116:119], v[36:39]\n\t"
    "ds_read_b64_tr_b16 a[184:185], %[ra11] offset:36864\n\t"
    "buffer_load_dwordx4 %[wv], %[srd2], s41 offen lds\n\t"
    "v_mfma_f32_16x16x32_bf16 v[44:47], a[148:151], a[116:119], v[44:47]\n\t"
    "ds_read_b64_tr_b16 a[186:187], %[ra11] offset:55296\n\t"
    "s_add_u32 m0, %[m0w], 74752\n\t"
    "s_add_u32 s41, s40, 128\n\t"
    "v_mfma_f32_16x16x32_bf16 v[40:43], a[148:151], a[112:115], v[40:43]\n\t"
    "ds_read_b64_tr_b16 a[188:189], %[ra10] offset:37376\n\t"
    "ds_read_b64_tr_b16 a[190:191], %[ra10] offset:55808\n\t"
    "buffer_load_dwordx4 %[wv], %[srd2], s41 offen lds\n\t"
    "v_mfma_f32_16x16x32_bf16 v[48:51], a[152:155], a[112:115], v[48:51]\n\t"
    "ds_read_b64_tr_b16 a[192:193], %[ra11] offset:37376\n\t"
    "s_add_u32 m0, %[m0w], 75776\n\t"
    "s_add_u32 s41, s40, 256\n\t"
    "v_mfma_f32_16x16x32_bf16 v[52:55], a[152:155], a[116:119], v[52:55]\n\t"
    "ds_read_b64_tr_b16 a[194:195], %[ra11] offset:55808\n\t"
    "buffer_load_dwordx4 %[wv], %[srd2], s41 offen lds\n\t"
    "v_mfma_f32_16x16x32_bf16 v[60:63], a[156:159], a[116:119], v[60:63]\n\t"
    "ds_read_b64_tr_b16 a[196:197], %[ra10] offset:37888\n\t"
    "s_add_u32 m0, %[m0w], 76800\n\t"
    "s_add_u32 s41, s40, 384\n\t"
    "v_mfma_f32_16x16x32_bf16 v[56:59], a[156:159], a[112:115], v[56:59]\n\t"
    "ds_read_b64_tr_b16 a[198:199], %[ra10] offset:56320\n\t"
    "ds_read_b64_tr_b16 a[200:201], %[ra11] offset:37888\n\t"
    "buffer_load_dwordx4 %[wv], %[srd2], s41 offen lds\n\t"
    "v_mfma_f32_16x16x32_bf16 v[64:67], a[160:163], a[112:115], v[64:67]\n\t"
    "ds_read_b64_tr_b16 a[202:203], %[ra11] offset:56320\n\t"
    "s_add_u32 m0, %[m0w], 77824\n\t"
    "s_add_u32 s41, s40, 512\n\t"
    "v_mfma_f32_16x16x32_bf16 v[68:71], a[160:163], a[116:119], v[68:71]\n\t"
    "ds_read_b64_tr_b16 a[204:205], %[ra10] offset:38400\n\t"
    "buffer_load_dwordx4 %[wv], %[srd2], s41 offen lds\n\t"
    "v_mfma_f32_16x16x32_bf16 v[76:79], a[164:167], a[116:119], v[76:79]\n\t"
    "ds_read_b64_tr_b16 a[206:207], %[ra10] offset:56832\n\t"
    "ds_read_b64_tr_b16 a[208:209], %[ra11] offset:38400\n\t"
    "s_add_u32 m0, %[m0w], 78848\n\t"
    "s_add_u32 s41, s40, 640\n\t"
    "v_mfma_f32_16x16x32_bf16 v[72:75], a[164:167], a[112:115], v[72:75]\n\t"
    "ds_read_b64_tr_b16 a[210:211], %[ra11] offset:56832\n\t"
    "buffer_load_dwordx4 %[wv], %[srd2], s41 offen lds\n\t"
    "v_mfma_f32_16x16x32_bf16 v[80:83], a[168:171], a[112:115], v[80:83]\n\t"
    "ds_read_b64_tr_b16 a[212:213], %[ra12] offset:36864\n\t"
    "s_add_u32 m0, %[m0w], 79872\n\t"
    "s_add_u32 s41, s40, 768\n\t"
    "v_mfma_f32_16x16x32_bf16 v[84:87], a[168:171], a[116:119], v[84:87]\n\t"
    "ds_read_b64_tr_b16 a[214:215], %[ra12] offset:55296\n\t"
    "buffer_load_dwordx4 %[wv], %[srd2], s41 offen lds\n\t"
    "v_mfma_f32_16x16x32_bf16 v[92:95], a[172:175], a[116:119], v[92:95]\n\t"
    "s_add_u32 m0, %[m0w], 80896\n\t"
    "s_add_u32 s41, s40, 896\n\t"
    "v_mfma_f32_16x16x32_bf16 v[88:91], a[172:175], a[112:115], v[88:91]\n\t"
    "buffer_load_dwordx4 %[wv], %[srd2], s41 offen lds\n\t"
    "v_mfma_f32_16x16x32_bf16 v[96:99], a[176:179], a[112:115], v[96:99]\n\t"
    "s_add_u32 m0, %[m0w], 81920\n\t"
    "s_add_u32 s41, s40, 1024\n\t"
    "v_mfma_f32_16x16x32_bf16 v[100:103], a[176:179], a[116:119], v[100:103]\n\t"
    "buffer_load_dwordx4 %[wv], %[srd2], s41 offen lds\n\t"
    "s_add_u32 s40, s40, 36864\n\t"
    "s_cmp_eq_u32 s40, 663552\n\t"
    "s_cselect_b32 s40, 0, s40\n\t"
    "s_waitcnt vmcnt(18) lgkmcnt(0)\n\t"
    "s_barrier\n\t"
    "v_mfma_f32_16x16x32_bf16 v[32:35], a[180:183], a[120:123], v[32:35]\n\t"
    "ds_read_b64_tr_b16 a[144:145], %[ra00] offset:0\n\t"
    "ds_read_b64_tr_b16 a[146:147], %[ra00] offset:18432\n\t"
    "s_add_u32 m0, %[m0w], 110592\n\t"
    "s_add_u32 s41, s40, 0\n\t"
    "v_mfma_f32_16x16x32_bf16 v[36:39], a[180:183], a[124:127], v[36:39]\n\t"
    "ds_read_b64_tr_b16 a[148:149], %[ra01] offset:0\n\t"
    "buffer_load_dwordx4 %[wv], %[srd2], s41 offen lds\n\t"
    "v_mfma_f32_16x16x32_bf16 v[44:47], a[184:187], a[124:127], v[44:47]\n\t"
    "ds_read_b64_tr_b16 a[150:151], %[ra01] offset:18432\n\t"
    "s_add_u32 m0, %[m0w], 111616\n\t"
    "s_add_u32 s41, s40, 128\n\t"
    "v_mfma_f32_16x16x32_bf16 v[40:43], a[184:187], a[120:123], v[40:43]\n\t"
    "ds_read_b64_tr_b16 a[152:153], %[ra00] offset:512\n\t"
    "ds_read_b64_tr_b16 a[154:155], %[ra00] offset:18944\n\t"
    "buffer_load_dwordx4 %[wv], %[srd2], s41 offen lds\n\t"
    "v_mfma_f32_16x16x32_bf16 v[48:51], a[188:191], a[120:123], v[48:51]\n\t"
    "ds_read_b64_tr_b16 a[156:157], %[ra01] offset:512\n\t"
    "s_add_u32 m0, %[m0w], 112640\n\t"
    "s_add_u32 s41, s40, 256\n\t"
    "v_mfma_f32_16x16x32_bf16 v[52:55], a[188:191], a[124:127], v[52:55]\n\t"
    "ds_read_b64_tr_b16 a[158:159], %[ra01] offset:18944\n\t"
    "buffer_load_dwordx4 %[wv], %[srd2], s41 offen lds\n\t"
    "v_mfma_f32_16x16x32_bf16 v[60:63], a[192:195], a[124:127], v[60:63]\n\t"
    "ds_read_b64_tr_b16 a[160:161], %[ra00] offset:1024\n\t"
    "s_add_u32 m0, %[m0w], 113664\n\t"
    "s_add_u32 s41, s40, 384\n\t"
    "v_mfma_f32_16x16x32_bf16 v[56:59], a[192:195], a[120:123], v[56:59]\n\t"
    "ds_read_b64_tr_b16 a[162:163], %[ra00] offset:19456\n\t"
    "ds_read_b64_tr_b16 a[164:165], %[ra01] offset:1024\n\t"
    "buffer_load_dwordx4 %[wv], %[srd2], s41 offen lds\n\t"
    "v_mfma_f32_16x16x32_bf16 v[64:67], a[196:199], a[120:123], v[64:67]\n\t"
    "ds_read_b64_tr_b16 a[166:167], %[ra01] offset:19456\n\t"
    "s_add_u32 m0, %[m0w], 114688\n\t"
    "s_add_u32 s41, s40, 512\n\t"
    "v_mfma_f32_16x16x32_bf16 v[68:71], a[196:199], a[124:127], v[68:71]\n\t"
    "ds_read_b64_tr_b16 a[168:169], %[ra00] offset:1536\n\t"
    "buffer_load_dwordx4 %[wv], %[srd2], s41 offen lds\n\t"
    "v_mfma_f32_16x16x32_bf16 v[76:79], a[200:203], a[124:127], v[76:79]\n\t"
    "ds_read_b64_tr_b16 a[170:171], %[ra00] offset:19968\n\t"
    "ds_read_b64_tr_b16 a[172:173], %[ra01] offset:1536\n\t"
    "s_add_u32 m0, %[m0w], 115712\n\t"
    "s_add_u32 s41, s40, 640\n\t"
    "v_mfma_f32_16x16x32_bf16 v[72:75], a[200:203], a[120:123], v[72:75]\n\t"
    "ds_read_b64_tr_b16 a[174:175], %[ra01] offset:19968\n\t"
    "buffer_load_dwordx4 %[wv], %[srd2], s41 offen lds\n\t"
    "v_mfma_f32_16x16x32_bf16 v[80:83], a[204:207], a[120:123], v[80:83]\n\t"
    "ds_read_b64_tr_b16 a[176:177], %[ra02] offset:0\n\t"
    "s_add_u32 m0, %[m0w], 116736\n\t"
    "s_add_u32 s41, s40, 768\n\t"
    "v_mfma_f32_16x16x32_bf16 v[84:87], a[204:207], a[124:127], v[84:87]\n\t"
    "ds_read_b64_tr_b16 a[178:179], %[ra02] offset:18432\n\t"
    "buffer_load_dwordx4 %[wv], %[srd2], s41 offen lds\n\t"
    "v_mfma_f32_16x16x32_bf16 v[92:95], a[208:211], a[124:127], v[92:95]\n\t"
    "s_add_u32 m0, %[m0w], 117760\n\t"
    "s_add_u32 s41, s40, 896\n\t"
    "v_mfma_f32_16x16x32_bf16 v[88:91], a[208:211], a[120:123], v[88:91]\n\t"
    "buffer_load_dwordx4 %[wv], %[srd2], s41 offen lds\n\t"
    "v_mfma_f32_16x16x32_bf16 v[96:99], a[212:215], a[120:123], v[96:99]\n\t"
    "s_add_u32 m0, %[m0w], 118784\n\t"
    "s_add_u32 s41, s40, 1024\n\t"
    "v_mfma_f32_16x16x32_bf16 v[100:103], a[212:215], a[124:127], v[100:103]\n\t"
    "buffer_load_dwordx4 %[wv], %[srd2], s41 offen lds\n\t"
    "s_add_u32 s40, s40, 36864\n\t"
    "s_cmp_eq_u32 s40, 663552\n\t"
    "s_cselect_b32 s40, 0, s40\n\t"
    "s_waitcnt vmcnt(18) lgkmcnt(0)\n\t"
    "s_barrier\n\t"
    "v_mfma_f32_16x16x32_bf16 v[32:35], a[144:147], a[128:131], v[32:35]\n\t"
    "ds_read_b64_tr_b16 a[180:181], %[ra00] offset:36864\n\t"
    "ds_read_b64_tr_b16 a[182:183], %[ra00] offset:55296\n\t"
    "s_add_u32 m0, %[m0w], 0\n\t"
    "s_add_u32 s41, s40, 0\n\t"
    "v_mfma_f32_16x16x32_bf16 v[36:39], a[144:147], a[132:135], v[36:39]\n\t"
    "ds_read_b64_tr_b16 a[184:185], %[ra01] offset:36864\n\t"
    "buffer_load_dwordx4 %[wv], %[srd2], s41 offen lds\n\t"
    "v_mfma_f32_16x16x32_bf16 v[44:47], a[148:151], a[132:135], v[44:47]\n\t"
    "ds_read_b64_tr_b16 a[186:187], %[ra01] offset:55296\n\t"
    "s_add_u32 m0, %[m0w], 1024\n\t"
    "s_add_u32 s41, s40, 128\n\t"
    "v_mfma_f32_16x16x32_bf16 v[40:43], a[148:151], a[128:131], v[40:43]\n\t"
    "ds_read_b64_tr_b16 a[188:189], %[ra00] offset:37376\n\t"
    "ds_read_b64_tr_b16 a[190:191], %[ra00] offset:55808\n\t"
    "buffer_load_dwordx4 %[wv], %[srd2], s41 offen lds\n\t"
    "v_mfma_f32_16x16x32_bf16 v[48:51], a[152:155], a[128:131], v[48:51]\n\t"
    "ds_read_b64_tr_b16 a[192:193], %[ra01] offset:37376\n\t"
    "s_add_u32 m0, %[m0w], 2048\n\t"
    "s_add_u32 s41, s40, 256\n\t"
    "v_mfma_f32_16x16x32_bf16 v[52:55], a[152:155], a[132:135], v[52:55]\n\t"
    "ds_read_b64_tr_b16 a[194:195], %[ra01] offset:55808\n\t"
    "buffer_load_dwordx4 %[wv], %[srd2], s41 offen lds\n\t"
    "v_mfma_f32_16x16x32_bf16 v[60:63], a[156:159], a[132:135], v[60:63]\n\t"
    "ds_read_b64_tr_b16 a[196:197], %[ra00] offset:37888\n\t"
    "s_add_u32 m0, %[m0w], 3072\n\t"
    "s_add_u32 s41, s40, 384\n\t"
    "v_mfma_f32_16x16x32_bf16 v[56:59], a[156:159], a[128:131], v[56:59]\n\t"
    "ds_read_b64_tr_b16 a[198:199], %[ra00] offset:56320\n\t"
    "ds_read_b64_tr_b16 a[200:201], %[ra01] offset:37888\n\t"
    "buffer_load_dwordx4 %[wv], %[srd2], s41 offen lds\n\t"
    "v_mfma_f32_16x16x32_bf16 v[64:67], a[160:163], a[128:131], v[64:67]\n\t"
    "ds_read_b64_tr_b16 a[202:203], %[ra01] offset:56320\n\t"
    "s_add_u32 m0, %[m0w], 4096\n\t"
    "s_add_u32 s41, s40, 512\n\t"
    "v_mfma_f32_16x16x32_bf16 v[68:71], a[160:163], a[132:135], v[68:71]\n\t"
    "ds_read_b64_tr_b16 a[204:205], %[ra00] offset:38400\n\t"
    "buffer_load_dwordx4 %[wv], %[srd2], s41 offen lds\n\t"
    "v_mfma_f32_16x16x32_bf16 v[76:79], a[164:167], a[132:135], v[76:79]\n\t"
    "ds_read_b64_tr_b16 a[206:207], %[ra00] offset:56832\n\t"
    "ds_read_b64_tr_b16 a[208:209], %[ra01] offset:38400\n\t"
    "s_add_u32 m0, %[m0w], 5120\n\t"
    "s_add_u32 s41, s40, 640\n\t"
    "v_mfma_f32_16x16x32_bf16 v[72:75], a[164:167], a[128:131], v[72:75]\n\t"
    "ds_read_b64_tr_b16 a[210:211], %[ra01] offset:56832\n\t"
    "buffer_load_dwordx4 %[wv], %[srd2], s41 offen lds\n\t"
    "v_mfma_f32_16x16x32_bf16 v[80:83], a[168:171], a[128:131], v[80:83]\n\t"
    "ds_read_b64_tr_b16 a[212:213], %[ra02] offset:36864\n\t"
    "s_add_u32 m0, %[m0w], 6144\n\t"
    "s_add_u32 s41, s40, 768\n\t"
    "v_mfma_f32_16x16x32_bf16 v[84:87], a[168:171], a[132:135], v[84:87]\n\t"
    "ds_read_b64_tr_b16 a[214:215], %[ra02] offset:55296\n\t"
    "buffer_load_dwordx4 %[wv], %[srd2], s41 offen lds\n\t"
    "v_mfma_f32_16x16x32_bf16 v[92:95], a[172:175], a[132:135], v[92:95]\n\t"
    "s_add_u32 m0, %[m0w], 7168\n\t"
    "s_add_u32 s41, s40, 896\n\t"
    "v_mfma_f32_16x16x32_bf16 v[88:91], a[172:175], a[128:131], v[88:91]\n\t"
    "buffer_load_dwordx4 %[wv], %[srd2], s41 offen lds\n\t"
    "v_mfma_f32_16x16x32_bf16 v[96:99], a[176:179], a[128:131], v[96:99]\n\t"
    "s_add_u32 m0, %[m0w], 8192\n\t"
    "s_add_u32 s41, s40, 1024\n\t"
    "v_mfma_f32_16x16x32_bf16 v[100:103], a[176:179], a[132:135], v[100:103]\n\t"
    "buffer_load_dwordx4 %[wv], %[srd2], s41 offen lds\n\t"
    "s_add_u32 s40, s40, 36864\n\t"
    "s_cmp_eq_u32 s40, 663552\n\t"
    "s_cselect_b32 s40, 0, s40\n\t"
    "s_waitcnt lgkmcnt(0)\n\t"
    "v_mfma_f32_16x16x32_bf16 v[32:35], a[180:183], a[136:139], v[32:35]\n\t"
    "v_mfma_f32_16x16x32_bf16 v[36:39], a[180:183], a[140:143], v[36:39]\n\t"
    "v_mfma_f32_16x16x32_bf16 v[44:47], a[184:187], a[140:143], v[44:47]\n\t"
    "v_mfma_f32_16x16x32_bf16 v[40:43], a[184:187], a[136:139], v[40:43]\n\t"
    "v_mfma_f32_16x16x32_bf16 v[48:51], a[188:191], a[136:139], v[48:51]\n\t"
    "v_mfma_f32_16x16x32_bf16 v[52:55], a[188:191], a[140:143], v[52:55]\n\t"
    "v_mfma_f32_16x16x32_bf16 v[60:63], a[192:195], a[140:143], v[60:63]\n\t"
    "v_mfma_f32_16x16x32_bf16 v[56:59], a[192:195], a[136:139], v[56:59]\n\t"
    "v_mfma_f32_16x16x32_bf16 v[64:67], a[196:199], a[136:139], v[64:67]\n\t"
    "v_mfma_f32_16x16x32_bf16 v[68:71], a[196:199], a[140:143], v[68:71]\n\t"
    "v_mfma_f32_16x16x32_bf16 v[76:79], a[200:203], a[140:143], v[76:79]\n\t"
    "v_mfma_f32_16x16x32_bf16 v[72:75], a[200:203], a[136:139], v[72:75]\n\t"
    "v_mfma_f32_16x16x32_bf16 v[80:83], a[204:207], a[136:139], v[80:83]\n\t"
    "v_mfma_f32_16x16x32_bf16 v[84:87], a[204:207], a[140:143], v[84:87]\n\t"
    "v_mfma_f32_16x16x32_bf16 v[92:95], a[208:211], a[140:143], v[92:95]\n\t"
    "v_mfma_f32_16x16x32_bf16 v[88:91], a[208:211], a[136:139], v[88:91]\n\t"
    "v_mfma_f32_16x16x32_bf16 v[96:99], a[212:215], a[136:139], v[96:99]\n\t"
    "v_mfma_f32_16x16x32_bf16 v[100:103], a[212:215], a[140:143], v[100:103]\n\t"
    "s_barrier\n\t"
    "v_mul_f32 v32, v32, v145\n\t"
    "v_mul_f32 v33, v33, v145\n\t"
    "v_mul_f32 v34, v34, v145\n\t"
    "v_mul_f32 v35, v35, v145\n\t"
    "v_fmaak_f32 v140, v32, v32, 0x409d30fc\n\t"
    "v_fmaak_f32 v141, v33, v33, 0x409d30fc\n\t"
    "v_fmaak_f32 v142, v34, v34, 0x409d30fc\n\t"
    "v_fmaak_f32 v143, v35, v35, 0x409d30fc\n\t"
    "v_mul_f32 v140, v140, v32\n\t"
    "v_mul_f32 v141, v141, v33\n\t"
    "v_mul_f32 v142, v142, v34\n\t"
    "v_mul_f32 v143, v143, v35\n\t"
    "v_exp_f32 v140, v140\n\t"
    "v_exp_f32 v141, v141\n\t"
    "v_exp_f32 v142, v142\n\t"
    "v_exp_f32 v143, v143\n\t"
    "v_fma_f32 v140, v140, v144, v144\n\t"
    "v_fma_f32 v141, v141, v144, v144\n\t"
    "v_fma_f32 v142, v142, v144, v144\n\t"
    "v_fma_f32 v143, v143, v144, v144\n\t"
    "v_rcp_f32 v140, v140\n\t"
    "v_rcp_f32 v141, v141\n\t"
    "v_rcp_f32 v142, v142\n\t"
    "v_rcp_f32 v143, v143\n\t"
    "v_mul_f32 v32, v32, v140\n\t"
    "v_mul_f32 v33, v33, v141\n\t"
    "v_mul_f32 v34, v34, v142\n\t"
    "v_mul_f32 v35, v35, v143\n\t"
    "v_cvt_pk_bf16_f32 v32, v32, v33\n\t"
    "v_cvt_pk_bf16_f32 v33, v34, v35\n\t"
    "ds_write_b64 %[hw0], v[32:33] offset:0\n\t"
    "v_mul_f32 v36, v36, v146\n\t"
    "v_mul_f32 v37, v37, v146\n\t"
    "v_mul_f32 v38, v38, v146\n\t"
    "v_mul_f32 v39, v39, v146\n\t"
    "v_fmaak_f32 v140, v36, v36, 0x409d30fc\n\t"
    "v_fmaak_f32 v141, v37, v37, 0x409d30fc\n\t"
    "v_fmaak_f32 v142, v38, v38, 0x409d30fc\n\t"
    "v_fmaak_f32 v143, v39, v39, 0x409d30fc\n\t"
    "v_mul_f32 v140, v140, v36\n\t"
    "v_mul_f32 v141, v141, v37\n\t"
    "v_mul_f32 v142, v142, v38\n\t"
    "v_mul_f32 v143, v143, v39\n\t"
    "v_exp_f32 v140, v140\n\t"
    "v_exp_f32 v141, v141\n\t"
    "v_exp_f32 v142, v142\n\t"
    "v_exp_f32 v143, v143\n\t"
    "v_fma_f32 v140, v140, v144, v144\n\t"
    "v_fma_f32 v141, v141, v144, v144\n\t"
    "v_fma_f32 v142, v142, v144, v144\n\t"
    "v_fma_f32 v143, v143, v144, v144\n\t"
    "v_rcp_f32 v140, v140\n\t"
    "v_rcp_f32 v141, v141\n\t"
    "v_rcp_f32 v142, v142\n\t"
    "v_rcp_f32 v143, v143\n\t"
    "v_mul_f32 v36, v36, v140\n\t"
    "v_mul_f32 v37, v37, v141\n\t"
    "v_mul_f32 v38, v38, v142\n\t"
    "v_mul_f32 v39, v39, v143\n\t"
    "v_cvt_pk_bf16_f32 v36, v36, v37\n\t"
    "v_cvt_pk_bf16_f32 v37, v38, v39\n\t"
    "ds_write_b64 %[hw0], v[36:37] offset:18432\n\t"
    "v_mul_f32 v40, v40, v145\n\t"
    "v_mul_f32 v41, v41, v145\n\t"
    "v_mul_f32 v42, v42, v145\n\t"
    "v_mul_f32 v43, v43, v145\n\t"
    "v_fmaak_f32 v140, v40, v40, 0x409d30fc\n\t"
    "v_fmaak_f32 v141, v41, v41, 0x409d30fc\n\t"
    "v_fmaak_f32 v142, v42, v42, 0x409d30fc\n\t"
    "v_fmaak_f32 v143, v43, v43, 0x409d30fc\n\t"
    "v_mul_f32 v140, v140, v40\n\t"
    "v_mul_f32 v141, v141, v41\n\t"
    "v_mul_f32 v142, v142, v42\n\t"
    "v_mul_f32 v143, v143, v43\n\t"
    "v_exp_f32 v140, v140\n\t"
    "v_exp_f32 v141, v141\n\t"
    "v_exp_f32 v142, v142\n\t"
    "v_exp_f32 v143, v143\n\t"
    "v_fma_f32 v140, v140, v144, v144\n\t"
    "v_fma_f32 v141, v141, v144, v144\n\t"
    "v_fma_f32 v142, v142, v144, v144\n\t"
    "v_fma_f32 v143, v143, v144, v144\n\t"
    "v_rcp_f32 v140, v140\n\t"
    "v_rcp_f32 v141, v141\n\t"
    "v_rcp_f32 v142, v142\n\t"
    "v_rcp_f32 v143, v143\n\t"
    "v_mul_f32 v40, v40, v140\n\t"
    "v_mul_f32 v41, v41, v141\n\t"
    "v_mul_f32 v42, v42, v142\n\t"
    "v_mul_f32 v43, v43, v143\n\t"
    "v_cvt_pk_bf16_f32 v40, v40, v41\n\t"
    "v_cvt_pk_bf16_f32 v41, v42, v43\n\t"
    "ds_write_b64 %[hw0], v[40:41] offset:8\n\t"
    "v_mul_f32 v44, v44, v146\n\t"
    "v_mul_f32 v45, v45, v146\n\t"
    "v_mul_f32 v46, v46, v146\n\t"
    "v_mul_f32 v47, v47, v146\n\t"
    "v_fmaak_f32 v140, v44, v44, 0x409d30fc\n\t"
    "v_fmaak_f32 v141, v45, v45, 0x409d30fc\n\t"
    "v_fmaak_f32 v142, v46, v46, 0x409d30fc\n\t"
    "v_fmaak_f32 v143, v47, v47, 0x409d30fc\n\t"
    "v_mul_f32 v140, v140, v44\n\t"
    "v_mul_f32 v141, v141, v45\n\t"
    "v_mul_f32 v142, v142, v46\n\t"
    "v_mul_f32 v143, v143, v47\n\t"
    "v_exp_f32 v140, v140\n\t"
    "v_exp_f32 v141, v141\n\t"
    "v_exp_f32 v142, v142\n\t"
    "v_exp_f32 v143, v143\n\t"
    "v_fma_f32 v140, v140, v144, v144\n\t"
    "v_fma_f32 v141, v141, v144, v144\n\t"
    "v_fma_f32 v142, v142, v144, v144\n\t"
    "v_fma_f32 v143, v143, v144, v144\n\t"
    "v_rcp_f32 v140, v140\n\t"
    "v_rcp_f32 v141, v141\n\t"
    "v_rcp_f32 v142, v142\n\t"
    "v_rcp_f32 v143, v143\n\t"
    "v_mul_f32 v44, v44, v140\n\t"
    "v_mul_f32 v45, v45, v141\n\t"
    "v_mul_f32 v46, v46, v142\n\t"
    "v_mul_f32 v47, v47, v143\n\t"
    "v_cvt_pk_bf16_f32 v44, v44, v45\n\t"
    "v_cvt_pk_bf16_f32 v45, v46, v47\n\t"
    "ds_write_b64 %[hw0], v[44:45] offset:18440\n\t"
    "v_mul_f32 v48, v48, v145\n\t"
    "v_mul_f32 v49, v49, v145\n\t"
    "v_mul_f32 v50, v50, v145\n\t"
    "v_mul_f32 v51, v51, v145\n\t"
    "v_fmaak_f32 v140, v48, v48, 0x409d30fc\n\t"
    "v_fmaak_f32 v141, v49, v49, 0x409d30fc\n\t"
    "v_fmaak_f32 v142, v50, v50, 0x409d30fc\n\t"
    "v_fmaak_f32 v143, v51, v51, 0x409d30fc\n\t"
    "v_mul_f32 v140, v140, v48\n\t"
    "v_mul_f32 v141, v141, v49\n\t"
    "v_mul_f32 v142, v142, v50\n\t"
    "v_mul_f32 v143, v143, v51\n\t"
    "v_exp_f32 v140, v140\n\t"
    "v_exp_f32 v141, v141\n\t"
    "v_exp_f32 v142, v142\n\t"
    "v_exp_f32 v143, v143\n\t"
    "v_fma_f32 v140, v140, v144, v144\n\t"
    "v_fma_f32 v141, v141, v144, v144\n\t"
    "v_fma_f32 v142, v142, v144, v144\n\t"
    "v_fma_f32 v143, v143, v144, v144\n\t"
    "v_rcp_f32 v140, v140\n\t"
    "v_rcp_f32 v141, v141\n\t"
    "v_rcp_f32 v142, v142\n\t"
    "v_rcp_f32 v143, v143\n\t"
    "v_mul_f32 v48, v48, v140\n\t"
    "v_mul_f32 v49, v49, v141\n\t"
    "v_mul_f32 v50, v50, v142\n\t"
    "v_mul_f32 v51, v51, v143\n\t"
    "v_cvt_pk_bf16_f32 v48, v48, v49\n\t"
    "v_cvt_pk_bf16_f32 v49, v50, v51\n\t"
    "ds_write_b64 %[hw0], v[48:49] offset:1024\n\t"
    "v_mul_f32 v52, v52, v146\n\t"
    "v_mul_f32 v53, v53, v146\n\t"
    "v_mul_f32 v54, v54, v146\n\t"
    "v_mul_f32 v55, v55, v146\n\t"
    "v_fmaak_f32 v140, v52, v52, 0x409d30fc\n\t"
    "v_fmaak_f32 v141, v53, v53, 0x409d30fc\n\t"
    "v_fmaak_f32 v142, v54, v54, 0x409d30fc\n\t"
    "v_fmaak_f32 v143, v55, v55, 0x409d30fc\n\t"
    "v_mul_f32 v140, v140, v52\n\t"
    "v_mul_f32 v141, v141, v53\n\t"
    "v_mul_f32 v142, v142, v54\n\t"
    "v_mul_f32 v143, v143, v55\n\t"
    "v_exp_f32 v140, v140\n\t"
    "v_exp_f32 v141, v141\n\t"
    "v_exp_f32 v142, v142\n\t"
    "v_exp_f32 v143, v143\n\t"
    "v_fma_f32 v140, v140, v144, v144\n\t"
    "v_fma_f32 v141, v141, v144, v144\n\t"
    "v_fma_f32 v142, v142, v144, v144\n\t"
    "v_fma_f32 v143, v143, v144, v144\n\t"
    "v_rcp_f32 v140, v140\n\t"
    "v_rcp_f32 v141, v141\n\t"
    "v_rcp_f32 v142, v142\n\t"
    "v_rcp_f32 v143, v143\n\t"
    "v_mul_f32 v52, v52, v140\n\t"
    "v_mul_f32 v53, v53, v141\n\t"
    "v_mul_f32 v54, v54, v142\n\t"
    "v_mul_f32 v55, v55, v143\n\t"
    "v_cvt_pk_bf16_f32 v52, v52, v53\n\t"
    "v_cvt_pk_bf16_f32 v53, v54, v55\n\t"
    "ds_write_b64 %[hw0], v[52:53] offset:19456\n\t"
    "v_mul_f32 v56, v56, v145\n\t"
    "v_mul_f32 v57, v57, v145\n\t"
    "v_mul_f32 v58, v58, v145\n\t"
    "v_mul_f32 v59, v59, v145\n\t"
    "v_fmaak_f32 v140, v56, v56, 0x409d30fc\n\t"
    "v_fmaak_f32 v141, v57, v57, 0x409d30fc\n\t"
    "v_fmaak_f32 v142, v58, v58, 0x409d30fc\n\t"
    "v_fmaak_f32 v143, v59, v59, 0x409d30fc\n\t"
    "v_mul_f32 v140, v140, v56\n\t"
    "v_mul_f32 v141, v141, v57\n\t"
    "v_mul_f32 v142, v142, v58\n\t"
    "v_mul_f32 v143, v143, v59\n\t"
    "v_exp_f32 v140, v140\n\t"
    "v_exp_f32 v141, v141\n\t"
    "v_exp_f32 v142, v142\n\t"
    "v_exp_f32 v143, v143\n\t"
    "v_fma_f32 v140, v140, v144, v144\n\t"
    "v_fma_f32 v141, v141, v144, v144\n\t"
    "v_fma_f32 v142, v142, v144, v144\n\t"
    "v_fma_f32 v143, v143, v144, v144\n\t"
    "v_rcp_f32 v140, v140\n\t"
    "v_rcp_f32 v141, v141\n\t"
    "v_rcp_f32 v142, v142\n\t"
    "v_rcp_f32 v143, v143\n\t"
    "v_mul_f32 v56, v56, v140\n\t"
    "v_mul_f32 v57, v57, v141\n\t"
    "v_mul_f32 v58, v58, v142\n\t"
    "v_mul_f32 v59, v59, v143\n\t"
    "v_cvt_pk_bf16_f32 v56, v56, v57\n\t"
    "v_cvt_pk_bf16_f32 v57, v58, v59\n\t"
    "ds_write_b64 %[hw0], v[56:57] offset:1032\n\t"
    "v_mul_f32 v60, v60, v146\n\t"
    "v_mul_f32 v61, v61, v146\n\t"
    "v_mul_f32 v62, v62, v146\n\t"
    "v_mul_f32 v63, v63, v146\n\t"
    "v_fmaak_f32 v140, v60, v60, 0x409d30fc\n\t"
    "v_fmaak_f32 v141, v61, v61, 0x409d30fc\n\t"
    "v_fmaak_f32 v142, v62, v62, 0x409d30fc\n\t"
    "v_fmaak_f32 v143, v63, v63, 0x409d30fc\n\t"
    "v_mul_f32 v140, v140, v60\n\t"
    "v_mul_f32 v141, v141, v61\n\t"
    "v_mul_f32 v142, v142, v62\n\t"
    "v_mul_f32 v143, v143, v63\n\t"
    "v_exp_f32 v140, v140\n\t"
    "v_exp_f32 v141, v141\n\t"
    "v_exp_f32 v142, v142\n\t"
    "v_exp_f32 v143, v143\n\t"
    "v_fma_f32 v140, v140, v144, v144\n\t"
    "v_fma_f32 v141, v141, v144, v144\n\t"
    "v_fma_f32 v142, v142, v144, v144\n\t"
    "v_fma_f32 v143, v143, v144, v144\n\t"
    "v_rcp_f32 v140, v140\n\t"
    "v_rcp_f32 v141, v141\n\t"
    "v_rcp_f32 v142, v142\n\t"
    "v_rcp_f32 v143, v143\n\t"
    "v_mul_f32 v60, v60, v140\n\t"
    "v_mul_f32 v61, v61, v141\n\t"
    "v_mul_f32 v62, v62, v142\n\t"
    "v_mul_f32 v63, v63, v143\n\t"
    "v_cvt_pk_bf16_f32 v60, v60, v61\n\t"
    "v_cvt_pk_bf16_f32 v61, v62, v63\n\t"
    "ds_write_b64 %[hw0], v[60:61] offset:19464\n\t"
    "v_mul_f32 v64, v64, v145\n\t"
    "v_mul_f32 v65, v65, v145\n\t"
    "v_mul_f32 v66, v66, v145\n\t"
    "v_mul_f32 v67, v67, v145\n\t"
    "v_fmaak_f32 v140, v64, v64, 0x409d30fc\n\t"
    "v_fmaak_f32 v141, v65, v65, 0x409d30fc\n\t"
    "v_fmaak_f32 v142, v66, v66, 0x409d30fc\n\t"
    "v_fmaak_f32 v143, v67, v67, 0x409d30fc\n\t"
    "v_mul_f32 v140, v140, v64\n\t"
    "v_mul_f32 v141, v141, v65\n\t"
    "v_mul_f32 v142, v142, v66\n\t"
    "v_mul_f32 v143, v143, v67\n\t"
    "v_exp_f32 v140, v140\n\t"
    "v_exp_f32 v141, v141\n\t"
    "v_exp_f32 v142, v142\n\t"
    "v_exp_f32 v143, v143\n\t"
    "v_fma_f32 v140, v140, v144, v144\n\t"
    "v_fma_f32 v141, v141, v144, v144\n\t"
    "v_fma_f32 v142, v142, v144, v144\n\t"
    "v_fma_f32 v143, v143, v144, v144\n\t"
    "v_rcp_f32 v140, v140\n\t"
    "v_rcp_f32 v141, v141\n\t"
    "v_rcp_f32 v142, v142\n\t"
    "v_rcp_f32 v143, v143\n\t"
    "v_mul_f32 v64, v64, v140\n\t"
    "v_mul_f32 v65, v65, v141\n\t"
    "v_mul_f32 v66, v66, v142\n\t"
    "v_mul_f32 v67, v67, v143\n\t"
    "v_cvt_pk_bf16_f32 v64, v64, v65\n\t"
    "v_cvt_pk_bf16_f32 v65, v66, v67\n\t"
    "ds_write_b64 %[hw0], v[64:65] offset:2048\n\t"
    "v_mul_f32 v68, v68, v146\n\t"
    "v_mul_f32 v69, v69, v146\n\t"
    "v_mul_f32 v70, v70, v146\n\t"
    "v_mul_f32 v71, v71, v146\n\t"
    "v_fmaak_f32 v140, v68, v68, 0x409d30fc\n\t"
    "v_fmaak_f32 v141, v69, v69, 0x409d30fc\n\t"
    "v_fmaak_f32 v142, v70, v70, 0x409d30fc\n\t"
    "v_fmaak_f32 v143, v71, v71, 0x409d30fc\n\t"
    "v_mul_f32 v140, v140, v68\n\t"
    "v_mul_f32 v141, v141, v69\n\t"
    "v_mul_f32 v142, v142, v70\n\t"
    "v_mul_f32 v143, v143, v71\n\t"
    "v_exp_f32 v140, v140\n\t"
    "v_exp_f32 v141, v141\n\t"
    "v_exp_f32 v142, v142\n\t"
    "v_exp_f32 v143, v143\n\t"
    "v_fma_f32 v140, v140, v144, v144\n\t"
    "v_fma_f32 v141, v141, v144, v144\n\t"
    "v_fma_f32 v142, v142, v144, v144\n\t"
    "v_fma_f32 v143, v143, v144, v144\n\t"
    "v_rcp_f32 v140, v140\n\t"
    "v_rcp_f32 v141, v141\n\t"
    "v_rcp_f32 v142, v142\n\t"
    "v_rcp_f32 v143, v143\n\t"
    "v_mul_f32 v68, v68, v140\n\t"
    "v_mul_f32 v69, v69, v141\n\t"
    "v_mul_f32 v70, v70, v142\n\t"
    "v_mul_f32 v71, v71, v143\n\t"
    "v_cvt_pk_bf16_f32 v68, v68, v69\n\t"
    "v_cvt_pk_bf16_f32 v69, v70, v71\n\t"
    "ds_write_b64 %[hw0], v[68:69] offset:20480\n\t"
    "v_mul_f32 v72, v72, v145\n\t"
    "v_mul_f32 v73, v73, v145\n\t"
    "v_mul_f32 v74, v74, v145\n\t"
    "v_mul_f32 v75, v75, v145\n\t"
    "v_fmaak_f32 v140, v72, v72, 0x409d30fc\n\t"
    "v_fmaak_f32 v141, v73, v73, 0x409d30fc\n\t"
    "v_fmaak_f32 v142, v74, v74, 0x409d30fc\n\t"
    "v_fmaak_f32 v143, v75, v75, 0x409d30fc\n\t"
    "v_mul_f32 v140, v140, v72\n\t"
    "v_mul_f32 v141, v141, v73\n\t"
    "v_mul_f32 v142, v142, v74\n\t"
    "v_mul_f32 v143, v143, v75\n\t"
    "v_exp_f32 v140, v140\n\t"
    "v_exp_f32 v141, v141\n\t"
    "v_exp_f32 v142, v142\n\t"
    "v_exp_f32 v143, v143\n\t"
    "v_fma_f32 v140, v140, v144, v144\n\t"
    "v_fma_f32 v141, v141, v144, v144\n\t"
    "v_fma_f32 v142, v142, v144, v144\n\t"
    "v_fma_f32 v143, v143, v144, v144\n\t"
    "v_rcp_f32 v140, v140\n\t"
    "v_rcp_f32 v141, v141\n\t"
    "v_rcp_f32 v142, v142\n\t"
    "v_rcp_f32 v143, v143\n\t"
    "v_mul_f32 v72, v72, v140\n\t"
    "v_mul_f32 v73, v73, v141\n\t"
    "v_mul_f32 v74, v74, v142\n\t"
    "v_mul_f32 v75, v75, v143\n\t"
    "v_cvt_pk_bf16_f32 v72, v72, v73\n\t"
    "v_cvt_pk_bf16_f32 v73, v74, v75\n\t"
    "ds_write_b64 %[hw0], v[72:73] offset:2056\n\t"
    "v_mul_f32 v76, v76, v146\n\t"
    "v_mul_f32 v77, v77, v146\n\t"
    "v_mul_f32 v78, v78, v146\n\t"
    "v_mul_f32 v79, v79, v146\n\t"
    "v_fmaak_f32 v140, v76, v76, 0x409d30fc\n\t"
    "v_fmaak_f32 v141, v77, v77, 0x409d30fc\n\t"
    "v_fmaak_f32 v142, v78, v78, 0x409d30fc\n\t"
    "v_fmaak_f32 v143, v79, v79, 0x409d30fc\n\t"
    "v_mul_f32 v140, v140, v76\n\t"
    "v_mul_f32 v141, v141, v77\n\t"
    "v_mul_f32 v142, v142, v78\n\t"
    "v_mul_f32 v143, v143, v79\n\t"
    "v_exp_f32 v140, v140\n\t"
    "v_exp_f32 v141, v141\n\t"
    "v_exp_f32 v142, v142\n\t"
    "v_exp_f32 v143, v143\n\t"
    "v_fma_f32 v140, v140, v144, v144\n\t"
    "v_fma_f32 v141, v141, v144, v144\n\t"
    "v_fma_f32 v142, v142, v144, v144\n\t"
    "v_fma_f32 v143, v143, v144, v144\n\t"
    "v_rcp_f32 v140, v140\n\t"
    "v_rcp_f32 v141, v141\n\t"
    "v_rcp_f32 v142, v142\n\t"
    "v_rcp_f32 v143, v143\n\t"
    "v_mul_f32 v76, v76, v140\n\t"
    "v_mul_f32 v77, v77, v141\n\t"
    "v_mul_f32 v78, v78, v142\n\t"
    "v_mul_f32 v79, v79, v143\n\t"
    "v_cvt_pk_bf16_f32 v76, v76, v77\n\t"
    "v_cvt_pk_bf16_f32 v77, v78, v79\n\t"
    "ds_write_b64 %[hw0], v[76:77] offset:20488\n\t"
    "v_mul_f32 v80, v80, v145\n\t"
    "v_mul_f32 v81, v81, v145\n\t"
    "v_mul_f32 v82, v82, v145\n\t"
    "v_mul_f32 v83, v83, v145\n\t"
    "v_fmaak_f32 v140, v80, v80, 0x409d30fc\n\t"
    "v_fmaak_f32 v141, v81, v81, 0x409d30fc\n\t"
    "v_fmaak_f32 v142, v82, v82, 0x409d30fc\n\t"
    "v_fmaak_f32 v143, v83, v83, 0x409d30fc\n\t"
    "v_mul_f32 v140, v140, v80\n\t"
    "v_mul_f32 v141, v141, v81\n\t"
    "v_mul_f32 v142, v142, v82\n\t"
    "v_mul_f32 v143, v143, v83\n\t"
    "v_exp_f32 v140, v140\n\t"
    "v_exp_f32 v141, v141\n\t"
    "v_exp_f32 v142, v142\n\t"
    "v_exp_f32 v143, v143\n\t"
    "v_fma_f32 v140, v140, v144, v144\n\t"
    "v_fma_f32 v141, v141, v144, v144\n\t"
    "v_fma_f32 v142, v142, v144, v144\n\t"
    "v_fma_f32 v143, v143, v144, v144\n\t"
    "v_rcp_f32 v140, v140\n\t"
    "v_rcp_f32 v141, v141\n\t"
    "v_rcp_f32 v142, v142\n\t"
    "v_rcp_f32 v143, v143\n\t"
    "v_mul_f32 v80, v80, v140\n\t"
    "v_mul_f32 v81, v81, v141\n\t"
    "v_mul_f32 v82, v82, v142\n\t"
    "v_mul_f32 v83, v83, v143\n\t"
    "v_cvt_pk_bf16_f32 v80, v80, v81\n\t"
    "v_cvt_pk_bf16_f32 v81, v82, v83\n\t"
    "ds_write_b64 %[hw0], v[80:81] offset:3072\n\t"
    "v_mul_f32 v84, v84, v146\n\t"
    "v_mul_f32 v85, v85, v146\n\t"
    "v_mul_f32 v86, v86, v146\n\t"
    "v_mul_f32 v87, v87, v146\n\t"
    "v_fmaak_f32 v140, v84, v84, 0x409d30fc\n\t"
    "v_fmaak_f32 v141, v85, v85, 0x409d30fc\n\t"
    "v_fmaak_f32 v142, v86, v86, 0x409d30fc\n\t"
    "v_fmaak_f32 v143, v87, v87, 0x409d30fc\n\t"
    "v_mul_f32 v140, v140, v84\n\t"
    "v_mul_f32 v141, v141, v85\n\t"
    "v_mul_f32 v142, v142, v86\n\t"
    "v_mul_f32 v143, v143, v87\n\t"
    "v_exp_f32 v140, v140\n\t"
    "v_exp_f32 v141, v141\n\t"
    "v_exp_f32 v142, v142\n\t"
    "v_exp_f32 v143, v143\n\t"
    "v_fma_f32 v140, v140, v144, v144\n\t"
    "v_fma_f32 v141, v141, v144, v144\n\t"
    "v_fma_f32 v142, v142, v144, v144\n\t"
    "v_fma_f32 v143, v143, v144, v144\n\t"
    "v_rcp_f32 v140, v140\n\t"
    "v_rcp_f32 v141, v141\n\t"
    "v_rcp_f32 v142, v142\n\t"
    "v_rcp_f32 v143, v143\n\t"
    "v_mul_f32 v84, v84, v140\n\t"
    "v_mul_f32 v85, v85, v141\n\t"
    "v_mul_f32 v86, v86, v142\n\t"
    "v_mul_f32 v87, v87, v143\n\t"
    "v_cvt_pk_bf16_f32 v84, v84, v85\n\t"
    "v_cvt_pk_bf16_f32 v85, v86, v87\n\t"
    "ds_write_b64 %[hw0], v[84:85] offset:21504\n\t"
    "v_mul_f32 v88, v88, v145\n\t"
    "v_mul_f32 v89, v89, v145\n\t"
    "v_mul_f32 v90, v90, v145\n\t"
    "v_mul_f32 v91, v91, v145\n\t"
    "v_fmaak_f32 v140, v88, v88, 0x409d30fc\n\t"
    "v_fmaak_f32 v141, v89, v89, 0x409d30fc\n\t"
    "v_fmaak_f32 v142, v90, v90, 0x409d30fc\n\t"
    "v_fmaak_f32 v143, v91, v91, 0x409d30fc\n\t"
    "v_mul_f32 v140, v140, v88\n\t"
    "v_mul_f32 v141, v141, v89\n\t"
    "v_mul_f32 v142, v142, v90\n\t"
    "v_mul_f32 v143, v143, v91\n\t"
    "v_exp_f32 v140, v140\n\t"
    "v_exp_f32 v141, v141\n\t"
    "v_exp_f32 v142, v142\n\t"
    "v_exp_f32 v143, v143\n\t"
    "v_fma_f32 v140, v140, v144, v144\n\t"
    "v_fma_f32 v141, v141, v144, v144\n\t"
    "v_fma_f32 v142, v142, v144, v144\n\t"
    "v_fma_f32 v143, v143, v144, v144\n\t"
    "v_rcp_f32 v140, v140\n\t"
    "v_rcp_f32 v141, v141\n\t"
    "v_rcp_f32 v142, v142\n\t"
    "v_rcp_f32 v143, v143\n\t"
    "v_mul_f32 v88, v88, v140\n\t"
    "v_mul_f32 v89, v89, v141\n\t"
    "v_mul_f32 v90, v90, v142\n\t"
    "v_mul_f32 v91, v91, v143\n\t"
    "v_cvt_pk_bf16_f32 v88, v88, v89\n\t"
    "v_cvt_pk_bf16_f32 v89, v90, v91\n\t"
    "ds_write_b64 %[hw0], v[88:89] offset:3080\n\t"
    "v_mul_f32 v92, v92, v146\n\t"
    "v_mul_f32 v93, v93, v146\n\t"
    "v_mul_f32 v94, v94, v146\n\t"
    "v_mul_f32 v95, v95, v146\n\t"
    "v_fmaak_f32 v140, v92, v92, 0x409d30fc\n\t"
    "v_fmaak_f32 v141, v93, v93, 0x409d30fc\n\t"
    "v_fmaak_f32 v142, v94, v94, 0x409d30fc\n\t"
    "v_fmaak_f32 v143, v95, v95, 0x409d30fc\n\t"
    "v_mul_f32 v140, v140, v92\n\t"
    "v_mul_f32 v141, v141, v93\n\t"
    "v_mul_f32 v142, v142, v94\n\t"
    "v_mul_f32 v143, v143, v95\n\t"
    "v_exp_f32 v140, v140\n\t"
    "v_exp_f32 v141, v141\n\t"
    "v_exp_f32 v142, v142\n\t"
    "v_exp_f32 v143, v143\n\t"
    "v_fma_f32 v140, v140, v144, v144\n\t"
    "v_fma_f32 v141, v141, v144, v144\n\t"
    "v_fma_f32 v142, v142, v144, v144\n\t"
    "v_fma_f32 v143, v143, v144, v144\n\t"
    "v_rcp_f32 v140, v140\n\t"
    "v_rcp_f32 v141, v141\n\t"
    "v_rcp_f32 v142, v142\n\t"
    "v_rcp_f32 v143, v143\n\t"
    "v_mul_f32 v92, v92, v140\n\t"
    "v_mul_f32 v93, v93, v141\n\t"
    "v_mul_f32 v94, v94, v142\n\t"
    "v_mul_f32 v95, v95, v143\n\t"
    "v_cvt_pk_bf16_f32 v92, v92, v93\n\t"
    "v_cvt_pk_bf16_f32 v93, v94, v95\n\t"
    "ds_write_b64 %[hw0], v[92:93] offset:21512\n\t"
    "v_mul_f32 v96, v96, v145\n\t"
    "v_mul_f32 v97, v97, v145\n\t"
    "v_mul_f32 v98, v98, v145\n\t"
    "v_mul_f32 v99, v99, v145\n\t"
    "v_fmaak_f32 v140, v96, v96, 0x409d30fc\n\t"
    "v_fmaak_f32 v141, v97, v97, 0x409d30fc\n\t"
    "v_fmaak_f32 v142, v98, v98, 0x409d30fc\n\t"
    "v_fmaak_f32 v143, v99, v99, 0x409d30fc\n\t"
    "v_mul_f32 v140, v140, v96\n\t"
    "v_mul_f32 v141, v141, v97\n\t"
    "v_mul_f32 v142, v142, v98\n\t"
    "v_mul_f32 v143, v143, v99\n\t"
    "v_exp_f32 v140, v140\n\t"
    "v_exp_f32 v141, v141\n\t"
    "v_exp_f32 v142, v142\n\t"
    "v_exp_f32 v143, v143\n\t"
    "v_fma_f32 v140, v140, v144, v144\n\t"
    "v_fma_f32 v141, v141, v144, v144\n\t"
    "v_fma_f32 v142, v142, v144, v144\n\t"
    "v_fma_f32 v143, v143, v144, v144\n\t"
    "v_rcp_f32 v140, v140\n\t"
    "v_rcp_f32 v141, v141\n\t"
    "v_rcp_f32 v142, v142\n\t"
    "v_rcp_f32 v143, v143\n\t"
    "v_mul_f32 v96, v96, v140\n\t"
    "v_mul_f32 v97, v97, v141\n\t"
    "v_mul_f32 v98, v98, v142\n\t"
    "v_mul_f32 v99, v99, v143\n\t"
    "v_cvt_pk_bf16_f32 v96, v96, v97\n\t"
    "v_cvt_pk_bf16_f32 v97, v98, v99\n\t"
    "ds_write_b64 %[hw8], v[96:97] offset:0\n\t"
    "v_mul_f32 v100, v100, v146\n\t"
    "v_mul_f32 v101, v101, v146\n\t"
    "v_mul_f32 v102, v102, v146\n\t"
    "v_mul_f32 v103, v103, v146\n\t"
    "v_fmaak_f32 v140, v100, v100, 0x409d30fc\n\t"
    "v_fmaak_f32 v141, v101, v101, 0x409d30fc\n\t"
    "v_fmaak_f32 v142, v102, v102, 0x409d30fc\n\t"
    "v_fmaak_f32 v143, v103, v103, 0x409d30fc\n\t"
    "v_mul_f32 v140, v140, v100\n\t"
    "v_mul_f32 v141, v141, v101\n\t"
    "v_mul_f32 v142, v142, v102\n\t"
    "v_mul_f32 v143, v143, v103\n\t"
    "v_exp_f32 v140, v140\n\t"
    "v_exp_f32 v141, v141\n\t"
    "v_exp_f32 v142, v142\n\t"
    "v_exp_f32 v143, v143\n\t"
    "v_fma_f32 v140, v140, v144, v144\n\t"
    "v_fma_f32 v141, v141, v144, v144\n\t"
    "v_fma_f32 v142, v142, v144, v144\n\t"
    "v_fma_f32 v143, v143, v144, v144\n\t"
    "v_rcp_f32 v140, v140\n\t"
    "v_rcp_f32 v141, v141\n\t"
    "v_rcp_f32 v142, v142\n\t"
    "v_rcp_f32 v143, v143\n\t"
    "v_mul_f32 v100, v100, v140\n\t"
    "v_mul_f32 v101, v101, v141\n\t"
    "v_mul_f32 v102, v102, v142\n\t"
    "v_mul_f32 v103, v103, v143\n\t"
    "v_cvt_pk_bf16_f32 v100, v100, v101\n\t"
    "v_cvt_pk_bf16_f32 v101, v102, v103\n\t"
    "ds_write_b64 %[hw8], v[100:101] offset:18432\n\t"
    "s_waitcnt vmcnt(18) lgkmcnt(0)\n\t"
    "s_barrier\n\t"
    "v_mov_b32 v140, %[hb]\n\t"
    "s_cmp_eq_u32 %[wrapat], 0\n\t"
    "s_cselect_b32 s42, 18432, 0\n\t"
    "v_subrev_u32 v140, s42, v140\n\t"
    "ds_read_b128 a[0:3], v140 offset:0\n\t"
    "ds_read_b128 a[4:7], v140 offset:18432\n\t"
    "s_cmp_eq_u32 %[wrapat], 1\n\t"
    "s_cselect_b32 s42, 18432, 0\n\t"
    "v_subrev_u32 v140, s42, v140\n\t"
    "ds_read_b128 a[8:11], v140 offset:1024\n\t"
    "ds_read_b128 a[12:15], v140 offset:19456\n\t"
    "s_cmp_eq_u32 %[wrapat], 2\n\t"
    "s_cselect_b32 s42, 18432, 0\n\t"
    "v_subrev_u32 v140, s42, v140\n\t"
    "ds_read_b128 a[16:19], v140 offset:2048\n\t"
    "ds_read_b128 a[20:23], v140 offset:20480\n\t"
    "s_cmp_eq_u32 %[wrapat], 3\n\t"
    "s_cselect_b32 s42, 18432, 0\n\t"
    "v_subrev_u32 v140, s42, v140\n\t"
    "ds_read_b128 a[24:27], v140 offset:3072\n\t"
    "ds_read_b128 a[28:31], v140 offset:21504\n\t"
    "s_cmp_eq_u32 %[wrapat], 4\n\t"
    "s_cselect_b32 s42, 18432, 0\n\t"
    "v_subrev_u32 v140, s42, v140\n\t"
    "ds_read_b128 a[32:35], v140 offset:4096\n\t"
    "ds_read_b128 a[36:39], v140 offset:22528\n\t"
    "s_cmp_eq_u32 %[wrapat], 5\n\t"
    "s_cselect_b32 s42, 18432, 0\n\t"
    "v_subrev_u32 v140, s42, v140\n\t"
    "ds_read_b128 a[40:43], v140 offset:5120\n\t"
    "ds_read_b128 a[44:47], v140 offset:23552\n\t"
    "s_cmp_eq_u32 %[wrapat], 6\n\t"
    "s_cselect_b32 s42, 18432, 0\n\t"
    "v_subrev_u32 v140, s42, v140\n\t"
    "ds_read_b128 a[48:51], v140 offset:6144\n\t"
    "ds_read_b128 a[52:55], v140 offset:24576\n\t"
    "s_cmp_eq_u32 %[wrapat], 7\n\t"
    "s_cselect_b32 s42, 18432, 0\n\t"
    "v_subrev_u32 v140, s42, v140\n\t"
    "ds_read_b128 a[56:59], v140 offset:7168\n\t"
    "ds_read_b128 a[60:63], v140 offset:25600\n\t"
    "s_cmp_eq_u32 %[wrapat], 8\n\t"
    "s_cselect_b32 s42, 18432, 0\n\t"
    "v_subrev_u32 v140, s42, v140\n\t"
    "ds_read_b128 a[64:67], v140 offset:8192\n\t"
    "ds_read_b128 a[68:71], v140 offset:26624\n\t"
    "s_cmp_eq_u32 %[wrapat], 9\n\t"
    "s_cselect_b32 s42, 18432, 0\n\t"
    "v_subrev_u32 v140, s42, v140\n\t"
    "ds_read_b128 a[72:75], v140 offset:9216\n\t"
    "ds_read_b128 a[76:79], v140 offset:27648\n\t"
    "s_cmp_eq_u32 %[wrapat], 10\n\t"
    "s_cselect_b32 s42, 18432, 0\n\t"
    "v_subrev_u32 v140, s42, v140\n\t"
    "ds_read_b128 a[80:83], v140 offset:10240\n\t"
    "ds_read_b128 a[84:87], v140 offset:28672\n\t"
    "s_cmp_eq_u32 %[wrapat], 11\n\t"
    "s_cselect_b32 s42, 18432, 0\n\t"
    "v_subrev_u32 v140, s42, v140\n\t"
    "ds_read_b128 a[88:91], v140 offset:11264\n\t"
    "ds_read_b128 a[92:95], v140 offset:29696\n\t"
    "s_cmp_eq_u32 %[wrapat], 12\n\t"
    "s_cselect_b32 s42, 18432, 0\n\t"
    "v_subrev_u32 v140, s42, v140\n\t"
    "ds_read_b128 a[96:99], v140 offset:12288\n\t"
    "ds_read_b128 a[100:103], v140 offset:30720\n\t"
    "s_cmp_eq_u32 %[wrapat], 13\n\t"
    "s_cselect_b32 s42, 18432, 0\n\t"
    "v_subrev_u32 v140, s42, v140\n\t"
    "ds_read_b128 a[104:107], v140 offset:13312\n\t"
    "ds_read_b128 a[108:111], v140 offset:31744\n\t"
    "s_cmp_eq_u32 %[wrapat], 14\n\t"
    "s_cselect_b32 s42, 18432, 0\n\t"
    "v_subrev_u32 v140, s42, v140\n\t"
    "ds_read_b128 a[112:115], v140 offset:14336\n\t"
    "ds_read_b128 a[116:119], v140 offset:32768\n\t"
    "s_cmp_eq_u32 %[wrapat], 15\n\t"
    "s_cselect_b32 s42, 18432, 0\n\t"
    "v_subrev_u32 v140, s42, v140\n\t"
    "ds_read_b128 a[120:123], v140 offset:15360\n\t"
    "ds_read_b128 a[124:127], v140 offset:33792\n\t"
    "s_cmp_eq_u32 %[wrapat], 16\n\t"
    "s_cselect_b32 s42, 18432, 0\n\t"
    "v_subrev_u32 v140, s42, v140\n\t"
    "ds_read_b128 a[128:131], v140 offset:16384\n\t"
    "ds_read_b128 a[132:135], v140 offset:34816\n\t"
    "s_cmp_eq_u32 %[wrapat], 17\n\t"
    "s_cselect_b32 s42, 18432, 0\n\t"
    "v_subrev_u32 v140, s42, v140\n\t"
    "ds_read_b128 a[136:139], v140 offset:17408\n\t"
    "ds_read_b128 a[140:143], v140 offset:35840\n\t"
    "ds_read_b64_tr_b16 a[144:145], %[ra10] offset:0\n\t"
    "ds_read_b64_tr_b16 a[146:147], %[ra10] offset:18432\n\t"
    "ds_read_b64_tr_b16 a[148:149], %[ra11] offset:0\n\t"
    "ds_read_b64_tr_b16 a[150:151], %[ra11] offset:18432\n\t"
    "ds_read_b64_tr_b16 a[152:153], %[ra10] offset:512\n\t"
    "ds_read_b64_tr_b16 a[154:155], %[ra10] offset:18944\n\t"
    "ds_read_b64_tr_b16 a[156:157], %[ra11] offset:512\n\t"
    "ds_read_b64_tr_b16 a[158:159], %[ra11] offset:18944\n\t"
    "ds_read_b64_tr_b16 a[160:161], %[ra10] offset:1024\n\t"
    "ds_read_b64_tr_b16 a[162:163], %[ra10] offset:19456\n\t"
    "ds_read_b64_tr_b16 a[164:165], %[ra11] offset:1024\n\t"
    "ds_read_b64_tr_b16 a[166:167], %[ra11] offset:19456\n\t"
    "ds_read_b64_tr_b16 a[168:169], %[ra10] offset:1536\n\t"
    "ds_read_b64_tr_b16 a[170:171], %[ra10] offset:19968\n\t"
    "ds_read_b64_tr_b16 a[172:173], %[ra11] offset:1536\n\t"
    "ds_read_b64_tr_b16 a[174:175], %[ra11] offset:19968\n\t"
    "ds_read_b64_tr_b16 a[176:177], %[ra12] offset:0\n\t"
    "ds_read_b64_tr_b16 a[178:179], %[ra12] offset:18432\n\t"
    "v_lshlrev_b32 v32, 16, v104\n\t"
    "v_and_b32 v33, 0xffff0000, v104\n\t"
    "v_lshlrev_b32 v34, 16, v105\n\t"
    "v_and_b32 v35, 0xffff0000, v105\n\t"
    "v_mul_f32 v32, 0x40faf233, v32\n\t"
    "v_mul_f32 v33, 0x40faf233, v33\n\t"
    "v_mul_f32 v34, 0x40faf233, v34\n\t"
    "v_mul_f32 v35, 0x40faf233, v35\n\t"
    "v_lshlrev_b32 v36, 16, v122\n\t"
    "v_and_b32 v37, 0xffff0000, v122\n\t"
    "v_lshlrev_b32 v38, 16, v123\n\t"
    "v_and_b32 v39, 0xffff0000, v123\n\t"
    "v_mul_f32 v36, 0x40faf233, v36\n\t"
    "v_mul_f32 v37, 0x40faf233, v37\n\t"
    "v_mul_f32 v38, 0x40faf233, v38\n\t"
    "v_mul_f32 v39, 0x40faf233, v39\n\t"
    "v_lshlrev_b32 v40, 16, v106\n\t"
    "v_and_b32 v41, 0xffff0000, v106\n\t"
    "v_lshlrev_b32 v42, 16, v107\n\t"
    "v_and_b32 v43, 0xffff0000, v107\n\t"
    "v_mul_f32 v40, 0x40faf233, v40\n\t"
    "v_mul_f32 v41, 0x40faf233, v41\n\t"
    "v_mul_f32 v42, 0x40faf233, v42\n\t"
    "v_mul_f32 v43, 0x40faf233, v43\n\t"
    "v_lshlrev_b32 v44, 16, v124\n\t"
    "v_and_b32 v45, 0xffff0000, v124\n\t"
    "v_lshlrev_b32 v46, 16, v125\n\t"
    "v_and_b32 v47, 0xffff0000, v125\n\t"
    "v_mul_f32 v44, 0x40faf233, v44\n\t"
    "v_mul_f32 v45, 0x40faf233, v45\n\t"
    "v_mul_f32 v46, 0x40faf233, v46\n\t"
    "v_mul_f32 v47, 0x40faf233, v47\n\t"
    "v_lshlrev_b32 v48, 16, v108\n\t"
    "v_and_b32 v49, 0xffff0000, v108\n\t"
    "v_lshlrev_b32 v50, 16, v109\n\t"
    "v_and_b32 v51, 0xffff0000, v109\n\t"
    "v_mul_f32 v48, 0x40faf233, v48\n\t"
    "v_mul_f32 v49, 0x40faf233, v49\n\t"
    "v_mul_f32 v50, 0x40faf233, v50\n\t"
    "v_mul_f32 v51, 0x40faf233, v51\n\t"
    "v_lshlrev_b32 v52, 16, v126\n\t"
    "v_and_b32 v53, 0xffff0000, v126\n\t"
    "v_lshlrev_b32 v54, 16, v127\n\t"
    "v_and_b32 v55, 0xffff0000, v127\n\t"
    "v_mul_f32 v52, 0x40faf233, v52\n\t"
    "v_mul_f32 v53, 0x40faf233, v53\n\t"
    "v_mul_f32 v54, 0x40faf233, v54\n\t"
    "v_mul_f32 v55, 0x40faf233, v55\n\t"
    "v_lshlrev_b32 v56, 16, v110\n\t"
    "v_and_b32 v57, 0xffff0000, v110\n\t"
    "v_lshlrev_b32 v58, 16, v111\n\t"
    "v_and_b32 v59, 0xffff0000, v111\n\t"
    "v_mul_f32 v56, 0x40faf233, v56\n\t"
    "v_mul_f32 v57, 0x40faf233, v57\n\t"
    "v_mul_f32 v58, 0x40faf233, v58\n\t"
    "v_mul_f32 v59, 0x40faf233, v59\n\t"
    "v_lshlrev_b32 v60, 16, v128\n\t"
    "v_and_b32 v61, 0xffff0000, v128\n\t"
    "v_lshlrev_b32 v62, 16, v129\n\t"
    "v_and_b32 v63, 0xffff0000, v129\n\t"
    "v_mul_f32 v60, 0x40faf233, v60\n\t"
    "v_mul_f32 v61, 0x40faf233, v61\n\t"
    "v_mul_f32 v62, 0x40faf233, v62\n\t"
    "v_mul_f32 v63, 0x40faf233, v63\n\t"
    "v_lshlrev_b32 v64, 16, v112\n\t"
    "v_and_b32 v65, 0xffff0000, v112\n\t"
    "v_lshlrev_b32 v66, 16, v113\n\t"
    "v_and_b32 v67, 0xffff0000, v113\n\t"
    "v_mul_f32 v64, 0x40faf233, v64\n\t"
    "v_mul_f32 v65, 0x40faf233, v65\n\t"
    "v_mul_f32 v66, 0x40faf233, v66\n\t"
    "v_mul_f32 v67, 0x40faf233, v67\n\t"
    "v_lshlrev_b32 v68, 16, v130\n\t"
    "v_and_b32 v69, 0xffff0000, v130\n\t"
    "v_lshlrev_b32 v70, 16, v131\n\t"
    "v_and_b32 v71, 0xffff0000, v131\n\t"
    "v_mul_f32 v68, 0x40faf233, v68\n\t"
    "v_mul_f32 v69, 0x40faf233, v69\n\t"
    "v_mul_f32 v70, 0x40faf233, v70\n\t"
    "v_mul_f32 v71, 0x40faf233, v71\n\t"
    "v_lshlrev_b32 v72, 16, v114\n\t"
    "v_and_b32 v73, 0xffff0000, v114\n\t"
    "v_lshlrev_b32 v74, 16, v115\n\t"
    "v_and_b32 v75, 0xffff0000, v115\n\t"
    "v_mul_f32 v72, 0x40faf233, v72\n\t"
    "v_mul_f32 v73, 0x40faf233, v73\n\t"
    "v_mul_f32 v74, 0x40faf233, v74\n\t"
    "v_mul_f32 v75, 0x40faf233, v75\n\t"
    "v_lshlrev_b32 v76, 16, v132\n\t"
    "v_and_b32 v77, 0xffff0000, v132\n\t"
    "v_lshlrev_b32 v78, 16, v133\n\t"
    "v_and_b32 v79, 0xffff0000, v133\n\t"
    "v_mul_f32 v76, 0x40faf233, v76\n\t"
    "v_mul_f32 v77, 0x40faf233, v77\n\t"
    "v_mul_f32 v78, 0x40faf233, v78\n\t"
    "v_mul_f32 v79, 0x40faf233, v79\n\t"
    "v_lshlrev_b32 v80, 16, v116\n\t"
    "v_and_b32 v81, 0xffff0000, v116\n\t"
    "v_lshlrev_b32 v82, 16, v117\n\t"
    "v_and_b32 v83, 0xffff0000, v117\n\t"
    "v_mul_f32 v80, 0x40faf233, v80\n\t"
    "v_mul_f32 v81, 0x40faf233, v81\n\t"
    "v_mul_f32 v82, 0x40faf233, v82\n\t"
    "v_mul_f32 v83, 0x40faf233, v83\n\t"
    "v_lshlrev_b32 v84, 16, v134\n\t"
    "v_and_b32 v85, 0xffff0000, v134\n\t"
    "v_lshlrev_b32 v86, 16, v135\n\t"
    "v_and_b32 v87, 0xffff0000, v135\n\t"
    "v_mul_f32 v84, 0x40faf233, v84\n\t"
    "v_mul_f32 v85, 0x40faf233, v85\n\t"
    "v_mul_f32 v86, 0x40faf233, v86\n\t"
    "v_mul_f32 v87, 0x40faf233, v87\n\t"
    "v_lshlrev_b32 v88, 16, v118\n\t"
    "v_and_b32 v89, 0xffff0000, v118\n\t"
    "v_lshlrev_b32 v90, 16, v119\n\t"
    "v_and_b32 v91, 0xffff0000, v119\n\t"
    "v_mul_f32 v88, 0x40faf233, v88\n\t"
    "v_mul_f32 v89, 0x40faf233, v89\n\t"
    "v_mul_f32 v90, 0x40faf233, v90\n\t"
    "v_mul_f32 v91, 0x40faf233, v91\n\t"
    "v_lshlrev_b32 v92, 16, v136\n\t"
    "v_and_b32 v93, 0xffff0000, v136\n\t"
    "v_lshlrev_b32 v94, 16, v137\n\t"
    "v_and_b32 v95, 0xffff0000, v137\n\t"
    "v_mul_f32 v92, 0x40faf233, v92\n\t"
    "v_mul_f32 v93, 0x40faf233, v93\n\t"
    "v_mul_f32 v94, 0x40faf233, v94\n\t"
    "v_mul_f32 v95, 0x40faf233, v95\n\t"
    "v_lshlrev_b32 v96, 16, v120\n\t"
    "v_and_b32 v97, 0xffff0000, v120\n\t"
    "v_lshlrev_b32 v98, 16, v121\n\t"
    "v_and_b32 v99, 0xffff0000, v121\n\t"
    "v_mul_f32 v96, 0x40faf233, v96\n\t"
    "v_mul_f32 v97, 0x40faf233, v97\n\t"
    "v_mul_f32 v98, 0x40faf233, v98\n\t"
    "v_mul_f32 v99, 0x40faf233, v99\n\t"
    "v_lshlrev_b32 v100, 16, v138\n\t"
    "v_and_b32 v101, 0xffff0000, v138\n\t"
    "v_lshlrev_b32 v102, 16, v139\n\t"
    "v_and_b32 v103, 0xffff0000, v139\n\t"
    "v_mul_f32 v100, 0x40faf233, v100\n\t"
    "v_mul_f32 v101, 0x40faf233, v101\n\t"
    "v_mul_f32 v102, 0x40faf233, v102\n\t"
    "v_mul_f32 v103, 0x40faf233, v103\n\t"
    "s_waitcnt vmcnt(9) lgkmcnt(0)\n\t"
    "s_barrier\n\t"
    "s_add_u32 m0, %[m0w], 36864\n\t"
    "s_add_u32 s41, s40, 0\n\t"
    "buffer_load_dwordx4 %[wv], %[srd2], s41 offen lds\n\t"
    "s_add_u32 m0, %[m0w], 37888\n\t"
    "s_add_u32 s41, s40, 128\n\t"
    "buffer_load_dwordx4 %[wv], %[srd2], s41 offen lds\n\t"
    "s_add_u32 m0, %[m0w], 38912\n\t"
    "s_add_u32 s41, s40, 256\n\t"
    "buffer_load_dwordx4 %[wv], %[srd2], s41 offen lds\n\t"
    "s_add_u32 m0, %[m0w], 39936\n\t"
    "s_add_u32 s41, s40, 384\n\t"
    "buffer_load_dwordx4 %[wv], %[srd2], s41 offen lds\n\t"
    "s_add_u32 m0, %[m0w], 40960\n\t"
    "s_add_u32 s41, s40, 512\n\t"
    "buffer_load_dwordx4 %[wv], %[srd2], s41 offen lds\n\t"
    "s_add_u32 m0, %[m0w], 41984\n\t"
    "s_add_u32 s41, s40, 640\n\t"
    "buffer_load_dwordx4 %[wv], %[srd2], s41 offen lds\n\t"
    "s_add_u32 m0, %[m0w], 43008\n\t"
    "s_add_u32 s41, s40, 768\n\t"
    "buffer_load_dwordx4 %[wv], %[srd2], s41 offen lds\n\t"
    "s_add_u32 m0, %[m0w], 44032\n\t"
    "s_add_u32 s41, s40, 896\n\t"
    "buffer_load_dwordx4 %[wv], %[srd2], s41 offen lds\n\t"
    "s_add_u32 m0, %[m0w], 45056\n\t"
    "s_add_u32 s41, s40, 1024\n\t"
    "buffer_load_dwordx4 %[wv], %[srd2], s41 offen lds\n\t"
    "s_add_u32 s40, s40, 36864\n\t"
    "s_cmp_eq_u32 s40, 663552\n\t"
    "s_cselect_b32 s40, 0, s40\n\t"
    "v_mfma_f32_16x16x32_bf16 v[32:35], a[144:147], a[0:3], v[32:35]\n\t"
    "ds_read_b64_tr_b16 a[180:181], %[ra10] offset:36864\n\t"
    "ds_read_b64_tr_b16 a[182:183], %[ra10] offset:55296\n\t"
    "s_add_u32 m0, %[m0w], 73728\n\t"
    "s_add_u32 s41, s40, 0\n\t"
    "v_mfma_f32_16x16x32_bf16 v[36:39], a[144:147], a[4:7], v[36:39]\n\t"
    "ds_read_b64_tr_b16 a[184:185], %[ra11] offset:36864\n\t"
    "buffer_load_dwordx4 %[wv], %[srd2], s41 offen lds\n\t"
    "v_mfma_f32_16x16x32_bf16 v[44:47], a[148:151], a[4:7], v[44:47]\n\t"
    "ds_read_b64_tr_b16 a[186:187], %[ra11] offset:55296\n\t"
    "s_add_u32 m0, %[m0w], 74752\n\t"
    "s_add_u32 s41, s40, 128\n\t"
    "v_mfma_f32_16x16x32_bf16 v[40:43], a[148:151], a[0:3], v[40:43]\n\t"
    "ds_read_b64_tr_b16 a[188:189], %[ra10] offset:37376\n\t"
    "ds_read_b64_tr_b16 a[190:191], %[ra10] offset:55808\n\t"
    "buffer_load_dwordx4 %[wv], %[srd2], s41 offen lds\n\t"
    "v_mfma_f32_16x16x32_bf16 v[48:51], a[152:155], a[0:3], v[48:51]\n\t"
    "ds_read_b64_tr_b16 a[192:193], %[ra11] offset:37376\n\t"
    "s_add_u32 m0, %[m0w], 75776\n\t"
    "s_add_u32 s41, s40, 256\n\t"
    "v_mfma_f32_16x16x32_bf16 v[52:55], a[152:155], a[4:7], v[52:55]\n\t"
    "ds_read_b64_tr_b16 a[194:195], %[ra11] offset:55808\n\t"
    "buffer_load_dwordx4 %[wv], %[srd2], s41 offen lds\n\t"
    "v_mfma_f32_16x16x32_bf16 v[60:63], a[156:159], a[4:7], v[60:63]\n\t"
    "ds_read_b64_tr_b16 a[196:197], %[ra10] offset:37888\n\t"
    "s_add_u32 m0, %[m0w], 76800\n\t"
    "s_add_u32 s41, s40, 384\n\t"
    "v_mfma_f32_16x16x32_bf16 v[56:59], a[156:159], a[0:3], v[56:59]\n\t"
    "ds_read_b64_tr_b16 a[198:199], %[ra10] offset:56320\n\t"
    "ds_read_b64_tr_b16 a[200:201], %[ra11] offset:37888\n\t"
    "buffer_load_dwordx4 %[wv], %[srd2], s41 offen lds\n\t"
    "v_mfma_f32_16x16x32_bf16 v[64:67], a[160:163], a[0:3], v[64:67]\n\t"
    "ds_read_b64_tr_b16 a[202:203], %[ra11] offset:56320\n\t"
    "s_add_u32 m0, %[m0w], 77824\n\t"
    "s_add_u32 s41, s40, 512\n\t"
    "v_mfma_f32_16x16x32_bf16 v[68:71], a[160:163], a[4:7], v[68:71]\n\t"
    "ds_read_b64_tr_b16 a[204:205], %[ra10] offset:38400\n\t"
    "buffer_load_dwordx4 %[wv], %[srd2], s41 offen lds\n\t"
    "v_mfma_f32_16x16x32_bf16 v[76:79], a[164:167], a[4:7], v[76:79]\n\t"
    "ds_read_b64_tr_b16 a[206:207], %[ra10] offset:56832\n\t"
    "ds_read_b64_tr_b16 a[208:209], %[ra11] offset:38400\n\t"
    "s_add_u32 m0, %[m0w], 78848\n\t"
    "s_add_u32 s41, s40, 640\n\t"
    "v_mfma_f32_16x16x32_bf16 v[72:75], a[164:167], a[0:3], v[72:75]\n\t"
    "ds_read_b64_tr_b16 a[210:211], %[ra11] offset:56832\n\t"
    "buffer_load_dwordx4 %[wv], %[srd2], s41 offen lds\n\t"
    "v_mfma_f32_16x16x32_bf16 v[80:83], a[168:171], a[0:3], v[80:83]\n\t"
    "ds_read_b64_tr_b16 a[212:213], %[ra12] offset:36864\n\t"
    "s_add_u32 m0, %[m0w], 79872\n\t"
    "s_add_u32 s41, s40, 768\n\t"
    "v_mfma_f32_16x16x32_bf16 v[84:87], a[168:171], a[4:7], v[84:87]\n\t"
    "ds_read_b64_tr_b16 a[214:215], %[ra12] offset:55296\n\t"
    "buffer_load_dwordx4 %[wv], %[srd2], s41 offen lds\n\t"
    "v_mfma_f32_16x16x32_bf16 v[92:95], a[172:175], a[4:7], v[92:95]\n\t"
    "s_add_u32 m0, %[m0w], 80896\n\t"
    "s_add_u32 s41, s40, 896\n\t"
    "v_mfma_f32_16x16x32_bf16 v[88:91], a[172:175], a[0:3], v[88:91]\n\t"
    "buffer_load_dwordx4 %[wv], %[srd2], s41 offen lds\n\t"
    "v_mfma_f32_16x16x32_bf16 v[96:99], a[176:179], a[0:3], v[96:99]\n\t"
    "s_add_u32 m0, %[m0w], 81920\n\t"
    "s_add_u32 s41, s40, 1024\n\t"
    "v_mfma_f32_16x16x32_bf16 v[100:103], a[176:179], a[4:7], v[100:103]\n\t"
    "buffer_load_dwordx4 %[wv], %[srd2], s41 offen lds\n\t"
    "s_add_u32 s40, s40, 36864\n\t"
    "s_cmp_eq_u32 s40, 663552\n\t"
    "s_cselect_b32 s40, 0, s40\n\t"
    "s_waitcnt vmcnt(18) lgkmcnt(0)\n\t"
    "s_barrier\n\t"
    "v_mfma_f32_16x16x32_bf16 v[32:35], a[180:183], a[8:11], v[32:35]\n\t"
    "ds_read_b64_tr_b16 a[144:145], %[ra00] offset:0\n\t"
    "ds_read_b64_tr_b16 a[146:147], %[ra00] offset:18432\n\t"
    "s_add_u32 m0, %[m0w], 110592\n\t"
    "s_add_u32 s41, s40, 0\n\t"
    "v_mfma_f32_16x16x32_bf16 v[36:39], a[180:183], a[12:15], v[36:39]\n\t"
    "ds_read_b64_tr_b16 a[148:149], %[ra01] offset:0\n\t"
    "buffer_load_dwordx4 %[wv], %[srd2], s41 offen lds\n\t"
    "v_mfma_f32_16x16x32_bf16 v[44:47], a[184:187], a[12:15], v[44:47]\n\t"
    "ds_read_b64_tr_b16 a[150:151], %[ra01] offset:18432\n\t"
    "s_add_u32 m0, %[m0w], 111616\n\t"
    "s_add_u32 s41, s40, 128\n\t"
    "v_mfma_f32_16x16x32_bf16 v[40:43], a[184:187], a[8:11], v[40:43]\n\t"
    "ds_read_b64_tr_b16 a[152:153], %[ra00] offset:512\n\t"
    "ds_read_b64_tr_b16 a[154:155], %[ra00] offset:18944\n\t"
    "buffer_load_dwordx4 %[wv], %[srd2], s41 offen lds\n\t"
    "v_mfma_f32_16x16x32_bf16 v[48:51], a[188:191], a[8:11], v[48:51]\n\t"
    "ds_read_b64_tr_b16 a[156:157], %[ra01] offset:512\n\t"
    "s_add_u32 m0, %[m0w], 112640\n\t"
    "s_add_u32 s41, s40, 256\n\t"
    "v_mfma_f32_16x16x32_bf16 v[52:55], a[188:191], a[12:15], v[52:55]\n\t"
    "ds_read_b64_tr_b16 a[158:159], %[ra01] offset:18944\n\t"
    "buffer_load_dwordx4 %[wv], %[srd2], s41 offen lds\n\t"
    "v_mfma_f32_16x16x32_bf16 v[60:63], a[192:195], a[12:15], v[60:63]\n\t"
    "ds_read_b64_tr_b16 a[160:161], %[ra00] offset:1024\n\t"
    "s_add_u32 m0, %[m0w], 113664\n\t"
    "s_add_u32 s41, s40, 384\n\t"
    "v_mfma_f32_16x16x32_bf16 v[56:59], a[192:195], a[8:11], v[56:59]\n\t"
    "ds_read_b64_tr_b16 a[162:163], %[ra00] offset:19456\n\t"
    "ds_read_b64_tr_b16 a[164:165], %[ra01] offset:1024\n\t"
    "buffer_load_dwordx4 %[wv], %[srd2], s41 offen lds\n\t"
    "v_mfma_f32_16x16x32_bf16 v[64:67], a[196:199], a[8:11], v[64:67]\n\t"
    "ds_read_b64_tr_b16 a[166:167], %[ra01] offset:19456\n\t"
    "s_add_u32 m0, %[m0w], 114688\n\t"
    "s_add_u32 s41, s40, 512\n\t"
    "v_mfma_f32_16x16x32_bf16 v[68:71], a[196:199], a[12:15], v[68:71]\n\t"
    "ds_read_b64_tr_b16 a[168:169], %[ra00] offset:1536\n\t"
    "buffer_load_dwordx4 %[wv], %[srd2], s41 offen lds\n\t"
    "v_mfma_f32_16x16x32_bf16 v[76:79], a[200:203], a[12:15], v[76:79]\n\t"
    "ds_read_b64_tr_b16 a[170:171], %[ra00] offset:19968\n\t"
    "ds_read_b64_tr_b16 a[172:173], %[ra01] offset:1536\n\t"
    "s_add_u32 m0, %[m0w], 115712\n\t"
    "s_add_u32 s41, s40, 640\n\t"
    "v_mfma_f32_16x16x32_bf16 v[72:75], a[200:203], a[8:11], v[72:75]\n\t"
    "ds_read_b64_tr_b16 a[174:175], %[ra01] offset:19968\n\t"
    "buffer_load_dwordx4 %[wv], %[srd2], s41 offen lds\n\t"
    "v_mfma_f32_16x16x32_bf16 v[80:83], a[204:207], a[8:11], v[80:83]\n\t"
    "ds_read_b64_tr_b16 a[176:177], %[ra02] offset:0\n\t"
    "s_add_u32 m0, %[m0w], 116736\n\t"
    "s_add_u32 s41, s40, 768\n\t"
    "v_mfma_f32_16x16x32_bf16 v[84:87], a[204:207], a[12:15], v[84:87]\n\t"
    "ds_read_b64_tr_b16 a[178:179], %[ra02] offset:18432\n\t"
    "buffer_load_dwordx4 %[wv], %[srd2], s41 offen lds\n\t"
    "v_mfma_f32_16x16x32_bf16 v[92:95], a[208:211], a[12:15], v[92:95]\n\t"
    "s_add_u32 m0, %[m0w], 117760\n\t"
    "s_add_u32 s41, s40, 896\n\t"
    "v_mfma_f32_16x16x32_bf16 v[88:91], a[208:211], a[8:11], v[88:91]\n\t"
    "buffer_load_dwordx4 %[wv], %[srd2], s41 offen lds\n\t"
    "v_mfma_f32_16x16x32_bf16 v[96:99], a[212:215], a[8:11], v[96:99]\n\t"
    "s_add_u32 m0, %[m0w], 118784\n\t"
    "s_add_u32 s41, s40, 1024\n\t"
    "v_mfma_f32_16x16x32_bf16 v[100:103], a[212:215], a[12:15], v[100:103]\n\t"
    "buffer_load_dwordx4 %[wv], %[srd2], s41 offen lds\n\t"
    "s_add_u32 s40, s40, 36864\n\t"
    "s_cmp_eq_u32 s40, 663552\n\t"
    "s_cselect_b32 s40, 0, s40\n\t"
    "s_waitcnt vmcnt(18) lgkmcnt(0)\n\t"
    "s_barrier\n\t"
    "v_mfma_f32_16x16x32_bf16 v[32:35], a[144:147], a[16:19], v[32:35]\n\t"
    "ds_read_b64_tr_b16 a[180:181], %[ra00] offset:36864\n\t"
    "ds_read_b64_tr_b16 a[182:183], %[ra00] offset:55296\n\t"
    "s_add_u32 m0, %[m0w], 0\n\t"
    "s_add_u32 s41, s40, 0\n\t"
    "v_mfma_f32_16x16x32_bf16 v[36:39], a[144:147], a[20:23], v[36:39]\n\t"
    "ds_read_b64_tr_b16 a[184:185], %[ra01] offset:36864\n\t"
    "buffer_load_dwordx4 %[wv], %[srd2], s41 offen lds\n\t"
    "v_mfma_f32_16x16x32_bf16 v[44:47], a[148:151], a[20:23], v[44:47]\n\t"
    "ds_read_b64_tr_b16 a[186:187], %[ra01] offset:55296\n\t"
    "s_add_u32 m0, %[m0w], 1024\n\t"
    "s_add_u32 s41, s40, 128\n\t"
    "v_mfma_f32_16x16x32_bf16 v[40:43], a[148:151], a[16:19], v[40:43]\n\t"
    "ds_read_b64_tr_b16 a[188:189], %[ra00] offset:37376\n\t"
    "ds_read_b64_tr_b16 a[190:191], %[ra00] offset:55808\n\t"
    "buffer_load_dwordx4 %[wv], %[srd2], s41 offen lds\n\t"
    "v_mfma_f32_16x16x32_bf16 v[48:51], a[152:155], a[16:19], v[48:51]\n\t"
    "ds_read_b64_tr_b16 a[192:193], %[ra01] offset:37376\n\t"
    "s_add_u32 m0, %[m0w], 2048\n\t"
    "s_add_u32 s41, s40, 256\n\t"
    "v_mfma_f32_16x16x32_bf16 v[52:55], a[152:155], a[20:23], v[52:55]\n\t"
    "ds_read_b64_tr_b16 a[194:195], %[ra01] offset:55808\n\t"
    "buffer_load_dwordx4 %[wv], %[srd2], s41 offen lds\n\t"
    "v_mfma_f32_16x16x32_bf16 v[60:63], a[156:159], a[20:23], v[60:63]\n\t"
    "ds_read_b64_tr_b16 a[196:197], %[ra00] offset:37888\n\t"
    "s_add_u32 m0, %[m0w], 3072\n\t"
    "s_add_u32 s41, s40, 384\n\t"
    "v_mfma_f32_16x16x32_bf16 v[56:59], a[156:159], a[16:19], v[56:59]\n\t"
    "ds_read_b64_tr_b16 a[198:199], %[ra00] offset:56320\n\t"
    "ds_read_b64_tr_b16 a[200:201], %[ra01] offset:37888\n\t"
    "buffer_load_dwordx4 %[wv], %[srd2], s41 offen lds\n\t"
    "v_mfma_f32_16x16x32_bf16 v[64:67], a[160:163], a[16:19], v[64:67]\n\t"
    "ds_read_b64_tr_b16 a[202:203], %[ra01] offset:56320\n\t"
    "s_add_u32 m0, %[m0w], 4096\n\t"
    "s_add_u32 s41, s40, 512\n\t"
    "v_mfma_f32_16x16x32_bf16 v[68:71], a[160:163], a[20:23], v[68:71]\n\t"
    "ds_read_b64_tr_b16 a[204:205], %[ra00] offset:38400\n\t"
    "buffer_load_dwordx4 %[wv], %[srd2], s41 offen lds\n\t"
    "v_mfma_f32_16x16x32_bf16 v[76:79], a[164:167], a[20:23], v[76:79]\n\t"
    "ds_read_b64_tr_b16 a[206:207], %[ra00] offset:56832\n\t"
    "ds_read_b64_tr_b16 a[208:209], %[ra01] offset:38400\n\t"
    "s_add_u32 m0, %[m0w], 5120\n\t"
    "s_add_u32 s41, s40, 640\n\t"
    "v_mfma_f32_16x16x32_bf16 v[72:75], a[164:167], a[16:19], v[72:75]\n\t"
    "ds_read_b64_tr_b16 a[210:211], %[ra01] offset:56832\n\t"
    "buffer_load_dwordx4 %[wv], %[srd2], s41 offen lds\n\t"
    "v_mfma_f32_16x16x32_bf16 v[80:83], a[168:171], a[16:19], v[80:83]\n\t"
    "ds_read_b64_tr_b16 a[212:213], %[ra02] offset:36864\n\t"
    "s_add_u32 m0, %[m0w], 6144\n\t"
    "s_add_u32 s41, s40, 768\n\t"
    "v_mfma_f32_16x16x32_bf16 v[84:87], a[168:171], a[20:23], v[84:87]\n\t"
    "ds_read_b64_tr_b16 a[214:215], %[ra02] offset:55296\n\t"
    "buffer_load_dwordx4 %[wv], %[srd2], s41 offen lds\n\t"
    "v_mfma_f32_16x16x32_bf16 v[92:95], a[172:175], a[20:23], v[92:95]\n\t"
    "s_add_u32 m0, %[m0w], 7168\n\t"
    "s_add_u32 s41, s40, 896\n\t"
    "v_mfma_f32_16x16x32_bf16 v[88:91], a[172:175], a[16:19], v[88:91]\n\t"
    "buffer_load_dwordx4 %[wv], %[srd2], s41 offen lds\n\t"
    "v_mfma_f32_16x16x32_bf16 v[96:99], a[176:179], a[16:19], v[96:99]\n\t"
    "s_add_u32 m0, %[m0w], 8192\n\t"
    "s_add_u32 s41, s40, 1024\n\t"
    "v_mfma_f32_16x16x32_bf16 v[100:103], a[176:179], a[20:23], v[100:103]\n\t"
    "buffer_load_dwordx4 %[wv], %[srd2], s41 offen lds\n\t"
    "s_add_u32 s40, s40, 36864\n\t"
    "s_cmp_eq_u32 s40, 663552\n\t"
    "s_cselect_b32 s40, 0, s40\n\t"
    "s_waitcnt vmcnt(18) lgkmcnt(0)\n\t"
    "s_barrier\n\t"
    "v_mfma_f32_16x16x32_bf16 v[32:35], a[180:183], a[24:27], v[32:35]\n\t"
    "ds_read_b64_tr_b16 a[144:145], %[ra10] offset:0\n\t"
    "ds_read_b64_tr_b16 a[146:147], %[ra10] offset:18432\n\t"
    "s_add_u32 m0, %[m0w], 36864\n\t"
    "s_add_u32 s41, s40, 0\n\t"
    "v_mfma_f32_16x16x32_bf16 v[36:39], a[180:183], a[28:31], v[36:39]\n\t"
    "ds_read_b64_tr_b16 a[148:149], %[ra11] offset:0\n\t"
    "buffer_load_dwordx4 %[wv], %[srd2], s41 offen lds\n\t"
    "v_mfma_f32_16x16x32_bf16 v[44:47], a[184:187], a[28:31], v[44:47]\n\t"
    "ds_read_b64_tr_b16 a[150:151], %[ra11] offset:18432\n\t"
    "s_add_u32 m0, %[m0w], 37888\n\t"
    "s_add_u32 s41, s40, 128\n\t"
    "v_mfma_f32_16x16x32_bf16 v[40:43], a[184:187], a[24:27], v[40:43]\n\t"
    "ds_read_b64_tr_b16 a[152:153], %[ra10] offset:512\n\t"
    "ds_read_b64_tr_b16 a[154:155], %[ra10] offset:18944\n\t"
    "buffer_load_dwordx4 %[wv], %[srd2], s41 offen lds\n\t"
    "v_mfma_f32_16x16x32_bf16 v[48:51], a[188:191], a[24:27], v[48:51]\n\t"
    "ds_read_b64_tr_b16 a[156:157], %[ra11] offset:512\n\t"
    "s_add_u32 m0, %[m0w], 38912\n\t"
    "s_add_u32 s41, s40, 256\n\t"
    "v_mfma_f32_16x16x32_bf16 v[52:55], a[188:191], a[28:31], v[52:55]\n\t"
    "ds_read_b64_tr_b16 a[158:159], %[ra11] offset:18944\n\t"
    "buffer_load_dwordx4 %[wv], %[srd2], s41 offen lds\n\t"
    "v_mfma_f32_16x16x32_bf16 v[60:63], a[192:195], a[28:31], v[60:63]\n\t"
    "ds_read_b64_tr_b16 a[160:161], %[ra10] offset:1024\n\t"
    "s_add_u32 m0, %[m0w], 39936\n\t"
    "s_add_u32 s41, s40, 384\n\t"
    "v_mfma_f32_16x16x32_bf16 v[56:59], a[192:195], a[24:27], v[56:59]\n\t"
    "ds_read_b64_tr_b16 a[162:163], %[ra10] offset:19456\n\t"
    "ds_read_b64_tr_b16 a[164:165], %[ra11] offset:1024\n\t"
    "buffer_load_dwordx4 %[wv], %[srd2], s41 offen lds\n\t"
    "v_mfma_f32_16x16x32_bf16 v[64:67], a[196:199], a[24:27], v[64:67]\n\t"
    "ds_read_b64_tr_b16 a[166:167], %[ra11] offset:19456\n\t"
    "s_add_u32 m0, %[m0w], 40960\n\t"
    "s_add_u32 s41, s40, 512\n\t"
    "v_mfma_f32_16x16x32_bf16 v[68:71], a[196:199], a[28:31], v[68:71]\n\t"
    "ds_read_b64_tr_b16 a[168:169], %[ra10] offset:1536\n\t"
    "buffer_load_dwordx4 %[wv], %[srd2], s41 offen lds\n\t"
    "v_mfma_f32_16x16x32_bf16 v[76:79], a[200:203], a[28:31], v[76:79]\n\t"
    "ds_read_b64_tr_b16 a[170:171], %[ra10] offset:19968\n\t"
    "ds_read_b64_tr_b16 a[172:173], %[ra11] offset:1536\n\t"
    "s_add_u32 m0, %[m0w], 41984\n\t"
    "s_add_u32 s41, s40, 640\n\t"
    "v_mfma_f32_16x16x32_bf16 v[72:75], a[200:203], a[24:27], v[72:75]\n\t"
    "ds_read_b64_tr_b16 a[174:175], %[ra11] offset:19968\n\t"
    "buffer_load_dwordx4 %[wv], %[srd2], s41 offen lds\n\t"
    "v_mfma_f32_16x16x32_bf16 v[80:83], a[204:207], a[24:27], v[80:83]\n\t"
    "ds_read_b64_tr_b16 a[176:177], %[ra12] offset:0\n\t"
    "s_add_u32 m0, %[m0w], 43008\n\t"
    "s_add_u32 s41, s40, 768\n\t"
    "v_mfma_f32_16x16x32_bf16 v[84:87], a[204:207], a[28:31], v[84:87]\n\t"
    "ds_read_b64_tr_b16 a[178:179], %[ra12] offset:18432\n\t"
    "buffer_load_dwordx4 %[wv], %[srd2], s41 offen lds\n\t"
    "v_mfma_f32_16x16x32_bf16 v[92:95], a[208:211], a[28:31], v[92:95]\n\t"
    "s_add_u32 m0, %[m0w], 44032\n\t"
    "s_add_u32 s41, s40, 896\n\t"
    "v_mfma_f32_16x16x32_bf16 v[88:91], a[208:211], a[24:27], v[88:91]\n\t"
    "buffer_load_dwordx4 %[wv], %[srd2], s41 offen lds\n\t"
    "v_mfma_f32_16x16x32_bf16 v[96:99], a[212:215], a[24:27], v[96:99]\n\t"
    "buffer_load_dword v141, %[tq], %[srdq], 0 offen\n\t"
    "s_add_u32 m0, %[m0w], 45056\n\t"
    "s_add_u32 s41, s40, 1024\n\t"
    "v_mfma_f32_16x16x32_bf16 v[100:103], a[212:215], a[28:31], v[100:103]\n\t"
    "buffer_load_dwordx4 %[wv], %[srd2], s41 offen lds\n\t"
    "s_add_u32 s40, s40, 36864\n\t"
    "s_cmp_eq_u32 s40, 663552\n\t"
    "s_cselect_b32 s40, 0, s40\n\t"
    "s_waitcnt vmcnt(19) lgkmcnt(0)\n\t"
    "s_barrier\n\t"
    "v_mfma_f32_16x16x32_bf16 v[32:35], a[144:147], a[32:35], v[32:35]\n\t"
    "ds_read_b64_tr_b16 a[180:181], %[ra10] offset:36864\n\t"
    "ds_read_b64_tr_b16 a[182:183], %[ra10] offset:55296\n\t"
    "s_add_u32 m0, %[m0w], 73728\n\t"
    "s_add_u32 s41, s40, 0\n\t"
    "v_mfma_f32_16x16x32_bf16 v[36:39], a[144:147], a[36:39], v[36:39]\n\t"
    "ds_read_b64_tr_b16 a[184:185], %[ra11] offset:36864\n\t"
    "buffer_load_dwordx4 %[wv], %[srd2], s41 offen lds\n\t"
    "v_mfma_f32_16x16x32_bf16 v[44:47], a[148:151], a[36:39], v[44:47]\n\t"
    "ds_read_b64_tr_b16 a[186:187], %[ra11] offset:55296\n\t"
    "s_add_u32 m0, %[m0w], 74752\n\t"
    "s_add_u32 s41, s40, 128\n\t"
    "v_mfma_f32_16x16x32_bf16 v[40:43], a[148:151], a[32:35], v[40:43]\n\t"
    "ds_read_b64_tr_b16 a[188:189], %[ra10] offset:37376\n\t"
    "ds_read_b64_tr_b16 a[190:191], %[ra10] offset:55808\n\t"
    "buffer_load_dwordx4 %[wv], %[srd2], s41 offen lds\n\t"
    "v_mfma_f32_16x16x32_bf16 v[48:51], a[152:155], a[32:35], v[48:51]\n\t"
    "ds_read_b64_tr_b16 a[192:193], %[ra11] offset:37376\n\t"
    "s_add_u32 m0, %[m0w], 75776\n\t"
    "s_add_u32 s41, s40, 256\n\t"
    "v_mfma_f32_16x16x32_bf16 v[52:55], a[152:155], a[36:39], v[52:55]\n\t"
    "ds_read_b64_tr_b16 a[194:195], %[ra11] offset:55808\n\t"
    "buffer_load_dwordx4 %[wv], %[srd2], s41 offen lds\n\t"
    "v_mfma_f32_16x16x32_bf16 v[60:63], a[156:159], a[36:39], v[60:63]\n\t"
    "ds_read_b64_tr_b16 a[196:197], %[ra10] offset:37888\n\t"
    "s_add_u32 m0, %[m0w], 76800\n\t"
    "s_add_u32 s41, s40, 384\n\t"
    "v_mfma_f32_16x16x32_bf16 v[56:59], a[156:159], a[32:35], v[56:59]\n\t"
    "ds_read_b64_tr_b16 a[198:199], %[ra10] offset:56320\n\t"
    "ds_read_b64_tr_b16 a[200:201], %[ra11] offset:37888\n\t"
    "buffer_load_dwordx4 %[wv], %[srd2], s41 offen lds\n\t"
    "v_mfma_f32_16x16x32_bf16 v[64:67], a[160:163], a[32:35], v[64:67]\n\t"
    "ds_read_b64_tr_b16 a[202:203], %[ra11] offset:56320\n\t"
    "s_add_u32 m0, %[m0w], 77824\n\t"
    "s_add_u32 s41, s40, 512\n\t"
    "v_mfma_f32_16x16x32_bf16 v[68:71], a[160:163], a[36:39], v[68:71]\n\t"
    "ds_read_b64_tr_b16 a[204:205], %[ra10] offset:38400\n\t"
    "buffer_load_dwordx4 %[wv], %[srd2], s41 offen lds\n\t"
    "v_mfma_f32_16x16x32_bf16 v[76:79], a[164:167], a[36:39], v[76:79]\n\t"
    "ds_read_b64_tr_b16 a[206:207], %[ra10] offset:56832\n\t"
    "ds_read_b64_tr_b16 a[208:209], %[ra11] offset:38400\n\t"
    "s_add_u32 m0, %[m0w], 78848\n\t"
    "s_add_u32 s41, s40, 640\n\t"
    "v_mfma_f32_16x16x32_bf16 v[72:75], a[164:167], a[32:35], v[72:75]\n\t"
    "ds_read_b64_tr_b16 a[210:211], %[ra11] offset:56832\n\t"
    "buffer_load_dwordx4 %[wv], %[srd2], s41 offen lds\n\t"
    "v_mfma_f32_16x16x32_bf16 v[80:83], a[168:171], a[32:35], v[80:83]\n\t"
    "ds_read_b64_tr_b16 a[212:213], %[ra12] offset:36864\n\t"
    "s_add_u32 m0, %[m0w], 79872\n\t"
    "s_add_u32 s41, s40, 768\n\t"
    "v_mfma_f32_16x16x32_bf16 v[84:87], a[168:171], a[36:39], v[84:87]\n\t"
    "ds_read_b64_tr_b16 a[214:215], %[ra12] offset:55296\n\t"
    "buffer_load_dwordx4 %[wv], %[srd2], s41 offen lds\n\t"
    "v_mfma_f32_16x16x32_bf16 v[92:95], a[172:175], a[36:39], v[92:95]\n\t"
    "s_add_u32 m0, %[m0w], 80896\n\t"
    "s_add_u32 s41, s40, 896\n\t"
    "v_mfma_f32_16x16x32_bf16 v[88:91], a[172:175], a[32:35], v[88:91]\n\t"
    "buffer_load_dwordx4 %[wv], %[srd2], s41 offen lds\n\t"
    "v_mfma_f32_16x16x32_bf16 v[96:99], a[176:179], a[32:35], v[96:99]\n\t"
    "s_add_u32 m0, %[m0w], 81920\n\t"
    "s_add_u32 s41, s40, 1024\n\t"
    "v_mfma_f32_16x16x32_bf16 v[100:103], a[176:179], a[36:39], v[100:103]\n\t"
    "buffer_load_dwordx4 %[wv], %[srd2], s41 offen lds\n\t"
    "s_add_u32 s40, s40, 36864\n\t"
    "s_cmp_eq_u32 s40, 663552\n\t"
    "s_cselect_b32 s40, 0, s40\n\t"
    "s_waitcnt vmcnt(19) lgkmcnt(0)\n\t"
    "s_barrier\n\t"
    "v_mfma_f32_16x16x32_bf16 v[32:35], a[180:183], a[40:43], v[32:35]\n\t"
    "ds_read_b64_tr_b16 a[144:145], %[ra00] offset:0\n\t"
    "ds_read_b64_tr_b16 a[146:147], %[ra00] offset:18432\n\t"
    "s_add_u32 m0, %[m0w], 110592\n\t"
    "s_add_u32 s41, s40, 0\n\t"
    "v_mfma_f32_16x16x32_bf16 v[36:39], a[180:183], a[44:47], v[36:39]\n\t"
    "ds_read_b64_tr_b16 a[148:149], %[ra01] offset:0\n\t"
    "buffer_load_dwordx4 %[wv], %[srd2], s41 offen lds\n\t"
    "v_mfma_f32_16x16x32_bf16 v[44:47], a[184:187], a[44:47], v[44:47]\n\t"
    "ds_read_b64_tr_b16 a[150:151], %[ra01] offset:18432\n\t"
    "s_add_u32 m0, %[m0w], 111616\n\t"
    "s_add_u32 s41, s40, 128\n\t"
    "v_mfma_f32_16x16x32_bf16 v[40:43], a[184:187], a[40:43], v[40:43]\n\t"
    "ds_read_b64_tr_b16 a[152:153], %[ra00] offset:512\n\t"
    "ds_read_b64_tr_b16 a[154:155], %[ra00] offset:18944\n\t"
    "buffer_load_dwordx4 %[wv], %[srd2], s41 offen lds\n\t"
    "v_mfma_f32_16x16x32_bf16 v[48:51], a[188:191], a[40:43], v[48:51]\n\t"
    "ds_read_b64_tr_b16 a[156:157], %[ra01] offset:512\n\t"
    "s_add_u32 m0, %[m0w], 112640\n\t"
    "s_add_u32 s41, s40, 256\n\t"
    "v_mfma_f32_16x16x32_bf16 v[52:55], a[188:191], a[44:47], v[52:55]\n\t"
    "ds_read_b64_tr_b16 a[158:159], %[ra01] offset:18944\n\t"
    "buffer_load_dwordx4 %[wv], %[srd2], s41 offen lds\n\t"
    "v_mfma_f32_16x16x32_bf16 v[60:63], a[192:195], a[44:47], v[60:63]\n\t"
    "ds_read_b64_tr_b16 a[160:161], %[ra00] offset:1024\n\t"
    "s_add_u32 m0, %[m0w], 113664\n\t"
    "s_add_u32 s41, s40, 384\n\t"
    "v_mfma_f32_16x16x32_bf16 v[56:59], a[192:195], a[40:43], v[56:59]\n\t"
    "ds_read_b64_tr_b16 a[162:163], %[ra00] offset:19456\n\t"
    "ds_read_b64_tr_b16 a[164:165], %[ra01] offset:1024\n\t"
    "buffer_load_dwordx4 %[wv], %[srd2], s41 offen lds\n\t"
    "v_mfma_f32_16x16x32_bf16 v[64:67], a[196:199], a[40:43], v[64:67]\n\t"
    "ds_read_b64_tr_b16 a[166:167], %[ra01] offset:19456\n\t"
    "s_add_u32 m0, %[m0w], 114688\n\t"
    "s_add_u32 s41, s40, 512\n\t"
    "v_mfma_f32_16x16x32_bf16 v[68:71], a[196:199], a[44:47], v[68:71]\n\t"
    "ds_read_b64_tr_b16 a[168:169], %[ra00] offset:1536\n\t"
    "buffer_load_dwordx4 %[wv], %[srd2], s41 offen lds\n\t"
    "v_mfma_f32_16x16x32_bf16 v[76:79], a[200:203], a[44:47], v[76:79]\n\t"
    "ds_read_b64_tr_b16 a[170:171], %[ra00] offset:19968\n\t"
    "ds_read_b64_tr_b16 a[172:173], %[ra01] offset:1536\n\t"
    "s_add_u32 m0, %[m0w], 115712\n\t"
    "s_add_u32 s41, s40, 640\n\t"
    "v_mfma_f32_16x16x32_bf16 v[72:75], a[200:203], a[40:43], v[72:75]\n\t"
    "ds_read_b64_tr_b16 a[174:175], %[ra01] offset:19968\n\t"
    "buffer_load_dwordx4 %[wv], %[srd2], s41 offen lds\n\t"
    "v_mfma_f32_16x16x32_bf16 v[80:83], a[204:207], a[40:43], v[80:83]\n\t"
    "ds_read_b64_tr_b16 a[176:177], %[ra02] offset:0\n\t"
    "s_add_u32 m0, %[m0w], 116736\n\t"
    "s_add_u32 s41, s40, 768\n\t"
    "v_mfma_f32_16x16x32_bf16 v[84:87], a[204:207], a[44:47], v[84:87]\n\t"
    "ds_read_b64_tr_b16 a[178:179], %[ra02] offset:18432\n\t"
    "buffer_load_dwordx4 %[wv], %[srd2], s41 offen lds\n\t"
    "v_mfma_f32_16x16x32_bf16 v[92:95], a[208:211], a[44:47], v[92:95]\n\t"
    "s_add_u32 m0, %[m0w], 117760\n\t"
    "s_add_u32 s41, s40, 896\n\t"
    "v_mfma_f32_16x16x32_bf16 v[88:91], a[208:211], a[40:43], v[88:91]\n\t"
    "buffer_load_dwordx4 %[wv], %[srd2], s41 offen lds\n\t"
    "v_mfma_f32_16x16x32_bf16 v[96:99], a[212:215], a[40:43], v[96:99]\n\t"
    "s_add_u32 m0, %[m0w], 118784\n\t"
    "s_add_u32 s41, s40, 1024\n\t"
    "v_mfma_f32_16x16x32_bf16 v[100:103], a[212:215], a[44:47], v[100:103]\n\t"
    "buffer_load_dwordx4 %[wv], %[srd2], s41 offen lds\n\t"
    "s_add_u32 s40, s40, 36864\n\t"
    "s_cmp_eq_u32 s40, 663552\n\t"
    "s_cselect_b32 s40, 0, s40\n\t"
    "s_waitcnt vmcnt(18) lgkmcnt(0)\n\t"
    "s_barrier\n\t"
    "v_mfma_f32_16x16x32_bf16 v[32:35], a[144:147], a[48:51], v[32:35]\n\t"
    "ds_read_b64_tr_b16 a[180:181], %[ra00] offset:36864\n\t"
    "ds_read_b64_tr_b16 a[182:183], %[ra00] offset:55296\n\t"
    "s_add_u32 m0, %[m0w], 0\n\t"
    "s_add_u32 s41, s40, 0\n\t"
    "v_mfma_f32_16x16x32_bf16 v[36:39], a[144:147], a[52:55], v[36:39]\n\t"
    "ds_read_b64_tr_b16 a[184:185], %[ra01] offset:36864\n\t"
    "buffer_load_dwordx4 %[wv], %[srd2], s41 offen lds\n\t"
    "v_mfma_f32_16x16x32_bf16 v[44:47], a[148:151], a[52:55], v[44:47]\n\t"
    "ds_read_b64_tr_b16 a[186:187], %[ra01] offset:55296\n\t"
    "s_add_u32 m0, %[m0w], 1024\n\t"
    "s_add_u32 s41, s40, 128\n\t"
    "v_mfma_f32_16x16x32_bf16 v[40:43], a[148:151], a[48:51], v[40:43]\n\t"
    "ds_read_b64_tr_b16 a[188:189], %[ra00] offset:37376\n\t"
    "ds_read_b64_tr_b16 a[190:191], %[ra00] offset:55808\n\t"
    "buffer_load_dwordx4 %[wv], %[srd2], s41 offen lds\n\t"
    "v_mfma_f32_16x16x32_bf16 v[48:51], a[152:155], a[48:51], v[48:51]\n\t"
    "ds_read_b64_tr_b16 a[192:193], %[ra01] offset:37376\n\t"
    "s_add_u32 m0, %[m0w], 2048\n\t"
    "s_add_u32 s41, s40, 256\n\t"
    "v_mfma_f32_16x16x32_bf16 v[52:55], a[152:155], a[52:55], v[52:55]\n\t"
    "ds_read_b64_tr_b16 a[194:195], %[ra01] offset:55808\n\t"
    "buffer_load_dwordx4 %[wv], %[srd2], s41 offen lds\n\t"
    "v_mfma_f32_16x16x32_bf16 v[60:63], a[156:159], a[52:55], v[60:63]\n\t"
    "ds_read_b64_tr_b16 a[196:197], %[ra00] offset:37888\n\t"
    "s_add_u32 m0, %[m0w], 3072\n\t"
    "s_add_u32 s41, s40, 384\n\t"
    "v_mfma_f32_16x16x32_bf16 v[56:59], a[156:159], a[48:51], v[56:59]\n\t"
    "ds_read_b64_tr_b16 a[198:199], %[ra00] offset:56320\n\t"
    "ds_read_b64_tr_b16 a[200:201], %[ra01] offset:37888\n\t"
    "buffer_load_dwordx4 %[wv], %[srd2], s41 offen lds\n\t"
    "v_mfma_f32_16x16x32_bf16 v[64:67], a[160:163], a[48:51], v[64:67]\n\t"
    "ds_read_b64_tr_b16 a[202:203], %[ra01] offset:56320\n\t"
    "s_add_u32 m0, %[m0w], 4096\n\t"
    "s_add_u32 s41, s40, 512\n\t"
    "v_mfma_f32_16x16x32_bf16 v[68:71], a[160:163], a[52:55], v[68:71]\n\t"
    "ds_read_b64_tr_b16 a[204:205], %[ra00] offset:38400\n\t"
    "buffer_load_dwordx4 %[wv], %[srd2], s41 offen lds\n\t"
    "v_mfma_f32_16x16x32_bf16 v[76:79], a[164:167], a[52:55], v[76:79]\n\t"
    "ds_read_b64_tr_b16 a[206:207], %[ra00] offset:56832\n\t"
    "ds_read_b64_tr_b16 a[208:209], %[ra01] offset:38400\n\t"
    "s_add_u32 m0, %[m0w], 5120\n\t"
    "s_add_u32 s41, s40, 640\n\t"
    "v_mfma_f32_16x16x32_bf16 v[72:75], a[164:167], a[48:51], v[72:75]\n\t"
    "ds_read_b64_tr_b16 a[210:211], %[ra01] offset:56832\n\t"
    "buffer_load_dwordx4 %[wv], %[srd2], s41 offen lds\n\t"
    "v_mfma_f32_16x16x32_bf16 v[80:83], a[168:171], a[48:51], v[80:83]\n\t"
    "ds_read_b64_tr_b16 a[212:213], %[ra02] offset:36864\n\t"
    "s_add_u32 m0, %[m0w], 6144\n\t"
    "s_add_u32 s41, s40, 768\n\t"
    "v_mfma_f32_16x16x32_bf16 v[84:87], a[168:171], a[52:55], v[84:87]\n\t"
    "ds_read_b64_tr_b16 a[214:215], %[ra02] offset:55296\n\t"
    "buffer_load_dwordx4 %[wv], %[srd2], s41 offen lds\n\t"
    "v_mfma_f32_16x16x32_bf16 v[92:95], a[172:175], a[52:55], v[92:95]\n\t"
    "s_add_u32 m0, %[m0w], 7168\n\t"
    "s_add_u32 s41, s40, 896\n\t"
    "v_mfma_f32_16x16x32_bf16 v[88:91], a[172:175], a[48:51], v[88:91]\n\t"
    "buffer_load_dwordx4 %[wv], %[srd2], s41 offen lds\n\t"
    "v_mfma_f32_16x16x32_bf16 v[96:99], a[176:179], a[48:51], v[96:99]\n\t"
    "s_add_u32 m0, %[m0w], 8192\n\t"
    "s_add_u32 s41, s40, 1024\n\t"
    "v_mfma_f32_16x16x32_bf16 v[100:103], a[176:179], a[52:55], v[100:103]\n\t"
    "buffer_load_dwordx4 %[wv], %[srd2], s41 offen lds\n\t"
    "s_add_u32 s40, s40, 36864\n\t"
    "s_cmp_eq_u32 s40, 663552\n\t"
    "s_cselect_b32 s40, 0, s40\n\t"
    "s_waitcnt vmcnt(18) lgkmcnt(0)\n\t"
    "s_barrier\n\t"
    "v_mfma_f32_16x16x32_bf16 v[32:35], a[180:183], a[56:59], v[32:35]\n\t"
    "ds_read_b64_tr_b16 a[144:145], %[ra10] offset:0\n\t"
    "ds_read_b64_tr_b16 a[146:147], %[ra10] offset:18432\n\t"
    "s_add_u32 m0, %[m0w], 36864\n\t"
    "s_add_u32 s41, s40, 0\n\t"
    "v_mfma_f32_16x16x32_bf16 v[36:39], a[180:183], a[60:63], v[36:39]\n\t"
    "ds_read_b64_tr_b16 a[148:149], %[ra11] offset:0\n\t"
    "buffer_load_dwordx4 %[wv], %[srd2], s41 offen lds\n\t"
    "v_mfma_f32_16x16x32_bf16 v[44:47], a[184:187], a[60:63], v[44:47]\n\t"
    "ds_read_b64_tr_b16 a[150:151], %[ra11] offset:18432\n\t"
    "s_add_u32 m0, %[m0w], 37888\n\t"
    "s_add_u32 s41, s40, 128\n\t"
    "v_mfma_f32_16x16x32_bf16 v[40:43], a[184:187], a[56:59], v[40:43]\n\t"
    "ds_read_b64_tr_b16 a[152:153], %[ra10] offset:512\n\t"
    "ds_read_b64_tr_b16 a[154:155], %[ra10] offset:18944\n\t"
    "buffer_load_dwordx4 %[wv], %[srd2], s41 offen lds\n\t"
    "v_mfma_f32_16x16x32_bf16 v[48:51], a[188:191], a[56:59], v[48:51]\n\t"
    "ds_read_b64_tr_b16 a[156:157], %[ra11] offset:512\n\t"
    "s_add_u32 m0, %[m0w], 38912\n\t"
    "s_add_u32 s41, s40, 256\n\t"
    "v_mfma_f32_16x16x32_bf16 v[52:55], a[188:191], a[60:63], v[52:55]\n\t"
    "ds_read_b64_tr_b16 a[158:159], %[ra11] offset:18944\n\t"
    "buffer_load_dwordx4 %[wv], %[srd2], s41 offen lds\n\t"
    "v_mfma_f32_16x16x32_bf16 v[60:63], a[192:195], a[60:63], v[60:63]\n\t"
    "ds_read_b64_tr_b16 a[160:161], %[ra10] offset:1024\n\t"
    "s_add_u32 m0, %[m0w], 39936\n\t"
    "s_add_u32 s41, s40, 384\n\t"
    "v_mfma_f32_16x16x32_bf16 v[56:59], a[192:195], a[56:59], v[56:59]\n\t"
    "ds_read_b64_tr_b16 a[162:163], %[ra10] offset:19456\n\t"
    "ds_read_b64_tr_b16 a[164:165], %[ra11] offset:1024\n\t"
    "buffer_load_dwordx4 %[wv], %[srd2], s41 offen lds\n\t"
    "v_mfma_f32_16x16x32_bf16 v[64:67], a[196:199], a[56:59], v[64:67]\n\t"
    "ds_read_b64_tr_b16 a[166:167], %[ra11] offset:19456\n\t"
    "s_add_u32 m0, %[m0w], 40960\n\t"
    "s_add_u32 s41, s40, 512\n\t"
    "v_mfma_f32_16x16x32_bf16 v[68:71], a[196:199], a[60:63], v[68:71]\n\t"
    "ds_read_b64_tr_b16 a[168:169], %[ra10] offset:1536\n\t"
    "buffer_load_dwordx4 %[wv], %[srd2], s41 offen lds\n\t"
    "v_mfma_f32_16x16x32_bf16 v[76:79], a[200:203], a[60:63], v[76:79]\n\t"
    "ds_read_b64_tr_b16 a[170:171], %[ra10] offset:19968\n\t"
    "ds_read_b64_tr_b16 a[172:173], %[ra11] offset:1536\n\t"
    "s_add_u32 m0, %[m0w], 41984\n\t"
    "s_add_u32 s41, s40, 640\n\t"
    "v_mfma_f32_16x16x32_bf16 v[72:75], a[200:203], a[56:59], v[72:75]\n\t"
    "ds_read_b64_tr_b16 a[174:175], %[ra11] offset:19968\n\t"
    "buffer_load_dwordx4 %[wv], %[srd2], s41 offen lds\n\t"
    "v_mfma_f32_16x16x32_bf16 v[80:83], a[204:207], a[56:59], v[80:83]\n\t"
    "ds_read_b64_tr_b16 a[176:177], %[ra12] offset:0\n\t"
    "s_add_u32 m0, %[m0w], 43008\n\t"
    "s_add_u32 s41, s40, 768\n\t"
    "v_mfma_f32_16x16x32_bf16 v[84:87], a[204:207], a[60:63], v[84:87]\n\t"
    "ds_read_b64_tr_b16 a[178:179], %[ra12] offset:18432\n\t"
    "buffer_load_dwordx4 %[wv], %[srd2], s41 offen lds\n\t"
    "v_mfma_f32_16x16x32_bf16 v[92:95], a[208:211], a[60:63], v[92:95]\n\t"
    "s_add_u32 m0, %[m0w], 44032\n\t"
    "s_add_u32 s41, s40, 896\n\t"
    "v_mfma_f32_16x16x32_bf16 v[88:91], a[208:211], a[56:59], v[88:91]\n\t"
    "buffer_load_dwordx4 %[wv], %[srd2], s41 offen lds\n\t"
    "v_mfma_f32_16x16x32_bf16 v[96:99], a[212:215], a[56:59], v[96:99]\n\t"
    "s_add_u32 m0, %[m0w], 45056\n\t"
    "s_add_u32 s41, s40, 1024\n\t"
    "v_mfma_f32_16x16x32_bf16 v[100:103], a[212:215], a[60:63], v[100:103]\n\t"
    "buffer_load_dwordx4 %[wv], %[srd2], s41 offen lds\n\t"
    "s_add_u32 s40, s40, 36864\n\t"
    "s_cmp_eq_u32 s40, 663552\n\t"
    "s_cselect_b32 s40, 0, s40\n\t"
    "s_waitcnt vmcnt(18) lgkmcnt(0)\n\t"
    "s_barrier\n\t"
    "v_mfma_f32_16x16x32_bf16 v[32:35], a[144:147], a[64:67], v[32:35]\n\t"
    "ds_read_b64_tr_b16 a[180:181], %[ra10] offset:36864\n\t"
    "ds_read_b64_tr_b16 a[182:183], %[ra10] offset:55296\n\t"
    "s_add_u32 m0, %[m0w], 73728\n\t"
    "s_add_u32 s41, s40, 0\n\t"
    "v_mfma_f32_16x16x32_bf16 v[36:39], a[144:147], a[68:71], v[36:39]\n\t"
    "ds_read_b64_tr_b16 a[184:185], %[ra11] offset:36864\n\t"
    "buffer_load_dwordx4 %[wv], %[srd2], s41 offen lds\n\t"
    "v_mfma_f32_16x16x32_bf16 v[44:47], a[148:151], a[68:71], v[44:47]\n\t"
    "ds_read_b64_tr_b16 a[186:187], %[ra11] offset:55296\n\t"
    "s_add_u32 m0, %[m0w], 74752\n\t"
    "s_add_u32 s41, s40, 128\n\t"
    "v_mfma_f32_16x16x32_bf16 v[40:43], a[148:151], a[64:67], v[40:43]\n\t"
    "ds_read_b64_tr_b16 a[188:189], %[ra10] offset:37376\n\t"
    "ds_read_b64_tr_b16 a[190:191], %[ra10] offset:55808\n\t"
    "buffer_load_dwordx4 %[wv], %[srd2], s41 offen lds\n\t"
    "v_mfma_f32_16x16x32_bf16 v[48:51], a[152:155], a[64:67], v[48:51]\n\t"
    "ds_read_b64_tr_b16 a[192:193], %[ra11] offset:37376\n\t"
    "s_add_u32 m0, %[m0w], 75776\n\t"
    "s_add_u32 s41, s40, 256\n\t"
    "v_mfma_f32_16x16x32_bf16 v[52:55], a[152:155], a[68:71], v[52:55]\n\t"
    "ds_read_b64_tr_b16 a[194:195], %[ra11] offset:55808\n\t"
    "buffer_load_dwordx4 %[wv], %[srd2], s41 offen lds\n\t"
    "v_mfma_f32_16x16x32_bf16 v[60:63], a[156:159], a[68:71], v[60:63]\n\t"
    "ds_read_b64_tr_b16 a[196:197], %[ra10] offset:37888\n\t"
    "s_add_u32 m0, %[m0w], 76800\n\t"
    "s_add_u32 s41, s40, 384\n\t"
    "v_mfma_f32_16x16x32_bf16 v[56:59], a[156:159], a[64:67], v[56:59]\n\t"
    "ds_read_b64_tr_b16 a[198:199], %[ra10] offset:56320\n\t"
    "ds_read_b64_tr_b16 a[200:201], %[ra11] offset:37888\n\t"
    "buffer_load_dwordx4 %[wv], %[srd2], s41 offen lds\n\t"
    "v_mfma_f32_16x16x32_bf16 v[64:67], a[160:163], a[64:67], v[64:67]\n\t"
    "ds_read_b64_tr_b16 a[202:203], %[ra11] offset:56320\n\t"
    "s_add_u32 m0, %[m0w], 77824\n\t"
    "s_add_u32 s41, s40, 512\n\t"
    "v_mfma_f32_16x16x32_bf16 v[68:71], a[160:163], a[68:71], v[68:71]\n\t"
    "ds_read_b64_tr_b16 a[204:205], %[ra10] offset:38400\n\t"
    "buffer_load_dwordx4 %[wv], %[srd2], s41 offen lds\n\t"
    "v_mfma_f32_16x16x32_bf16 v[76:79], a[164:167], a[68:71], v[76:79]\n\t"
    "ds_read_b64_tr_b16 a[206:207], %[ra10] offset:56832\n\t"
    "ds_read_b64_tr_b16 a[208:209], %[ra11] offset:38400\n\t"
    "s_add_u32 m0, %[m0w], 78848\n\t"
    "s_add_u32 s41, s40, 640\n\t"
    "v_mfma_f32_16x16x32_bf16 v[72:75], a[164:167], a[64:67], v[72:75]\n\t"
    "ds_read_b64_tr_b16 a[210:211], %[ra11] offset:56832\n\t"
    "buffer_load_dwordx4 %[wv], %[srd2], s41 offen lds\n\t"
    "v_mfma_f32_16x16x32_bf16 v[80:83], a[168:171], a[64:67], v[80:83]\n\t"
    "ds_read_b64_tr_b16 a[212:213], %[ra12] offset:36864\n\t"
    "s_add_u32 m0, %[m0w], 79872\n\t"
    "s_add_u32 s41, s40, 768\n\t"
    "v_mfma_f32_16x16x32_bf16 v[84:87], a[168:171], a[68:71], v[84:87]\n\t"
    "ds_read_b64_tr_b16 a[214:215], %[ra12] offset:55296\n\t"
    "buffer_load_dwordx4 %[wv], %[srd2], s41 offen lds\n\t"
    "v_mfma_f32_16x16x32_bf16 v[92:95], a[172:175], a[68:71], v[92:95]\n\t"
    "s_add_u32 m0, %[m0w], 80896\n\t"
    "s_add_u32 s41, s40, 896\n\t"
    "v_mfma_f32_16x16x32_bf16 v[88:91], a[172:175], a[64:67], v[88:91]\n\t"
    "buffer_load_dwordx4 %[wv], %[srd2], s41 offen lds\n\t"
    "v_mfma_f32_16x16x32_bf16 v[96:99], a[176:179], a[64:67], v[96:99]\n\t"
    "s_add_u32 m0, %[m0w], 81920\n\t"
    "s_add_u32 s41, s40, 1024\n\t"
    "v_mfma_f32_16x16x32_bf16 v[100:103], a[176:179], a[68:71], v[100:103]\n\t"
    "buffer_load_dwordx4 %[wv], %[srd2], s41 offen lds\n\t"
    "s_add_u32 s40, s40, 36864\n\t"
    "s_cmp_eq_u32 s40, 663552\n\t"
    "s_cselect_b32 s40, 0, s40\n\t"
    "s_waitcnt vmcnt(18) lgkmcnt(0)\n\t"
    "s_barrier\n\t"
    "v_mfma_f32_16x16x32_bf16 v[32:35], a[180:183], a[72:75], v[32:35]\n\t"
    "ds_read_b64_tr_b16 a[144:145], %[ra00] offset:0\n\t"
    "ds_read_b64_tr_b16 a[146:147], %[ra00] offset:18432\n\t"
    "s_add_u32 m0, %[m0w], 110592\n\t"
    "s_add_u32 s41, s40, 0\n\t"
    "v_mfma_f32_16x16x32_bf16 v[36:39], a[180:183], a[76:79], v[36:39]\n\t"
    "ds_read_b64_tr_b16 a[148:149], %[ra01] offset:0\n\t"
    "buffer_load_dwordx4 %[wv], %[srd2], s41 offen lds\n\t"
    "v_mfma_f32_16x16x32_bf16 v[44:47], a[184:187], a[76:79], v[44:47]\n\t"
    "ds_read_b64_tr_b16 a[150:151], %[ra01] offset:18432\n\t"
    "s_add_u32 m0, %[m0w], 111616\n\t"
    "s_add_u32 s41, s40, 128\n\t"
    "v_mfma_f32_16x16x32_bf16 v[40:43], a[184:187], a[72:75], v[40:43]\n\t"
    "ds_read_b64_tr_b16 a[152:153], %[ra00] offset:512\n\t"
    "ds_read_b64_tr_b16 a[154:155], %[ra00] offset:18944\n\t"
    "buffer_load_dwordx4 %[wv], %[srd2], s41 offen lds\n\t"
    "v_mfma_f32_16x16x32_bf16 v[48:51], a[188:191], a[72:75], v[48:51]\n\t"
    "ds_read_b64_tr_b16 a[156:157], %[ra01] offset:512\n\t"
    "s_add_u32 m0, %[m0w], 112640\n\t"
    "s_add_u32 s41, s40, 256\n\t"
    "v_mfma_f32_16x16x32_bf16 v[52:55], a[188:191], a[76:79], v[52:55]\n\t"
    "ds_read_b64_tr_b16 a[158:159], %[ra01] offset:18944\n\t"
    "buffer_load_dwordx4 %[wv], %[srd2], s41 offen lds\n\t"
    "v_mfma_f32_16x16x32_bf16 v[60:63], a[192:195], a[76:79], v[60:63]\n\t"
    "ds_read_b64_tr_b16 a[160:161], %[ra00] offset:1024\n\t"
    "s_add_u32 m0, %[m0w], 113664\n\t"
    "s_add_u32 s41, s40, 384\n\t"
    "v_mfma_f32_16x16x32_bf16 v[56:59], a[192:195], a[72:75], v[56:59]\n\t"
    "ds_read_b64_tr_b16 a[162:163], %[ra00] offset:19456\n\t"
    "ds_read_b64_tr_b16 a[164:165], %[ra01] offset:1024\n\t"
    "buffer_load_dwordx4 %[wv], %[srd2], s41 offen lds\n\t"
    "v_mfma_f32_16x16x32_bf16 v[64:67], a[196:199], a[72:75], v[64:67]\n\t"
    "ds_read_b64_tr_b16 a[166:167], %[ra01] offset:19456\n\t"
    "s_add_u32 m0, %[m0w], 114688\n\t"
    "s_add_u32 s41, s40, 512\n\t"
    "v_mfma_f32_16x16x32_bf16 v[68:71], a[196:199], a[76:79], v[68:71]\n\t"
    "ds_read_b64_tr_b16 a[168:169], %[ra00] offset:1536\n\t"
    "buffer_load_dwordx4 %[wv], %[srd2], s41 offen lds\n\t"
    "v_mfma_f32_16x16x32_bf16 v[76:79], a[200:203], a[76:79], v[76:79]\n\t"
    "ds_read_b64_tr_b16 a[170:171], %[ra00] offset:19968\n\t"
    "ds_read_b64_tr_b16 a[172:173], %[ra01] offset:1536\n\t"
    "s_add_u32 m0, %[m0w], 115712\n\t"
    "s_add_u32 s41, s40, 640\n\t"
    "v_mfma_f32_16x16x32_bf16 v[72:75], a[200:203], a[72:75], v[72:75]\n\t"
    "ds_read_b64_tr_b16 a[174:175], %[ra01] offset:19968\n\t"
    "buffer_load_dwordx4 %[wv], %[srd2], s41 offen lds\n\t"
    "v_mfma_f32_16x16x32_bf16 v[80:83], a[204:207], a[72:75], v[80:83]\n\t"
    "ds_read_b64_tr_b16 a[176:177], %[ra02] offset:0\n\t"
    "s_add_u32 m0, %[m0w], 116736\n\t"
    "s_add_u32 s41, s40, 768\n\t"
    "v_mfma_f32_16x16x32_bf16 v[84:87], a[204:207], a[76:79], v[84:87]\n\t"
    "ds_read_b64_tr_b16 a[178:179], %[ra02] offset:18432\n\t"
    "buffer_load_dwordx4 %[wv], %[srd2], s41 offen lds\n\t"
    "v_mfma_f32_16x16x32_bf16 v[92:95], a[208:211], a[76:79], v[92:95]\n\t"
    "s_add_u32 m0, %[m0w], 117760\n\t"
    "s_add_u32 s41, s40, 896\n\t"
    "v_mfma_f32_16x16x32_bf16 v[88:91], a[208:211], a[72:75], v[88:91]\n\t"
    "buffer_load_dwordx4 %[wv], %[srd2], s41 offen lds\n\t"
    "v_mfma_f32_16x16x32_bf16 v[96:99], a[212:215], a[72:75], v[96:99]\n\t"
    "s_add_u32 m0, %[m0w], 118784\n\t"
    "s_add_u32 s41, s40, 1024\n\t"
    "v_mfma_f32_16x16x32_bf16 v[100:103], a[212:215], a[76:79], v[100:103]\n\t"
    "buffer_load_dwordx4 %[wv], %[srd2], s41 offen lds\n\t"
    "s_add_u32 s40, s40, 36864\n\t"
    "s_cmp_eq_u32 s40, 663552\n\t"
    "s_cselect_b32 s40, 0, s40\n\t"
    "s_waitcnt vmcnt(18) lgkmcnt(0)\n\t"
    "s_barrier\n\t"
    "v_mfma_f32_16x16x32_bf16 v[32:35], a[144:147], a[80:83], v[32:35]\n\t"
    "ds_read_b64_tr_b16 a[180:181], %[ra00] offset:36864\n\t"
    "ds_read_b64_tr_b16 a[182:183], %[ra00] offset:55296\n\t"
    "s_add_u32 m0, %[m0w], 0\n\t"
    "s_add_u32 s41, s40, 0\n\t"
    "v_mfma_f32_16x16x32_bf16 v[36:39], a[144:147], a[84:87], v[36:39]\n\t"
    "ds_read_b64_tr_b16 a[184:185], %[ra01] offset:36864\n\t"
    "buffer_load_dwordx4 %[wv], %[srd2], s41 offen lds\n\t"
    "v_mfma_f32_16x16x32_bf16 v[44:47], a[148:151], a[84:87], v[44:47]\n\t"
    "ds_read_b64_tr_b16 a[186:187], %[ra01] offset:55296\n\t"
    "s_add_u32 m0, %[m0w], 1024\n\t"
    "s_add_u32 s41, s40, 128\n\t"
    "v_mfma_f32_16x16x32_bf16 v[40:43], a[148:151], a[80:83], v[40:43]\n\t"
    "ds_read_b64_tr_b16 a[188:189], %[ra00] offset:37376\n\t"
    "ds_read_b64_tr_b16 a[190:191], %[ra00] offset:55808\n\t"
    "buffer_load_dwordx4 %[wv], %[srd2], s41 offen lds\n\t"
    "v_mfma_f32_16x16x32_bf16 v[48:51], a[152:155], a[80:83], v[48:51]\n\t"
    "ds_read_b64_tr_b16 a[192:193], %[ra01] offset:37376\n\t"
    "s_add_u32 m0, %[m0w], 2048\n\t"
    "s_add_u32 s41, s40, 256\n\t"
    "v_mfma_f32_16x16x32_bf16 v[52:55], a[152:155], a[84:87], v[52:55]\n\t"
    "ds_read_b64_tr_b16 a[194:195], %[ra01] offset:55808\n\t"
    "buffer_load_dwordx4 %[wv], %[srd2], s41 offen lds\n\t"
    "v_mfma_f32_16x16x32_bf16 v[60:63], a[156:159], a[84:87], v[60:63]\n\t"
    "ds_read_b64_tr_b16 a[196:197], %[ra00] offset:37888\n\t"
    "s_add_u32 m0, %[m0w], 3072\n\t"
    "s_add_u32 s41, s40, 384\n\t"
    "v_mfma_f32_16x16x32_bf16 v[56:59], a[156:159], a[80:83], v[56:59]\n\t"
    "ds_read_b64_tr_b16 a[198:199], %[ra00] offset:56320\n\t"
    "ds_read_b64_tr_b16 a[200:201], %[ra01] offset:37888\n\t"
    "buffer_load_dwordx4 %[wv], %[srd2], s41 offen lds\n\t"
    "v_mfma_f32_16x16x32_bf16 v[64:67], a[160:163], a[80:83], v[64:67]\n\t"
    "ds_read_b64_tr_b16 a[202:203], %[ra01] offset:56320\n\t"
    "s_add_u32 m0, %[m0w], 4096\n\t"
    "s_add_u32 s41, s40, 512\n\t"
    "v_mfma_f32_16x16x32_bf16 v[68:71], a[160:163], a[84:87], v[68:71]\n\t"
    "ds_read_b64_tr_b16 a[204:205], %[ra00] offset:38400\n\t"
    "buffer_load_dwordx4 %[wv], %[srd2], s41 offen lds\n\t"
    "v_mfma_f32_16x16x32_bf16 v[76:79], a[164:167], a[84:87], v[76:79]\n\t"
    "ds_read_b64_tr_b16 a[206:207], %[ra00] offset:56832\n\t"
    "ds_read_b64_tr_b16 a[208:209], %[ra01] offset:38400\n\t"
    "s_add_u32 m0, %[m0w], 5120\n\t"
    "s_add_u32 s41, s40, 640\n\t"
    "v_mfma_f32_16x16x32_bf16 v[72:75], a[164:167], a[80:83], v[72:75]\n\t"
    "ds_read_b64_tr_b16 a[210:211], %[ra01] offset:56832\n\t"
    "buffer_load_dwordx4 %[wv], %[srd2], s41 offen lds\n\t"
    "v_mfma_f32_16x16x32_bf16 v[80:83], a[168:171], a[80:83], v[80:83]\n\t"
    "ds_read_b64_tr_b16 a[212:213], %[ra02] offset:36864\n\t"
    "s_add_u32 m0, %[m0w], 6144\n\t"
    "s_add_u32 s41, s40, 768\n\t"
    "v_mfma_f32_16x16x32_bf16 v[84:87], a[168:171], a[84:87], v[84:87]\n\t"
    "ds_read_b64_tr_b16 a[214:215], %[ra02] offset:55296\n\t"
    "buffer_load_dwordx4 %[wv], %[srd2], s41 offen lds\n\t"
    "v_mfma_f32_16x16x32_bf16 v[92:95], a[172:175], a[84:87], v[92:95]\n\t"
    "s_add_u32 m0, %[m0w], 7168\n\t"
    "s_add_u32 s41, s40, 896\n\t"
    "v_mfma_f32_16x16x32_bf16 v[88:91], a[172:175], a[80:83], v[88:91]\n\t"
    "buffer_load_dwordx4 %[wv], %[srd2], s41 offen lds\n\t"
    "v_mfma_f32_16x16x32_bf16 v[96:99], a[176:179], a[80:83], v[96:99]\n\t"
    "s_add_u32 m0, %[m0w], 8192\n\t"
    "s_add_u32 s41, s40, 1024\n\t"
    "v_mfma_f32_16x16x32_bf16 v[100:103], a[176:179], a[84:87], v[100:103]\n\t"
    "buffer_load_dwordx4 %[wv], %[srd2], s41 offen lds\n\t"
    "s_add_u32 s40, s40, 36864\n\t"
    "s_cmp_eq_u32 s40, 663552\n\t"
    "s_cselect_b32 s40, 0, s40\n\t"
    "s_waitcnt vmcnt(18) lgkmcnt(0)\n\t"
    "s_barrier\n\t"
    "v_mfma_f32_16x16x32_bf16 v[32:35], a[180:183], a[88:91], v[32:35]\n\t"
    "ds_read_b64_tr_b16 a[144:145], %[ra10] offset:0\n\t"
    "ds_read_b64_tr_b16 a[146:147], %[ra10] offset:18432\n\t"
    "s_add_u32 m0, %[m0w], 36864\n\t"
    "s_add_u32 s41, s40, 0\n\t"
    "v_mfma_f32_16x16x32_bf16 v[36:39], a[180:183], a[92:95], v[36:39]\n\t"
    "ds_read_b64_tr_b16 a[148:149], %[ra11] offset:0\n\t"
    "buffer_load_dwordx4 %[wv], %[srd2], s41 offen lds\n\t"
    "v_mfma_f32_16x16x32_bf16 v[44:47], a[184:187], a[92:95], v[44:47]\n\t"
    "ds_read_b64_tr_b16 a[150:151], %[ra11] offset:18432\n\t"
    "s_add_u32 m0, %[m0w], 37888\n\t"
    "s_add_u32 s41, s40, 128\n\t"
    "v_mfma_f32_16x16x32_bf16 v[40:43], a[184:187], a[88:91], v[40:43]\n\t"
    "ds_read_b64_tr_b16 a[152:153], %[ra10] offset:512\n\t"
    "ds_read_b64_tr_b16 a[154:155], %[ra10] offset:18944\n\t"
    "buffer_load_dwordx4 %[wv], %[srd2], s41 offen lds\n\t"
    "v_mfma_f32_16x16x32_bf16 v[48:51], a[188:191], a[88:91], v[48:51]\n\t"
    "ds_read_b64_tr_b16 a[156:157], %[ra11] offset:512\n\t"
    "s_add_u32 m0, %[m0w], 38912\n\t"
    "s_add_u32 s41, s40, 256\n\t"
    "v_mfma_f32_16x16x32_bf16 v[52:55], a[188:191], a[92:95], v[52:55]\n\t"
    "ds_read_b64_tr_b16 a[158:159], %[ra11] offset:18944\n\t"
    "buffer_load_dwordx4 %[wv], %[srd2], s41 offen lds\n\t"
    "v_mfma_f32_16x16x32_bf16 v[60:63], a[192:195], a[92:95], v[60:63]\n\t"
    "ds_read_b64_tr_b16 a[160:161], %[ra10] offset:1024\n\t"
    "s_add_u32 m0, %[m0w], 39936\n\t"
    "s_add_u32 s41, s40, 384\n\t"
    "v_mfma_f32_16x16x32_bf16 v[56:59], a[192:195], a[88:91], v[56:59]\n\t"
    "ds_read_b64_tr_b16 a[162:163], %[ra10] offset:19456\n\t"
    "ds_read_b64_tr_b16 a[164:165], %[ra11] offset:1024\n\t"
    "buffer_load_dwordx4 %[wv], %[srd2], s41 offen lds\n\t"
    "v_mfma_f32_16x16x32_bf16 v[64:67], a[196:199], a[88:91], v[64:67]\n\t"
    "ds_read_b64_tr_b16 a[166:167], %[ra11] offset:19456\n\t"
    "s_add_u32 m0, %[m0w], 40960\n\t"
    "s_add_u32 s41, s40, 512\n\t"
    "v_mfma_f32_16x16x32_bf16 v[68:71], a[196:199], a[92:95], v[68:71]\n\t"
    "ds_read_b64_tr_b16 a[168:169], %[ra10] offset:1536\n\t"
    "buffer_load_dwordx4 %[wv], %[srd2], s41 offen lds\n\t"
    "v_mfma_f32_16x16x32_bf16 v[76:79], a[200:203], a[92:95], v[76:79]\n\t"
    "ds_read_b64_tr_b16 a[170:171], %[ra10] offset:19968\n\t"
    "ds_read_b64_tr_b16 a[172:173], %[ra11] offset:1536\n\t"
    "s_add_u32 m0, %[m0w], 41984\n\t"
    "s_add_u32 s41, s40, 640\n\t"
    "v_mfma_f32_16x16x32_bf16 v[72:75], a[200:203], a[88:91], v[72:75]\n\t"
    "ds_read_b64_tr_b16 a[174:175], %[ra11] offset:19968\n\t"
    "buffer_load_dwordx4 %[wv], %[srd2], s41 offen lds\n\t"
    "v_mfma_f32_16x16x32_bf16 v[80:83], a[204:207], a[88:91], v[80:83]\n\t"
    "ds_read_b64_tr_b16 a[176:177], %[ra12] offset:0\n\t"
    "s_add_u32 m0, %[m0w], 43008\n\t"
    "s_add_u32 s41, s40, 768\n\t"
    "v_mfma_f32_16x16x32_bf16 v[84:87], a[204:207], a[92:95], v[84:87]\n\t"
    "ds_read_b64_tr_b16 a[178:179], %[ra12] offset:18432\n\t"
    "buffer_load_dwordx4 %[wv], %[srd2], s41 offen lds\n\t"
    "v_mfma_f32_16x16x32_bf16 v[92:95], a[208:211], a[92:95], v[92:95]\n\t"
    "s_add_u32 m0, %[m0w], 44032\n\t"
    "s_add_u32 s41, s40, 896\n\t"
    "v_mfma_f32_16x16x32_bf16 v[88:91], a[208:211], a[88:91], v[88:91]\n\t"
    "buffer_load_dwordx4 %[wv], %[srd2], s41 offen lds\n\t"
    "v_mfma_f32_16x16x32_bf16 v[96:99], a[212:215], a[88:91], v[96:99]\n\t"
    "s_add_u32 m0, %[m0w], 45056\n\t"
    "s_add_u32 s41, s40, 1024\n\t"
    "v_mfma_f32_16x16x32_bf16 v[100:103], a[212:215], a[92:95], v[100:103]\n\t"
    "buffer_load_dwordx4 %[wv], %[srd2], s41 offen lds\n\t"
    "s_add_u32 s40, s40, 36864\n\t"
    "s_cmp_eq_u32 s40, 663552\n\t"
    "s_cselect_b32 s40, 0, s40\n\t"
    "s_waitcnt vmcnt(18) lgkmcnt(0)\n\t"
    "s_barrier\n\t"
    "v_mfma_f32_16x16x32_bf16 v[32:35], a[144:147], a[96:99], v[32:35]\n\t"
    "ds_read_b64_tr_b16 a[180:181], %[ra10] offset:36864\n\t"
    "ds_read_b64_tr_b16 a[182:183], %[ra10] offset:55296\n\t"
    "s_add_u32 m0, %[m0w], 73728\n\t"
    "s_add_u32 s41, s40, 0\n\t"
    "v_mfma_f32_16x16x32_bf16 v[36:39], a[144:147], a[100:103], v[36:39]\n\t"
    "ds_read_b64_tr_b16 a[184:185], %[ra11] offset:36864\n\t"
    "buffer_load_dwordx4 %[wv], %[srd2], s41 offen lds\n\t"
    "v_mfma_f32_16x16x32_bf16 v[44:47], a[148:151], a[100:103], v[44:47]\n\t"
    "ds_read_b64_tr_b16 a[186:187], %[ra11] offset:55296\n\t"
    "s_add_u32 m0, %[m0w], 74752\n\t"
    "s_add_u32 s41, s40, 128\n\t"
    "v_mfma_f32_16x16x32_bf16 v[40:43], a[148:151], a[96:99], v[40:43]\n\t"
    "ds_read_b64_tr_b16 a[188:189], %[ra10] offset:37376\n\t"
    "ds_read_b64_tr_b16 a[190:191], %[ra10] offset:55808\n\t"
    "buffer_load_dwordx4 %[wv], %[srd2], s41 offen lds\n\t"
    "v_mfma_f32_16x16x32_bf16 v[48:51], a[152:155], a[96:99], v[48:51]\n\t"
    "ds_read_b64_tr_b16 a[192:193], %[ra11] offset:37376\n\t"
    "s_add_u32 m0, %[m0w], 75776\n\t"
    "s_add_u32 s41, s40, 256\n\t"
    "v_mfma_f32_16x16x32_bf16 v[52:55], a[152:155], a[100:103], v[52:55]\n\t"
    "ds_read_b64_tr_b16 a[194:195], %[ra11] offset:55808\n\t"
    "buffer_load_dwordx4 %[wv], %[srd2], s41 offen lds\n\t"
    "v_mfma_f32_16x16x32_bf16 v[60:63], a[156:159], a[100:103], v[60:63]\n\t"
    "ds_read_b64_tr_b16 a[196:197], %[ra10] offset:37888\n\t"
    "s_add_u32 m0, %[m0w], 76800\n\t"
    "s_add_u32 s41, s40, 384\n\t"
    "v_mfma_f32_16x16x32_bf16 v[56:59], a[156:159], a[96:99], v[56:59]\n\t"
    "ds_read_b64_tr_b16 a[198:199], %[ra10] offset:56320\n\t"
    "ds_read_b64_tr_b16 a[200:201], %[ra11] offset:37888\n\t"
    "buffer_load_dwordx4 %[wv], %[srd2], s41 offen lds\n\t"
    "v_mfma_f32_16x16x32_bf16 v[64:67], a[160:163], a[96:99], v[64:67]\n\t"
    "ds_read_b64_tr_b16 a[202:203], %[ra11] offset:56320\n\t"
    "s_add_u32 m0, %[m0w], 77824\n\t"
    "s_add_u32 s41, s40, 512\n\t"
    "v_mfma_f32_16x16x32_bf16 v[68:71], a[160:163], a[100:103], v[68:71]\n\t"
    "ds_read_b64_tr_b16 a[204:205], %[ra10] offset:38400\n\t"
    "buffer_load_dwordx4 %[wv], %[srd2], s41 offen lds\n\t"
    "v_mfma_f32_16x16x32_bf16 v[76:79], a[164:167], a[100:103], v[76:79]\n\t"
    "ds_read_b64_tr_b16 a[206:207], %[ra10] offset:56832\n\t"
    "ds_read_b64_tr_b16 a[208:209], %[ra11] offset:38400\n\t"
    "s_add_u32 m0, %[m0w], 78848\n\t"
    "s_add_u32 s41, s40, 640\n\t"
    "v_mfma_f32_16x16x32_bf16 v[72:75], a[164:167], a[96:99], v[72:75]\n\t"
    "ds_read_b64_tr_b16 a[210:211], %[ra11] offset:56832\n\t"
    "buffer_load_dwordx4 %[wv], %[srd2], s41 offen lds\n\t"
    "v_mfma_f32_16x16x32_bf16 v[80:83], a[168:171], a[96:99], v[80:83]\n\t"
    "ds_read_b64_tr_b16 a[212:213], %[ra12] offset:36864\n\t"
    "s_add_u32 m0, %[m0w], 79872\n\t"
    "s_add_u32 s41, s40, 768\n\t"
    "v_mfma_f32_16x16x32_bf16 v[84:87], a[168:171], a[100:103], v[84:87]\n\t"
    "ds_read_b64_tr_b16 a[214:215], %[ra12] offset:55296\n\t"
    "buffer_load_dwordx4 %[wv], %[srd2], s41 offen lds\n\t"
    "v_mfma_f32_16x16x32_bf16 v[92:95], a[172:175], a[100:103], v[92:95]\n\t"
    "s_add_u32 m0, %[m0w], 80896\n\t"
    "s_add_u32 s41, s40, 896\n\t"
    "v_mfma_f32_16x16x32_bf16 v[88:91], a[172:175], a[96:99], v[88:91]\n\t"
    "buffer_load_dwordx4 %[wv], %[srd2], s41 offen lds\n\t"
    "v_mfma_f32_16x16x32_bf16 v[96:99], a[176:179], a[96:99], v[96:99]\n\t"
    "s_add_u32 m0, %[m0w], 81920\n\t"
    "s_add_u32 s41, s40, 1024\n\t"
    "v_mfma_f32_16x16x32_bf16 v[100:103], a[176:179], a[100:103], v[100:103]\n\t"
    "buffer_load_dwordx4 %[wv], %[srd2], s41 offen lds\n\t"
    "s_add_u32 s40, s40, 36864\n\t"
    "s_cmp_eq_u32 s40, 663552\n\t"
    "s_cselect_b32 s40, 0, s40\n\t"
    "s_waitcnt vmcnt(18) lgkmcnt(0)\n\t"
    "s_barrier\n\t"
    "v_mfma_f32_16x16x32_bf16 v[32:35], a[180:183], a[104:107], v[32:35]\n\t"
    "ds_read_b64_tr_b16 a[144:145], %[ra00] offset:0\n\t"
    "ds_read_b64_tr_b16 a[146:147], %[ra00] offset:18432\n\t"
    "s_add_u32 m0, %[m0w], 110592\n\t"
    "s_add_u32 s41, s40, 0\n\t"
    "v_mfma_f32_16x16x32_bf16 v[36:39], a[180:183], a[108:111], v[36:39]\n\t"
    "ds_read_b64_tr_b16 a[148:149], %[ra01] offset:0\n\t"
    "buffer_load_dwordx4 %[wv], %[srd2], s41 offen lds\n\t"
    "v_mfma_f32_16x16x32_bf16 v[44:47], a[184:187], a[108:111], v[44:47]\n\t"
    "ds_read_b64_tr_b16 a[150:151], %[ra01] offset:18432\n\t"
    "s_add_u32 m0, %[m0w], 111616\n\t"
    "s_add_u32 s41, s40, 128\n\t"
    "v_mfma_f32_16x16x32_bf16 v[40:43], a[184:187], a[104:107], v[40:43]\n\t"
    "ds_read_b64_tr_b16 a[152:153], %[ra00] offset:512\n\t"
    "ds_read_b64_tr_b16 a[154:155], %[ra00] offset:18944\n\t"
    "buffer_load_dwordx4 %[wv], %[srd2], s41 offen lds\n\t"
    "v_mfma_f32_16x16x32_bf16 v[48:51], a[188:191], a[104:107], v[48:51]\n\t"
    "ds_read_b64_tr_b16 a[156:157], %[ra01] offset:512\n\t"
    "s_add_u32 m0, %[m0w], 112640\n\t"
    "s_add_u32 s41, s40, 256\n\t"
    "v_mfma_f32_16x16x32_bf16 v[52:55], a[188:191], a[108:111], v[52:55]\n\t"
    "ds_read_b64_tr_b16 a[158:159], %[ra01] offset:18944\n\t"
    "buffer_load_dwordx4 %[wv], %[srd2], s41 offen lds\n\t"
    "v_mfma_f32_16x16x32_bf16 v[60:63], a[192:195], a[108:111], v[60:63]\n\t"
    "ds_read_b64_tr_b16 a[160:161], %[ra00] offset:1024\n\t"
    "s_add_u32 m0, %[m0w], 113664\n\t"
    "s_add_u32 s41, s40, 384\n\t"
    "v_mfma_f32_16x16x32_bf16 v[56:59], a[192:195], a[104:107], v[56:59]\n\t"
    "ds_read_b64_tr_b16 a[162:163], %[ra00] offset:19456\n\t"
    "ds_read_b64_tr_b16 a[164:165], %[ra01] offset:1024\n\t"
    "buffer_load_dwordx4 %[wv], %[srd2], s41 offen lds\n\t"
    "v_mfma_f32_16x16x32_bf16 v[64:67], a[196:199], a[104:107], v[64:67]\n\t"
    "ds_read_b64_tr_b16 a[166:167], %[ra01] offset:19456\n\t"
    "s_add_u32 m0, %[m0w], 114688\n\t"
    "s_add_u32 s41, s40, 512\n\t"
    "v_mfma_f32_16x16x32_bf16 v[68:71], a[196:199], a[108:111], v[68:71]\n\t"
    "ds_read_b64_tr_b16 a[168:169], %[ra00] offset:1536\n\t"
    "buffer_load_dwordx4 %[wv], %[srd2], s41 offen lds\n\t"
    "v_mfma_f32_16x16x32_bf16 v[76:79], a[200:203], a[108:111], v[76:79]\n\t"
    "ds_read_b64_tr_b16 a[170:171], %[ra00] offset:19968\n\t"
    "ds_read_b64_tr_b16 a[172:173], %[ra01] offset:1536\n\t"
    "s_add_u32 m0, %[m0w], 115712\n\t"
    "s_add_u32 s41, s40, 640\n\t"
    "v_mfma_f32_16x16x32_bf16 v[72:75], a[200:203], a[104:107], v[72:75]\n\t"
    "ds_read_b64_tr_b16 a[174:175], %[ra01] offset:19968\n\t"
    "buffer_load_dwordx4 %[wv], %[srd2], s41 offen lds\n\t"
    "v_mfma_f32_16x16x32_bf16 v[80:83], a[204:207], a[104:107], v[80:83]\n\t"
    "ds_read_b64_tr_b16 a[176:177], %[ra02] offset:0\n\t"
    "s_add_u32 m0, %[m0w], 116736\n\t"
    "s_add_u32 s41, s40, 768\n\t"
    "v_mfma_f32_16x16x32_bf16 v[84:87], a[204:207], a[108:111], v[84:87]\n\t"
    "ds_read_b64_tr_b16 a[178:179], %[ra02] offset:18432\n\t"
    "buffer_load_dwordx4 %[wv], %[srd2], s41 offen lds\n\t"
    "v_mfma_f32_16x16x32_bf16 v[92:95], a[208:211], a[108:111], v[92:95]\n\t"
    "s_add_u32 m0, %[m0w], 117760\n\t"
    "s_add_u32 s41, s40, 896\n\t"
    "v_mfma_f32_16x16x32_bf16 v[88:91], a[208:211], a[104:107], v[88:91]\n\t"
    "buffer_load_dwordx4 %[wv], %[srd2], s41 offen lds\n\t"
    "v_mfma_f32_16x16x32_bf16 v[96:99], a[212:215], a[104:107], v[96:99]\n\t"
    "s_add_u32 m0, %[m0w], 118784\n\t"
    "s_add_u32 s41, s40, 1024\n\t"
    "v_mfma_f32_16x16x32_bf16 v[100:103], a[212:215], a[108:111], v[100:103]\n\t"
    "buffer_load_dwordx4 %[wv], %[srd2], s41 offen lds\n\t"
    "s_add_u32 s40, s40, 36864\n\t"
    "s_cmp_eq_u32 s40, 663552\n\t"
    "s_cselect_b32 s40, 0, s40\n\t"
    "s_waitcnt vmcnt(18) lgkmcnt(0)\n\t"
    "s_barrier\n\t"
    "v_mfma_f32_16x16x32_bf16 v[32:35], a[144:147], a[112:115], v[32:35]\n\t"
    "ds_read_b64_tr_b16 a[180:181], %[ra00] offset:36864\n\t"
    "ds_read_b64_tr_b16 a[182:183], %[ra00] offset:55296\n\t"
    "v_mfma_f32_16x16x32_bf16 v[36:39], a[144:147], a[116:119], v[36:39]\n\t"
    "ds_read_b64_tr_b16 a[184:185], %[ra01] offset:36864\n\t"
    "v_mfma_f32_16x16x32_bf16 v[44:47], a[148:151], a[116:119], v[44:47]\n\t"
    "ds_read_b64_tr_b16 a[186:187], %[ra01] offset:55296\n\t"
    "v_mfma_f32_16x16x32_bf16 v[40:43], a[148:151], a[112:115], v[40:43]\n\t"
    "ds_read_b64_tr_b16 a[188:189], %[ra00] offset:37376\n\t"
    "ds_read_b64_tr_b16 a[190:191], %[ra00] offset:55808\n\t"
    "v_mfma_f32_16x16x32_bf16 v[48:51], a[152:155], a[112:115], v[48:51]\n\t"
    "ds_read_b64_tr_b16 a[192:193], %[ra01] offset:37376\n\t"
    "v_mfma_f32_16x16x32_bf16 v[52:55], a[152:155], a[116:119], v[52:55]\n\t"
    "ds_read_b64_tr_b16 a[194:195], %[ra01] offset:55808\n\t"
    "v_mfma_f32_16x16x32_bf16 v[60:63], a[156:159], a[116:119], v[60:63]\n\t"
    "ds_read_b64_tr_b16 a[196:197], %[ra00] offset:37888\n\t"
    "v_mfma_f32_16x16x32_bf16 v[56:59], a[156:159], a[112:115], v[56:59]\n\t"
    "ds_read_b64_tr_b16 a[198:199], %[ra00] offset:56320\n\t"
    "ds_read_b64_tr_b16 a[200:201], %[ra01] offset:37888\n\t"
    "v_mfma_f32_16x16x32_bf16 v[64:67], a[160:163], a[112:115], v[64:67]\n\t"
    "ds_read_b64_tr_b16 a[202:203], %[ra01] offset:56320\n\t"
    "v_mfma_f32_16x16x32_bf16 v[68:71], a[160:163], a[116:119], v[68:71]\n\t"
    "ds_read_b64_tr_b16 a[204:205], %[ra00] offset:38400\n\t"
    "v_mfma_f32_16x16x32_bf16 v[76:79], a[164:167], a[116:119], v[76:79]\n\t"
    "ds_read_b64_tr_b16 a[206:207], %[ra00] offset:56832\n\t"
    "ds_read_b64_tr_b16 a[208:209], %[ra01] offset:38400\n\t"
    "v_mfma_f32_16x16x32_bf16 v[72:75], a[164:167], a[112:115], v[72:75]\n\t"
    "ds_read_b64_tr_b16 a[210:211], %[ra01] offset:56832\n\t"
    "v_mfma_f32_16x16x32_bf16 v[80:83], a[168:171], a[112:115], v[80:83]\n\t"
    "ds_read_b64_tr_b16 a[212:213], %[ra02] offset:36864\n\t"
    "v_mfma_f32_16x16x32_bf16 v[84:87], a[168:171], a[116:119], v[84:87]\n\t"
    "ds_read_b64_tr_b16 a[214:215], %[ra02] offset:55296\n\t"
    "v_mfma_f32_16x16x32_bf16 v[92:95], a[172:175], a[116:119], v[92:95]\n\t"
    "v_mfma_f32_16x16x32_bf16 v[88:91], a[172:175], a[112:115], v[88:91]\n\t"
    "v_mfma_f32_16x16x32_bf16 v[96:99], a[176:179], a[112:115], v[96:99]\n\t"
    "v_mfma_f32_16x16x32_bf16 v[100:103], a[176:179], a[116:119], v[100:103]\n\t"
    "s_waitcnt vmcnt(9) lgkmcnt(0)\n\t"
    "s_barrier\n\t"
    "v_mfma_f32_16x16x32_bf16 v[32:35], a[180:183], a[120:123], v[32:35]\n\t"
    "ds_read_b64_tr_b16 a[144:145], %[ra10] offset:0\n\t"
    "ds_read_b64_tr_b16 a[146:147], %[ra10] offset:18432\n\t"
    "v_mfma_f32_16x16x32_bf16 v[36:39], a[180:183], a[124:127], v[36:39]\n\t"
    "ds_read_b64_tr_b16 a[148:149], %[ra11] offset:0\n\t"
    "v_mfma_f32_16x16x32_bf16 v[44:47], a[184:187], a[124:127], v[44:47]\n\t"
    "ds_read_b64_tr_b16 a[150:151], %[ra11] offset:18432\n\t"
    "v_mfma_f32_16x16x32_bf16 v[40:43], a[184:187], a[120:123], v[40:43]\n\t"
    "ds_read_b64_tr_b16 a[152:153], %[ra10] offset:512\n\t"
    "ds_read_b64_tr_b16 a[154:155], %[ra10] offset:18944\n\t"
    "v_mfma_f32_16x16x32_bf16 v[48:51], a[188:191], a[120:123], v[48:51]\n\t"
    "ds_read_b64_tr_b16 a[156:157], %[ra11] offset:512\n\t"
    "v_mfma_f32_16x16x32_bf16 v[52:55], a[188:191], a[124:127], v[52:55]\n\t"
    "ds_read_b64_tr_b16 a[158:159], %[ra11] offset:18944\n\t"
    "v_mfma_f32_16x16x32_bf16 v[60:63], a[192:195], a[124:127], v[60:63]\n\t"
    "ds_read_b64_tr_b16 a[160:161], %[ra10] offset:1024\n\t"
    "v_mfma_f32_16x16x32_bf16 v[56:59], a[192:195], a[120:123], v[56:59]\n\t"
    "ds_read_b64_tr_b16 a[162:163], %[ra10] offset:19456\n\t"
    "ds_read_b64_tr_b16 a[164:165], %[ra11] offset:1024\n\t"
    "v_mfma_f32_16x16x32_bf16 v[64:67], a[196:199], a[120:123], v[64:67]\n\t"
    "ds_read_b64_tr_b16 a[166:167], %[ra11] offset:19456\n\t"
    "v_mfma_f32_16x16x32_bf16 v[68:71], a[196:199], a[124:127], v[68:71]\n\t"
    "ds_read_b64_tr_b16 a[168:169], %[ra10] offset:1536\n\t"
    "v_mfma_f32_16x16x32_bf16 v[76:79], a[200:203], a[124:127], v[76:79]\n\t"
    "ds_read_b64_tr_b16 a[170:171], %[ra10] offset:19968\n\t"
    "ds_read_b64_tr_b16 a[172:173], %[ra11] offset:1536\n\t"
    "v_mfma_f32_16x16x32_bf16 v[72:75], a[200:203], a[120:123], v[72:75]\n\t"
    "ds_read_b64_tr_b16 a[174:175], %[ra11] offset:19968\n\t"
    "v_mfma_f32_16x16x32_bf16 v[80:83], a[204:207], a[120:123], v[80:83]\n\t"
    "ds_read_b64_tr_b16 a[176:177], %[ra12] offset:0\n\t"
    "v_mfma_f32_16x16x32_bf16 v[84:87], a[204:207], a[124:127], v[84:87]\n\t"
    "ds_read_b64_tr_b16 a[178:179], %[ra12] offset:18432\n\t"
    "v_mfma_f32_16x16x32_bf16 v[92:95], a[208:211], a[124:127], v[92:95]\n\t"
    "v_mfma_f32_16x16x32_bf16 v[88:91], a[208:211], a[120:123], v[88:91]\n\t"
    "v_mfma_f32_16x16x32_bf16 v[96:99], a[212:215], a[120:123], v[96:99]\n\t"
    "v_mfma_f32_16x16x32_bf16 v[100:103], a[212:215], a[124:127], v[100:103]\n\t"
    "s_waitcnt vmcnt(0) lgkmcnt(0)\n\t"
    "s_barrier\n\t"
    "v_mfma_f32_16x16x32_bf16 v[32:35], a[144:147], a[128:131], v[32:35]\n\t"
    "ds_read_b64_tr_b16 a[180:181], %[ra10] offset:36864\n\t"
    "ds_read_b64_tr_b16 a[182:183], %[ra10] offset:55296\n\t"
    "v_mfma_f32_16x16x32_bf16 v[36:39], a[144:147], a[132:135], v[36:39]\n\t"
    "ds_read_b64_tr_b16 a[184:185], %[ra11] offset:36864\n\t"
    "v_mfma_f32_16x16x32_bf16 v[44:47], a[148:151], a[132:135], v[44:47]\n\t"
    "ds_read_b64_tr_b16 a[186:187], %[ra11] offset:55296\n\t"
    "v_mfma_f32_16x16x32_bf16 v[40:43], a[148:151], a[128:131], v[40:43]\n\t"
    "ds_read_b64_tr_b16 a[188:189], %[ra10] offset:37376\n\t"
    "ds_read_b64_tr_b16 a[190:191], %[ra10] offset:55808\n\t"
    "v_mfma_f32_16x16x32_bf16 v[48:51], a[152:155], a[128:131], v[48:51]\n\t"
    "ds_read_b64_tr_b16 a[192:193], %[ra11] offset:37376\n\t"
    "v_mfma_f32_16x16x32_bf16 v[52:55], a[152:155], a[132:135], v[52:55]\n\t"
    "ds_read_b64_tr_b16 a[194:195], %[ra11] offset:55808\n\t"
    "v_mfma_f32_16x16x32_bf16 v[60:63], a[156:159], a[132:135], v[60:63]\n\t"
    "ds_read_b64_tr_b16 a[196:197], %[ra10] offset:37888\n\t"
    "v_mfma_f32_16x16x32_bf16 v[56:59], a[156:159], a[128:131], v[56:59]\n\t"
    "ds_read_b64_tr_b16 a[198:199], %[ra10] offset:56320\n\t"
    "ds_read_b64_tr_b16 a[200:201], %[ra11] offset:37888\n\t"
    "v_mfma_f32_16x16x32_bf16 v[64:67], a[160:163], a[128:131], v[64:67]\n\t"
    "ds_read_b64_tr_b16 a[202:203], %[ra11] offset:56320\n\t"
    "v_mfma_f32_16x16x32_bf16 v[68:71], a[160:163], a[132:135], v[68:71]\n\t"
    "ds_read_b64_tr_b16 a[204:205], %[ra10] offset:38400\n\t"
    "v_mfma_f32_16x16x32_bf16 v[76:79], a[164:167], a[132:135], v[76:79]\n\t"
    "ds_read_b64_tr_b16 a[206:207], %[ra10] offset:56832\n\t"
    "ds_read_b64_tr_b16 a[208:209], %[ra11] offset:38400\n\t"
    "v_mfma_f32_16x16x32_bf16 v[72:75], a[164:167], a[128:131], v[72:75]\n\t"
    "ds_read_b64_tr_b16 a[210:211], %[ra11] offset:56832\n\t"
    "v_mfma_f32_16x16x32_bf16 v[80:83], a[168:171], a[128:131], v[80:83]\n\t"
    "ds_read_b64_tr_b16 a[212:213], %[ra12] offset:36864\n\t"
    "v_mfma_f32_16x16x32_bf16 v[84:87], a[168:171], a[132:135], v[84:87]\n\t"
    "ds_read_b64_tr_b16 a[214:215], %[ra12] offset:55296\n\t"
    "v_mfma_f32_16x16x32_bf16 v[92:95], a[172:175], a[132:135], v[92:95]\n\t"
    "v_mfma_f32_16x16x32_bf16 v[88:91], a[172:175], a[128:131], v[88:91]\n\t"
    "v_mfma_f32_16x16x32_bf16 v[96:99], a[176:179], a[128:131], v[96:99]\n\t"
    "v_mfma_f32_16x16x32_bf16 v[100:103], a[176:179], a[132:135], v[100:103]\n\t"
    "s_waitcnt lgkmcnt(0)\n\t"
    "v_mfma_f32_16x16x32_bf16 v[32:35], a[180:183], a[136:139], v[32:35]\n\t"
    "v_mfma_f32_16x16x32_bf16 v[36:39], a[180:183], a[140:143], v[36:39]\n\t"
    "v_mfma_f32_16x16x32_bf16 v[44:47], a[184:187], a[140:143], v[44:47]\n\t"
    "v_mfma_f32_16x16x32_bf16 v[40:43], a[184:187], a[136:139], v[40:43]\n\t"
    "v_mfma_f32_16x16x32_bf16 v[48:51], a[188:191], a[136:139], v[48:51]\n\t"
    "v_mfma_f32_16x16x32_bf16 v[52:55], a[188:191], a[140:143], v[52:55]\n\t"
    "v_mfma_f32_16x16x32_bf16 v[60:63], a[192:195], a[140:143], v[60:63]\n\t"
    "v_mfma_f32_16x16x32_bf16 v[56:59], a[192:195], a[136:139], v[56:59]\n\t"
    "v_mfma_f32_16x16x32_bf16 v[64:67], a[196:199], a[136:139], v[64:67]\n\t"
    "v_mfma_f32_16x16x32_bf16 v[68:71], a[196:199], a[140:143], v[68:71]\n\t"
    "v_mfma_f32_16x16x32_bf16 v[76:79], a[200:203], a[140:143], v[76:79]\n\t"
    "v_mfma_f32_16x16x32_bf16 v[72:75], a[200:203], a[136:139], v[72:75]\n\t"
    "v_mfma_f32_16x16x32_bf16 v[80:83], a[204:207], a[136:139], v[80:83]\n\t"
    "v_mfma_f32_16x16x32_bf16 v[84:87], a[204:207], a[140:143], v[84:87]\n\t"
    "v_mfma_f32_16x16x32_bf16 v[92:95], a[208:211], a[140:143], v[92:95]\n\t"
    "v_mfma_f32_16x16x32_bf16 v[88:91], a[208:211], a[136:139], v[88:91]\n\t"
    "v_mfma_f32_16x16x32_bf16 v[96:99], a[212:215], a[136:139], v[96:99]\n\t"
    "v_mfma_f32_16x16x32_bf16 v[100:103], a[212:215], a[140:143], v[100:103]\n\t"
    "s_nop 7\n\t"
    "s_waitcnt vmcnt(0)\n\t"
    : "=&{v[32:39]}"(acc[0]), "=&{v[40:47]}"(acc[1]), "=&{v[48:55]}"(acc[2]), "=&{v[56:63]}"(acc[3]), "=&{v[64:71]}"(acc[4]),
      "=&{v[72:79]}"(acc[5]), "=&{v[80:87]}"(acc[6]), "=&{v[88:95]}"(acc[7]), "=&{v[96:103]}"(acc[8])
    : [ra00] "v"(ra[0]), [ra01] "v"(ra[1]), [ra02] "v"(ra[2]), [ra10] "v"(ra[0] + 73728u), [ra11] "v"(ra[1] + 73728u), [ra12] "v"(ra[2] + 73728u), [wv] "v"(wv), [xl] "v"(xl),
      [xb] "v"(ldsb + 73728u + rot * 1024u + (rho >> 4) * 18432u + (16u * g + (rho & 15u)) * 16u), [hb] "v"(ldsb + 36864u + rot * 1024u + xl),
      [hw0] "v"(ldsb + 36864u + c0 + xl), [hw8] "v"(ldsb + 36864u + c8 + xl),
      [srd1] "s"(srd1), [srd2] "s"(srd2), [srdx] "s"(srdx), [srdn] "s"(srdn), [m0w] "s"(ldsb + 9216u * w), [srds] "s"(srds), [xw] "v"(ldsb + 73728u + 18432u * w + xl),
      [sco0] "v"(((32u * j + rho - 16u) & (S - 1)) * 4u), [sco1] "v"(((32u * j + rho) & (S - 1)) * 4u),
      [xn0] "s"(j * 36864u + c0), [xn8] "s"(j * 36864u + c8),
      [tch] "v"(128u * l + ((j >> 3) * 4u + w) * 24576u), [tq] "v"(l < 36u ? (((j >> 3) * 4u + w) * 18u + (l >> 1)) * 8192u + (24u + (j & 7u)) * 256u + 128u * (l & 1u) : OOB),
      [srdq] "s"(mk_srd(wq, D * HD * 2)), [wrot] "s"(rot * 36864u), [wrapat] "s"(18u - rot)
    : "v104", "v105", "v106", "v107", "v108", "v109", "v110", "v111", "v112", "v113", "v114", "v115", "v116", "v117", "v118", "v119", "v120", "v121", "v122", "v123", "v124", "v125", "v126", "v127", "v128", "v129", "v130", "v131", "v132", "v133", "v134", "v135", "v136", "v137", "v138", "v139", "v140", "v141", "v142", "v143", "v144", "v145", "v146", "a0", "a1", "a2", "a3", "a4", "a5", "a6", "a7", "a8", "a9", "a10", "a11", "a12", "a13", "a14", "a15", "a16", "a17", "a18", "a19", "a20", "a21", "a22", "a23", "a24", "a25", "a26", "a27", "a28", "a29", "a30", "a31", "a32", "a33", "a34", "a35", "a36", "a37", "a38", "a39", "a40", "a41", "a42", "a43", "a44", "a45", "a46", "a47", "a48", "a49", "a50", "a51", "a52", "a53", "a54", "a55", "a56", "a57", "a58", "a59", "a60", "a61", "a62", "a63", "a64", "a65", "a66", "a67", "a68", "a69", "a70", "a71", "a72", "a73", "a74", "a75", "a76", "a77", "a78", "a79", "a80", "a81", "a82", "a83", "a84", "a85", "a86", "a87", "a88", "a89", "a90", "a91", "a92", "a93", "a94", "a95", "a96", "a97", "a98", "a99", "a100", "a101", "a102", "a103", "a104", "a105", "a106", "a107", "a108", "a109", "a110", "a111", "a112", "a113", "a114", "a115", "a116", "a117", "a118", "a119", "a120", "a121", "a122", "a123", "a124", "a125", "a126", "a127", "a128", "a129", "a130", "a131", "a132", "a133", "a134", "a135", "a136", "a137", "a138", "a139", "a140", "a141", "a142", "a143", "a144", "a145", "a146", "a147", "a148", "a149", "a150", "a151", "a152", "a153", "a154", "a155", "a156", "a157", "a158", "a159", "a160", "a161", "a162", "a163", "a164", "a165", "a166", "a167", "a168", "a169", "a170", "a171", "a172", "a173", "a174", "a175", "a176", "a177", "a178", "a179", "a180", "a181", "a182", "a183", "a184", "a185", "a186", "a187", "a188", "a189", "a190", "a191", "a192", "a193", "a194", "a195", "a196", "a197", "a198", "a199", "a200", "a201", "a202", "a203", "a204", "a205", "a206", "a207", "a208", "a209", "a210", "a211", "a212", "a213", "a214", "a215", "s40", "s41", "s42", "s43", "vcc", "scc", "memory");
  asm volatile("s_barrier" ::: "memory");
  for (unsigned i = 0; i < 3; ++i) early_loads(wq, 24u + (j & 7u), i, w, lds);
  const srd_t srdo1 = mk_srd(x1f, S * D * 2), srdo2 = mk_srd(x1nf, S * D * 2);
  f32x2* red = (f32x2*)(lds + ROFF);
  f32x2 v[2][18];
  auto put = [&](const srd_t srd, const int b, const unsigned (&o)[18]) {
#pragma unroll
    for (unsigned p = 0; p < 4; ++p)
      __builtin_amdgcn_raw_buffer_store_b128((u32x4){o[4 * p], o[4 * p + 1], o[4 * p + 2], o[4 * p + 3]}, srd, xl + (2u * j + b) * 18432u + c0 + p * 1024u, 0, 16);
    __builtin_amdgcn_raw_buffer_store_b64((u32x2){o[16], o[17]}, srd, xl + (2u * j + b) * 18432u + c8, 0, 16);
  };
#pragma unroll
  for (int b = 0; b < 2; ++b) {
    f32x2 s = {0.f, 0.f}; unsigned o[18];
#pragma unroll
    for (int i = 0; i < 18; ++i) { v[b][i] = (f32x2){acc[i >> 1][4 * b + 2 * (i & 1)], acc[i >> 1][4 * b + 2 * (i & 1) + 1]}; o[i] = cvtpk(v[b][i].x, v[b][i].y); s += v[b][i]; }
    put(srdo1, b, o);
    const float mw = sum_g(s.x + s.y) * (1.f / 144.f);
    f32x2 q = {0.f, 0.f};
#pragma unroll
    for (int i = 0; i < 18; ++i) { v[b][i] -= mw; q += v[b][i] * v[b][i]; }
    const float qq = sum_g(q.x + q.y);
    if (l < 16) red[(w * 2 + b) * 16 + l] = (f32x2){mw, qq};
  }
  asm volatile("s_waitcnt lgkmcnt(0)\n\ts_barrier" ::: "memory");
#pragma unroll
  for (int b = 0; b < 2; ++b) {
    f32x2 p[4]; float mean = 0.f, m2 = 0.f, own = 0.f; unsigned o[18];
#pragma unroll
    for (int k = 0; k < 4; ++k) { p[k] = red[(k * 2 + b) * 16 + i16]; mean += p[k].x; }
    mean *= 0.25f;
#pragma unroll
    for (int k = 0; k < 4; ++k) { const float d = p[k].x - mean; m2 += p[k].y + 144.f * d * d; own = (unsigned)k == w ? d : own; }
    const float r = __builtin_amdgcn_rsqf(m2 * (1.f / 576.f) + 1e-5f) * GELU_A, os = own * r;
#pragma unroll
    for (int i = 0; i < 18; ++i) { const f32x2 t = v[b][i] * r + os; o[i] = cvtpk(t.x, t.y); }
    put(srdo2, b, o);
  }
  asm volatile("s_waitcnt lgkmcnt(0)\n\ts_barrier" ::: "memory");
}

DI unsigned long long mask64(const bool b) { const unsigned m = rfl(b ? ~0u : 0u); return ((unsigned long long)m << 32) | m; }
DI unsigned long long uni64(const void* p) { const unsigned long long a = (unsigned long long)p; return ((unsigned long long)rfl((unsigned)(a >> 32)) << 32) | rfl((unsigned)a); }
struct Unit { unsigned h, qblk, tile, mi, rb, cb; bool y; };

DI void attn_unit(char* __restrict__ ws, const u16* __restrict__ wq, const u16* __restrict__ Kc, const u16* __restrict__ Vc,
                  const u16* __restrict__ w3, const u16* __restrict__ wo, const u16* __restrict__ w4, const Unit u, char* lds) {
  f32x16 q0, q1, q2, q3; f32x4 ls;
  float mg[4] = {-__builtin_inff(), -__builtin_inff(), -__builtin_inff(), -__builtin_inff()};
#pragma nounroll
  for (unsigned retry = 0;; ++retry) {
    asm volatile("" : "+s"(retry));
    unsigned tid = threadIdx.x;
    asm volatile("" : "+v"(tid));
    const unsigned l = tid & 63u, w = rfl(tid >> 6);
    const unsigned g = l >> 4, i16 = l & 15u, q4 = (l >> 2) & 3u, p4 = l & 3u;
    const unsigned ldsb = (unsigned)(size_t)lds;
    unsigned kb[4], kv[4], vb[2], vv[2];
#pragma unroll
    for (unsigned d = 0; d < 4; ++d) {
      kb[d] = ldsb + 256u * i16 + 16u * ((4u * d + g) ^ i16);
      kv[d] = 256u * g + 16u * (i16 ^ (4u * d + g));
    }
    const unsigned sw = 64u * (l >> 5) + 16u * (p4 ^ (2u * (g & 1u))), r8 = (l >> 2) & 7u;
#pragma unroll
    for (unsigned e = 0; e < 2; ++e) {
      vb[e] = ldsb + 65536u + 2048u * (g >> 1) + 256u * (g & 1u) + 64u * q4 + 16u * ((2u * e + (p4 >> 1)) ^ (2u * (g & 1u))) + 8u * (p4 & 1u);
      vv[e] = (e ? 128u : 1024u) + 256u * r8 + sw;
    }
    const unsigned wb = ldsb + 2048u * (g >> 1) + 256u * (g & 1u) + 64u * q4 + 16u * (p4 ^ (2u * (g & 1u)));
    const unsigned xl = 16u * l, inbox = u.tile * 4u + u.mi;
    const srd_t srdk = mk_srd((const char*)Kc + (size_t)u.h * 524288, 524288u);
    const srd_t srdv = mk_srd((const char*)Vc + (size_t)u.h * 524288 - 1024, 524288u + 1024u);
    const srd_t srdw = mk_srd((const char*)wq + u.h * 256u, 576u * 8192u - u.h * 256u);
    const srd_t srd = mk_srd(ws, WS_END);
    const srd_t srdp = mk_srd(ws + WS_EX + (size_t)(u.y ? inbox : 0u) * INBOX, u.y ? INBOX : 0u);
    const unsigned ui = inbox >> 6, ul = ui == 1u ? 73728u : 82944u;
    const srd_t srdu = mk_srd((const char*)(ui == 0u ? w3 : ui == 1u ? wo : w4) + (inbox & 63u) * ul, u.y ? ul : 0u);
    const srd_t usrdw = mk_srd((const char*)w3 + u.cb * 384u, u.y ? (unsigned)(D * F * 2) - u.cb * 384u : 0u);
    const unsigned kvs = 4096u * w;
    if (u.y) {
      wait_flag(ws, FL_L + 32u * u.qblk + 8u * w + (l & 7u));
      asm volatile("s_waitcnt vmcnt(0)" ::: "memory");
    }
    asm volatile(
    "s_nop 4\n\t"
    "s_nop 0\n\t"
    "s_cmp_eq_u32 %[retry], 1\n\t"
    "s_cbranch_scc0 Lqproj_%=\n\t"
    "s_add_u32 s41, %[kvs], 0\n\t"
    "s_add_u32 m0, %[m0b], 0\n\t"
    "s_nop 0\n\t"
    "buffer_load_dwordx4 %[kv0], %[srdk], s41 offen lds\n\t"
    "buffer_load_dwordx4 %[kv1], %[srdk], s41 offen offset:1024 lds\n\t"
    "buffer_load_dwordx4 %[kv2], %[srdk], s41 offen offset:2048 lds\n\t"
    "buffer_load_dwordx4 %[kv3], %[srdk], s41 offen offset:3072 lds\n\t"
    "s_add_u32 m0, %[m0b], 65536\n\t"
    "s_nop 0\n\t"
    "buffer_load_dwordx4 %[vv0], %[srdv], s41 offen lds\n\t"
    "buffer_load_dwordx4 %[vv1], %[srdv], s41 offen offset:1024 lds\n\t"
    "buffer_load_dwordx4 %[vv0], %[srdv], s41 offen offset:2048 lds\n\t"
    "buffer_load_dwordx4 %[vv1], %[srdv], s41 offen offset:3072 lds\n\t"
    "s_add_u32 s41, %[kvs], 16384\n\t"
    "s_add_u32 m0, %[m0b], 16384\n\t"
    "s_nop 0\n\t"
    "buffer_load_dwordx4 %[kv0], %[srdk], s41 offen lds\n\t"
    "buffer_load_dwordx4 %[kv1], %[srdk], s41 offen offset:1024 lds\n\t"
    "buffer_load_dwordx4 %[kv2], %[srdk], s41 offen offset:2048 lds\n\t"
    "buffer_load_dwordx4 %[kv3], %[srdk], s41 offen offset:3072 lds\n\t"
    "s_add_u32 m0, %[m0b], 81920\n\t"
    "s_nop 0\n\t"
    "buffer_load_dwordx4 %[vv0], %[srdv], s41 offen lds\n\t"
    "buffer_load_dwordx4 %[vv1], %[srdv], s41 offen offset:1024 lds\n\t"
    "buffer_load_dwordx4 %[vv0], %[srdv], s41 offen offset:2048 lds\n\t"
    "buffer_load_dwordx4 %[vv1], %[srdv], s41 offen offset:3072 lds\n\t"
    "s_add_u32 s41, %[kvs], 32768\n\t"
    "s_add_u32 m0, %[m0b], 32768\n\t"
    "s_nop 0\n\t"
    "buffer_load_dwordx4 %[kv0], %[srdk], s41 offen lds\n\t"
    "buffer_load_dwordx4 %[kv1], %[srdk], s41 offen offset:1024 lds\n\t"
    "buffer_load_dwordx4 %[kv2], %[srdk], s41 offen offset:2048 lds\n\t"
    "buffer_load_dwordx4 %[kv3], %[srdk], s41 offen offset:3072 lds\n\t"
    "s_add_u32 m0, %[m0b], 98304\n\t"
    "s_nop 0\n\t"
    "buffer_load_dwordx4 %[vv0], %[srdv], s41 offen lds\n\t"
    "buffer_load_dwordx4 %[vv1], %[srdv], s41 offen offset:1024 lds\n\t"
    "buffer_load_dwordx4 %[vv0], %[srdv], s41 offen offset:2048 lds\n\t"
    "buffer_load_dwordx4 %[vv1], %[srdv], s41 offen offset:3072 lds\n\t"
    "s_add_u32 s41, %[kvs], 49152\n\t"
    "s_add_u32 m0, %[m0b], 49152\n\t"
    "s_nop 0\n\t"
    "buffer_load_dwordx4 %[kv0], %[srdk], s41 offen lds\n\t"
    "buffer_load_dwordx4 %[kv1], %[srdk], s41 offen offset:1024 lds\n\t"
    "buffer_load_dwordx4 %[kv2], %[srdk], s41 offen offset:2048 lds\n\t"
    "buffer_load_dwordx4 %[kv3], %[srdk], s41 offen offset:3072 lds\n\t"
    "s_waitcnt vmcnt(0)\n\t"
    "s_barrier\n\t"
    "ds_read_b128 a[192:195], %[kb0] offset:0\n\t"
    "ds_read_b128 a[196:199], %[kb1] offset:0\n\t"
    "ds_read_b128 a[200:203], %[kb2] offset:0\n\t"
    "ds_read_b128 a[204:207], %[kb3] offset:0\n\t"
    "s_waitcnt lgkmcnt(0)\n\t"
    "s_branch Lp0_%=\n\t"
    "Lfxs_%=:\n\t"
    "s_mov_b32 s41, 0x40000\n\t"
    "Lfxp_%=:\n\t"
    "s_sleep 4\n\t"
    "buffer_load_dword v227, %[fxv], %[srd], 0 offen sc1\n\t"
    "s_waitcnt vmcnt(0)\n\t"
    "v_cmp_ne_u32 vcc, 1, v227\n\t"
    "s_sub_u32 s41, s41, 1\n\t"
    "s_cbranch_scc1 Lfxk_%=\n\t"
    "s_cbranch_vccnz Lfxp_%=\n\t"
    "s_branch Lfxk_%=\n\t"
    "Lqproj_%=:\n\t"
    "s_add_u32 s41, %[xs], 0\n\t"
    "buffer_load_dwordx4 a[0:3], %[xl], %[srd], s41 offen sc1\n\t"
    "buffer_load_dwordx4 a[4:7], %[xl], %[srd], s41 offen offset:1024 sc1\n\t"
    "buffer_load_dwordx4 a[8:11], %[xl], %[srd], s41 offen offset:2048 sc1\n\t"
    "s_add_u32 s41, %[xs], 18432\n\t"
    "buffer_load_dwordx4 a[12:15], %[xl], %[srd], s41 offen sc1\n\t"
    "buffer_load_dwordx4 a[16:19], %[xl], %[srd], s41 offen offset:1024 sc1\n\t"
    "buffer_load_dwordx4 a[20:23], %[xl], %[srd], s41 offen offset:2048 sc1\n\t"
    "s_add_u32 s41, %[xs], 36864\n\t"
    "buffer_load_dwordx4 a[24:27], %[xl], %[srd], s41 offen sc1\n\t"
    "buffer_load_dwordx4 a[28:31], %[xl], %[srd], s41 offen offset:1024 sc1\n\t"
    "buffer_load_dwordx4 a[32:35], %[xl], %[srd], s41 offen offset:2048 sc1\n\t"
    "s_add_u32 s41, %[xs], 55296\n\t"
    "buffer_load_dwordx4 a[36:39], %[xl], %[srd], s41 offen sc1\n\t"
    "buffer_load_dwordx4 a[40:43], %[xl], %[srd], s41 offen offset:1024 sc1\n\t"
    "buffer_load_dwordx4 a[44:47], %[xl], %[srd], s41 offen offset:2048 sc1\n\t"
    "s_add_u32 s41, %[xs], 3072\n\t"
    "buffer_load_dwordx4 a[48:51], %[xl], %[srd], s41 offen sc1\n\t"
    "buffer_load_dwordx4 a[52:55], %[xl], %[srd], s41 offen offset:1024 sc1\n\t"
    "buffer_load_dwordx4 a[56:59], %[xl], %[srd], s41 offen offset:2048 sc1\n\t"
    "s_add_u32 s41, %[xs], 21504\n\t"
    "buffer_load_dwordx4 a[60:63], %[xl], %[srd], s41 offen sc1\n\t"
    "buffer_load_dwordx4 a[64:67], %[xl], %[srd], s41 offen offset:1024 sc1\n\t"
    "buffer_load_dwordx4 a[68:71], %[xl], %[srd], s41 offen offset:2048 sc1\n\t"
    "s_add_u32 s41, %[xs], 39936\n\t"
    "buffer_load_dwordx4 a[72:75], %[xl], %[srd], s41 offen sc1\n\t"
    "buffer_load_dwordx4 a[76:79], %[xl], %[srd], s41 offen offset:1024 sc1\n\t"
    "buffer_load_dwordx4 a[80:83], %[xl], %[srd], s41 offen offset:2048 sc1\n\t"
    "s_add_u32 s41, %[xs], 58368\n\t"
    "buffer_load_dwordx4 a[84:87], %[xl], %[srd], s41 offen sc1\n\t"
    "buffer_load_dwordx4 a[88:91], %[xl], %[srd], s41 offen offset:1024 sc1\n\t"
    "buffer_load_dwordx4 a[92:95], %[xl], %[srd], s41 offen offset:2048 sc1\n\t"
    "v_add_u32 v225, 49152, %[wb]\n\t"
    "v_add_u32 v226, 98304, %[wb]\n\t"
    "v_mov_b32 v224, %[wb]\n\t"
    "v_mov_b32 v228, 1\n\t"
    "v_mov_b32 v229, 0\n\t"
    "s_waitcnt vmcnt(36)\n\t"
    "s_barrier\n\t"
    "ds_read_b64_tr_b16 a[144:145], v224 offset:16384\n\t"
    "ds_read_b64_tr_b16 a[146:147], v224 offset:20480\n\t"
    "ds_read_b64_tr_b16 a[148:149], v224 offset:24576\n\t"
    "ds_read_b64_tr_b16 a[150:151], v224 offset:28672\n\t"
    "ds_read_b64_tr_b16 a[152:153], v224 offset:32768\n\t"
    "ds_read_b64_tr_b16 a[154:155], v224 offset:36864\n\t"
    "ds_read_b64_tr_b16 a[156:157], v224 offset:16392\n\t"
    "ds_read_b64_tr_b16 a[158:159], v224 offset:20488\n\t"
    "ds_read_b64_tr_b16 a[160:161], v224 offset:24584\n\t"
    "ds_read_b64_tr_b16 a[162:163], v224 offset:28680\n\t"
    "ds_read_b64_tr_b16 a[164:165], v224 offset:32776\n\t"
    "ds_read_b64_tr_b16 a[166:167], v224 offset:36872\n\t"
    "s_waitcnt vmcnt(12) lgkmcnt(6)\n\t"
    "v_mfma_f32_16x16x32_bf16 v[96:99], a[144:147], a[0:3], 0\n\t"
    "ds_read_b64_tr_b16 a[168:169], v224 offset:16896\n\t"
    "v_mfma_f32_16x16x32_bf16 v[96:99], a[148:151], a[4:7], v[96:99]\n\t"
    "s_add_u32 s41, %[xs], 6144\n\t"
    "v_mfma_f32_16x16x32_bf16 v[96:99], a[152:155], a[8:11], v[96:99]\n\t"
    "ds_read_b64_tr_b16 a[170:171], v224 offset:20992\n\t"
    "v_mfma_f32_16x16x32_bf16 v[100:103], a[152:155], a[20:23], 0\n\t"
    "buffer_load_dwordx4 a[96:99], %[xl], %[srd], s41 offen sc1\n\t"
    "v_mfma_f32_16x16x32_bf16 v[100:103], a[148:151], a[16:19], v[100:103]\n\t"
    "ds_read_b64_tr_b16 a[172:173], v224 offset:25088\n\t"
    "v_mfma_f32_16x16x32_bf16 v[100:103], a[144:147], a[12:15], v[100:103]\n\t"
    "v_mfma_f32_16x16x32_bf16 v[104:107], a[144:147], a[24:27], 0\n\t"
    "ds_read_b64_tr_b16 a[174:175], v224 offset:29184\n\t"
    "v_mfma_f32_16x16x32_bf16 v[104:107], a[148:151], a[28:31], v[104:107]\n\t"
    "buffer_load_dwordx4 a[100:103], %[xl], %[srd], s41 offen offset:1024 sc1\n\t"
    "v_mfma_f32_16x16x32_bf16 v[104:107], a[152:155], a[32:35], v[104:107]\n\t"
    "ds_read_b64_tr_b16 a[176:177], v224 offset:33280\n\t"
    "v_mfma_f32_16x16x32_bf16 v[108:111], a[152:155], a[44:47], 0\n\t"
    "v_mfma_f32_16x16x32_bf16 v[108:111], a[148:151], a[40:43], v[108:111]\n\t"
    "ds_read_b64_tr_b16 a[178:179], v224 offset:37376\n\t"
    "v_mfma_f32_16x16x32_bf16 v[108:111], a[144:147], a[36:39], v[108:111]\n\t"
    "buffer_load_dwordx4 a[104:107], %[xl], %[srd], s41 offen offset:2048 sc1\n\t"
    "s_waitcnt lgkmcnt(6)\n\t"
    "v_mfma_f32_16x16x32_bf16 v[112:115], a[156:159], a[0:3], 0\n\t"
    "ds_read_b64_tr_b16 a[144:145], v224 offset:16904\n\t"
    "v_mfma_f32_16x16x32_bf16 v[112:115], a[160:163], a[4:7], v[112:115]\n\t"
    "s_add_u32 s41, %[xs], 24576\n\t"
    "v_mfma_f32_16x16x32_bf16 v[112:115], a[164:167], a[8:11], v[112:115]\n\t"
    "ds_read_b64_tr_b16 a[146:147], v224 offset:21000\n\t"
    "v_mfma_f32_16x16x32_bf16 v[116:119], a[164:167], a[20:23], 0\n\t"
    "buffer_load_dwordx4 a[108:111], %[xl], %[srd], s41 offen sc1\n\t"
    "v_mfma_f32_16x16x32_bf16 v[116:119], a[160:163], a[16:19], v[116:119]\n\t"
    "ds_read_b64_tr_b16 a[148:149], v224 offset:25096\n\t"
    "v_mfma_f32_16x16x32_bf16 v[116:119], a[156:159], a[12:15], v[116:119]\n\t"
    "v_mfma_f32_16x16x32_bf16 v[120:123], a[156:159], a[24:27], 0\n\t"
    "ds_read_b64_tr_b16 a[150:151], v224 offset:29192\n\t"
    "v_mfma_f32_16x16x32_bf16 v[120:123], a[160:163], a[28:31], v[120:123]\n\t"
    "buffer_load_dwordx4 a[112:115], %[xl], %[srd], s41 offen offset:1024 sc1\n\t"
    "v_mfma_f32_16x16x32_bf16 v[120:123], a[164:167], a[32:35], v[120:123]\n\t"
    "ds_read_b64_tr_b16 a[152:153], v224 offset:33288\n\t"
    "v_mfma_f32_16x16x32_bf16 v[124:127], a[164:167], a[44:47], 0\n\t"
    "v_mfma_f32_16x16x32_bf16 v[124:127], a[160:163], a[40:43], v[124:127]\n\t"
    "ds_read_b64_tr_b16 a[154:155], v224 offset:37384\n\t"
    "v_mfma_f32_16x16x32_bf16 v[124:127], a[156:159], a[36:39], v[124:127]\n\t"
    "buffer_load_dwordx4 a[116:119], %[xl], %[srd], s41 offen offset:2048 sc1\n\t"
    "s_waitcnt lgkmcnt(6)\n\t"
    "v_mfma_f32_16x16x32_bf16 v[128:131], a[168:171], a[0:3], 0\n\t"
    "ds_read_b64_tr_b16 a[156:157], v224 offset:17408\n\t"
    "v_mfma_f32_16x16x32_bf16 v[128:131], a[172:175], a[4:7], v[128:131]\n\t"
    "s_add_u32 s41, %[xs], 43008\n\t"
    "v_mfma_f32_16x16x32_bf16 v[128:131], a[176:179], a[8:11], v[128:131]\n\t"
    "ds_read_b64_tr_b16 a[158:159], v224 offset:21504\n\t"
    "v_mfma_f32_16x16x32_bf16 v[132:135], a[176:179], a[20:23], 0\n\t"
    "buffer_load_dwordx4 a[120:123], %[xl], %[srd], s41 offen sc1\n\t"
    "v_mfma_f32_16x16x32_bf16 v[132:135], a[172:175], a[16:19], v[132:135]\n\t"
    "ds_read_b64_tr_b16 a[160:161], v224 offset:25600\n\t"
    "v_mfma_f32_16x16x32_bf16 v[132:135], a[168:171], a[12:15], v[132:135]\n\t"
    "v_mfma_f32_16x16x32_bf16 v[136:139], a[168:171], a[24:27], 0\n\t"
    "ds_read_b64_tr_b16 a[162:163], v224 offset:29696\n\t"
    "v_mfma_f32_16x16x32_bf16 v[136:139], a[172:175], a[28:31], v[136:139]\n\t"
    "buffer_load_dwordx4 a[124:127], %[xl], %[srd], s41 offen offset:1024 sc1\n\t"
    "v_mfma_f32_16x16x32_bf16 v[136:139], a[176:179], a[32:35], v[136:139]\n\t"
    "ds_read_b64_tr_b16 a[164:165], v224 offset:33792\n\t"
    "v_mfma_f32_16x16x32_bf16 v[140:143], a[176:179], a[44:47], 0\n\t"
    "v_mfma_f32_16x16x32_bf16 v[140:143], a[172:175], a[40:43], v[140:143]\n\t"
    "ds_read_b64_tr_b16 a[166:167], v224 offset:37888\n\t"
    "v_mfma_f32_16x16x32_bf16 v[140:143], a[168:171], a[36:39], v[140:143]\n\t"
    "buffer_load_dwordx4 a[128:131], %[xl], %[srd], s41 offen offset:2048 sc1\n\t"
    "s_waitcnt lgkmcnt(6)\n\t"
    "v_mfma_f32_16x16x32_bf16 v[144:147], a[144:147], a[0:3], 0\n\t"
    "ds_read_b64_tr_b16 a[168:169], v224 offset:17416\n\t"
    "v_mfma_f32_16x16x32_bf16 v[144:147], a[148:151], a[4:7], v[144:147]\n\t"
    "s_add_u32 s41, %[xs], 61440\n\t"
    "v_mfma_f32_16x16x32_bf16 v[144:147], a[152:155], a[8:11], v[144:147]\n\t"
    "ds_read_b64_tr_b16 a[170:171], v224 offset:21512\n\t"
    "v_mfma_f32_16x16x32_bf16 v[148:151], a[152:155], a[20:23], 0\n\t"
    "buffer_load_dwordx4 a[132:135], %[xl], %[srd], s41 offen sc1\n\t"
    "v_mfma_f32_16x16x32_bf16 v[148:151], a[148:151], a[16:19], v[148:151]\n\t"
    "ds_read_b64_tr_b16 a[172:173], v224 offset:25608\n\t"
    "v_mfma_f32_16x16x32_bf16 v[148:151], a[144:147], a[12:15], v[148:151]\n\t"
    "v_mfma_f32_16x16x32_bf16 v[152:155], a[144:147], a[24:27], 0\n\t"
    "ds_read_b64_tr_b16 a[174:175], v224 offset:29704\n\t"
    "v_mfma_f32_16x16x32_bf16 v[152:155], a[148:151], a[28:31], v[152:155]\n\t"
    "buffer_load_dwordx4 a[136:139], %[xl], %[srd], s41 offen offset:1024 sc1\n\t"
    "v_mfma_f32_16x16x32_bf16 v[152:155], a[152:155], a[32:35], v[152:155]\n\t"
    "ds_read_b64_tr_b16 a[176:177], v224 offset:33800\n\t"
    "v_mfma_f32_16x16x32_bf16 v[156:159], a[152:155], a[44:47], 0\n\t"
    "v_mfma_f32_16x16x32_bf16 v[156:159], a[148:151], a[40:43], v[156:159]\n\t"
    "ds_read_b64_tr_b16 a[178:179], v224 offset:37896\n\t"
    "v_mfma_f32_16x16x32_bf16 v[156:159], a[144:147], a[36:39], v[156:159]\n\t"
    "buffer_load_dwordx4 a[140:143], %[xl], %[srd], s41 offen offset:2048 sc1\n\t"
    "s_waitcnt lgkmcnt(6)\n\t"
    "v_mfma_f32_16x16x32_bf16 v[160:163], a[156:159], a[0:3], 0\n\t"
    "ds_read_b64_tr_b16 a[144:145], v224 offset:17920\n\t"
    "v_mfma_f32_16x16x32_bf16 v[160:163], a[160:163], a[4:7], v[160:163]\n\t"
    "v_mfma_f32_16x16x32_bf16 v[160:163], a[164:167], a[8:11], v[160:163]\n\t"
    "ds_read_b64_tr_b16 a[146:147], v224 offset:22016\n\t"
    "v_mfma_f32_16x16x32_bf16 v[164:167], a[164:167], a[20:23], 0\n\t"
    "v_mfma_f32_16x16x32_bf16 v[164:167], a[160:163], a[16:19], v[164:167]\n\t"
    "ds_read_b64_tr_b16 a[148:149], v224 offset:26112\n\t"
    "v_mfma_f32_16x16x32_bf16 v[164:167], a[156:159], a[12:15], v[164:167]\n\t"
    "v_mfma_f32_16x16x32_bf16 v[168:171], a[156:159], a[24:27], 0\n\t"
    "ds_read_b64_tr_b16 a[150:151], v224 offset:30208\n\t"
    "v_mfma_f32_16x16x32_bf16 v[168:171], a[160:163], a[28:31], v[168:171]\n\t"
    "v_mfma_f32_16x16x32_bf16 v[168:171], a[164:167], a[32:35], v[168:171]\n\t"
    "ds_read_b64_tr_b16 a[152:153], v224 offset:34304\n\t"
    "v_mfma_f32_16x16x32_bf16 v[172:175], a[164:167], a[44:47], 0\n\t"
    "v_mfma_f32_16x16x32_bf16 v[172:175], a[160:163], a[40:43], v[172:175]\n\t"
    "ds_read_b64_tr_b16 a[154:155], v224 offset:38400\n\t"
    "v_mfma_f32_16x16x32_bf16 v[172:175], a[156:159], a[36:39], v[172:175]\n\t"
    "s_waitcnt lgkmcnt(6)\n\t"
    "v_mfma_f32_16x16x32_bf16 v[176:179], a[168:171], a[0:3], 0\n\t"
    "ds_read_b64_tr_b16 a[156:157], v224 offset:17928\n\t"
    "v_mfma_f32_16x16x32_bf16 v[176:179], a[172:175], a[4:7], v[176:179]\n\t"
    "v_mfma_f32_16x16x32_bf16 v[176:179], a[176:179], a[8:11], v[176:179]\n\t"
    "ds_read_b64_tr_b16 a[158:159], v224 offset:22024\n\t"
    "v_mfma_f32_16x16x32_bf16 v[180:183], a[176:179], a[20:23], 0\n\t"
    "v_mfma_f32_16x16x32_bf16 v[180:183], a[172:175], a[16:19], v[180:183]\n\t"
    "ds_read_b64_tr_b16 a[160:161], v224 offset:26120\n\t"
    "v_mfma_f32_16x16x32_bf16 v[180:183], a[168:171], a[12:15], v[180:183]\n\t"
    "v_mfma_f32_16x16x32_bf16 v[184:187], a[168:171], a[24:27], 0\n\t"
    "ds_read_b64_tr_b16 a[162:163], v224 offset:30216\n\t"
    "v_mfma_f32_16x16x32_bf16 v[184:187], a[172:175], a[28:31], v[184:187]\n\t"
    "v_mfma_f32_16x16x32_bf16 v[184:187], a[176:179], a[32:35], v[184:187]\n\t"
    "ds_read_b64_tr_b16 a[164:165], v224 offset:34312\n\t"
    "v_mfma_f32_16x16x32_bf16 v[188:191], a[176:179], a[44:47], 0\n\t"
    "v_mfma_f32_16x16x32_bf16 v[188:191], a[172:175], a[40:43], v[188:191]\n\t"
    "ds_read_b64_tr_b16 a[166:167], v224 offset:38408\n\t"
    "v_mfma_f32_16x16x32_bf16 v[188:191], a[168:171], a[36:39], v[188:191]\n\t"
    "s_waitcnt lgkmcnt(6)\n\t"
    "s_waitcnt vmcnt(42)\n\t"
    "s_barrier\n\t"
    "v_mfma_f32_16x16x32_bf16 v[192:195], a[144:147], a[0:3], 0\n\t"
    "ds_read_b64_tr_b16 a[168:169], v224 offset:40960\n\t"
    "v_mfma_f32_16x16x32_bf16 v[192:195], a[148:151], a[4:7], v[192:195]\n\t"
    "v_mfma_f32_16x16x32_bf16 v[192:195], a[152:155], a[8:11], v[192:195]\n\t"
    "ds_read_b64_tr_b16 a[170:171], v224 offset:45056\n\t"
    "v_mfma_f32_16x16x32_bf16 v[196:199], a[152:155], a[20:23], 0\n\t"
    "v_mfma_f32_16x16x32_bf16 v[196:199], a[148:151], a[16:19], v[196:199]\n\t"
    "ds_read_b64_tr_b16 a[172:173], v224 offset:49152\n\t"
    "v_mfma_f32_16x16x32_bf16 v[196:199], a[144:147], a[12:15], v[196:199]\n\t"
    "v_mfma_f32_16x16x32_bf16 v[200:203], a[144:147], a[24:27], 0\n\t"
    "ds_read_b64_tr_b16 a[174:175], v224 offset:53248\n\t"
    "v_mfma_f32_16x16x32_bf16 v[200:203], a[148:151], a[28:31], v[200:203]\n\t"
    "v_mfma_f32_16x16x32_bf16 v[200:203], a[152:155], a[32:35], v[200:203]\n\t"
    "ds_read_b64_tr_b16 a[176:177], v224 offset:57344\n\t"
    "v_mfma_f32_16x16x32_bf16 v[204:207], a[152:155], a[44:47], 0\n\t"
    "v_mfma_f32_16x16x32_bf16 v[204:207], a[148:151], a[40:43], v[204:207]\n\t"
    "ds_read_b64_tr_b16 a[178:179], v224 offset:61440\n\t"
    "v_mfma_f32_16x16x32_bf16 v[204:207], a[144:147], a[36:39], v[204:207]\n\t"
    "s_waitcnt lgkmcnt(6)\n\t"
    "v_mfma_f32_16x16x32_bf16 v[208:211], a[156:159], a[0:3], 0\n\t"
    "ds_read_b64_tr_b16 a[144:145], v224 offset:40968\n\t"
    "v_mfma_f32_16x16x32_bf16 v[208:211], a[160:163], a[4:7], v[208:211]\n\t"
    "v_mfma_f32_16x16x32_bf16 v[208:211], a[164:167], a[8:11], v[208:211]\n\t"
    "ds_read_b64_tr_b16 a[146:147], v224 offset:45064\n\t"
    "v_mfma_f32_16x16x32_bf16 v[212:215], a[164:167], a[20:23], 0\n\t"
    "v_mfma_f32_16x16x32_bf16 v[212:215], a[160:163], a[16:19], v[212:215]\n\t"
    "ds_read_b64_tr_b16 a[148:149], v224 offset:49160\n\t"
    "v_mfma_f32_16x16x32_bf16 v[212:215], a[156:159], a[12:15], v[212:215]\n\t"
    "v_mfma_f32_16x16x32_bf16 v[216:219], a[156:159], a[24:27], 0\n\t"
    "ds_read_b64_tr_b16 a[150:151], v224 offset:53256\n\t"
    "v_mfma_f32_16x16x32_bf16 v[216:219], a[160:163], a[28:31], v[216:219]\n\t"
    "v_mfma_f32_16x16x32_bf16 v[216:219], a[164:167], a[32:35], v[216:219]\n\t"
    "ds_read_b64_tr_b16 a[152:153], v224 offset:57352\n\t"
    "v_mfma_f32_16x16x32_bf16 v[220:223], a[164:167], a[44:47], 0\n\t"
    "v_mfma_f32_16x16x32_bf16 v[220:223], a[160:163], a[40:43], v[220:223]\n\t"
    "ds_read_b64_tr_b16 a[154:155], v224 offset:61448\n\t"
    "v_mfma_f32_16x16x32_bf16 v[220:223], a[156:159], a[36:39], v[220:223]\n\t"
    "s_waitcnt vmcnt(12) lgkmcnt(6)\n\t"
    "v_mfma_f32_16x16x32_bf16 v[96:99], a[168:171], a[48:51], v[96:99]\n\t"
    "ds_read_b64_tr_b16 a[156:157], v224 offset:41472\n\t"
    "v_mfma_f32_16x16x32_bf16 v[96:99], a[172:175], a[52:55], v[96:99]\n\t"
    "s_add_u32 m0, %[m0w], 73728\n\t"
    "s_add_u32 s41, %[ws], 2359296\n\t"
    "v_mfma_f32_16x16x32_bf16 v[96:99], a[176:179], a[56:59], v[96:99]\n\t"
    "ds_read_b64_tr_b16 a[158:159], v224 offset:45568\n\t"
    "v_mfma_f32_16x16x32_bf16 v[100:103], a[176:179], a[68:71], v[100:103]\n\t"
    "buffer_load_dwordx4 %[wv], %[srdw], s41 offen lds\n\t"
    "v_mfma_f32_16x16x32_bf16 v[100:103], a[172:175], a[64:67], v[100:103]\n\t"
    "ds_read_b64_tr_b16 a[160:161], v224 offset:49664\n\t"
    "v_mfma_f32_16x16x32_bf16 v[100:103], a[168:171], a[60:63], v[100:103]\n\t"
    "s_add_u32 m0, %[m0w], 74752\n\t"
    "s_add_u32 s41, %[ws], 2359424\n\t"
    "v_mfma_f32_16x16x32_bf16 v[104:107], a[168:171], a[72:75], v[104:107]\n\t"
    "ds_read_b64_tr_b16 a[162:163], v224 offset:53760\n\t"
    "v_mfma_f32_16x16x32_bf16 v[104:107], a[172:175], a[76:79], v[104:107]\n\t"
    "buffer_load_dwordx4 %[wv], %[srdw], s41 offen lds\n\t"
    "v_mfma_f32_16x16x32_bf16 v[104:107], a[176:179], a[80:83], v[104:107]\n\t"
    "ds_read_b64_tr_b16 a[164:165], v224 offset:57856\n\t"
    "v_mfma_f32_16x16x32_bf16 v[108:111], a[176:179], a[92:95], v[108:111]\n\t"
    "s_add_u32 m0, %[m0w], 75776\n\t"
    "s_add_u32 s41, %[ws], 2424832\n\t"
    "v_mfma_f32_16x16x32_bf16 v[108:111], a[172:175], a[88:91], v[108:111]\n\t"
    "ds_read_b64_tr_b16 a[166:167], v224 offset:61952\n\t"
    "v_mfma_f32_16x16x32_bf16 v[108:111], a[168:171], a[84:87], v[108:111]\n\t"
    "buffer_load_dwordx4 %[wv], %[srdw], s41 offen lds\n\t"
    "s_waitcnt lgkmcnt(6)\n\t"
    "v_mfma_f32_16x16x32_bf16 v[112:115], a[144:147], a[48:51], v[112:115]\n\t"
    "ds_read_b64_tr_b16 a[168:169], v224 offset:41480\n\t"
    "v_mfma_f32_16x16x32_bf16 v[112:115], a[148:151], a[52:55], v[112:115]\n\t"
    "s_add_u32 m0, %[m0w], 76800\n\t"
    "s_add_u32 s41, %[ws], 2424960\n\t"
    "v_mfma_f32_16x16x32_bf16 v[112:115], a[152:155], a[56:59], v[112:115]\n\t"
    "ds_read_b64_tr_b16 a[170:171], v224 offset:45576\n\t"
    "v_mfma_f32_16x16x32_bf16 v[116:119], a[152:155], a[68:71], v[116:119]\n\t"
    "buffer_load_dwordx4 %[wv], %[srdw], s41 offen lds\n\t"
    "v_mfma_f32_16x16x32_bf16 v[116:119], a[148:151], a[64:67], v[116:119]\n\t"
    "ds_read_b64_tr_b16 a[172:173], v224 offset:49672\n\t"
    "v_mfma_f32_16x16x32_bf16 v[116:119], a[144:147], a[60:63], v[116:119]\n\t"
    "s_add_u32 m0, %[m0w], 77824\n\t"
    "s_add_u32 s41, %[ws], 2490368\n\t"
    "v_mfma_f32_16x16x32_bf16 v[120:123], a[144:147], a[72:75], v[120:123]\n\t"
    "ds_read_b64_tr_b16 a[174:175], v224 offset:53768\n\t"
    "v_mfma_f32_16x16x32_bf16 v[120:123], a[148:151], a[76:79], v[120:123]\n\t"
    "buffer_load_dwordx4 %[wv], %[srdw], s41 offen lds\n\t"
    "v_mfma_f32_16x16x32_bf16 v[120:123], a[152:155], a[80:83], v[120:123]\n\t"
    "ds_read_b64_tr_b16 a[176:177], v224 offset:57864\n\t"
    "v_mfma_f32_16x16x32_bf16 v[124:127], a[152:155], a[92:95], v[124:127]\n\t"
    "s_add_u32 m0, %[m0w], 78848\n\t"
    "s_add_u32 s41, %[ws], 2490496\n\t"
    "v_mfma_f32_16x16x32_bf16 v[124:127], a[148:151], a[88:91], v[124:127]\n\t"
    "ds_read_b64_tr_b16 a[178:179], v224 offset:61960\n\t"
    "v_mfma_f32_16x16x32_bf16 v[124:127], a[144:147], a[84:87], v[124:127]\n\t"
    "buffer_load_dwordx4 %[wv], %[srdw], s41 offen lds\n\t"
    "s_waitcnt lgkmcnt(6)\n\t"
    "v_mfma_f32_16x16x32_bf16 v[128:131], a[156:159], a[48:51], v[128:131]\n\t"
    "ds_read_b64_tr_b16 a[144:145], v224 offset:41984\n\t"
    "v_mfma_f32_16x16x32_bf16 v[128:131], a[160:163], a[52:55], v[128:131]\n\t"
    "s_add_u32 s41, %[xs], 9216\n\t"
    "v_mfma_f32_16x16x32_bf16 v[128:131], a[164:167], a[56:59], v[128:131]\n\t"
    "ds_read_b64_tr_b16 a[146:147], v224 offset:46080\n\t"
    "v_mfma_f32_16x16x32_bf16 v[132:135], a[164:167], a[68:71], v[132:135]\n\t"
    "buffer_load_dwordx4 a[0:3], %[xl], %[srd], s41 offen sc1\n\t"
    "v_mfma_f32_16x16x32_bf16 v[132:135], a[160:163], a[64:67], v[132:135]\n\t"
    "ds_read_b64_tr_b16 a[148:149], v224 offset:50176\n\t"
    "v_mfma_f32_16x16x32_bf16 v[132:135], a[156:159], a[60:63], v[132:135]\n\t"
    "v_mfma_f32_16x16x32_bf16 v[136:139], a[156:159], a[72:75], v[136:139]\n\t"
    "ds_read_b64_tr_b16 a[150:151], v224 offset:54272\n\t"
    "v_mfma_f32_16x16x32_bf16 v[136:139], a[160:163], a[76:79], v[136:139]\n\t"
    "buffer_load_dwordx4 a[4:7], %[xl], %[srd], s41 offen offset:1024 sc1\n\t"
    "v_mfma_f32_16x16x32_bf16 v[136:139], a[164:167], a[80:83], v[136:139]\n\t"
    "ds_read_b64_tr_b16 a[152:153], v224 offset:58368\n\t"
    "v_mfma_f32_16x16x32_bf16 v[140:143], a[164:167], a[92:95], v[140:143]\n\t"
    "v_mfma_f32_16x16x32_bf16 v[140:143], a[160:163], a[88:91], v[140:143]\n\t"
    "ds_read_b64_tr_b16 a[154:155], v224 offset:62464\n\t"
    "v_mfma_f32_16x16x32_bf16 v[140:143], a[156:159], a[84:87], v[140:143]\n\t"
    "buffer_load_dwordx4 a[8:11], %[xl], %[srd], s41 offen offset:2048 sc1\n\t"
    "s_waitcnt lgkmcnt(6)\n\t"
    "v_mfma_f32_16x16x32_bf16 v[144:147], a[168:171], a[48:51], v[144:147]\n\t"
    "ds_read_b64_tr_b16 a[156:157], v224 offset:41992\n\t"
    "v_mfma_f32_16x16x32_bf16 v[144:147], a[172:175], a[52:55], v[144:147]\n\t"
    "s_add_u32 s41, %[xs], 27648\n\t"
    "v_mfma_f32_16x16x32_bf16 v[144:147], a[176:179], a[56:59], v[144:147]\n\t"
    "ds_read_b64_tr_b16 a[158:159], v224 offset:46088\n\t"
    "v_mfma_f32_16x16x32_bf16 v[148:151], a[176:179], a[68:71], v[148:151]\n\t"
    "buffer_load_dwordx4 a[12:15], %[xl], %[srd], s41 offen sc1\n\t"
    "v_mfma_f32_16x16x32_bf16 v[148:151], a[172:175], a[64:67], v[148:151]\n\t"
    "ds_read_b64_tr_b16 a[160:161], v224 offset:50184\n\t"
    "v_mfma_f32_16x16x32_bf16 v[148:151], a[168:171], a[60:63], v[148:151]\n\t"
    "v_mfma_f32_16x16x32_bf16 v[152:155], a[168:171], a[72:75], v[152:155]\n\t"
    "ds_read_b64_tr_b16 a[162:163], v224 offset:54280\n\t"
    "v_mfma_f32_16x16x32_bf16 v[152:155], a[172:175], a[76:79], v[152:155]\n\t"
    "buffer_load_dwordx4 a[16:19], %[xl], %[srd], s41 offen offset:1024 sc1\n\t"
    "v_mfma_f32_16x16x32_bf16 v[152:155], a[176:179], a[80:83], v[152:155]\n\t"
    "ds_read_b64_tr_b16 a[164:165], v224 offset:58376\n\t"
    "v_mfma_f32_16x16x32_bf16 v[156:159], a[176:179], a[92:95], v[156:159]\n\t"
    "v_mfma_f32_16x16x32_bf16 v[156:159], a[172:175], a[88:91], v[156:159]\n\t"
    "ds_read_b64_tr_b16 a[166:167], v224 offset:62472\n\t"
    "v_mfma_f32_16x16x32_bf16 v[156:159], a[168:171], a[84:87], v[156:159]\n\t"
    "buffer_load_dwordx4 a[20:23], %[xl], %[srd], s41 offen offset:2048 sc1\n\t"
    "s_waitcnt lgkmcnt(6)\n\t"
    "v_mfma_f32_16x16x32_bf16 v[160:163], a[144:147], a[48:51], v[160:163]\n\t"
    "ds_read_b64_tr_b16 a[168:169], v224 offset:42496\n\t"
    "v_mfma_f32_16x16x32_bf16 v[160:163], a[148:151], a[52:55], v[160:163]\n\t"
    "s_add_u32 s41, %[xs], 46080\n\t"
    "v_mfma_f32_16x16x32_bf16 v[160:163], a[152:155], a[56:59], v[160:163]\n\t"
    "ds_read_b64_tr_b16 a[170:171], v224 offset:46592\n\t"
    "v_mfma_f32_16x16x32_bf16 v[164:167], a[152:155], a[68:71], v[164:167]\n\t"
    "buffer_load_dwordx4 a[24:27], %[xl], %[srd], s41 offen sc1\n\t"
    "v_mfma_f32_16x16x32_bf16 v[164:167], a[148:151], a[64:67], v[164:167]\n\t"
    "ds_read_b64_tr_b16 a[172:173], v224 offset:50688\n\t"
    "v_mfma_f32_16x16x32_bf16 v[164:167], a[144:147], a[60:63], v[164:167]\n\t"
    "v_mfma_f32_16x16x32_bf16 v[168:171], a[144:147], a[72:75], v[168:171]\n\t"
    "ds_read_b64_tr_b16 a[174:175], v224 offset:54784\n\t"
    "v_mfma_f32_16x16x32_bf16 v[168:171], a[148:151], a[76:79], v[168:171]\n\t"
    "buffer_load_dwordx4 a[28:31], %[xl], %[srd], s41 offen offset:1024 sc1\n\t"
    "v_mfma_f32_16x16x32_bf16 v[168:171], a[152:155], a[80:83], v[168:171]\n\t"
    "ds_read_b64_tr_b16 a[176:177], v224 offset:58880\n\t"
    "v_mfma_f32_16x16x32_bf16 v[172:175], a[152:155], a[92:95], v[172:175]\n\t"
    "v_mfma_f32_16x16x32_bf16 v[172:175], a[148:151], a[88:91], v[172:175]\n\t"
    "ds_read_b64_tr_b16 a[178:179], v224 offset:62976\n\t"
    "v_mfma_f32_16x16x32_bf16 v[172:175], a[144:147], a[84:87], v[172:175]\n\t"
    "buffer_load_dwordx4 a[32:35], %[xl], %[srd], s41 offen offset:2048 sc1\n\t"
    "s_waitcnt lgkmcnt(6)\n\t"
    "v_mfma_f32_16x16x32_bf16 v[176:179], a[156:159], a[48:51], v[176:179]\n\t"
    "ds_read_b64_tr_b16 a[144:145], v224 offset:42504\n\t"
    "v_mfma_f32_16x16x32_bf16 v[176:179], a[160:163], a[52:55], v[176:179]\n\t"
    "s_add_u32 s41, %[xs], 64512\n\t"
    "v_mfma_f32_16x16x32_bf16 v[176:179], a[164:167], a[56:59], v[176:179]\n\t"
    "ds_read_b64_tr_b16 a[146:147], v224 offset:46600\n\t"
    "v_mfma_f32_16x16x32_bf16 v[180:183], a[164:167], a[68:71], v[180:183]\n\t"
    "buffer_load_dwordx4 a[36:39], %[xl], %[srd], s41 offen sc1\n\t"
    "v_mfma_f32_16x16x32_bf16 v[180:183], a[160:163], a[64:67], v[180:183]\n\t"
    "ds_read_b64_tr_b16 a[148:149], v224 offset:50696\n\t"
    "v_mfma_f32_16x16x32_bf16 v[180:183], a[156:159], a[60:63], v[180:183]\n\t"
    "v_mfma_f32_16x16x32_bf16 v[184:187], a[156:159], a[72:75], v[184:187]\n\t"
    "ds_read_b64_tr_b16 a[150:151], v224 offset:54792\n\t"
    "v_mfma_f32_16x16x32_bf16 v[184:187], a[160:163], a[76:79], v[184:187]\n\t"
    "buffer_load_dwordx4 a[40:43], %[xl], %[srd], s41 offen offset:1024 sc1\n\t"
    "v_mfma_f32_16x16x32_bf16 v[184:187], a[164:167], a[80:83], v[184:187]\n\t"
    "ds_read_b64_tr_b16 a[152:153], v224 offset:58888\n\t"
    "v_mfma_f32_16x16x32_bf16 v[188:191], a[164:167], a[92:95], v[188:191]\n\t"
    "v_mfma_f32_16x16x32_bf16 v[188:191], a[160:163], a[88:91], v[188:191]\n\t"
    "ds_read_b64_tr_b16 a[154:155], v224 offset:62984\n\t"
    "v_mfma_f32_16x16x32_bf16 v[188:191], a[156:159], a[84:87], v[188:191]\n\t"
    "buffer_load_dwordx4 a[44:47], %[xl], %[srd], s41 offen offset:2048 sc1\n\t"
    "s_waitcnt lgkmcnt(6)\n\t"
    "s_waitcnt vmcnt(54)\n\t"
    "s_barrier\n\t"
    "v_mfma_f32_16x16x32_bf16 v[192:195], a[168:171], a[48:51], v[192:195]\n\t"
    "ds_read_b64_tr_b16 a[156:157], v225 offset:16384\n\t"
    "v_mfma_f32_16x16x32_bf16 v[192:195], a[172:175], a[52:55], v[192:195]\n\t"
    "v_mfma_f32_16x16x32_bf16 v[192:195], a[176:179], a[56:59], v[192:195]\n\t"
    "ds_read_b64_tr_b16 a[158:159], v225 offset:20480\n\t"
    "v_mfma_f32_16x16x32_bf16 v[196:199], a[176:179], a[68:71], v[196:199]\n\t"
    "v_mfma_f32_16x16x32_bf16 v[196:199], a[172:175], a[64:67], v[196:199]\n\t"
    "ds_read_b64_tr_b16 a[160:161], v225 offset:24576\n\t"
    "v_mfma_f32_16x16x32_bf16 v[196:199], a[168:171], a[60:63], v[196:199]\n\t"
    "v_mfma_f32_16x16x32_bf16 v[200:203], a[168:171], a[72:75], v[200:203]\n\t"
    "ds_read_b64_tr_b16 a[162:163], v225 offset:28672\n\t"
    "v_mfma_f32_16x16x32_bf16 v[200:203], a[172:175], a[76:79], v[200:203]\n\t"
    "v_mfma_f32_16x16x32_bf16 v[200:203], a[176:179], a[80:83], v[200:203]\n\t"
    "ds_read_b64_tr_b16 a[164:165], v225 offset:32768\n\t"
    "v_mfma_f32_16x16x32_bf16 v[204:207], a[176:179], a[92:95], v[204:207]\n\t"
    "v_mfma_f32_16x16x32_bf16 v[204:207], a[172:175], a[88:91], v[204:207]\n\t"
    "ds_read_b64_tr_b16 a[166:167], v225 offset:36864\n\t"
    "v_mfma_f32_16x16x32_bf16 v[204:207], a[168:171], a[84:87], v[204:207]\n\t"
    "s_waitcnt lgkmcnt(6)\n\t"
    "v_mfma_f32_16x16x32_bf16 v[208:211], a[144:147], a[48:51], v[208:211]\n\t"
    "ds_read_b64_tr_b16 a[168:169], v225 offset:16392\n\t"
    "v_mfma_f32_16x16x32_bf16 v[208:211], a[148:151], a[52:55], v[208:211]\n\t"
    "v_mfma_f32_16x16x32_bf16 v[208:211], a[152:155], a[56:59], v[208:211]\n\t"
    "ds_read_b64_tr_b16 a[170:171], v225 offset:20488\n\t"
    "v_mfma_f32_16x16x32_bf16 v[212:215], a[152:155], a[68:71], v[212:215]\n\t"
    "v_mfma_f32_16x16x32_bf16 v[212:215], a[148:151], a[64:67], v[212:215]\n\t"
    "ds_read_b64_tr_b16 a[172:173], v225 offset:24584\n\t"
    "v_mfma_f32_16x16x32_bf16 v[212:215], a[144:147], a[60:63], v[212:215]\n\t"
    "v_mfma_f32_16x16x32_bf16 v[216:219], a[144:147], a[72:75], v[216:219]\n\t"
    "ds_read_b64_tr_b16 a[174:175], v225 offset:28680\n\t"
    "v_mfma_f32_16x16x32_bf16 v[216:219], a[148:151], a[76:79], v[216:219]\n\t"
    "v_mfma_f32_16x16x32_bf16 v[216:219], a[152:155], a[80:83], v[216:219]\n\t"
    "ds_read_b64_tr_b16 a[176:177], v225 offset:32776\n\t"
    "v_mfma_f32_16x16x32_bf16 v[220:223], a[152:155], a[92:95], v[220:223]\n\t"
    "v_mfma_f32_16x16x32_bf16 v[220:223], a[148:151], a[88:91], v[220:223]\n\t"
    "ds_read_b64_tr_b16 a[178:179], v225 offset:36872\n\t"
    "v_mfma_f32_16x16x32_bf16 v[220:223], a[144:147], a[84:87], v[220:223]\n\t"
    "s_waitcnt vmcnt(18) lgkmcnt(6)\n\t"
    "v_mfma_f32_16x16x32_bf16 v[96:99], a[156:159], a[96:99], v[96:99]\n\t"
    "ds_read_b64_tr_b16 a[144:145], v225 offset:16896\n\t"
    "v_mfma_f32_16x16x32_bf16 v[96:99], a[160:163], a[100:103], v[96:99]\n\t"
    "s_add_u32 m0, %[m0w], 98304\n\t"
    "s_add_u32 s41, %[ws], 3145728\n\t"
    "v_mfma_f32_16x16x32_bf16 v[96:99], a[164:167], a[104:107], v[96:99]\n\t"
    "ds_read_b64_tr_b16 a[146:147], v225 offset:20992\n\t"
    "v_mfma_f32_16x16x32_bf16 v[100:103], a[164:167], a[116:119], v[100:103]\n\t"
    "buffer_load_dwordx4 %[wv], %[srdw], s41 offen lds\n\t"
    "v_mfma_f32_16x16x32_bf16 v[100:103], a[160:163], a[112:115], v[100:103]\n\t"
    "ds_read_b64_tr_b16 a[148:149], v225 offset:25088\n\t"
    "v_mfma_f32_16x16x32_bf16 v[100:103], a[156:159], a[108:111], v[100:103]\n\t"
    "s_add_u32 m0, %[m0w], 99328\n\t"
    "s_add_u32 s41, %[ws], 3145856\n\t"
    "v_mfma_f32_16x16x32_bf16 v[104:107], a[156:159], a[120:123], v[104:107]\n\t"
    "ds_read_b64_tr_b16 a[150:151], v225 offset:29184\n\t"
    "v_mfma_f32_16x16x32_bf16 v[104:107], a[160:163], a[124:127], v[104:107]\n\t"
    "buffer_load_dwordx4 %[wv], %[srdw], s41 offen lds\n\t"
    "v_mfma_f32_16x16x32_bf16 v[104:107], a[164:167], a[128:131], v[104:107]\n\t"
    "ds_read_b64_tr_b16 a[152:153], v225 offset:33280\n\t"
    "v_mfma_f32_16x16x32_bf16 v[108:111], a[164:167], a[140:143], v[108:111]\n\t"
    "s_add_u32 m0, %[m0w], 100352\n\t"
    "s_add_u32 s41, %[ws], 3211264\n\t"
    "v_mfma_f32_16x16x32_bf16 v[108:111], a[160:163], a[136:139], v[108:111]\n\t"
    "ds_read_b64_tr_b16 a[154:155], v225 offset:37376\n\t"
    "v_mfma_f32_16x16x32_bf16 v[108:111], a[156:159], a[132:135], v[108:111]\n\t"
    "buffer_load_dwordx4 %[wv], %[srdw], s41 offen lds\n\t"
    "s_waitcnt lgkmcnt(6)\n\t"
    "v_mfma_f32_16x16x32_bf16 v[112:115], a[168:171], a[96:99], v[112:115]\n\t"
    "ds_read_b64_tr_b16 a[156:157], v225 offset:16904\n\t"
    "v_mfma_f32_16x16x32_bf16 v[112:115], a[172:175], a[100:103], v[112:115]\n\t"
    "s_add_u32 m0, %[m0w], 101376\n\t"
    "s_add_u32 s41, %[ws], 3211392\n\t"
    "v_mfma_f32_16x16x32_bf16 v[112:115], a[176:179], a[104:107], v[112:115]\n\t"
    "ds_read_b64_tr_b16 a[158:159], v225 offset:21000\n\t"
    "v_mfma_f32_16x16x32_bf16 v[116:119], a[176:179], a[116:119], v[116:119]\n\t"
    "buffer_load_dwordx4 %[wv], %[srdw], s41 offen lds\n\t"
    "v_mfma_f32_16x16x32_bf16 v[116:119], a[172:175], a[112:115], v[116:119]\n\t"
    "ds_read_b64_tr_b16 a[160:161], v225 offset:25096\n\t"
    "v_mfma_f32_16x16x32_bf16 v[116:119], a[168:171], a[108:111], v[116:119]\n\t"
    "s_add_u32 m0, %[m0w], 102400\n\t"
    "s_add_u32 s41, %[ws], 3276800\n\t"
    "v_mfma_f32_16x16x32_bf16 v[120:123], a[168:171], a[120:123], v[120:123]\n\t"
    "ds_read_b64_tr_b16 a[162:163], v225 offset:29192\n\t"
    "v_mfma_f32_16x16x32_bf16 v[120:123], a[172:175], a[124:127], v[120:123]\n\t"
    "buffer_load_dwordx4 %[wv], %[srdw], s41 offen lds\n\t"
    "v_mfma_f32_16x16x32_bf16 v[120:123], a[176:179], a[128:131], v[120:123]\n\t"
    "ds_read_b64_tr_b16 a[164:165], v225 offset:33288\n\t"
    "v_mfma_f32_16x16x32_bf16 v[124:127], a[176:179], a[140:143], v[124:127]\n\t"
    "s_add_u32 m0, %[m0w], 103424\n\t"
    "s_add_u32 s41, %[ws], 3276928\n\t"
    "v_mfma_f32_16x16x32_bf16 v[124:127], a[172:175], a[136:139], v[124:127]\n\t"
    "ds_read_b64_tr_b16 a[166:167], v225 offset:37384\n\t"
    "v_mfma_f32_16x16x32_bf16 v[124:127], a[168:171], a[132:135], v[124:127]\n\t"
    "buffer_load_dwordx4 %[wv], %[srdw], s41 offen lds\n\t"
    "s_waitcnt lgkmcnt(6)\n\t"
    "v_mfma_f32_16x16x32_bf16 v[128:131], a[144:147], a[96:99], v[128:131]\n\t"
    "ds_read_b64_tr_b16 a[168:169], v225 offset:17408\n\t"
    "v_mfma_f32_16x16x32_bf16 v[128:131], a[148:151], a[100:103], v[128:131]\n\t"
    "s_add_u32 s41, %[xs], 12288\n\t"
    "v_mfma_f32_16x16x32_bf16 v[128:131], a[152:155], a[104:107], v[128:131]\n\t"
    "ds_read_b64_tr_b16 a[170:171], v225 offset:21504\n\t"
    "v_mfma_f32_16x16x32_bf16 v[132:135], a[152:155], a[116:119], v[132:135]\n\t"
    "buffer_load_dwordx4 a[48:51], %[xl], %[srd], s41 offen sc1\n\t"
    "v_mfma_f32_16x16x32_bf16 v[132:135], a[148:151], a[112:115], v[132:135]\n\t"
    "ds_read_b64_tr_b16 a[172:173], v225 offset:25600\n\t"
    "v_mfma_f32_16x16x32_bf16 v[132:135], a[144:147], a[108:111], v[132:135]\n\t"
    "v_mfma_f32_16x16x32_bf16 v[136:139], a[144:147], a[120:123], v[136:139]\n\t"
    "ds_read_b64_tr_b16 a[174:175], v225 offset:29696\n\t"
    "v_mfma_f32_16x16x32_bf16 v[136:139], a[148:151], a[124:127], v[136:139]\n\t"
    "buffer_load_dwordx4 a[52:55], %[xl], %[srd], s41 offen offset:1024 sc1\n\t"
    "v_mfma_f32_16x16x32_bf16 v[136:139], a[152:155], a[128:131], v[136:139]\n\t"
    "ds_read_b64_tr_b16 a[176:177], v225 offset:33792\n\t"
    "v_mfma_f32_16x16x32_bf16 v[140:143], a[152:155], a[140:143], v[140:143]\n\t"
    "v_mfma_f32_16x16x32_bf16 v[140:143], a[148:151], a[136:139], v[140:143]\n\t"
    "ds_read_b64_tr_b16 a[178:179], v225 offset:37888\n\t"
    "v_mfma_f32_16x16x32_bf16 v[140:143], a[144:147], a[132:135], v[140:143]\n\t"
    "buffer_load_dwordx4 a[56:59], %[xl], %[srd], s41 offen offset:2048 sc1\n\t"
    "s_waitcnt lgkmcnt(6)\n\t"
    "v_mfma_f32_16x16x32_bf16 v[144:147], a[156:159], a[96:99], v[144:147]\n\t"
    "ds_read_b64_tr_b16 a[144:145], v225 offset:17416\n\t"
    "v_mfma_f32_16x16x32_bf16 v[144:147], a[160:163], a[100:103], v[144:147]\n\t"
    "s_add_u32 s41, %[xs], 30720\n\t"
    "v_mfma_f32_16x16x32_bf16 v[144:147], a[164:167], a[104:107], v[144:147]\n\t"
    "ds_read_b64_tr_b16 a[146:147], v225 offset:21512\n\t"
    "v_mfma_f32_16x16x32_bf16 v[148:151], a[164:167], a[116:119], v[148:151]\n\t"
    "buffer_load_dwordx4 a[60:63], %[xl], %[srd], s41 offen sc1\n\t"
    "v_mfma_f32_16x16x32_bf16 v[148:151], a[160:163], a[112:115], v[148:151]\n\t"
    "ds_read_b64_tr_b16 a[148:149], v225 offset:25608\n\t"
    "v_mfma_f32_16x16x32_bf16 v[148:151], a[156:159], a[108:111], v[148:151]\n\t"
    "v_mfma_f32_16x16x32_bf16 v[152:155], a[156:159], a[120:123], v[152:155]\n\t"
    "ds_read_b64_tr_b16 a[150:151], v225 offset:29704\n\t"
    "v_mfma_f32_16x16x32_bf16 v[152:155], a[160:163], a[124:127], v[152:155]\n\t"
    "buffer_load_dwordx4 a[64:67], %[xl], %[srd], s41 offen offset:1024 sc1\n\t"
    "v_mfma_f32_16x16x32_bf16 v[152:155], a[164:167], a[128:131], v[152:155]\n\t"
    "ds_read_b64_tr_b16 a[152:153], v225 offset:33800\n\t"
    "v_mfma_f32_16x16x32_bf16 v[156:159], a[164:167], a[140:143], v[156:159]\n\t"
    "v_mfma_f32_16x16x32_bf16 v[156:159], a[160:163], a[136:139], v[156:159]\n\t"
    "ds_read_b64_tr_b16 a[154:155], v225 offset:37896\n\t"
    "v_mfma_f32_16x16x32_bf16 v[156:159], a[156:159], a[132:135], v[156:159]\n\t"
    "buffer_load_dwordx4 a[68:71], %[xl], %[srd], s41 offen offset:2048 sc1\n\t"
    "s_waitcnt lgkmcnt(6)\n\t"
    "v_mfma_f32_16x16x32_bf16 v[160:163], a[168:171], a[96:99], v[160:163]\n\t"
    "ds_read_b64_tr_b16 a[156:157], v225 offset:17920\n\t"
    "v_mfma_f32_16x16x32_bf16 v[160:163], a[172:175], a[100:103], v[160:163]\n\t"
    "s_add_u32 s41, %[xs], 49152\n\t"
    "v_mfma_f32_16x16x32_bf16 v[160:163], a[176:179], a[104:107], v[160:163]\n\t"
    "ds_read_b64_tr_b16 a[158:159], v225 offset:22016\n\t"
    "v_mfma_f32_16x16x32_bf16 v[164:167], a[176:179], a[116:119], v[164:167]\n\t"
    "buffer_load_dwordx4 a[72:75], %[xl], %[srd], s41 offen sc1\n\t"
    "v_mfma_f32_16x16x32_bf16 v[164:167], a[172:175], a[112:115], v[164:167]\n\t"
    "ds_read_b64_tr_b16 a[160:161], v225 offset:26112\n\t"
    "v_mfma_f32_16x16x32_bf16 v[164:167], a[168:171], a[108:111], v[164:167]\n\t"
    "v_mfma_f32_16x16x32_bf16 v[168:171], a[168:171], a[120:123], v[168:171]\n\t"
    "ds_read_b64_tr_b16 a[162:163], v225 offset:30208\n\t"
    "v_mfma_f32_16x16x32_bf16 v[168:171], a[172:175], a[124:127], v[168:171]\n\t"
    "buffer_load_dwordx4 a[76:79], %[xl], %[srd], s41 offen offset:1024 sc1\n\t"
    "v_mfma_f32_16x16x32_bf16 v[168:171], a[176:179], a[128:131], v[168:171]\n\t"
    "ds_read_b64_tr_b16 a[164:165], v225 offset:34304\n\t"
    "v_mfma_f32_16x16x32_bf16 v[172:175], a[176:179], a[140:143], v[172:175]\n\t"
    "v_mfma_f32_16x16x32_bf16 v[172:175], a[172:175], a[136:139], v[172:175]\n\t"
    "ds_read_b64_tr_b16 a[166:167], v225 offset:38400\n\t"
    "v_mfma_f32_16x16x32_bf16 v[172:175], a[168:171], a[132:135], v[172:175]\n\t"
    "buffer_load_dwordx4 a[80:83], %[xl], %[srd], s41 offen offset:2048 sc1\n\t"
    "s_waitcnt lgkmcnt(6)\n\t"
    "v_mfma_f32_16x16x32_bf16 v[176:179], a[144:147], a[96:99], v[176:179]\n\t"
    "ds_read_b64_tr_b16 a[168:169], v225 offset:17928\n\t"
    "v_mfma_f32_16x16x32_bf16 v[176:179], a[148:151], a[100:103], v[176:179]\n\t"
    "s_add_u32 s41, %[xs], 67584\n\t"
    "v_mfma_f32_16x16x32_bf16 v[176:179], a[152:155], a[104:107], v[176:179]\n\t"
    "ds_read_b64_tr_b16 a[170:171], v225 offset:22024\n\t"
    "v_mfma_f32_16x16x32_bf16 v[180:183], a[152:155], a[116:119], v[180:183]\n\t"
    "buffer_load_dwordx4 a[84:87], %[xl], %[srd], s41 offen sc1\n\t"
    "v_mfma_f32_16x16x32_bf16 v[180:183], a[148:151], a[112:115], v[180:183]\n\t"
    "ds_read_b64_tr_b16 a[172:173], v225 offset:26120\n\t"
    "v_mfma_f32_16x16x32_bf16 v[180:183], a[144:147], a[108:111], v[180:183]\n\t"
    "v_mfma_f32_16x16x32_bf16 v[184:187], a[144:147], a[120:123], v[184:187]\n\t"
    "ds_read_b64_tr_b16 a[174:175], v225 offset:30216\n\t"
    "v_mfma_f32_16x16x32_bf16 v[184:187], a[148:151], a[124:127], v[184:187]\n\t"
    "buffer_load_dwordx4 a[88:91], %[xl], %[srd], s41 offen offset:1024 sc1\n\t"
    "v_mfma_f32_16x16x32_bf16 v[184:187], a[152:155], a[128:131], v[184:187]\n\t"
    "ds_read_b64_tr_b16 a[176:177], v225 offset:34312\n\t"
    "v_mfma_f32_16x16x32_bf16 v[188:191], a[152:155], a[140:143], v[188:191]\n\t"
    "v_mfma_f32_16x16x32_bf16 v[188:191], a[148:151], a[136:139], v[188:191]\n\t"
    "ds_read_b64_tr_b16 a[178:179], v225 offset:38408\n\t"
    "v_mfma_f32_16x16x32_bf16 v[188:191], a[144:147], a[132:135], v[188:191]\n\t"
    "buffer_load_dwordx4 a[92:95], %[xl], %[srd], s41 offen offset:2048 sc1\n\t"
    "s_waitcnt lgkmcnt(6)\n\t"
    "s_waitcnt vmcnt(30)\n\t"
    "s_barrier\n\t"
    "v_mfma_f32_16x16x32_bf16 v[192:195], a[156:159], a[96:99], v[192:195]\n\t"
    "ds_read_b64_tr_b16 a[144:145], v225 offset:40960\n\t"
    "v_mfma_f32_16x16x32_bf16 v[192:195], a[160:163], a[100:103], v[192:195]\n\t"
    "buffer_store_dword v228, v229, %[srd], %[fxo] offen sc1\n\t"
    "v_mfma_f32_16x16x32_bf16 v[192:195], a[164:167], a[104:107], v[192:195]\n\t"
    "ds_read_b64_tr_b16 a[146:147], v225 offset:45056\n\t"
    "v_mfma_f32_16x16x32_bf16 v[196:199], a[164:167], a[116:119], v[196:199]\n\t"
    "v_mfma_f32_16x16x32_bf16 v[196:199], a[160:163], a[112:115], v[196:199]\n\t"
    "ds_read_b64_tr_b16 a[148:149], v225 offset:49152\n\t"
    "v_mfma_f32_16x16x32_bf16 v[196:199], a[156:159], a[108:111], v[196:199]\n\t"
    "v_mfma_f32_16x16x32_bf16 v[200:203], a[156:159], a[120:123], v[200:203]\n\t"
    "ds_read_b64_tr_b16 a[150:151], v225 offset:53248\n\t"
    "v_mfma_f32_16x16x32_bf16 v[200:203], a[160:163], a[124:127], v[200:203]\n\t"
    "v_mfma_f32_16x16x32_bf16 v[200:203], a[164:167], a[128:131], v[200:203]\n\t"
    "ds_read_b64_tr_b16 a[152:153], v225 offset:57344\n\t"
    "v_mfma_f32_16x16x32_bf16 v[204:207], a[164:167], a[140:143], v[204:207]\n\t"
    "v_mfma_f32_16x16x32_bf16 v[204:207], a[160:163], a[136:139], v[204:207]\n\t"
    "ds_read_b64_tr_b16 a[154:155], v225 offset:61440\n\t"
    "v_mfma_f32_16x16x32_bf16 v[204:207], a[156:159], a[132:135], v[204:207]\n\t"
    "s_waitcnt lgkmcnt(6)\n\t"
    "v_mfma_f32_16x16x32_bf16 v[208:211], a[168:171], a[96:99], v[208:211]\n\t"
    "ds_read_b64_tr_b16 a[156:157], v225 offset:40968\n\t"
    "v_mfma_f32_16x16x32_bf16 v[208:211], a[172:175], a[100:103], v[208:211]\n\t"
    "v_mfma_f32_16x16x32_bf16 v[208:211], a[176:179], a[104:107], v[208:211]\n\t"
    "ds_read_b64_tr_b16 a[158:159], v225 offset:45064\n\t"
    "v_mfma_f32_16x16x32_bf16 v[212:215], a[176:179], a[116:119], v[212:215]\n\t"
    "v_mfma_f32_16x16x32_bf16 v[212:215], a[172:175], a[112:115], v[212:215]\n\t"
    "ds_read_b64_tr_b16 a[160:161], v225 offset:49160\n\t"
    "v_mfma_f32_16x16x32_bf16 v[212:215], a[168:171], a[108:111], v[212:215]\n\t"
    "v_mfma_f32_16x16x32_bf16 v[216:219], a[168:171], a[120:123], v[216:219]\n\t"
    "ds_read_b64_tr_b16 a[162:163], v225 offset:53256\n\t"
    "v_mfma_f32_16x16x32_bf16 v[216:219], a[172:175], a[124:127], v[216:219]\n\t"
    "v_mfma_f32_16x16x32_bf16 v[216:219], a[176:179], a[128:131], v[216:219]\n\t"
    "ds_read_b64_tr_b16 a[164:165], v225 offset:57352\n\t"
    "v_mfma_f32_16x16x32_bf16 v[220:223], a[176:179], a[140:143], v[220:223]\n\t"
    "v_mfma_f32_16x16x32_bf16 v[220:223], a[172:175], a[136:139], v[220:223]\n\t"
    "ds_read_b64_tr_b16 a[166:167], v225 offset:61448\n\t"
    "v_mfma_f32_16x16x32_bf16 v[220:223], a[168:171], a[132:135], v[220:223]\n\t"
    "s_waitcnt vmcnt(19) lgkmcnt(6)\n\t"
    "v_mfma_f32_16x16x32_bf16 v[96:99], a[144:147], a[0:3], v[96:99]\n\t"
    "ds_read_b64_tr_b16 a[168:169], v225 offset:41472\n\t"
    "v_mfma_f32_16x16x32_bf16 v[96:99], a[148:151], a[4:7], v[96:99]\n\t"
    "s_add_u32 m0, %[m0w], 122880\n\t"
    "s_add_u32 s41, %[ws], 3932160\n\t"
    "v_mfma_f32_16x16x32_bf16 v[96:99], a[152:155], a[8:11], v[96:99]\n\t"
    "ds_read_b64_tr_b16 a[170:171], v225 offset:45568\n\t"
    "v_mfma_f32_16x16x32_bf16 v[100:103], a[152:155], a[20:23], v[100:103]\n\t"
    "buffer_load_dwordx4 %[wv], %[srdw], s41 offen lds\n\t"
    "v_mfma_f32_16x16x32_bf16 v[100:103], a[148:151], a[16:19], v[100:103]\n\t"
    "ds_read_b64_tr_b16 a[172:173], v225 offset:49664\n\t"
    "v_mfma_f32_16x16x32_bf16 v[100:103], a[144:147], a[12:15], v[100:103]\n\t"
    "s_add_u32 m0, %[m0w], 123904\n\t"
    "s_add_u32 s41, %[ws], 3932288\n\t"
    "v_mfma_f32_16x16x32_bf16 v[104:107], a[144:147], a[24:27], v[104:107]\n\t"
    "ds_read_b64_tr_b16 a[174:175], v225 offset:53760\n\t"
    "v_mfma_f32_16x16x32_bf16 v[104:107], a[148:151], a[28:31], v[104:107]\n\t"
    "buffer_load_dwordx4 %[wv], %[srdw], s41 offen lds\n\t"
    "v_mfma_f32_16x16x32_bf16 v[104:107], a[152:155], a[32:35], v[104:107]\n\t"
    "ds_read_b64_tr_b16 a[176:177], v225 offset:57856\n\t"
    "v_mfma_f32_16x16x32_bf16 v[108:111], a[152:155], a[44:47], v[108:111]\n\t"
    "s_add_u32 m0, %[m0w], 124928\n\t"
    "s_add_u32 s41, %[ws], 3997696\n\t"
    "v_mfma_f32_16x16x32_bf16 v[108:111], a[148:151], a[40:43], v[108:111]\n\t"
    "ds_read_b64_tr_b16 a[178:179], v225 offset:61952\n\t"
    "v_mfma_f32_16x16x32_bf16 v[108:111], a[144:147], a[36:39], v[108:111]\n\t"
    "buffer_load_dwordx4 %[wv], %[srdw], s41 offen lds\n\t"
    "s_waitcnt lgkmcnt(6)\n\t"
    "v_mfma_f32_16x16x32_bf16 v[112:115], a[156:159], a[0:3], v[112:115]\n\t"
    "ds_read_b64_tr_b16 a[144:145], v225 offset:41480\n\t"
    "v_mfma_f32_16x16x32_bf16 v[112:115], a[160:163], a[4:7], v[112:115]\n\t"
    "s_add_u32 m0, %[m0w], 125952\n\t"
    "s_add_u32 s41, %[ws], 3997824\n\t"
    "v_mfma_f32_16x16x32_bf16 v[112:115], a[164:167], a[8:11], v[112:115]\n\t"
    "ds_read_b64_tr_b16 a[146:147], v225 offset:45576\n\t"
    "v_mfma_f32_16x16x32_bf16 v[116:119], a[164:167], a[20:23], v[116:119]\n\t"
    "buffer_load_dwordx4 %[wv], %[srdw], s41 offen lds\n\t"
    "v_mfma_f32_16x16x32_bf16 v[116:119], a[160:163], a[16:19], v[116:119]\n\t"
    "ds_read_b64_tr_b16 a[148:149], v225 offset:49672\n\t"
    "v_mfma_f32_16x16x32_bf16 v[116:119], a[156:159], a[12:15], v[116:119]\n\t"
    "s_add_u32 m0, %[m0w], 126976\n\t"
    "s_add_u32 s41, %[ws], 4063232\n\t"
    "v_mfma_f32_16x16x32_bf16 v[120:123], a[156:159], a[24:27], v[120:123]\n\t"
    "ds_read_b64_tr_b16 a[150:151], v225 offset:53768\n\t"
    "v_mfma_f32_16x16x32_bf16 v[120:123], a[160:163], a[28:31], v[120:123]\n\t"
    "buffer_load_dwordx4 %[wv], %[srdw], s41 offen lds\n\t"
    "v_mfma_f32_16x16x32_bf16 v[120:123], a[164:167], a[32:35], v[120:123]\n\t"
    "ds_read_b64_tr_b16 a[152:153], v225 offset:57864\n\t"
    "v_mfma_f32_16x16x32_bf16 v[124:127], a[164:167], a[44:47], v[124:127]\n\t"
    "s_add_u32 m0, %[m0w], 128000\n\t"
    "s_add_u32 s41, %[ws], 4063360\n\t"
    "v_mfma_f32_16x16x32_bf16 v[124:127], a[160:163], a[40:43], v[124:127]\n\t"
    "ds_read_b64_tr_b16 a[154:155], v225 offset:61960\n\t"
    "v_mfma_f32_16x16x32_bf16 v[124:127], a[156:159], a[36:39], v[124:127]\n\t"
    "buffer_load_dwordx4 %[wv], %[srdw], s41 offen lds\n\t"
    "s_waitcnt lgkmcnt(6)\n\t"
    "v_mfma_f32_16x16x32_bf16 v[128:131], a[168:171], a[0:3], v[128:131]\n\t"
    "ds_read_b64_tr_b16 a[156:157], v225 offset:41984\n\t"
    "v_mfma_f32_16x16x32_bf16 v[128:131], a[172:175], a[4:7], v[128:131]\n\t"
    "s_add_u32 s41, %[xs], 15360\n\t"
    "v_mfma_f32_16x16x32_bf16 v[128:131], a[176:179], a[8:11], v[128:131]\n\t"
    "ds_read_b64_tr_b16 a[158:159], v225 offset:46080\n\t"
    "v_mfma_f32_16x16x32_bf16 v[132:135], a[176:179], a[20:23], v[132:135]\n\t"
    "buffer_load_dwordx4 a[96:99], %[xl], %[srd], s41 offen sc1\n\t"
    "v_mfma_f32_16x16x32_bf16 v[132:135], a[172:175], a[16:19], v[132:135]\n\t"
    "ds_read_b64_tr_b16 a[160:161], v225 offset:50176\n\t"
    "v_mfma_f32_16x16x32_bf16 v[132:135], a[168:171], a[12:15], v[132:135]\n\t"
    "v_mfma_f32_16x16x32_bf16 v[136:139], a[168:171], a[24:27], v[136:139]\n\t"
    "ds_read_b64_tr_b16 a[162:163], v225 offset:54272\n\t"
    "v_mfma_f32_16x16x32_bf16 v[136:139], a[172:175], a[28:31], v[136:139]\n\t"
    "buffer_load_dwordx4 a[100:103], %[xl], %[srd], s41 offen offset:1024 sc1\n\t"
    "v_mfma_f32_16x16x32_bf16 v[136:139], a[176:179], a[32:35], v[136:139]\n\t"
    "ds_read_b64_tr_b16 a[164:165], v225 offset:58368\n\t"
    "v_mfma_f32_16x16x32_bf16 v[140:143], a[176:179], a[44:47], v[140:143]\n\t"
    "v_mfma_f32_16x16x32_bf16 v[140:143], a[172:175], a[40:43], v[140:143]\n\t"
    "ds_read_b64_tr_b16 a[166:167], v225 offset:62464\n\t"
    "v_mfma_f32_16x16x32_bf16 v[140:143], a[168:171], a[36:39], v[140:143]\n\t"
    "buffer_load_dwordx4 a[104:107], %[xl], %[srd], s41 offen offset:2048 sc1\n\t"
    "s_waitcnt lgkmcnt(6)\n\t"
    "v_mfma_f32_16x16x32_bf16 v[144:147], a[144:147], a[0:3], v[144:147]\n\t"
    "ds_read_b64_tr_b16 a[168:169], v225 offset:41992\n\t"
    "v_mfma_f32_16x16x32_bf16 v[144:147], a[148:151], a[4:7], v[144:147]\n\t"
    "s_add_u32 s41, %[xs], 33792\n\t"
    "v_mfma_f32_16x16x32_bf16 v[144:147], a[152:155], a[8:11], v[144:147]\n\t"
    "ds_read_b64_tr_b16 a[170:171], v225 offset:46088\n\t"
    "v_mfma_f32_16x16x32_bf16 v[148:151], a[152:155], a[20:23], v[148:151]\n\t"
    "buffer_load_dwordx4 a[108:111], %[xl], %[srd], s41 offen sc1\n\t"
    "v_mfma_f32_16x16x32_bf16 v[148:151], a[148:151], a[16:19], v[148:151]\n\t"
    "ds_read_b64_tr_b16 a[172:173], v225 offset:50184\n\t"
    "v_mfma_f32_16x16x32_bf16 v[148:151], a[144:147], a[12:15], v[148:151]\n\t"
    "v_mfma_f32_16x16x32_bf16 v[152:155], a[144:147], a[24:27], v[152:155]\n\t"
    "ds_read_b64_tr_b16 a[174:175], v225 offset:54280\n\t"
    "v_mfma_f32_16x16x32_bf16 v[152:155], a[148:151], a[28:31], v[152:155]\n\t"
    "buffer_load_dwordx4 a[112:115], %[xl], %[srd], s41 offen offset:1024 sc1\n\t"
    "v_mfma_f32_16x16x32_bf16 v[152:155], a[152:155], a[32:35], v[152:155]\n\t"
    "ds_read_b64_tr_b16 a[176:177], v225 offset:58376\n\t"
    "v_mfma_f32_16x16x32_bf16 v[156:159], a[152:155], a[44:47], v[156:159]\n\t"
    "v_mfma_f32_16x16x32_bf16 v[156:159], a[148:151], a[40:43], v[156:159]\n\t"
    "ds_read_b64_tr_b16 a[178:179], v225 offset:62472\n\t"
    "v_mfma_f32_16x16x32_bf16 v[156:159], a[144:147], a[36:39], v[156:159]\n\t"
    "buffer_load_dwordx4 a[116:119], %[xl], %[srd], s41 offen offset:2048 sc1\n\t"
    "s_waitcnt lgkmcnt(6)\n\t"
    "v_mfma_f32_16x16x32_bf16 v[160:163], a[156:159], a[0:3], v[160:163]\n\t"
    "ds_read_b64_tr_b16 a[144:145], v225 offset:42496\n\t"
    "v_mfma_f32_16x16x32_bf16 v[160:163], a[160:163], a[4:7], v[160:163]\n\t"
    "s_add_u32 s41, %[xs], 52224\n\t"
    "v_mfma_f32_16x16x32_bf16 v[160:163], a[164:167], a[8:11], v[160:163]\n\t"
    "ds_read_b64_tr_b16 a[146:147], v225 offset:46592\n\t"
    "v_mfma_f32_16x16x32_bf16 v[164:167], a[164:167], a[20:23], v[164:167]\n\t"
    "buffer_load_dwordx4 a[120:123], %[xl], %[srd], s41 offen sc1\n\t"
    "v_mfma_f32_16x16x32_bf16 v[164:167], a[160:163], a[16:19], v[164:167]\n\t"
    "ds_read_b64_tr_b16 a[148:149], v225 offset:50688\n\t"
    "v_mfma_f32_16x16x32_bf16 v[164:167], a[156:159], a[12:15], v[164:167]\n\t"
    "v_mfma_f32_16x16x32_bf16 v[168:171], a[156:159], a[24:27], v[168:171]\n\t"
    "ds_read_b64_tr_b16 a[150:151], v225 offset:54784\n\t"
    "v_mfma_f32_16x16x32_bf16 v[168:171], a[160:163], a[28:31], v[168:171]\n\t"
    "buffer_load_dwordx4 a[124:127], %[xl], %[srd], s41 offen offset:1024 sc1\n\t"
    "v_mfma_f32_16x16x32_bf16 v[168:171], a[164:167], a[32:35], v[168:171]\n\t"
    "ds_read_b64_tr_b16 a[152:153], v225 offset:58880\n\t"
    "v_mfma_f32_16x16x32_bf16 v[172:175], a[164:167], a[44:47], v[172:175]\n\t"
    "v_mfma_f32_16x16x32_bf16 v[172:175], a[160:163], a[40:43], v[172:175]\n\t"
    "ds_read_b64_tr_b16 a[154:155], v225 offset:62976\n\t"
    "v_mfma_f32_16x16x32_bf16 v[172:175], a[156:159], a[36:39], v[172:175]\n\t"
    "buffer_load_dwordx4 a[128:131], %[xl], %[srd], s41 offen offset:2048 sc1\n\t"
    "s_waitcnt lgkmcnt(6)\n\t"
    "v_mfma_f32_16x16x32_bf16 v[176:179], a[168:171], a[0:3], v[176:179]\n\t"
    "ds_read_b64_tr_b16 a[156:157], v225 offset:42504\n\t"
    "v_mfma_f32_16x16x32_bf16 v[176:179], a[172:175], a[4:7], v[176:179]\n\t"
    "s_add_u32 s41, %[xs], 70656\n\t"
    "v_mfma_f32_16x16x32_bf16 v[176:179], a[176:179], a[8:11], v[176:179]\n\t"
    "ds_read_b64_tr_b16 a[158:159], v225 offset:46600\n\t"
    "v_mfma_f32_16x16x32_bf16 v[180:183], a[176:179], a[20:23], v[180:183]\n\t"
    "buffer_load_dwordx4 a[132:135], %[xl], %[srd], s41 offen sc1\n\t"
    "v_mfma_f32_16x16x32_bf16 v[180:183], a[172:175], a[16:19], v[180:183]\n\t"
    "ds_read_b64_tr_b16 a[160:161], v225 offset:50696\n\t"
    "v_mfma_f32_16x16x32_bf16 v[180:183], a[168:171], a[12:15], v[180:183]\n\t"
    "v_mfma_f32_16x16x32_bf16 v[184:187], a[168:171], a[24:27], v[184:187]\n\t"
    "ds_read_b64_tr_b16 a[162:163], v225 offset:54792\n\t"
    "v_mfma_f32_16x16x32_bf16 v[184:187], a[172:175], a[28:31], v[184:187]\n\t"
    "buffer_load_dwordx4 a[136:139], %[xl], %[srd], s41 offen offset:1024 sc1\n\t"
    "v_mfma_f32_16x16x32_bf16 v[184:187], a[176:179], a[32:35], v[184:187]\n\t"
    "ds_read_b64_tr_b16 a[164:165], v225 offset:58888\n\t"
    "v_mfma_f32_16x16x32_bf16 v[188:191], a[176:179], a[44:47], v[188:191]\n\t"
    "v_mfma_f32_16x16x32_bf16 v[188:191], a[172:175], a[40:43], v[188:191]\n\t"
    "ds_read_b64_tr_b16 a[166:167], v225 offset:62984\n\t"
    "v_mfma_f32_16x16x32_bf16 v[188:191], a[168:171], a[36:39], v[188:191]\n\t"
    "buffer_load_dwordx4 a[140:143], %[xl], %[srd], s41 offen offset:2048 sc1\n\t"
    "s_waitcnt lgkmcnt(6)\n\t"
    "s_waitcnt vmcnt(31)\n\t"
    "s_barrier\n\t"
    "v_mfma_f32_16x16x32_bf16 v[192:195], a[144:147], a[0:3], v[192:195]\n\t"
    "ds_read_b64_tr_b16 a[168:169], v226 offset:16384\n\t"
    "v_mfma_f32_16x16x32_bf16 v[192:195], a[148:151], a[4:7], v[192:195]\n\t"
    "v_mfma_f32_16x16x32_bf16 v[192:195], a[152:155], a[8:11], v[192:195]\n\t"
    "ds_read_b64_tr_b16 a[170:171], v226 offset:20480\n\t"
    "v_mfma_f32_16x16x32_bf16 v[196:199], a[152:155], a[20:23], v[196:199]\n\t"
    "v_mfma_f32_16x16x32_bf16 v[196:199], a[148:151], a[16:19], v[196:199]\n\t"
    "ds_read_b64_tr_b16 a[172:173], v226 offset:24576\n\t"
    "v_mfma_f32_16x16x32_bf16 v[196:199], a[144:147], a[12:15], v[196:199]\n\t"
    "v_mfma_f32_16x16x32_bf16 v[200:203], a[144:147], a[24:27], v[200:203]\n\t"
    "ds_read_b64_tr_b16 a[174:175], v226 offset:28672\n\t"
    "v_mfma_f32_16x16x32_bf16 v[200:203], a[148:151], a[28:31], v[200:203]\n\t"
    "v_mfma_f32_16x16x32_bf16 v[200:203], a[152:155], a[32:35], v[200:203]\n\t"
    "ds_read_b64_tr_b16 a[176:177], v226 offset:32768\n\t"
    "v_mfma_f32_16x16x32_bf16 v[204:207], a[152:155], a[44:47], v[204:207]\n\t"
    "v_mfma_f32_16x16x32_bf16 v[204:207], a[148:151], a[40:43], v[204:207]\n\t"
    "ds_read_b64_tr_b16 a[178:179], v226 offset:36864\n\t"
    "v_mfma_f32_16x16x32_bf16 v[204:207], a[144:147], a[36:39], v[204:207]\n\t"
    "s_waitcnt lgkmcnt(6)\n\t"
    "v_mfma_f32_16x16x32_bf16 v[208:211], a[156:159], a[0:3], v[208:211]\n\t"
    "ds_read_b64_tr_b16 a[144:145], v226 offset:16392\n\t"
    "v_mfma_f32_16x16x32_bf16 v[208:211], a[160:163], a[4:7], v[208:211]\n\t"
    "v_mfma_f32_16x16x32_bf16 v[208:211], a[164:167], a[8:11], v[208:211]\n\t"
    "ds_read_b64_tr_b16 a[146:147], v226 offset:20488\n\t"
    "v_mfma_f32_16x16x32_bf16 v[212:215], a[164:167], a[20:23], v[212:215]\n\t"
    "v_mfma_f32_16x16x32_bf16 v[212:215], a[160:163], a[16:19], v[212:215]\n\t"
    "ds_read_b64_tr_b16 a[148:149], v226 offset:24584\n\t"
    "v_mfma_f32_16x16x32_bf16 v[212:215], a[156:159], a[12:15], v[212:215]\n\t"
    "v_mfma_f32_16x16x32_bf16 v[216:219], a[156:159], a[24:27], v[216:219]\n\t"
    "ds_read_b64_tr_b16 a[150:151], v226 offset:28680\n\t"
    "v_mfma_f32_16x16x32_bf16 v[216:219], a[160:163], a[28:31], v[216:219]\n\t"
    "v_mfma_f32_16x16x32_bf16 v[216:219], a[164:167], a[32:35], v[216:219]\n\t"
    "ds_read_b64_tr_b16 a[152:153], v226 offset:32776\n\t"
    "v_mfma_f32_16x16x32_bf16 v[220:223], a[164:167], a[44:47], v[220:223]\n\t"
    "v_mfma_f32_16x16x32_bf16 v[220:223], a[160:163], a[40:43], v[220:223]\n\t"
    "ds_read_b64_tr_b16 a[154:155], v226 offset:36872\n\t"
    "v_mfma_f32_16x16x32_bf16 v[220:223], a[156:159], a[36:39], v[220:223]\n\t"
    "s_waitcnt vmcnt(19) lgkmcnt(6)\n\t"
    "v_mfma_f32_16x16x32_bf16 v[96:99], a[168:171], a[48:51], v[96:99]\n\t"
    "ds_read_b64_tr_b16 a[156:157], v226 offset:16896\n\t"
    "v_mfma_f32_16x16x32_bf16 v[96:99], a[172:175], a[52:55], v[96:99]\n\t"
    "s_add_u32 s41, %[kvs], 0\n\t"
    "s_add_u32 m0, %[m0b], 0\n\t"
    "v_mfma_f32_16x16x32_bf16 v[96:99], a[176:179], a[56:59], v[96:99]\n\t"
    "ds_read_b64_tr_b16 a[158:159], v226 offset:20992\n\t"
    "v_mfma_f32_16x16x32_bf16 v[100:103], a[176:179], a[68:71], v[100:103]\n\t"
    "buffer_load_dwordx4 %[kv0], %[srdk], s41 offen lds\n\t"
    "v_mfma_f32_16x16x32_bf16 v[100:103], a[172:175], a[64:67], v[100:103]\n\t"
    "ds_read_b64_tr_b16 a[160:161], v226 offset:25088\n\t"
    "v_mfma_f32_16x16x32_bf16 v[100:103], a[168:171], a[60:63], v[100:103]\n\t"
    "v_mfma_f32_16x16x32_bf16 v[104:107], a[168:171], a[72:75], v[104:107]\n\t"
    "ds_read_b64_tr_b16 a[162:163], v226 offset:29184\n\t"
    "v_mfma_f32_16x16x32_bf16 v[104:107], a[172:175], a[76:79], v[104:107]\n\t"
    "buffer_load_dwordx4 %[kv1], %[srdk], s41 offen offset:1024 lds\n\t"
    "v_mfma_f32_16x16x32_bf16 v[104:107], a[176:179], a[80:83], v[104:107]\n\t"
    "ds_read_b64_tr_b16 a[164:165], v226 offset:33280\n\t"
    "v_mfma_f32_16x16x32_bf16 v[108:111], a[176:179], a[92:95], v[108:111]\n\t"
    "v_mfma_f32_16x16x32_bf16 v[108:111], a[172:175], a[88:91], v[108:111]\n\t"
    "ds_read_b64_tr_b16 a[166:167], v226 offset:37376\n\t"
    "v_mfma_f32_16x16x32_bf16 v[108:111], a[168:171], a[84:87], v[108:111]\n\t"
    "buffer_load_dwordx4 %[kv2], %[srdk], s41 offen offset:2048 lds\n\t"
    "s_waitcnt lgkmcnt(6)\n\t"
    "v_mfma_f32_16x16x32_bf16 v[112:115], a[144:147], a[48:51], v[112:115]\n\t"
    "ds_read_b64_tr_b16 a[168:169], v226 offset:16904\n\t"
    "v_mfma_f32_16x16x32_bf16 v[112:115], a[148:151], a[52:55], v[112:115]\n\t"
    "v_mfma_f32_16x16x32_bf16 v[112:115], a[152:155], a[56:59], v[112:115]\n\t"
    "ds_read_b64_tr_b16 a[170:171], v226 offset:21000\n\t"
    "v_mfma_f32_16x16x32_bf16 v[116:119], a[152:155], a[68:71], v[116:119]\n\t"
    "buffer_load_dwordx4 %[kv3], %[srdk], s41 offen offset:3072 lds\n\t"
    "v_mfma_f32_16x16x32_bf16 v[116:119], a[148:151], a[64:67], v[116:119]\n\t"
    "ds_read_b64_tr_b16 a[172:173], v226 offset:25096\n\t"
    "v_mfma_f32_16x16x32_bf16 v[116:119], a[144:147], a[60:63], v[116:119]\n\t"
    "s_add_u32 s41, %[kvs], 16384\n\t"
    "s_add_u32 m0, %[m0b], 16384\n\t"
    "v_mfma_f32_16x16x32_bf16 v[120:123], a[144:147], a[72:75], v[120:123]\n\t"
    "ds_read_b64_tr_b16 a[174:175], v226 offset:29192\n\t"
    "v_mfma_f32_16x16x32_bf16 v[120:123], a[148:151], a[76:79], v[120:123]\n\t"
    "buffer_load_dwordx4 %[kv0], %[srdk], s41 offen lds\n\t"
    "v_mfma_f32_16x16x32_bf16 v[120:123], a[152:155], a[80:83], v[120:123]\n\t"
    "ds_read_b64_tr_b16 a[176:177], v226 offset:33288\n\t"
    "v_mfma_f32_16x16x32_bf16 v[124:127], a[152:155], a[92:95], v[124:127]\n\t"
    "v_mfma_f32_16x16x32_bf16 v[124:127], a[148:151], a[88:91], v[124:127]\n\t"
    "ds_read_b64_tr_b16 a[178:179], v226 offset:37384\n\t"
    "v_mfma_f32_16x16x32_bf16 v[124:127], a[144:147], a[84:87], v[124:127]\n\t"
    "buffer_load_dwordx4 %[kv1], %[srdk], s41 offen offset:1024 lds\n\t"
    "s_waitcnt lgkmcnt(6)\n\t"
    "v_mfma_f32_16x16x32_bf16 v[128:131], a[156:159], a[48:51], v[128:131]\n\t"
    "ds_read_b64_tr_b16 a[144:145], v226 offset:17408\n\t"
    "v_mfma_f32_16x16x32_bf16 v[128:131], a[160:163], a[52:55], v[128:131]\n\t"
    "v_mfma_f32_16x16x32_bf16 v[128:131], a[164:167], a[56:59], v[128:131]\n\t"
    "ds_read_b64_tr_b16 a[146:147], v226 offset:21504\n\t"
    "v_mfma_f32_16x16x32_bf16 v[132:135], a[164:167], a[68:71], v[132:135]\n\t"
    "buffer_load_dwordx4 %[kv2], %[srdk], s41 offen offset:2048 lds\n\t"
    "v_mfma_f32_16x16x32_bf16 v[132:135], a[160:163], a[64:67], v[132:135]\n\t"
    "ds_read_b64_tr_b16 a[148:149], v226 offset:25600\n\t"
    "v_mfma_f32_16x16x32_bf16 v[132:135], a[156:159], a[60:63], v[132:135]\n\t"
    "v_mfma_f32_16x16x32_bf16 v[136:139], a[156:159], a[72:75], v[136:139]\n\t"
    "ds_read_b64_tr_b16 a[150:151], v226 offset:29696\n\t"
    "v_mfma_f32_16x16x32_bf16 v[136:139], a[160:163], a[76:79], v[136:139]\n\t"
    "buffer_load_dwordx4 %[kv3], %[srdk], s41 offen offset:3072 lds\n\t"
    "v_mfma_f32_16x16x32_bf16 v[136:139], a[164:167], a[80:83], v[136:139]\n\t"
    "ds_read_b64_tr_b16 a[152:153], v226 offset:33792\n\t"
    "v_mfma_f32_16x16x32_bf16 v[140:143], a[164:167], a[92:95], v[140:143]\n\t"
    "s_add_u32 s41, %[kvs], 0\n\t"
    "s_add_u32 m0, %[m0b], 65536\n\t"
    "v_mfma_f32_16x16x32_bf16 v[140:143], a[160:163], a[88:91], v[140:143]\n\t"
    "ds_read_b64_tr_b16 a[154:155], v226 offset:37888\n\t"
    "v_mfma_f32_16x16x32_bf16 v[140:143], a[156:159], a[84:87], v[140:143]\n\t"
    "buffer_load_dwordx4 %[vv0], %[srdv], s41 offen lds\n\t"
    "s_waitcnt lgkmcnt(6)\n\t"
    "v_mfma_f32_16x16x32_bf16 v[144:147], a[168:171], a[48:51], v[144:147]\n\t"
    "ds_read_b64_tr_b16 a[156:157], v226 offset:17416\n\t"
    "v_mfma_f32_16x16x32_bf16 v[144:147], a[172:175], a[52:55], v[144:147]\n\t"
    "v_mfma_f32_16x16x32_bf16 v[144:147], a[176:179], a[56:59], v[144:147]\n\t"
    "ds_read_b64_tr_b16 a[158:159], v226 offset:21512\n\t"
    "v_mfma_f32_16x16x32_bf16 v[148:151], a[176:179], a[68:71], v[148:151]\n\t"
    "buffer_load_dwordx4 %[vv1], %[srdv], s41 offen offset:1024 lds\n\t"
    "v_mfma_f32_16x16x32_bf16 v[148:151], a[172:175], a[64:67], v[148:151]\n\t"
    "ds_read_b64_tr_b16 a[160:161], v226 offset:25608\n\t"
    "v_mfma_f32_16x16x32_bf16 v[148:151], a[168:171], a[60:63], v[148:151]\n\t"
    "v_mfma_f32_16x16x32_bf16 v[152:155], a[168:171], a[72:75], v[152:155]\n\t"
    "ds_read_b64_tr_b16 a[162:163], v226 offset:29704\n\t"
    "v_mfma_f32_16x16x32_bf16 v[152:155], a[172:175], a[76:79], v[152:155]\n\t"
    "buffer_load_dwordx4 %[vv0], %[srdv], s41 offen offset:2048 lds\n\t"
    "v_mfma_f32_16x16x32_bf16 v[152:155], a[176:179], a[80:83], v[152:155]\n\t"
    "ds_read_b64_tr_b16 a[164:165], v226 offset:33800\n\t"
    "v_mfma_f32_16x16x32_bf16 v[156:159], a[176:179], a[92:95], v[156:159]\n\t"
    "v_mfma_f32_16x16x32_bf16 v[156:159], a[172:175], a[88:91], v[156:159]\n\t"
    "ds_read_b64_tr_b16 a[166:167], v226 offset:37896\n\t"
    "v_mfma_f32_16x16x32_bf16 v[156:159], a[168:171], a[84:87], v[156:159]\n\t"
    "buffer_load_dwordx4 %[vv1], %[srdv], s41 offen offset:3072 lds\n\t"
    "s_waitcnt lgkmcnt(6)\n\t"
    "v_mfma_f32_16x16x32_bf16 v[160:163], a[144:147], a[48:51], v[160:163]\n\t"
    "ds_read_b64_tr_b16 a[168:169], v226 offset:17920\n\t"
    "v_mfma_f32_16x16x32_bf16 v[160:163], a[148:151], a[52:55], v[160:163]\n\t"
    "s_add_u32 s41, %[kvs], 32768\n\t"
    "s_add_u32 m0, %[m0b], 32768\n\t"
    "v_mfma_f32_16x16x32_bf16 v[160:163], a[152:155], a[56:59], v[160:163]\n\t"
    "ds_read_b64_tr_b16 a[170:171], v226 offset:22016\n\t"
    "v_mfma_f32_16x16x32_bf16 v[164:167], a[152:155], a[68:71], v[164:167]\n\t"
    "buffer_load_dwordx4 %[kv0], %[srdk], s41 offen lds\n\t"
    "v_mfma_f32_16x16x32_bf16 v[164:167], a[148:151], a[64:67], v[164:167]\n\t"
    "ds_read_b64_tr_b16 a[172:173], v226 offset:26112\n\t"
    "v_mfma_f32_16x16x32_bf16 v[164:167], a[144:147], a[60:63], v[164:167]\n\t"
    "v_mfma_f32_16x16x32_bf16 v[168:171], a[144:147], a[72:75], v[168:171]\n\t"
    "ds_read_b64_tr_b16 a[174:175], v226 offset:30208\n\t"
    "v_mfma_f32_16x16x32_bf16 v[168:171], a[148:151], a[76:79], v[168:171]\n\t"
    "buffer_load_dwordx4 %[kv1], %[srdk], s41 offen offset:1024 lds\n\t"
    "v_mfma_f32_16x16x32_bf16 v[168:171], a[152:155], a[80:83], v[168:171]\n\t"
    "ds_read_b64_tr_b16 a[176:177], v226 offset:34304\n\t"
    "v_mfma_f32_16x16x32_bf16 v[172:175], a[152:155], a[92:95], v[172:175]\n\t"
    "v_mfma_f32_16x16x32_bf16 v[172:175], a[148:151], a[88:91], v[172:175]\n\t"
    "ds_read_b64_tr_b16 a[178:179], v226 offset:38400\n\t"
    "v_mfma_f32_16x16x32_bf16 v[172:175], a[144:147], a[84:87], v[172:175]\n\t"
    "buffer_load_dwordx4 %[kv2], %[srdk], s41 offen offset:2048 lds\n\t"
    "s_waitcnt lgkmcnt(6)\n\t"
    "v_mfma_f32_16x16x32_bf16 v[176:179], a[156:159], a[48:51], v[176:179]\n\t"
    "ds_read_b64_tr_b16 a[144:145], v226 offset:17928\n\t"
    "v_mfma_f32_16x16x32_bf16 v[176:179], a[160:163], a[52:55], v[176:179]\n\t"
    "v_mfma_f32_16x16x32_bf16 v[176:179], a[164:167], a[56:59], v[176:179]\n\t"
    "ds_read_b64_tr_b16 a[146:147], v226 offset:22024\n\t"
    "v_mfma_f32_16x16x32_bf16 v[180:183], a[164:167], a[68:71], v[180:183]\n\t"
    "buffer_load_dwordx4 %[kv3], %[srdk], s41 offen offset:3072 lds\n\t"
    "v_mfma_f32_16x16x32_bf16 v[180:183], a[160:163], a[64:67], v[180:183]\n\t"
    "ds_read_b64_tr_b16 a[148:149], v226 offset:26120\n\t"
    "v_mfma_f32_16x16x32_bf16 v[180:183], a[156:159], a[60:63], v[180:183]\n\t"
    "v_mfma_f32_16x16x32_bf16 v[184:187], a[156:159], a[72:75], v[184:187]\n\t"
    "ds_read_b64_tr_b16 a[150:151], v226 offset:30216\n\t"
    "v_mfma_f32_16x16x32_bf16 v[184:187], a[160:163], a[76:79], v[184:187]\n\t"
    "v_mfma_f32_16x16x32_bf16 v[184:187], a[164:167], a[80:83], v[184:187]\n\t"
    "ds_read_b64_tr_b16 a[152:153], v226 offset:34312\n\t"
    "v_mfma_f32_16x16x32_bf16 v[188:191], a[164:167], a[92:95], v[188:191]\n\t"
    "v_mfma_f32_16x16x32_bf16 v[188:191], a[160:163], a[88:91], v[188:191]\n\t"
    "ds_read_b64_tr_b16 a[154:155], v226 offset:38408\n\t"
    "v_mfma_f32_16x16x32_bf16 v[188:191], a[156:159], a[84:87], v[188:191]\n\t"
    "s_waitcnt lgkmcnt(6)\n\t"
    "s_waitcnt vmcnt(28)\n\t"
    "s_barrier\n\t"
    "v_mfma_f32_16x16x32_bf16 v[192:195], a[168:171], a[48:51], v[192:195]\n\t"
    "ds_read_b64_tr_b16 a[156:157], v226 offset:40960\n\t"
    "v_mfma_f32_16x16x32_bf16 v[192:195], a[172:175], a[52:55], v[192:195]\n\t"
    "v_mfma_f32_16x16x32_bf16 v[192:195], a[176:179], a[56:59], v[192:195]\n\t"
    "ds_read_b64_tr_b16 a[158:159], v226 offset:45056\n\t"
    "v_mfma_f32_16x16x32_bf16 v[196:199], a[176:179], a[68:71], v[196:199]\n\t"
    "v_mfma_f32_16x16x32_bf16 v[196:199], a[172:175], a[64:67], v[196:199]\n\t"
    "ds_read_b64_tr_b16 a[160:161], v226 offset:49152\n\t"
    "v_mfma_f32_16x16x32_bf16 v[196:199], a[168:171], a[60:63], v[196:199]\n\t"
    "v_mfma_f32_16x16x32_bf16 v[200:203], a[168:171], a[72:75], v[200:203]\n\t"
    "ds_read_b64_tr_b16 a[162:163], v226 offset:53248\n\t"
    "v_mfma_f32_16x16x32_bf16 v[200:203], a[172:175], a[76:79], v[200:203]\n\t"
    "v_mfma_f32_16x16x32_bf16 v[200:203], a[176:179], a[80:83], v[200:203]\n\t"
    "ds_read_b64_tr_b16 a[164:165], v226 offset:57344\n\t"
    "v_mfma_f32_16x16x32_bf16 v[204:207], a[176:179], a[92:95], v[204:207]\n\t"
    "v_mfma_f32_16x16x32_bf16 v[204:207], a[172:175], a[88:91], v[204:207]\n\t"
    "ds_read_b64_tr_b16 a[166:167], v226 offset:61440\n\t"
    "v_mfma_f32_16x16x32_bf16 v[204:207], a[168:171], a[84:87], v[204:207]\n\t"
    "s_waitcnt lgkmcnt(6)\n\t"
    "v_mfma_f32_16x16x32_bf16 v[208:211], a[144:147], a[48:51], v[208:211]\n\t"
    "ds_read_b64_tr_b16 a[168:169], v226 offset:40968\n\t"
    "v_mfma_f32_16x16x32_bf16 v[208:211], a[148:151], a[52:55], v[208:211]\n\t"
    "v_mfma_f32_16x16x32_bf16 v[208:211], a[152:155], a[56:59], v[208:211]\n\t"
    "ds_read_b64_tr_b16 a[170:171], v226 offset:45064\n\t"
    "v_mfma_f32_16x16x32_bf16 v[212:215], a[152:155], a[68:71], v[212:215]\n\t"
    "v_mfma_f32_16x16x32_bf16 v[212:215], a[148:151], a[64:67], v[212:215]\n\t"
    "ds_read_b64_tr_b16 a[172:173], v226 offset:49160\n\t"
    "v_mfma_f32_16x16x32_bf16 v[212:215], a[144:147], a[60:63], v[212:215]\n\t"
    "v_mfma_f32_16x16x32_bf16 v[216:219], a[144:147], a[72:75], v[216:219]\n\t"
    "ds_read_b64_tr_b16 a[174:175], v226 offset:53256\n\t"
    "v_mfma_f32_16x16x32_bf16 v[216:219], a[148:151], a[76:79], v[216:219]\n\t"
    "v_mfma_f32_16x16x32_bf16 v[216:219], a[152:155], a[80:83], v[216:219]\n\t"
    "ds_read_b64_tr_b16 a[176:177], v226 offset:57352\n\t"
    "v_mfma_f32_16x16x32_bf16 v[220:223], a[152:155], a[92:95], v[220:223]\n\t"
    "v_mfma_f32_16x16x32_bf16 v[220:223], a[148:151], a[88:91], v[220:223]\n\t"
    "ds_read_b64_tr_b16 a[178:179], v226 offset:61448\n\t"
    "v_mfma_f32_16x16x32_bf16 v[220:223], a[144:147], a[84:87], v[220:223]\n\t"
    "s_waitcnt vmcnt(16) lgkmcnt(6)\n\t"
    "v_mfma_f32_16x16x32_bf16 v[96:99], a[156:159], a[96:99], v[96:99]\n\t"
    "ds_read_b64_tr_b16 a[144:145], v226 offset:41472\n\t"
    "v_mfma_f32_16x16x32_bf16 v[96:99], a[160:163], a[100:103], v[96:99]\n\t"
    "s_add_u32 s41, %[kvs], 16384\n\t"
    "s_add_u32 m0, %[m0b], 81920\n\t"
    "v_mfma_f32_16x16x32_bf16 v[96:99], a[164:167], a[104:107], v[96:99]\n\t"
    "ds_read_b64_tr_b16 a[146:147], v226 offset:45568\n\t"
    "v_mfma_f32_16x16x32_bf16 v[100:103], a[164:167], a[116:119], v[100:103]\n\t"
    "buffer_load_dwordx4 %[vv0], %[srdv], s41 offen lds\n\t"
    "v_mfma_f32_16x16x32_bf16 v[100:103], a[160:163], a[112:115], v[100:103]\n\t"
    "ds_read_b64_tr_b16 a[148:149], v226 offset:49664\n\t"
    "v_mfma_f32_16x16x32_bf16 v[100:103], a[156:159], a[108:111], v[100:103]\n\t"
    "v_mfma_f32_16x16x32_bf16 v[104:107], a[156:159], a[120:123], v[104:107]\n\t"
    "ds_read_b64_tr_b16 a[150:151], v226 offset:53760\n\t"
    "v_mfma_f32_16x16x32_bf16 v[104:107], a[160:163], a[124:127], v[104:107]\n\t"
    "buffer_load_dwordx4 %[vv1], %[srdv], s41 offen offset:1024 lds\n\t"
    "v_mfma_f32_16x16x32_bf16 v[104:107], a[164:167], a[128:131], v[104:107]\n\t"
    "ds_read_b64_tr_b16 a[152:153], v226 offset:57856\n\t"
    "v_mfma_f32_16x16x32_bf16 v[108:111], a[164:167], a[140:143], v[108:111]\n\t"
    "v_mfma_f32_16x16x32_bf16 v[108:111], a[160:163], a[136:139], v[108:111]\n\t"
    "ds_read_b64_tr_b16 a[154:155], v226 offset:61952\n\t"
    "v_mfma_f32_16x16x32_bf16 v[108:111], a[156:159], a[132:135], v[108:111]\n\t"
    "buffer_load_dwordx4 %[vv0], %[srdv], s41 offen offset:2048 lds\n\t"
    "s_waitcnt lgkmcnt(6)\n\t"
    "v_mfma_f32_16x16x32_bf16 v[112:115], a[168:171], a[96:99], v[112:115]\n\t"
    "ds_read_b64_tr_b16 a[156:157], v226 offset:41480\n\t"
    "v_mfma_f32_16x16x32_bf16 v[112:115], a[172:175], a[100:103], v[112:115]\n\t"
    "v_mfma_f32_16x16x32_bf16 v[112:115], a[176:179], a[104:107], v[112:115]\n\t"
    "ds_read_b64_tr_b16 a[158:159], v226 offset:45576\n\t"
    "v_mfma_f32_16x16x32_bf16 v[116:119], a[176:179], a[116:119], v[116:119]\n\t"
    "buffer_load_dwordx4 %[vv1], %[srdv], s41 offen offset:3072 lds\n\t"
    "v_cvt_pk_bf16_f32 v32, v96, v97\n\t"
    "v_mfma_f32_16x16x32_bf16 v[116:119], a[172:175], a[112:115], v[116:119]\n\t"
    "ds_read_b64_tr_b16 a[160:161], v226 offset:49672\n\t"
    "v_cvt_pk_bf16_f32 v33, v98, v99\n\t"
    "v_mfma_f32_16x16x32_bf16 v[116:119], a[168:171], a[108:111], v[116:119]\n\t"
    "s_add_u32 s41, %[kvs], 49152\n\t"
    "s_add_u32 m0, %[m0b], 49152\n\t"
    "v_cvt_pk_bf16_f32 v48, v100, v101\n\t"
    "v_mfma_f32_16x16x32_bf16 v[120:123], a[168:171], a[120:123], v[120:123]\n\t"
    "ds_read_b64_tr_b16 a[162:163], v226 offset:53768\n\t"
    "v_cvt_pk_bf16_f32 v49, v102, v103\n\t"
    "v_mfma_f32_16x16x32_bf16 v[120:123], a[172:175], a[124:127], v[120:123]\n\t"
    "buffer_load_dwordx4 %[kv0], %[srdk], s41 offen lds\n\t"
    "v_cvt_pk_bf16_f32 v64, v104, v105\n\t"
    "v_mfma_f32_16x16x32_bf16 v[120:123], a[176:179], a[128:131], v[120:123]\n\t"
    "ds_read_b64_tr_b16 a[164:165], v226 offset:57864\n\t"
    "v_cvt_pk_bf16_f32 v65, v106, v107\n\t"
    "v_mfma_f32_16x16x32_bf16 v[124:127], a[176:179], a[140:143], v[124:127]\n\t"
    "v_cvt_pk_bf16_f32 v80, v108, v109\n\t"
    "v_mfma_f32_16x16x32_bf16 v[124:127], a[172:175], a[136:139], v[124:127]\n\t"
    "ds_read_b64_tr_b16 a[166:167], v226 offset:61960\n\t"
    "v_cvt_pk_bf16_f32 v81, v110, v111\n\t"
    "v_mfma_f32_16x16x32_bf16 v[124:127], a[168:171], a[132:135], v[124:127]\n\t"
    "buffer_load_dwordx4 %[kv1], %[srdk], s41 offen offset:1024 lds\n\t"
    "s_waitcnt lgkmcnt(6)\n\t"
    "v_mfma_f32_16x16x32_bf16 v[128:131], a[144:147], a[96:99], v[128:131]\n\t"
    "ds_read_b64_tr_b16 a[168:169], v226 offset:41984\n\t"
    "v_mfma_f32_16x16x32_bf16 v[128:131], a[148:151], a[100:103], v[128:131]\n\t"
    "v_mfma_f32_16x16x32_bf16 v[128:131], a[152:155], a[104:107], v[128:131]\n\t"
    "ds_read_b64_tr_b16 a[170:171], v226 offset:46080\n\t"
    "v_mfma_f32_16x16x32_bf16 v[132:135], a[152:155], a[116:119], v[132:135]\n\t"
    "buffer_load_dwordx4 %[kv2], %[srdk], s41 offen offset:2048 lds\n\t"
    "v_cvt_pk_bf16_f32 v34, v112, v113\n\t"
    "v_mfma_f32_16x16x32_bf16 v[132:135], a[148:151], a[112:115], v[132:135]\n\t"
    "ds_read_b64_tr_b16 a[172:173], v226 offset:50176\n\t"
    "v_cvt_pk_bf16_f32 v35, v114, v115\n\t"
    "v_mfma_f32_16x16x32_bf16 v[132:135], a[144:147], a[108:111], v[132:135]\n\t"
    "v_cvt_pk_bf16_f32 v50, v116, v117\n\t"
    "v_mfma_f32_16x16x32_bf16 v[136:139], a[144:147], a[120:123], v[136:139]\n\t"
    "ds_read_b64_tr_b16 a[174:175], v226 offset:54272\n\t"
    "v_cvt_pk_bf16_f32 v51, v118, v119\n\t"
    "v_mfma_f32_16x16x32_bf16 v[136:139], a[148:151], a[124:127], v[136:139]\n\t"
    "buffer_load_dwordx4 %[kv3], %[srdk], s41 offen offset:3072 lds\n\t"
    "v_cvt_pk_bf16_f32 v66, v120, v121\n\t"
    "v_mfma_f32_16x16x32_bf16 v[136:139], a[152:155], a[128:131], v[136:139]\n\t"
    "ds_read_b64_tr_b16 a[176:177], v226 offset:58368\n\t"
    "v_cvt_pk_bf16_f32 v67, v122, v123\n\t"
    "v_mfma_f32_16x16x32_bf16 v[140:143], a[152:155], a[140:143], v[140:143]\n\t"
    "s_add_u32 s41, %[kvs], 32768\n\t"
    "s_add_u32 m0, %[m0b], 98304\n\t"
    "v_cvt_pk_bf16_f32 v82, v124, v125\n\t"
    "v_mfma_f32_16x16x32_bf16 v[140:143], a[148:151], a[136:139], v[140:143]\n\t"
    "ds_read_b64_tr_b16 a[178:179], v226 offset:62464\n\t"
    "v_cvt_pk_bf16_f32 v83, v126, v127\n\t"
    "v_mfma_f32_16x16x32_bf16 v[140:143], a[144:147], a[132:135], v[140:143]\n\t"
    "buffer_load_dwordx4 %[vv0], %[srdv], s41 offen lds\n\t"
    "s_waitcnt lgkmcnt(6)\n\t"
    "v_mfma_f32_16x16x32_bf16 v[144:147], a[156:159], a[96:99], v[144:147]\n\t"
    "ds_read_b64_tr_b16 a[144:145], v226 offset:41992\n\t"
    "v_mfma_f32_16x16x32_bf16 v[144:147], a[160:163], a[100:103], v[144:147]\n\t"
    "v_mfma_f32_16x16x32_bf16 v[144:147], a[164:167], a[104:107], v[144:147]\n\t"
    "ds_read_b64_tr_b16 a[146:147], v226 offset:46088\n\t"
    "v_mfma_f32_16x16x32_bf16 v[148:151], a[164:167], a[116:119], v[148:151]\n\t"
    "buffer_load_dwordx4 %[vv1], %[srdv], s41 offen offset:1024 lds\n\t"
    "v_cvt_pk_bf16_f32 v36, v128, v129\n\t"
    "v_mfma_f32_16x16x32_bf16 v[148:151], a[160:163], a[112:115], v[148:151]\n\t"
    "ds_read_b64_tr_b16 a[148:149], v226 offset:50184\n\t"
    "v_cvt_pk_bf16_f32 v37, v130, v131\n\t"
    "v_mfma_f32_16x16x32_bf16 v[148:151], a[156:159], a[108:111], v[148:151]\n\t"
    "v_cvt_pk_bf16_f32 v52, v132, v133\n\t"
    "v_mfma_f32_16x16x32_bf16 v[152:155], a[156:159], a[120:123], v[152:155]\n\t"
    "ds_read_b64_tr_b16 a[150:151], v226 offset:54280\n\t"
    "v_cvt_pk_bf16_f32 v53, v134, v135\n\t"
    "v_mfma_f32_16x16x32_bf16 v[152:155], a[160:163], a[124:127], v[152:155]\n\t"
    "buffer_load_dwordx4 %[vv0], %[srdv], s41 offen offset:2048 lds\n\t"
    "v_cvt_pk_bf16_f32 v68, v136, v137\n\t"
    "v_mfma_f32_16x16x32_bf16 v[152:155], a[164:167], a[128:131], v[152:155]\n\t"
    "ds_read_b64_tr_b16 a[152:153], v226 offset:58376\n\t"
    "v_cvt_pk_bf16_f32 v69, v138, v139\n\t"
    "v_mfma_f32_16x16x32_bf16 v[156:159], a[164:167], a[140:143], v[156:159]\n\t"
    "v_cvt_pk_bf16_f32 v84, v140, v141\n\t"
    "v_mfma_f32_16x16x32_bf16 v[156:159], a[160:163], a[136:139], v[156:159]\n\t"
    "ds_read_b64_tr_b16 a[154:155], v226 offset:62472\n\t"
    "v_cvt_pk_bf16_f32 v85, v142, v143\n\t"
    "v_mfma_f32_16x16x32_bf16 v[156:159], a[156:159], a[132:135], v[156:159]\n\t"
    "buffer_load_dwordx4 %[vv1], %[srdv], s41 offen offset:3072 lds\n\t"
    "s_waitcnt lgkmcnt(6)\n\t"
    "v_mfma_f32_16x16x32_bf16 v[160:163], a[168:171], a[96:99], v[160:163]\n\t"
    "ds_read_b64_tr_b16 a[156:157], v226 offset:42496\n\t"
    "v_mfma_f32_16x16x32_bf16 v[160:163], a[172:175], a[100:103], v[160:163]\n\t"
    "v_mfma_f32_16x16x32_bf16 v[160:163], a[176:179], a[104:107], v[160:163]\n\t"
    "ds_read_b64_tr_b16 a[158:159], v226 offset:46592\n\t"
    "v_mfma_f32_16x16x32_bf16 v[164:167], a[176:179], a[116:119], v[164:167]\n\t"
    "v_cvt_pk_bf16_f32 v38, v144, v145\n\t"
    "v_mfma_f32_16x16x32_bf16 v[164:167], a[172:175], a[112:115], v[164:167]\n\t"
    "ds_read_b64_tr_b16 a[160:161], v226 offset:50688\n\t"
    "v_cvt_pk_bf16_f32 v39, v146, v147\n\t"
    "v_mfma_f32_16x16x32_bf16 v[164:167], a[168:171], a[108:111], v[164:167]\n\t"
    "v_cvt_pk_bf16_f32 v54, v148, v149\n\t"
    "v_mfma_f32_16x16x32_bf16 v[168:171], a[168:171], a[120:123], v[168:171]\n\t"
    "ds_read_b64_tr_b16 a[162:163], v226 offset:54784\n\t"
    "v_cvt_pk_bf16_f32 v55, v150, v151\n\t"
    "v_mfma_f32_16x16x32_bf16 v[168:171], a[172:175], a[124:127], v[168:171]\n\t"
    "v_cvt_pk_bf16_f32 v70, v152, v153\n\t"
    "v_mfma_f32_16x16x32_bf16 v[168:171], a[176:179], a[128:131], v[168:171]\n\t"
    "ds_read_b64_tr_b16 a[164:165], v226 offset:58880\n\t"
    "v_cvt_pk_bf16_f32 v71, v154, v155\n\t"
    "v_mfma_f32_16x16x32_bf16 v[172:175], a[176:179], a[140:143], v[172:175]\n\t"
    "v_cvt_pk_bf16_f32 v86, v156, v157\n\t"
    "v_mfma_f32_16x16x32_bf16 v[172:175], a[172:175], a[136:139], v[172:175]\n\t"
    "ds_read_b64_tr_b16 a[166:167], v226 offset:62976\n\t"
    "v_cvt_pk_bf16_f32 v87, v158, v159\n\t"
    "v_mfma_f32_16x16x32_bf16 v[172:175], a[168:171], a[132:135], v[172:175]\n\t"
    "s_waitcnt lgkmcnt(6)\n\t"
    "v_mfma_f32_16x16x32_bf16 v[176:179], a[144:147], a[96:99], v[176:179]\n\t"
    "ds_read_b64_tr_b16 a[168:169], v226 offset:42504\n\t"
    "v_mfma_f32_16x16x32_bf16 v[176:179], a[148:151], a[100:103], v[176:179]\n\t"
    "v_mfma_f32_16x16x32_bf16 v[176:179], a[152:155], a[104:107], v[176:179]\n\t"
    "ds_read_b64_tr_b16 a[170:171], v226 offset:46600\n\t"
    "v_mfma_f32_16x16x32_bf16 v[180:183], a[152:155], a[116:119], v[180:183]\n\t"
    "v_cvt_pk_bf16_f32 v40, v160, v161\n\t"
    "v_mfma_f32_16x16x32_bf16 v[180:183], a[148:151], a[112:115], v[180:183]\n\t"
    "ds_read_b64_tr_b16 a[172:173], v226 offset:50696\n\t"
    "v_cvt_pk_bf16_f32 v41, v162, v163\n\t"
    "v_mfma_f32_16x16x32_bf16 v[180:183], a[144:147], a[108:111], v[180:183]\n\t"
    "v_cvt_pk_bf16_f32 v56, v164, v165\n\t"
    "v_mfma_f32_16x16x32_bf16 v[184:187], a[144:147], a[120:123], v[184:187]\n\t"
    "ds_read_b64_tr_b16 a[174:175], v226 offset:54792\n\t"
    "v_cvt_pk_bf16_f32 v57, v166, v167\n\t"
    "v_mfma_f32_16x16x32_bf16 v[184:187], a[148:151], a[124:127], v[184:187]\n\t"
    "v_cvt_pk_bf16_f32 v72, v168, v169\n\t"
    "v_mfma_f32_16x16x32_bf16 v[184:187], a[152:155], a[128:131], v[184:187]\n\t"
    "ds_read_b64_tr_b16 a[176:177], v226 offset:58888\n\t"
    "v_cvt_pk_bf16_f32 v73, v170, v171\n\t"
    "v_mfma_f32_16x16x32_bf16 v[188:191], a[152:155], a[140:143], v[188:191]\n\t"
    "v_cvt_pk_bf16_f32 v88, v172, v173\n\t"
    "v_mfma_f32_16x16x32_bf16 v[188:191], a[148:151], a[136:139], v[188:191]\n\t"
    "ds_read_b64_tr_b16 a[178:179], v226 offset:62984\n\t"
    "v_cvt_pk_bf16_f32 v89, v174, v175\n\t"
    "v_mfma_f32_16x16x32_bf16 v[188:191], a[144:147], a[132:135], v[188:191]\n\t"
    "s_waitcnt lgkmcnt(6)\n\t"
    "s_waitcnt vmcnt(24)\n\t"
    "s_barrier\n\t"
    "v_mfma_f32_16x16x32_bf16 v[192:195], a[156:159], a[96:99], v[192:195]\n\t"
    "ds_read_b128 a[192:195], %[kb0] offset:0\n\t"
    "v_mfma_f32_16x16x32_bf16 v[192:195], a[160:163], a[100:103], v[192:195]\n\t"
    "v_mfma_f32_16x16x32_bf16 v[192:195], a[164:167], a[104:107], v[192:195]\n\t"
    "ds_read_b128 a[196:199], %[kb1] offset:0\n\t"
    "v_mfma_f32_16x16x32_bf16 v[196:199], a[164:167], a[116:119], v[196:199]\n\t"
    "v_cvt_pk_bf16_f32 v42, v176, v177\n\t"
    "v_mfma_f32_16x16x32_bf16 v[196:199], a[160:163], a[112:115], v[196:199]\n\t"
    "ds_read_b128 a[200:203], %[kb2] offset:0\n\t"
    "v_cvt_pk_bf16_f32 v43, v178, v179\n\t"
    "v_mfma_f32_16x16x32_bf16 v[196:199], a[156:159], a[108:111], v[196:199]\n\t"
    "v_cvt_pk_bf16_f32 v58, v180, v181\n\t"
    "v_mfma_f32_16x16x32_bf16 v[200:203], a[156:159], a[120:123], v[200:203]\n\t"
    "ds_read_b128 a[204:207], %[kb3] offset:0\n\t"
    "v_cvt_pk_bf16_f32 v59, v182, v183\n\t"
    "v_mfma_f32_16x16x32_bf16 v[200:203], a[160:163], a[124:127], v[200:203]\n\t"
    "v_cvt_pk_bf16_f32 v74, v184, v185\n\t"
    "v_mfma_f32_16x16x32_bf16 v[200:203], a[164:167], a[128:131], v[200:203]\n\t"
    "v_cvt_pk_bf16_f32 v75, v186, v187\n\t"
    "v_mfma_f32_16x16x32_bf16 v[204:207], a[164:167], a[140:143], v[204:207]\n\t"
    "v_cvt_pk_bf16_f32 v90, v188, v189\n\t"
    "v_mfma_f32_16x16x32_bf16 v[204:207], a[160:163], a[136:139], v[204:207]\n\t"
    "v_cvt_pk_bf16_f32 v91, v190, v191\n\t"
    "v_mfma_f32_16x16x32_bf16 v[204:207], a[156:159], a[132:135], v[204:207]\n\t"
    "s_waitcnt lgkmcnt(4)\n\t"
    "v_mfma_f32_16x16x32_bf16 v[208:211], a[168:171], a[96:99], v[208:211]\n\t"
    "v_mfma_f32_16x16x32_bf16 v[208:211], a[172:175], a[100:103], v[208:211]\n\t"
    "v_mfma_f32_16x16x32_bf16 v[208:211], a[176:179], a[104:107], v[208:211]\n\t"
    "v_mfma_f32_16x16x32_bf16 v[212:215], a[176:179], a[116:119], v[212:215]\n\t"
    "v_cvt_pk_bf16_f32 v44, v192, v193\n\t"
    "v_mfma_f32_16x16x32_bf16 v[212:215], a[172:175], a[112:115], v[212:215]\n\t"
    "v_cvt_pk_bf16_f32 v45, v194, v195\n\t"
    "v_mfma_f32_16x16x32_bf16 v[212:215], a[168:171], a[108:111], v[212:215]\n\t"
    "v_cvt_pk_bf16_f32 v60, v196, v197\n\t"
    "v_mfma_f32_16x16x32_bf16 v[216:219], a[168:171], a[120:123], v[216:219]\n\t"
    "v_cvt_pk_bf16_f32 v61, v198, v199\n\t"
    "v_mfma_f32_16x16x32_bf16 v[216:219], a[172:175], a[124:127], v[216:219]\n\t"
    "v_cvt_pk_bf16_f32 v76, v200, v201\n\t"
    "v_mfma_f32_16x16x32_bf16 v[216:219], a[176:179], a[128:131], v[216:219]\n\t"
    "v_cvt_pk_bf16_f32 v77, v202, v203\n\t"
    "v_mfma_f32_16x16x32_bf16 v[220:223], a[176:179], a[140:143], v[220:223]\n\t"
    "v_cvt_pk_bf16_f32 v92, v204, v205\n\t"
    "v_mfma_f32_16x16x32_bf16 v[220:223], a[172:175], a[136:139], v[220:223]\n\t"
    "v_cvt_pk_bf16_f32 v93, v206, v207\n\t"
    "v_mfma_f32_16x16x32_bf16 v[220:223], a[168:171], a[132:135], v[220:223]\n\t"
    "v_cvt_pk_bf16_f32 v46, v208, v209\n\t"
    "v_cvt_pk_bf16_f32 v47, v210, v211\n\t"
    "v_cvt_pk_bf16_f32 v62, v212, v213\n\t"
    "v_cvt_pk_bf16_f32 v63, v214, v215\n\t"
    "v_cvt_pk_bf16_f32 v78, v216, v217\n\t"
    "v_cvt_pk_bf16_f32 v79, v218, v219\n\t"
    "s_nop 1\n\t"
    "v_cvt_pk_bf16_f32 v94, v220, v221\n\t"
    "v_cvt_pk_bf16_f32 v95, v222, v223\n\t"
    "s_waitcnt lgkmcnt(0)\n\t"
    "s_cmp_eq_u32 %[retry], 2\n\t"
    "s_cbranch_scc1 Lend_%=\n\t"
    "Lp0_%=:\n\t"
    "v_mfma_f32_16x16x32_bf16 v[160:163], a[192:195], v[32:35], 0\n\t"
    "v_mov_b32 v208, 0\n\t"
    "v_mfma_f32_16x16x32_bf16 v[160:163], a[196:199], v[36:39], v[160:163]\n\t"
    "ds_read_b128 a[208:211], %[kb0] offset:4096\n\t"
    "v_mov_b32 v209, 0\n\t"
    "v_mfma_f32_16x16x32_bf16 v[160:163], a[200:203], v[40:43], v[160:163]\n\t"
    "ds_read_b128 a[212:215], %[kb1] offset:4096\n\t"
    "s_add_u32 s40, %[kvs], 32768\n\t"
    "v_mov_b32 v210, 0\n\t"
    "v_mfma_f32_16x16x32_bf16 v[160:163], a[204:207], v[44:47], v[160:163]\n\t"
    "ds_read_b128 a[216:219], %[kb2] offset:4096\n\t"
    "s_lshr_b32 s46, %[kvs], 2\n\t"
    "v_mov_b32 v211, 0\n\t"
    "v_mfma_f32_16x16x32_bf16 v[164:167], a[204:207], v[60:63], 0\n\t"
    "ds_read_b128 a[220:223], %[kb3] offset:4096\n\t"
    "s_sub_u32 s46, s46, 4096\n\t"
    "v_mov_b32 v212, 0\n\t"
    "v_mov_b32 v236, 0\n\t"
    "v_mfma_f32_16x16x32_bf16 v[164:167], a[200:203], v[56:59], v[164:167]\n\t"
    "v_mov_b32 v213, 0\n\t"
    "v_mov_b32 v237, 0\n\t"
    "v_mfma_f32_16x16x32_bf16 v[164:167], a[196:199], v[52:55], v[164:167]\n\t"
    "v_mov_b32 v214, 0\n\t"
    "v_mov_b32 v238, 0\n\t"
    "v_mfma_f32_16x16x32_bf16 v[164:167], a[192:195], v[48:51], v[164:167]\n\t"
    "v_mov_b32 v215, 0\n\t"
    "v_mov_b32 v239, 0\n\t"
    "v_mfma_f32_16x16x32_bf16 v[168:171], a[192:195], v[64:67], 0\n\t"
    "v_mov_b32 v216, 0\n\t"
    "v_mfma_f32_16x16x32_bf16 v[168:171], a[196:199], v[68:71], v[168:171]\n\t"
    "v_mov_b32 v217, 0\n\t"
    "v_mfma_f32_16x16x32_bf16 v[168:171], a[200:203], v[72:75], v[168:171]\n\t"
    "v_mov_b32 v218, 0\n\t"
    "v_mfma_f32_16x16x32_bf16 v[168:171], a[204:207], v[76:79], v[168:171]\n\t"
    "v_mov_b32 v219, 0\n\t"
    "v_mfma_f32_16x16x32_bf16 v[172:175], a[204:207], v[92:95], 0\n\t"
    "v_mov_b32 v220, 0\n\t"
    "v_mfma_f32_16x16x32_bf16 v[172:175], a[200:203], v[88:91], v[172:175]\n\t"
    "v_mov_b32 v221, 0\n\t"
    "v_mfma_f32_16x16x32_bf16 v[172:175], a[196:199], v[84:87], v[172:175]\n\t"
    "v_mov_b32 v222, 0\n\t"
    "v_mfma_f32_16x16x32_bf16 v[172:175], a[192:195], v[80:83], v[172:175]\n\t"
    "v_mov_b32 v223, 0\n\t"
    "v_max3_f32 v228, v160, v161, v162\n\t"
    "v_max3_f32 v229, v164, v165, v166\n\t"
    "v_max3_f32 v230, v168, v169, v170\n\t"
    "s_nop 3\n\t"
    "v_max3_f32 v231, v172, v173, v174\n\t"
    "v_max3_f32 v228, v228, v163, %[mg0]\n\t"
    "v_max3_f32 v229, v229, v167, %[mg1]\n\t"
    "v_max3_f32 v230, v230, v171, %[mg2]\n\t"
    "v_max3_f32 v231, v231, v175, %[mg3]\n\t"
    "v_mov_b32 v232, v228\n\t"
    "v_mov_b32 v233, v229\n\t"
    "v_mov_b32 v234, v230\n\t"
    "v_mov_b32 v235, v231\n\t"
    "v_permlane32_swap_b32 v228, v232\n\t"
    "v_permlane32_swap_b32 v229, v233\n\t"
    "v_permlane32_swap_b32 v230, v234\n\t"
    "v_permlane32_swap_b32 v231, v235\n\t"
    "v_max_f32 v228, v228, v232\n\t"
    "v_max_f32 v229, v229, v233\n\t"
    "v_max_f32 v230, v230, v234\n\t"
    "v_max_f32 v231, v231, v235\n\t"
    "v_mov_b32 v232, v228\n\t"
    "v_mov_b32 v233, v229\n\t"
    "v_mov_b32 v234, v230\n\t"
    "v_mov_b32 v235, v231\n\t"
    "v_permlane16_swap_b32 v228, v232\n\t"
    "v_permlane16_swap_b32 v229, v233\n\t"
    "v_permlane16_swap_b32 v230, v234\n\t"
    "v_permlane16_swap_b32 v231, v235\n\t"
    "v_max_f32 v228, v228, v232\n\t"
    "v_max_f32 v229, v229, v233\n\t"
    "v_max_f32 v230, v230, v234\n\t"
    "v_max_f32 v231, v231, v235\n\t"
    "v_sub_f32 v160, v160, v228\n\t"
    "v_sub_f32 v161, v161, v228\n\t"
    "v_sub_f32 v162, v162, v228\n\t"
    "v_sub_f32 v163, v163, v228\n\t"
    "v_sub_f32 v164, v164, v229\n\t"
    "v_sub_f32 v165, v165, v229\n\t"
    "v_sub_f32 v166, v166, v229\n\t"
    "v_sub_f32 v167, v167, v229\n\t"
    "v_sub_f32 v168, v168, v230\n\t"
    "v_sub_f32 v169, v169, v230\n\t"
    "v_sub_f32 v170, v170, v230\n\t"
    "v_sub_f32 v171, v171, v230\n\t"
    "v_sub_f32 v172, v172, v231\n\t"
    "v_sub_f32 v173, v173, v231\n\t"
    "v_sub_f32 v174, v174, v231\n\t"
    "v_sub_f32 v175, v175, v231\n\t"
    "v_sub_f32 v192, 0, v228\n\t"
    "v_sub_f32 v193, 0, v228\n\t"
    "v_sub_f32 v194, 0, v228\n\t"
    "v_sub_f32 v195, 0, v228\n\t"
    "v_sub_f32 v196, 0, v229\n\t"
    "v_sub_f32 v197, 0, v229\n\t"
    "v_sub_f32 v198, 0, v229\n\t"
    "v_sub_f32 v199, 0, v229\n\t"
    "v_sub_f32 v200, 0, v230\n\t"
    "v_sub_f32 v201, 0, v230\n\t"
    "v_sub_f32 v202, 0, v230\n\t"
    "v_sub_f32 v203, 0, v230\n\t"
    "v_sub_f32 v204, 0, v231\n\t"
    "v_sub_f32 v205, 0, v231\n\t"
    "v_sub_f32 v206, 0, v231\n\t"
    "v_sub_f32 v207, 0, v231\n\t"
    "v_mfma_f32_16x16x32_bf16 a[0:3], v[236:239], v[236:239], 0\n\t"
    "v_exp_f32 v160, v160\n\t"
    "v_exp_f32 v161, v161\n\t"
    "v_exp_f32 v162, v162\n\t"
    "v_exp_f32 v163, v163\n\t"
    "v_exp_f32 v164, v164\n\t"
    "v_cvt_pk_bf16_f32 v96, v160, v161\n\t"
    "v_cvt_pk_bf16_f32 v97, v162, v163\n\t"
    "v_mfma_f32_16x16x32_bf16 a[4:7], v[236:239], v[236:239], 0\n\t"
    "v_exp_f32 v165, v165\n\t"
    "s_nop 0\n\t"
    "v_cvt_pk_bf16_f32 v104, v164, v165\n\t"
    "v_mfma_f32_16x16x32_bf16 a[8:11], v[236:239], v[236:239], 0\n\t"
    "v_exp_f32 v166, v166\n\t"
    "v_mfma_f32_16x16x32_bf16 a[12:15], v[236:239], v[236:239], 0\n\t"
    "v_exp_f32 v167, v167\n\t"
    "s_nop 0\n\t"
    "v_cvt_pk_bf16_f32 v105, v166, v167\n\t"
    "v_mfma_f32_16x16x32_bf16 a[16:19], v[236:239], v[236:239], 0\n\t"
    "v_exp_f32 v168, v168\n\t"
    "v_mfma_f32_16x16x32_bf16 a[20:23], v[236:239], v[236:239], 0\n\t"
    "v_exp_f32 v169, v169\n\t"
    "v_add_f32 v216, v216, v168\n\t"
    "v_cvt_pk_bf16_f32 v112, v168, v169\n\t"
    "v_mfma_f32_16x16x32_bf16 a[24:27], v[236:239], v[236:239], 0\n\t"
    "v_exp_f32 v170, v170\n\t"
    "v_add_f32 v216, v216, v169\n\t"
    "v_mfma_f32_16x16x32_bf16 a[28:31], v[236:239], v[236:239], 0\n\t"
    "v_exp_f32 v171, v171\n\t"
    "v_add_f32 v216, v216, v170\n\t"
    "v_cvt_pk_bf16_f32 v113, v170, v171\n\t"
    "v_exp_f32 v172, v172\n\t"
    "v_add_f32 v216, v216, v171\n\t"
    "s_waitcnt lgkmcnt(0)\n\t"
    "v_mfma_f32_16x16x32_bf16 v[176:179], a[208:211], v[32:35], v[192:195]\n\t"
    "v_exp_f32 v173, v173\n\t"
    "v_mfma_f32_16x16x32_bf16 v[176:179], a[212:215], v[36:39], v[176:179]\n\t"
    "ds_read_b128 a[192:195], %[kb0] offset:8192\n\t"
    "v_mfma_f32_16x16x32_bf16 v[176:179], a[216:219], v[40:43], v[176:179]\n\t"
    "v_exp_f32 v174, v174\n\t"
    "v_mfma_f32_16x16x32_bf16 v[176:179], a[220:223], v[44:47], v[176:179]\n\t"
    "ds_read_b128 a[196:199], %[kb1] offset:8192\n\t"
    "v_cvt_pk_bf16_f32 v120, v172, v173\n\t"
    "v_mfma_f32_16x16x32_bf16 v[180:183], a[220:223], v[60:63], v[196:199]\n\t"
    "v_exp_f32 v175, v175\n\t"
    "v_mfma_f32_16x16x32_bf16 v[180:183], a[216:219], v[56:59], v[180:183]\n\t"
    "ds_read_b128 a[200:203], %[kb2] offset:8192\n\t"
    "v_cvt_pk_bf16_f32 v121, v174, v175\n\t"
    "v_mfma_f32_16x16x32_bf16 v[180:183], a[212:215], v[52:55], v[180:183]\n\t"
    "ds_read_b128 a[204:207], %[kb3] offset:8192\n\t"
    "v_add_f32 v220, v220, v172\n\t"
    "v_mfma_f32_16x16x32_bf16 v[180:183], a[208:211], v[48:51], v[180:183]\n\t"
    "v_add_f32 v220, v220, v173\n\t"
    "v_mfma_f32_16x16x32_bf16 v[184:187], a[208:211], v[64:67], v[200:203]\n\t"
    "v_exp_f32 v176, v176\n\t"
    "v_mfma_f32_16x16x32_bf16 v[184:187], a[212:215], v[68:71], v[184:187]\n\t"
    "v_add_f32 v220, v220, v174\n\t"
    "v_mfma_f32_16x16x32_bf16 v[184:187], a[216:219], v[72:75], v[184:187]\n\t"
    "v_exp_f32 v177, v177\n\t"
    "v_mfma_f32_16x16x32_bf16 v[184:187], a[220:223], v[76:79], v[184:187]\n\t"
    "v_cvt_pk_bf16_f32 v98, v176, v177\n\t"
    "v_mfma_f32_16x16x32_bf16 v[188:191], a[220:223], v[92:95], v[204:207]\n\t"
    "v_exp_f32 v178, v178\n\t"
    "v_mfma_f32_16x16x32_bf16 v[188:191], a[216:219], v[88:91], v[188:191]\n\t"
    "v_add_f32 v220, v220, v175\n\t"
    "v_mfma_f32_16x16x32_bf16 v[188:191], a[212:215], v[84:87], v[188:191]\n\t"
    "v_exp_f32 v179, v179\n\t"
    "v_mfma_f32_16x16x32_bf16 v[188:191], a[208:211], v[80:83], v[188:191]\n\t"
    "v_cvt_pk_bf16_f32 v99, v178, v179\n\t"
    "v_mfma_f32_16x16x32_bf16 a[32:35], v[236:239], v[236:239], 0\n\t"
    "v_exp_f32 v180, v180\n\t"
    "v_mfma_f32_16x16x32_bf16 a[36:39], v[236:239], v[236:239], 0\n\t"
    "v_exp_f32 v181, v181\n\t"
    "s_nop 0\n\t"
    "v_cvt_pk_bf16_f32 v106, v180, v181\n\t"
    "v_mfma_f32_16x16x32_bf16 a[40:43], v[236:239], v[236:239], 0\n\t"
    "v_exp_f32 v182, v182\n\t"
    "v_mfma_f32_16x16x32_bf16 a[44:47], v[236:239], v[236:239], 0\n\t"
    "v_exp_f32 v183, v183\n\t"
    "s_nop 0\n\t"
    "v_cvt_pk_bf16_f32 v107, v182, v183\n\t"
    "v_mfma_f32_16x16x32_bf16 a[48:51], v[236:239], v[236:239], 0\n\t"
    "v_exp_f32 v184, v184\n\t"
    "s_nop 0\n\t"
    "v_add_f32 v216, v216, v184\n\t"
    "v_mfma_f32_16x16x32_bf16 a[52:55], v[236:239], v[236:239], 0\n\t"
    "v_exp_f32 v185, v185\n\t"
    "s_nop 0\n\t"
    "v_cvt_pk_bf16_f32 v114, v184, v185\n\t"
    "v_mfma_f32_16x16x32_bf16 a[56:59], v[236:239], v[236:239], 0\n\t"
    "v_exp_f32 v186, v186\n\t"
    "v_add_f32 v216, v216, v185\n\t"
    "v_add_f32 v216, v216, v186\n\t"
    "v_mfma_f32_16x16x32_bf16 a[60:63], v[236:239], v[236:239], 0\n\t"
    "v_exp_f32 v187, v187\n\t"
    "s_nop 0\n\t"
    "v_cvt_pk_bf16_f32 v115, v186, v187\n\t"
    "s_waitcnt lgkmcnt(0)\n\t"
    "v_mfma_f32_16x16x32_bf16 v[160:163], a[192:195], v[32:35], v[192:195]\n\t"
    "v_exp_f32 v188, v188\n\t"
    "v_mfma_f32_16x16x32_bf16 v[160:163], a[196:199], v[36:39], v[160:163]\n\t"
    "ds_read_b128 a[208:211], %[kb0] offset:12288\n\t"
    "v_add_f32 v216, v216, v187\n\t"
    "v_mfma_f32_16x16x32_bf16 v[160:163], a[200:203], v[40:43], v[160:163]\n\t"
    "v_exp_f32 v189, v189\n\t"
    "v_mfma_f32_16x16x32_bf16 v[160:163], a[204:207], v[44:47], v[160:163]\n\t"
    "ds_read_b128 a[212:215], %[kb1] offset:12288\n\t"
    "v_cvt_pk_bf16_f32 v122, v188, v189\n\t"
    "v_mfma_f32_16x16x32_bf16 v[164:167], a[204:207], v[60:63], v[196:199]\n\t"
    "v_exp_f32 v190, v190\n\t"
    "v_mfma_f32_16x16x32_bf16 v[164:167], a[200:203], v[56:59], v[164:167]\n\t"
    "ds_read_b128 a[216:219], %[kb2] offset:12288\n\t"
    "v_add_f32 v220, v220, v188\n\t"
    "v_mfma_f32_16x16x32_bf16 v[164:167], a[196:199], v[52:55], v[164:167]\n\t"
    "v_exp_f32 v191, v191\n\t"
    "v_mfma_f32_16x16x32_bf16 v[164:167], a[192:195], v[48:51], v[164:167]\n\t"
    "ds_read_b128 a[220:223], %[kb3] offset:12288\n\t"
    "v_cvt_pk_bf16_f32 v123, v190, v191\n\t"
    "v_mfma_f32_16x16x32_bf16 v[168:171], a[192:195], v[64:67], v[200:203]\n\t"
    "v_exp_f32 v160, v160\n\t"
    "v_mfma_f32_16x16x32_bf16 v[168:171], a[196:199], v[68:71], v[168:171]\n\t"
    "v_add_f32 v220, v220, v189\n\t"
    "v_mfma_f32_16x16x32_bf16 v[168:171], a[200:203], v[72:75], v[168:171]\n\t"
    "v_exp_f32 v161, v161\n\t"
    "v_mfma_f32_16x16x32_bf16 v[168:171], a[204:207], v[76:79], v[168:171]\n\t"
    "v_cvt_pk_bf16_f32 v100, v160, v161\n\t"
    "v_mfma_f32_16x16x32_bf16 v[172:175], a[204:207], v[92:95], v[204:207]\n\t"
    "v_exp_f32 v162, v162\n\t"
    "v_mfma_f32_16x16x32_bf16 v[172:175], a[200:203], v[88:91], v[172:175]\n\t"
    "v_add_f32 v220, v220, v190\n\t"
    "v_mfma_f32_16x16x32_bf16 v[172:175], a[196:199], v[84:87], v[172:175]\n\t"
    "v_exp_f32 v163, v163\n\t"
    "v_mfma_f32_16x16x32_bf16 v[172:175], a[192:195], v[80:83], v[172:175]\n\t"
    "v_cvt_pk_bf16_f32 v101, v162, v163\n\t"
    "v_mfma_f32_16x16x32_bf16 a[64:67], v[236:239], v[236:239], 0\n\t"
    "v_exp_f32 v164, v164\n\t"
    "v_add_f32 v220, v220, v191\n\t"
    "v_mfma_f32_16x16x32_bf16 a[68:71], v[236:239], v[236:239], 0\n\t"
    "v_exp_f32 v165, v165\n\t"
    "s_nop 0\n\t"
    "v_cvt_pk_bf16_f32 v108, v164, v165\n\t"
    "v_mfma_f32_16x16x32_bf16 a[72:75], v[236:239], v[236:239], 0\n\t"
    "v_exp_f32 v166, v166\n\t"
    "v_mfma_f32_16x16x32_bf16 a[76:79], v[236:239], v[236:239], 0\n\t"
    "v_exp_f32 v167, v167\n\t"
    "s_nop 0\n\t"
    "v_cvt_pk_bf16_f32 v109, v166, v167\n\t"
    "v_mfma_f32_16x16x32_bf16 a[80:83], v[236:239], v[236:239], 0\n\t"
    "v_exp_f32 v168, v168\n\t"
    "s_nop 0\n\t"
    "v_add_f32 v216, v216, v168\n\t"
    "v_mfma_f32_16x16x32_bf16 a[84:87], v[236:239], v[236:239], 0\n\t"
    "v_exp_f32 v169, v169\n\t"
    "s_nop 0\n\t"
    "v_cvt_pk_bf16_f32 v116, v168, v169\n\t"
    "v_mfma_f32_16x16x32_bf16 a[88:91], v[236:239], v[236:239], 0\n\t"
    "v_exp_f32 v170, v170\n\t"
    "v_add_f32 v216, v216, v169\n\t"
    "v_mfma_f32_16x16x32_bf16 a[92:95], v[236:239], v[236:239], 0\n\t"
    "v_exp_f32 v171, v171\n\t"
    "v_add_f32 v216, v216, v170\n\t"
    "v_cvt_pk_bf16_f32 v117, v170, v171\n\t"
    "v_exp_f32 v172, v172\n\t"
    "s_waitcnt vmcnt(16) lgkmcnt(0)\n\t"
    "s_barrier\n\t"
    "v_mfma_f32_16x16x32_bf16 v[176:179], a[208:211], v[32:35], v[192:195]\n\t"
    "v_exp_f32 v173, v173\n\t"
    "v_mfma_f32_16x16x32_bf16 v[176:179], a[212:215], v[36:39], v[176:179]\n\t"
    "ds_read_b128 a[192:195], %[kb0] offset:16384\n\t"
    "s_add_u32 s40, s40, 16384\n\t"
    "v_mfma_f32_16x16x32_bf16 v[176:179], a[216:219], v[40:43], v[176:179]\n\t"
    "v_exp_f32 v174, v174\n\t"
    "v_mfma_f32_16x16x32_bf16 v[176:179], a[220:223], v[44:47], v[176:179]\n\t"
    "ds_read_b128 a[196:199], %[kb1] offset:16384\n\t"
    "s_add_u32 m0, %[m0b], 0\n\t"
    "v_mfma_f32_16x16x32_bf16 v[180:183], a[220:223], v[60:63], v[196:199]\n\t"
    "v_exp_f32 v175, v175\n\t"
    "v_mfma_f32_16x16x32_bf16 v[180:183], a[216:219], v[56:59], v[180:183]\n\t"
    "ds_read_b128 a[200:203], %[kb2] offset:16384\n\t"
    "v_cvt_pk_bf16_f32 v124, v172, v173\n\t"
    "v_mfma_f32_16x16x32_bf16 v[180:183], a[212:215], v[52:55], v[180:183]\n\t"
    "ds_read_b128 a[204:207], %[kb3] offset:16384\n\t"
    "v_cvt_pk_bf16_f32 v125, v174, v175\n\t"
    "v_mfma_f32_16x16x32_bf16 v[180:183], a[208:211], v[48:51], v[180:183]\n\t"
    "ds_read_b64_tr_b16 a[224:225], %[vb0] offset:0\n\t"
    "v_add_f32 v216, v216, v171\n\t"
    "v_mfma_f32_16x16x32_bf16 v[184:187], a[208:211], v[64:67], v[200:203]\n\t"
    "v_exp_f32 v176, v176\n\t"
    "v_mfma_f32_16x16x32_bf16 v[184:187], a[212:215], v[68:71], v[184:187]\n\t"
    "ds_read_b64_tr_b16 a[226:227], %[vb0] offset:4096\n\t"
    "v_add_f32 v220, v220, v172\n\t"
    "v_mfma_f32_16x16x32_bf16 v[184:187], a[216:219], v[72:75], v[184:187]\n\t"
    "v_exp_f32 v177, v177\n\t"
    "v_mfma_f32_16x16x32_bf16 v[184:187], a[220:223], v[76:79], v[184:187]\n\t"
    "ds_read_b64_tr_b16 a[228:229], %[vb0] offset:8192\n\t"
    "v_cvt_pk_bf16_f32 v102, v176, v177\n\t"
    "v_mfma_f32_16x16x32_bf16 v[188:191], a[220:223], v[92:95], v[204:207]\n\t"
    "v_exp_f32 v178, v178\n\t"
    "v_mfma_f32_16x16x32_bf16 v[188:191], a[216:219], v[88:91], v[188:191]\n\t"
    "ds_read_b64_tr_b16 a[230:231], %[vb0] offset:12288\n\t"
    "v_add_f32 v220, v220, v173\n\t"
    "v_mfma_f32_16x16x32_bf16 v[188:191], a[212:215], v[84:87], v[188:191]\n\t"
    "v_exp_f32 v179, v179\n\t"
    "v_mfma_f32_16x16x32_bf16 v[188:191], a[208:211], v[80:83], v[188:191]\n\t"
    "ds_read_b64_tr_b16 a[232:233], %[vb1] offset:0\n\t"
    "v_cvt_pk_bf16_f32 v103, v178, v179\n\t"
    "v_mfma_f32_16x16x32_bf16 a[96:99], v[236:239], v[236:239], 0\n\t"
    "v_exp_f32 v180, v180\n\t"
    "ds_read_b64_tr_b16 a[234:235], %[vb1] offset:4096\n\t"
    "v_add_f32 v220, v220, v174\n\t"
    "v_mfma_f32_16x16x32_bf16 a[100:103], v[236:239], v[236:239], 0\n\t"
    "v_exp_f32 v181, v181\n\t"
    "ds_read_b64_tr_b16 a[236:237], %[vb1] offset:8192\n\t"
    "v_cvt_pk_bf16_f32 v110, v180, v181\n\t"
    "v_mfma_f32_16x16x32_bf16 a[104:107], v[236:239], v[236:239], 0\n\t"
    "v_exp_f32 v182, v182\n\t"
    "ds_read_b64_tr_b16 a[238:239], %[vb1] offset:12288\n\t"
    "v_add_f32 v220, v220, v175\n\t"
    "v_mfma_f32_16x16x32_bf16 a[108:111], v[236:239], v[236:239], 0\n\t"
    "v_exp_f32 v183, v183\n\t"
    "buffer_load_dwordx4 %[kv0], %[srdk1], s40 offen lds\n\t"
    "v_cvt_pk_bf16_f32 v111, v182, v183\n\t"
    "v_mfma_f32_16x16x32_bf16 a[112:115], v[236:239], v[236:239], 0\n\t"
    "v_exp_f32 v184, v184\n\t"
    "s_nop 0\n\t"
    "v_add_f32 v216, v216, v184\n\t"
    "v_mfma_f32_16x16x32_bf16 a[116:119], v[236:239], v[236:239], 0\n\t"
    "v_exp_f32 v185, v185\n\t"
    "buffer_load_dwordx4 %[kv1], %[srdk1], s40 offen offset:1024 lds\n\t"
    "v_cvt_pk_bf16_f32 v118, v184, v185\n\t"
    "v_mfma_f32_16x16x32_bf16 a[120:123], v[236:239], v[236:239], 0\n\t"
    "v_exp_f32 v186, v186\n\t"
    "v_add_f32 v216, v216, v185\n\t"
    "v_add_f32 v216, v216, v186\n\t"
    "v_mov_b32 v232, -1\n\t"
    "v_mov_b32 v233, -1\n\t"
    "v_mov_b32 v234, -1\n\t"
    "v_mov_b32 v235, -1\n\t"
    "v_mfma_f32_16x16x32_bf16 a[124:127], v[236:239], v[236:239], 0\n\t"
    "v_exp_f32 v187, v187\n\t"
    "v_mov_b32 v236, 0x3f803f80\n\t"
    "v_mov_b32 v237, 0x3f803f80\n\t"
    "v_mov_b32 v238, 0x3f803f80\n\t"
    "v_mov_b32 v239, 0x3f803f80\n\t"
    "v_cvt_pk_bf16_f32 v119, v186, v187\n\t"
    "s_waitcnt lgkmcnt(0)\n\t"
    "s_branch Lp1_%=\n\t"
    "Lloop_%=:\n\t"
    "v_mfma_f32_16x16x32_bf16 v[160:163], a[192:195], v[32:35], v[192:195]\n\t"
    "v_exp_f32 v188, v188\n\t"
    "v_mfma_f32_16x16x32_bf16 v[160:163], a[196:199], v[36:39], v[160:163]\n\t"
    "ds_read_b128 a[208:211], %[kb0] offset:4096\n\t"
    "s_add_u32 s46, s46, 4096\n\t"
    "v_mfma_f32_16x16x32_bf16 v[160:163], a[200:203], v[40:43], v[160:163]\n\t"
    "v_exp_f32 v189, v189\n\t"
    "v_mfma_f32_16x16x32_bf16 v[160:163], a[204:207], v[44:47], v[160:163]\n\t"
    "ds_read_b128 a[212:215], %[kb1] offset:4096\n\t"
    "v_cvt_pk_bf16_f32 v151, v186, v187\n\t"
    "v_mfma_f32_16x16x32_bf16 v[164:167], a[204:207], v[60:63], v[196:199]\n\t"
    "v_exp_f32 v190, v190\n\t"
    "v_mfma_f32_16x16x32_bf16 v[164:167], a[200:203], v[56:59], v[164:167]\n\t"
    "ds_read_b128 a[216:219], %[kb2] offset:4096\n\t"
    "v_cvt_pk_bf16_f32 v158, v188, v189\n\t"
    "v_mfma_f32_16x16x32_bf16 v[164:167], a[196:199], v[52:55], v[164:167]\n\t"
    "v_exp_f32 v191, v191\n\t"
    "v_mfma_f32_16x16x32_bf16 v[164:167], a[192:195], v[48:51], v[164:167]\n\t"
    "ds_read_b128 a[220:223], %[kb3] offset:4096\n\t"
    "v_cvt_pk_bf16_f32 v159, v190, v191\n\t"
    "v_mfma_f32_16x16x32_bf16 v[168:171], a[192:195], v[64:67], v[200:203]\n\t"
    "v_exp_f32 v160, v160\n\t"
    "v_mfma_f32_16x16x32_bf16 v[168:171], a[196:199], v[68:71], v[168:171]\n\t"
    "ds_read_b64_tr_b16 a[240:241], %[vb0] offset:49664\n\t"
    "v_add_f32 v216, v216, v187\n\t"
    "v_mfma_f32_16x16x32_bf16 v[168:171], a[200:203], v[72:75], v[168:171]\n\t"
    "v_exp_f32 v161, v161\n\t"
    "v_mfma_f32_16x16x32_bf16 v[168:171], a[204:207], v[76:79], v[168:171]\n\t"
    "ds_read_b64_tr_b16 a[242:243], %[vb0] offset:53760\n\t"
    "v_cvt_pk_bf16_f32 v96, v160, v161\n\t"
    "v_mfma_f32_16x16x32_bf16 v[172:175], a[204:207], v[92:95], v[204:207]\n\t"
    "v_exp_f32 v162, v162\n\t"
    "v_mfma_f32_16x16x32_bf16 v[172:175], a[200:203], v[88:91], v[172:175]\n\t"
    "ds_read_b64_tr_b16 a[244:245], %[vb0] offset:57856\n\t"
    "v_add_f32 v220, v220, v188\n\t"
    "v_mfma_f32_16x16x32_bf16 v[172:175], a[196:199], v[84:87], v[172:175]\n\t"
    "v_exp_f32 v163, v163\n\t"
    "v_mfma_f32_16x16x32_bf16 v[172:175], a[192:195], v[80:83], v[172:175]\n\t"
    "ds_read_b64_tr_b16 a[246:247], %[vb0] offset:61952\n\t"
    "v_cvt_pk_bf16_f32 v97, v162, v163\n\t"
    "v_mfma_f32_16x16x32_bf16 a[0:3], a[224:227], v[128:131], a[0:3]\n\t"
    "v_exp_f32 v164, v164\n\t"
    "v_mfma_f32_16x16x32_bf16 a[0:3], a[228:231], v[132:135], a[0:3]\n\t"
    "ds_read_b64_tr_b16 a[248:249], %[vb1] offset:49664\n\t"
    "v_add_f32 v220, v220, v189\n\t"
    "v_mfma_f32_16x16x32_bf16 a[4:7], a[228:231], v[140:143], a[4:7]\n\t"
    "v_exp_f32 v165, v165\n\t"
    "v_mfma_f32_16x16x32_bf16 a[4:7], a[224:227], v[136:139], a[4:7]\n\t"
    "ds_read_b64_tr_b16 a[250:251], %[vb1] offset:53760\n\t"
    "v_cvt_pk_bf16_f32 v104, v164, v165\n\t"
    "v_mfma_f32_16x16x32_bf16 a[8:11], a[224:227], v[144:147], a[8:11]\n\t"
    "v_exp_f32 v166, v166\n\t"
    "v_mfma_f32_16x16x32_bf16 a[8:11], a[228:231], v[148:151], a[8:11]\n\t"
    "ds_read_b64_tr_b16 a[252:253], %[vb1] offset:57856\n\t"
    "v_add_f32 v220, v220, v190\n\t"
    "v_mfma_f32_16x16x32_bf16 a[12:15], a[228:231], v[156:159], a[12:15]\n\t"
    "v_exp_f32 v167, v167\n\t"
    "v_mfma_f32_16x16x32_bf16 a[12:15], a[224:227], v[152:155], a[12:15]\n\t"
    "ds_read_b64_tr_b16 a[254:255], %[vb1] offset:61952\n\t"
    "v_cvt_pk_bf16_f32 v105, v166, v167\n\t"
    "v_mfma_f32_16x16x32_bf16 a[28:31], a[232:235], v[152:155], a[28:31]\n\t"
    "v_exp_f32 v168, v168\n\t"
    "v_mfma_f32_16x16x32_bf16 a[28:31], a[236:239], v[156:159], a[28:31]\n\t"
    "buffer_load_dwordx4 %[kv2], %[srdk1], s40 offen offset:2048 lds\n\t"
    "v_add_f32 v220, v220, v191\n\t"
    "v_mfma_f32_16x16x32_bf16 a[24:27], a[236:239], v[148:151], a[24:27]\n\t"
    "v_exp_f32 v169, v169\n\t"
    "v_mfma_f32_16x16x32_bf16 a[24:27], a[232:235], v[144:147], a[24:27]\n\t"
    "v_add_f32 v216, v216, v168\n\t"
    "v_cvt_pk_bf16_f32 v112, v168, v169\n\t"
    "v_mfma_f32_16x16x32_bf16 a[20:23], a[232:235], v[136:139], a[20:23]\n\t"
    "v_exp_f32 v170, v170\n\t"
    "v_mfma_f32_16x16x32_bf16 a[20:23], a[236:239], v[140:143], a[20:23]\n\t"
    "buffer_load_dwordx4 %[kv3], %[srdk1], s40 offen offset:3072 lds\n\t"
    "v_add_f32 v216, v216, v169\n\t"
    "v_mfma_f32_16x16x32_bf16 a[16:19], a[236:239], v[132:135], a[16:19]\n\t"
    "v_exp_f32 v171, v171\n\t"
    "v_mfma_f32_16x16x32_bf16 a[16:19], a[232:235], v[128:131], a[16:19]\n\t"
    "v_add_f32 v216, v216, v170\n\t"
    "v_cvt_pk_bf16_f32 v113, v170, v171\n\t"
    "v_mfma_f32_16x16x32_bf16 v[208:211], v[236:239], v[128:131], v[208:211]\n\t"
    "v_exp_f32 v172, v172\n\t"
    "v_mfma_f32_16x16x32_bf16 v[208:211], v[236:239], v[132:135], v[208:211]\n\t"
    "v_add_f32 v216, v216, v171\n\t"
    "s_waitcnt lgkmcnt(0)\n\t"
    "v_mfma_f32_16x16x32_bf16 v[176:179], a[208:211], v[32:35], v[192:195]\n\t"
    "v_exp_f32 v173, v173\n\t"
    "v_mfma_f32_16x16x32_bf16 v[176:179], a[212:215], v[36:39], v[176:179]\n\t"
    "ds_read_b128 a[192:195], %[kb0] offset:8192\n\t"
    "s_add_u32 m0, %[m0b], 98304\n\t"
    "v_mfma_f32_16x16x32_bf16 v[176:179], a[216:219], v[40:43], v[176:179]\n\t"
    "v_exp_f32 v174, v174\n\t"
    "v_mfma_f32_16x16x32_bf16 v[176:179], a[220:223], v[44:47], v[176:179]\n\t"
    "ds_read_b128 a[196:199], %[kb1] offset:8192\n\t"
    "v_cvt_pk_bf16_f32 v120, v172, v173\n\t"
    "v_mfma_f32_16x16x32_bf16 v[180:183], a[220:223], v[60:63], v[196:199]\n\t"
    "v_exp_f32 v175, v175\n\t"
    "v_mfma_f32_16x16x32_bf16 v[180:183], a[216:219], v[56:59], v[180:183]\n\t"
    "ds_read_b128 a[200:203], %[kb2] offset:8192\n\t"
    "v_cvt_pk_bf16_f32 v121, v174, v175\n\t"
    "v_mfma_f32_16x16x32_bf16 v[180:183], a[212:215], v[52:55], v[180:183]\n\t"
    "ds_read_b128 a[204:207], %[kb3] offset:8192\n\t"
    "v_add_f32 v220, v220, v172\n\t"
    "v_mfma_f32_16x16x32_bf16 v[180:183], a[208:211], v[48:51], v[180:183]\n\t"
    "ds_read_b64_tr_b16 a[224:225], %[vb0] offset:50176\n\t"
    "v_add_f32 v220, v220, v173\n\t"
    "v_mfma_f32_16x16x32_bf16 v[184:187], a[208:211], v[64:67], v[200:203]\n\t"
    "v_exp_f32 v176, v176\n\t"
    "v_mfma_f32_16x16x32_bf16 v[184:187], a[212:215], v[68:71], v[184:187]\n\t"
    "ds_read_b64_tr_b16 a[226:227], %[vb0] offset:54272\n\t"
    "v_add_f32 v220, v220, v174\n\t"
    "v_mfma_f32_16x16x32_bf16 v[184:187], a[216:219], v[72:75], v[184:187]\n\t"
    "v_exp_f32 v177, v177\n\t"
    "v_mfma_f32_16x16x32_bf16 v[184:187], a[220:223], v[76:79], v[184:187]\n\t"
    "ds_read_b64_tr_b16 a[228:229], %[vb0] offset:58368\n\t"
    "v_cvt_pk_bf16_f32 v98, v176, v177\n\t"
    "v_mfma_f32_16x16x32_bf16 v[188:191], a[220:223], v[92:95], v[204:207]\n\t"
    "v_exp_f32 v178, v178\n\t"
    "v_mfma_f32_16x16x32_bf16 v[188:191], a[216:219], v[88:91], v[188:191]\n\t"
    "ds_read_b64_tr_b16 a[230:231], %[vb0] offset:62464\n\t"
    "v_add_f32 v220, v220, v175\n\t"
    "v_mfma_f32_16x16x32_bf16 v[188:191], a[212:215], v[84:87], v[188:191]\n\t"
    "v_exp_f32 v179, v179\n\t"
    "v_mfma_f32_16x16x32_bf16 v[188:191], a[208:211], v[80:83], v[188:191]\n\t"
    "ds_read_b64_tr_b16 a[232:233], %[vb1] offset:50176\n\t"
    "v_cvt_pk_bf16_f32 v99, v178, v179\n\t"
    "v_mfma_f32_16x16x32_bf16 a[32:35], a[240:243], v[128:131], a[32:35]\n\t"
    "v_exp_f32 v180, v180\n\t"
    "v_mfma_f32_16x16x32_bf16 a[32:35], a[244:247], v[132:135], a[32:35]\n\t"
    "ds_read_b64_tr_b16 a[234:235], %[vb1] offset:54272\n\t"
    "v_mfma_f32_16x16x32_bf16 a[36:39], a[244:247], v[140:143], a[36:39]\n\t"
    "v_exp_f32 v181, v181\n\t"
    "v_mfma_f32_16x16x32_bf16 a[36:39], a[240:243], v[136:139], a[36:39]\n\t"
    "ds_read_b64_tr_b16 a[236:237], %[vb1] offset:58368\n\t"
    "v_cvt_pk_bf16_f32 v106, v180, v181\n\t"
    "v_mfma_f32_16x16x32_bf16 a[40:43], a[240:243], v[144:147], a[40:43]\n\t"
    "v_exp_f32 v182, v182\n\t"
    "v_mfma_f32_16x16x32_bf16 a[40:43], a[244:247], v[148:151], a[40:43]\n\t"
    "ds_read_b64_tr_b16 a[238:239], %[vb1] offset:62464\n\t"
    "v_mfma_f32_16x16x32_bf16 a[44:47], a[244:247], v[156:159], a[44:47]\n\t"
    "v_exp_f32 v183, v183\n\t"
    "v_mfma_f32_16x16x32_bf16 a[44:47], a[240:243], v[152:155], a[44:47]\n\t"
    "buffer_load_dwordx4 %[vv0], %[srdv], s40 offen lds\n\t"
    "v_cvt_pk_bf16_f32 v107, v182, v183\n\t"
    "v_mfma_f32_16x16x32_bf16 a[60:63], a[248:251], v[152:155], a[60:63]\n\t"
    "v_exp_f32 v184, v184\n\t"
    "v_mfma_f32_16x16x32_bf16 a[60:63], a[252:255], v[156:159], a[60:63]\n\t"
    "buffer_load_dword v224, %[xl], %[srdu], s46 offen\n\t"
    "v_add_f32 v216, v216, v184\n\t"
    "v_mfma_f32_16x16x32_bf16 a[56:59], a[252:255], v[148:151], a[56:59]\n\t"
    "v_exp_f32 v185, v185\n\t"
    "v_mfma_f32_16x16x32_bf16 a[56:59], a[248:251], v[144:147], a[56:59]\n\t"
    "buffer_load_dwordx4 %[vv1], %[srdv], s40 offen offset:1024 lds\n\t"
    "v_cvt_pk_bf16_f32 v114, v184, v185\n\t"
    "v_mfma_f32_16x16x32_bf16 a[52:55], a[248:251], v[136:139], a[52:55]\n\t"
    "v_exp_f32 v186, v186\n\t"
    "v_mfma_f32_16x16x32_bf16 a[52:55], a[252:255], v[140:143], a[52:55]\n\t"
    "v_add_f32 v216, v216, v185\n\t"
    "v_add_f32 v216, v216, v186\n\t"
    "v_mfma_f32_16x16x32_bf16 a[48:51], a[252:255], v[132:135], a[48:51]\n\t"
    "v_exp_f32 v187, v187\n\t"
    "v_mfma_f32_16x16x32_bf16 a[48:51], a[248:251], v[128:131], a[48:51]\n\t"
    "v_cvt_pk_bf16_f32 v115, v186, v187\n\t"
    "s_waitcnt lgkmcnt(0)\n\t"
    "v_mfma_f32_16x16x32_bf16 v[160:163], a[192:195], v[32:35], v[192:195]\n\t"
    "v_exp_f32 v188, v188\n\t"
    "v_mfma_f32_16x16x32_bf16 v[160:163], a[196:199], v[36:39], v[160:163]\n\t"
    "ds_read_b128 a[208:211], %[kb0] offset:12288\n\t"
    "v_add_f32 v216, v216, v187\n\t"
    "v_mfma_f32_16x16x32_bf16 v[160:163], a[200:203], v[40:43], v[160:163]\n\t"
    "v_exp_f32 v189, v189\n\t"
    "v_mfma_f32_16x16x32_bf16 v[160:163], a[204:207], v[44:47], v[160:163]\n\t"
    "ds_read_b128 a[212:215], %[kb1] offset:12288\n\t"
    "v_cvt_pk_bf16_f32 v122, v188, v189\n\t"
    "v_mfma_f32_16x16x32_bf16 v[164:167], a[204:207], v[60:63], v[196:199]\n\t"
    "v_exp_f32 v190, v190\n\t"
    "v_mfma_f32_16x16x32_bf16 v[164:167], a[200:203], v[56:59], v[164:167]\n\t"
    "ds_read_b128 a[216:219], %[kb2] offset:12288\n\t"
    "v_add_f32 v220, v220, v188\n\t"
    "v_mfma_f32_16x16x32_bf16 v[164:167], a[196:199], v[52:55], v[164:167]\n\t"
    "v_exp_f32 v191, v191\n\t"
    "v_mfma_f32_16x16x32_bf16 v[164:167], a[192:195], v[48:51], v[164:167]\n\t"
    "ds_read_b128 a[220:223], %[kb3] offset:12288\n\t"
    "v_cvt_pk_bf16_f32 v123, v190, v191\n\t"
    "v_mfma_f32_16x16x32_bf16 v[168:171], a[192:195], v[64:67], v[200:203]\n\t"
    "v_exp_f32 v160, v160\n\t"
    "v_mfma_f32_16x16x32_bf16 v[168:171], a[196:199], v[68:71], v[168:171]\n\t"
    "ds_read_b64_tr_b16 a[240:241], %[vb0] offset:50688\n\t"
    "v_add_f32 v220, v220, v189\n\t"
    "v_mfma_f32_16x16x32_bf16 v[168:171], a[200:203], v[72:75], v[168:171]\n\t"
    "v_exp_f32 v161, v161\n\t"
    "v_mfma_f32_16x16x32_bf16 v[168:171], a[204:207], v[76:79], v[168:171]\n\t"
    "ds_read_b64_tr_b16 a[242:243], %[vb0] offset:54784\n\t"
    "v_cvt_pk_bf16_f32 v100, v160, v161\n\t"
    "v_mfma_f32_16x16x32_bf16 v[172:175], a[204:207], v[92:95], v[204:207]\n\t"
    "v_exp_f32 v162, v162\n\t"
    "v_mfma_f32_16x16x32_bf16 v[172:175], a[200:203], v[88:91], v[172:175]\n\t"
    "ds_read_b64_tr_b16 a[244:245], %[vb0] offset:58880\n\t"
    "v_add_f32 v220, v220, v190\n\t"
    "v_mfma_f32_16x16x32_bf16 v[172:175], a[196:199], v[84:87], v[172:175]\n\t"
    "v_exp_f32 v163, v163\n\t"
    "v_mfma_f32_16x16x32_bf16 v[172:175], a[192:195], v[80:83], v[172:175]\n\t"
    "ds_read_b64_tr_b16 a[246:247], %[vb0] offset:62976\n\t"
    "v_cvt_pk_bf16_f32 v101, v162, v163\n\t"
    "v_mfma_f32_16x16x32_bf16 a[64:67], a[224:227], v[128:131], a[64:67]\n\t"
    "v_exp_f32 v164, v164\n\t"
    "v_mfma_f32_16x16x32_bf16 a[64:67], a[228:231], v[132:135], a[64:67]\n\t"
    "ds_read_b64_tr_b16 a[248:249], %[vb1] offset:50688\n\t"
    "v_add_f32 v220, v220, v191\n\t"
    "v_mfma_f32_16x16x32_bf16 a[68:71], a[228:231], v[140:143], a[68:71]\n\t"
    "v_exp_f32 v165, v165\n\t"
    "v_mfma_f32_16x16x32_bf16 a[68:71], a[224:227], v[136:139], a[68:71]\n\t"
    "ds_read_b64_tr_b16 a[250:251], %[vb1] offset:54784\n\t"
    "v_cvt_pk_bf16_f32 v108, v164, v165\n\t"
    "v_mfma_f32_16x16x32_bf16 a[72:75], a[224:227], v[144:147], a[72:75]\n\t"
    "v_exp_f32 v166, v166\n\t"
    "v_mfma_f32_16x16x32_bf16 a[72:75], a[228:231], v[148:151], a[72:75]\n\t"
    "ds_read_b64_tr_b16 a[252:253], %[vb1] offset:58880\n\t"
    "v_mfma_f32_16x16x32_bf16 a[76:79], a[228:231], v[156:159], a[76:79]\n\t"
    "v_exp_f32 v167, v167\n\t"
    "v_mfma_f32_16x16x32_bf16 a[76:79], a[224:227], v[152:155], a[76:79]\n\t"
    "ds_read_b64_tr_b16 a[254:255], %[vb1] offset:62976\n\t"
    "v_cvt_pk_bf16_f32 v109, v166, v167\n\t"
    "v_mfma_f32_16x16x32_bf16 a[92:95], a[232:235], v[152:155], a[92:95]\n\t"
    "v_exp_f32 v168, v168\n\t"
    "v_mfma_f32_16x16x32_bf16 a[92:95], a[236:239], v[156:159], a[92:95]\n\t"
    "buffer_load_dwordx4 %[vv0], %[srdv], s40 offen offset:2048 lds\n\t"
    "v_add_f32 v216, v216, v168\n\t"
    "v_mfma_f32_16x16x32_bf16 a[88:91], a[236:239], v[148:151], a[88:91]\n\t"
    "v_exp_f32 v169, v169\n\t"
    "v_mfma_f32_16x16x32_bf16 a[88:91], a[232:235], v[144:147], a[88:91]\n\t"
    "buffer_store_dwordx4 v[232:235], %[xl], %[srdp], s46 offen sc1\n\t"
    "v_cvt_pk_bf16_f32 v116, v168, v169\n\t"
    "v_mfma_f32_16x16x32_bf16 a[84:87], a[232:235], v[136:139], a[84:87]\n\t"
    "v_exp_f32 v170, v170\n\t"
    "v_mfma_f32_16x16x32_bf16 a[84:87], a[236:239], v[140:143], a[84:87]\n\t"
    "buffer_load_dwordx4 %[vv1], %[srdv], s40 offen offset:3072 lds\n\t"
    "v_add_f32 v216, v216, v169\n\t"
    "v_mfma_f32_16x16x32_bf16 a[80:83], a[236:239], v[132:135], a[80:83]\n\t"
    "v_exp_f32 v171, v171\n\t"
    "v_mfma_f32_16x16x32_bf16 a[80:83], a[232:235], v[128:131], a[80:83]\n\t"
    "v_add_f32 v216, v216, v170\n\t"
    "v_cvt_pk_bf16_f32 v117, v170, v171\n\t"
    "v_mfma_f32_16x16x32_bf16 v[212:215], v[236:239], v[136:139], v[212:215]\n\t"
    "v_exp_f32 v172, v172\n\t"
    "v_mfma_f32_16x16x32_bf16 v[212:215], v[236:239], v[140:143], v[212:215]\n\t"
    "s_waitcnt vmcnt(18) lgkmcnt(0)\n\t"
    "s_barrier\n\t"
    "v_mfma_f32_16x16x32_bf16 v[176:179], a[208:211], v[32:35], v[192:195]\n\t"
    "v_exp_f32 v173, v173\n\t"
    "v_mfma_f32_16x16x32_bf16 v[176:179], a[212:215], v[36:39], v[176:179]\n\t"
    "ds_read_b128 a[192:195], %[kb0] offset:16384\n\t"
    "s_add_u32 s40, s40, 16384\n\t"
    "v_mfma_f32_16x16x32_bf16 v[176:179], a[216:219], v[40:43], v[176:179]\n\t"
    "v_exp_f32 v174, v174\n\t"
    "v_mfma_f32_16x16x32_bf16 v[176:179], a[220:223], v[44:47], v[176:179]\n\t"
    "ds_read_b128 a[196:199], %[kb1] offset:16384\n\t"
    "s_add_u32 m0, %[m0b], 0\n\t"
    "v_mfma_f32_16x16x32_bf16 v[180:183], a[220:223], v[60:63], v[196:199]\n\t"
    "v_exp_f32 v175, v175\n\t"
    "v_mfma_f32_16x16x32_bf16 v[180:183], a[216:219], v[56:59], v[180:183]\n\t"
    "ds_read_b128 a[200:203], %[kb2] offset:16384\n\t"
    "v_cvt_pk_bf16_f32 v124, v172, v173\n\t"
    "v_mfma_f32_16x16x32_bf16 v[180:183], a[212:215], v[52:55], v[180:183]\n\t"
    "ds_read_b128 a[204:207], %[kb3] offset:16384\n\t"
    "v_cvt_pk_bf16_f32 v125, v174, v175\n\t"
    "v_mfma_f32_16x16x32_bf16 v[180:183], a[208:211], v[48:51], v[180:183]\n\t"
    "ds_read_b64_tr_b16 a[224:225], %[vb0] offset:0\n\t"
    "v_add_f32 v216, v216, v171\n\t"
    "v_mfma_f32_16x16x32_bf16 v[184:187], a[208:211], v[64:67], v[200:203]\n\t"
    "v_exp_f32 v176, v176\n\t"
    "v_mfma_f32_16x16x32_bf16 v[184:187], a[212:215], v[68:71], v[184:187]\n\t"
    "ds_read_b64_tr_b16 a[226:227], %[vb0] offset:4096\n\t"
    "v_add_f32 v220, v220, v172\n\t"
    "v_mfma_f32_16x16x32_bf16 v[184:187], a[216:219], v[72:75], v[184:187]\n\t"
    "v_exp_f32 v177, v177\n\t"
    "v_mfma_f32_16x16x32_bf16 v[184:187], a[220:223], v[76:79], v[184:187]\n\t"
    "ds_read_b64_tr_b16 a[228:229], %[vb0] offset:8192\n\t"
    "v_cvt_pk_bf16_f32 v102, v176, v177\n\t"
    "v_mfma_f32_16x16x32_bf16 v[188:191], a[220:223], v[92:95], v[204:207]\n\t"
    "v_exp_f32 v178, v178\n\t"
    "v_mfma_f32_16x16x32_bf16 v[188:191], a[216:219], v[88:91], v[188:191]\n\t"
    "ds_read_b64_tr_b16 a[230:231], %[vb0] offset:12288\n\t"
    "v_add_f32 v220, v220, v173\n\t"
    "v_mfma_f32_16x16x32_bf16 v[188:191], a[212:215], v[84:87], v[188:191]\n\t"
    "v_exp_f32 v179, v179\n\t"
    "v_mfma_f32_16x16x32_bf16 v[188:191], a[208:211], v[80:83], v[188:191]\n\t"
    "ds_read_b64_tr_b16 a[232:233], %[vb1] offset:0\n\t"
    "v_cvt_pk_bf16_f32 v103, v178, v179\n\t"
    "v_mfma_f32_16x16x32_bf16 a[96:99], a[240:243], v[128:131], a[96:99]\n\t"
    "v_exp_f32 v180, v180\n\t"
    "v_mfma_f32_16x16x32_bf16 a[96:99], a[244:247], v[132:135], a[96:99]\n\t"
    "ds_read_b64_tr_b16 a[234:235], %[vb1] offset:4096\n\t"
    "v_add_f32 v220, v220, v174\n\t"
    "v_mfma_f32_16x16x32_bf16 a[100:103], a[244:247], v[140:143], a[100:103]\n\t"
    "v_exp_f32 v181, v181\n\t"
    "v_mfma_f32_16x16x32_bf16 a[100:103], a[240:243], v[136:139], a[100:103]\n\t"
    "ds_read_b64_tr_b16 a[236:237], %[vb1] offset:8192\n\t"
    "v_cvt_pk_bf16_f32 v110, v180, v181\n\t"
    "v_mfma_f32_16x16x32_bf16 a[104:107], a[240:243], v[144:147], a[104:107]\n\t"
    "v_exp_f32 v182, v182\n\t"
    "v_mfma_f32_16x16x32_bf16 a[104:107], a[244:247], v[148:151], a[104:107]\n\t"
    "ds_read_b64_tr_b16 a[238:239], %[vb1] offset:12288\n\t"
    "v_add_f32 v220, v220, v175\n\t"
    "v_mfma_f32_16x16x32_bf16 a[108:111], a[244:247], v[156:159], a[108:111]\n\t"
    "v_exp_f32 v183, v183\n\t"
    "v_mfma_f32_16x16x32_bf16 a[108:111], a[240:243], v[152:155], a[108:111]\n\t"
    "buffer_load_dwordx4 %[kv0], %[srdk1], s40 offen lds\n\t"
    "v_cvt_pk_bf16_f32 v111, v182, v183\n\t"
    "v_mfma_f32_16x16x32_bf16 a[124:127], a[248:251], v[152:155], a[124:127]\n\t"
    "v_exp_f32 v184, v184\n\t"
    "v_mfma_f32_16x16x32_bf16 a[124:127], a[252:255], v[156:159], a[124:127]\n\t"
    "v_add_f32 v216, v216, v184\n\t"
    "v_mfma_f32_16x16x32_bf16 a[120:123], a[252:255], v[148:151], a[120:123]\n\t"
    "v_exp_f32 v185, v185\n\t"
    "v_mfma_f32_16x16x32_bf16 a[120:123], a[248:251], v[144:147], a[120:123]\n\t"
    "buffer_load_dwordx4 %[kv1], %[srdk1], s40 offen offset:1024 lds\n\t"
    "v_cvt_pk_bf16_f32 v118, v184, v185\n\t"
    "v_mfma_f32_16x16x32_bf16 a[116:119], a[248:251], v[136:139], a[116:119]\n\t"
    "v_exp_f32 v186, v186\n\t"
    "v_mfma_f32_16x16x32_bf16 a[116:119], a[252:255], v[140:143], a[116:119]\n\t"
    "v_add_f32 v216, v216, v185\n\t"
    "v_add_f32 v216, v216, v186\n\t"
    "v_mfma_f32_16x16x32_bf16 a[112:115], a[252:255], v[132:135], a[112:115]\n\t"
    "v_exp_f32 v187, v187\n\t"
    "v_mfma_f32_16x16x32_bf16 a[112:115], a[248:251], v[128:131], a[112:115]\n\t"
    "v_cvt_pk_bf16_f32 v119, v186, v187\n\t"
    "s_waitcnt lgkmcnt(0)\n\t"
    "Lp1_%=:\n\t"
    "v_mfma_f32_16x16x32_bf16 v[160:163], a[192:195], v[32:35], v[192:195]\n\t"
    "v_exp_f32 v188, v188\n\t"
    "v_mfma_f32_16x16x32_bf16 v[160:163], a[196:199], v[36:39], v[160:163]\n\t"
    "ds_read_b128 a[208:211], %[kb0] offset:20480\n\t"
    "s_add_u32 s46, s46, 4096\n\t"
    "v_mfma_f32_16x16x32_bf16 v[160:163], a[200:203], v[40:43], v[160:163]\n\t"
    "v_exp_f32 v189, v189\n\t"
    "v_mfma_f32_16x16x32_bf16 v[160:163], a[204:207], v[44:47], v[160:163]\n\t"
    "ds_read_b128 a[212:215], %[kb1] offset:20480\n\t"
    "v_cvt_pk_bf16_f32 v126, v188, v189\n\t"
    "v_mfma_f32_16x16x32_bf16 v[164:167], a[204:207], v[60:63], v[196:199]\n\t"
    "v_exp_f32 v190, v190\n\t"
    "v_mfma_f32_16x16x32_bf16 v[164:167], a[200:203], v[56:59], v[164:167]\n\t"
    "ds_read_b128 a[216:219], %[kb2] offset:20480\n\t"
    "v_add_f32 v216, v216, v187\n\t"
    "v_mfma_f32_16x16x32_bf16 v[164:167], a[196:199], v[52:55], v[164:167]\n\t"
    "v_exp_f32 v191, v191\n\t"
    "v_mfma_f32_16x16x32_bf16 v[164:167], a[192:195], v[48:51], v[164:167]\n\t"
    "ds_read_b128 a[220:223], %[kb3] offset:20480\n\t"
    "v_cvt_pk_bf16_f32 v127, v190, v191\n\t"
    "v_mfma_f32_16x16x32_bf16 v[168:171], a[192:195], v[64:67], v[200:203]\n\t"
    "v_exp_f32 v160, v160\n\t"
    "v_mfma_f32_16x16x32_bf16 v[168:171], a[196:199], v[68:71], v[168:171]\n\t"
    "ds_read_b64_tr_b16 a[240:241], %[vb0] offset:512\n\t"
    "v_add_f32 v220, v220, v188\n\t"
    "v_mfma_f32_16x16x32_bf16 v[168:171], a[200:203], v[72:75], v[168:171]\n\t"
    "v_exp_f32 v161, v161\n\t"
    "v_mfma_f32_16x16x32_bf16 v[168:171], a[204:207], v[76:79], v[168:171]\n\t"
    "ds_read_b64_tr_b16 a[242:243], %[vb0] offset:4608\n\t"
    "v_cvt_pk_bf16_f32 v128, v160, v161\n\t"
    "v_mfma_f32_16x16x32_bf16 v[172:175], a[204:207], v[92:95], v[204:207]\n\t"
    "v_exp_f32 v162, v162\n\t"
    "v_mfma_f32_16x16x32_bf16 v[172:175], a[200:203], v[88:91], v[172:175]\n\t"
    "ds_read_b64_tr_b16 a[244:245], %[vb0] offset:8704\n\t"
    "v_add_f32 v220, v220, v189\n\t"
    "v_mfma_f32_16x16x32_bf16 v[172:175], a[196:199], v[84:87], v[172:175]\n\t"
    "v_exp_f32 v163, v163\n\t"
    "v_mfma_f32_16x16x32_bf16 v[172:175], a[192:195], v[80:83], v[172:175]\n\t"
    "ds_read_b64_tr_b16 a[246:247], %[vb0] offset:12800\n\t"
    "v_cvt_pk_bf16_f32 v129, v162, v163\n\t"
    "v_mfma_f32_16x16x32_bf16 a[0:3], a[224:227], v[96:99], a[0:3]\n\t"
    "v_exp_f32 v164, v164\n\t"
    "v_mfma_f32_16x16x32_bf16 a[0:3], a[228:231], v[100:103], a[0:3]\n\t"
    "ds_read_b64_tr_b16 a[248:249], %[vb1] offset:512\n\t"
    "v_add_f32 v220, v220, v190\n\t"
    "v_mfma_f32_16x16x32_bf16 a[4:7], a[228:231], v[108:111], a[4:7]\n\t"
    "v_exp_f32 v165, v165\n\t"
    "v_mfma_f32_16x16x32_bf16 a[4:7], a[224:227], v[104:107], a[4:7]\n\t"
    "ds_read_b64_tr_b16 a[250:251], %[vb1] offset:4608\n\t"
    "v_cvt_pk_bf16_f32 v136, v164, v165\n\t"
    "v_mfma_f32_16x16x32_bf16 a[8:11], a[224:227], v[112:115], a[8:11]\n\t"
    "v_exp_f32 v166, v166\n\t"
    "v_mfma_f32_16x16x32_bf16 a[8:11], a[228:231], v[116:119], a[8:11]\n\t"
    "ds_read_b64_tr_b16 a[252:253], %[vb1] offset:8704\n\t"
    "v_add_f32 v220, v220, v191\n\t"
    "v_mfma_f32_16x16x32_bf16 a[12:15], a[228:231], v[124:127], a[12:15]\n\t"
    "v_exp_f32 v167, v167\n\t"
    "v_mfma_f32_16x16x32_bf16 a[12:15], a[224:227], v[120:123], a[12:15]\n\t"
    "ds_read_b64_tr_b16 a[254:255], %[vb1] offset:12800\n\t"
    "v_cvt_pk_bf16_f32 v137, v166, v167\n\t"
    "v_mfma_f32_16x16x32_bf16 a[28:31], a[232:235], v[120:123], a[28:31]\n\t"
    "v_exp_f32 v168, v168\n\t"
    "v_mfma_f32_16x16x32_bf16 a[28:31], a[236:239], v[124:127], a[28:31]\n\t"
    "buffer_load_dwordx4 %[kv2], %[srdk1], s40 offen offset:2048 lds\n\t"
    "v_add_f32 v216, v216, v168\n\t"
    "v_mfma_f32_16x16x32_bf16 a[24:27], a[236:239], v[116:119], a[24:27]\n\t"
    "v_exp_f32 v169, v169\n\t"
    "v_mfma_f32_16x16x32_bf16 a[24:27], a[232:235], v[112:115], a[24:27]\n\t"
    "v_cvt_pk_bf16_f32 v144, v168, v169\n\t"
    "v_add_f32 v216, v216, v169\n\t"
    "v_mfma_f32_16x16x32_bf16 a[20:23], a[232:235], v[104:107], a[20:23]\n\t"
    "v_exp_f32 v170, v170\n\t"
    "v_mfma_f32_16x16x32_bf16 a[20:23], a[236:239], v[108:111], a[20:23]\n\t"
    "buffer_load_dwordx4 %[kv3], %[srdk1], s40 offen offset:3072 lds\n\t"
    "v_add_f32 v216, v216, v170\n\t"
    "v_mfma_f32_16x16x32_bf16 a[16:19], a[236:239], v[100:103], a[16:19]\n\t"
    "v_exp_f32 v171, v171\n\t"
    "v_mfma_f32_16x16x32_bf16 a[16:19], a[232:235], v[96:99], a[16:19]\n\t"
    "v_cvt_pk_bf16_f32 v145, v170, v171\n\t"
    "v_add_f32 v216, v216, v171\n\t"
    "v_mfma_f32_16x16x32_bf16 v[208:211], v[236:239], v[96:99], v[208:211]\n\t"
    "v_exp_f32 v172, v172\n\t"
    "v_mfma_f32_16x16x32_bf16 v[208:211], v[236:239], v[100:103], v[208:211]\n\t"
    "v_add_f32 v220, v220, v172\n\t"
    "s_waitcnt lgkmcnt(0)\n\t"
    "v_mfma_f32_16x16x32_bf16 v[176:179], a[208:211], v[32:35], v[192:195]\n\t"
    "v_exp_f32 v173, v173\n\t"
    "v_mfma_f32_16x16x32_bf16 v[176:179], a[212:215], v[36:39], v[176:179]\n\t"
    "ds_read_b128 a[192:195], %[kb0] offset:24576\n\t"
    "s_add_u32 m0, %[m0b], 114688\n\t"
    "v_mfma_f32_16x16x32_bf16 v[176:179], a[216:219], v[40:43], v[176:179]\n\t"
    "v_exp_f32 v174, v174\n\t"
    "v_mfma_f32_16x16x32_bf16 v[176:179], a[220:223], v[44:47], v[176:179]\n\t"
    "ds_read_b128 a[196:199], %[kb1] offset:24576\n\t"
    "v_cvt_pk_bf16_f32 v152, v172, v173\n\t"
    "v_mfma_f32_16x16x32_bf16 v[180:183], a[220:223], v[60:63], v[196:199]\n\t"
    "v_exp_f32 v175, v175\n\t"
    "v_mfma_f32_16x16x32_bf16 v[180:183], a[216:219], v[56:59], v[180:183]\n\t"
    "ds_read_b128 a[200:203], %[kb2] offset:24576\n\t"
    "v_cvt_pk_bf16_f32 v153, v174, v175\n\t"
    "v_mfma_f32_16x16x32_bf16 v[180:183], a[212:215], v[52:55], v[180:183]\n\t"
    "ds_read_b128 a[204:207], %[kb3] offset:24576\n\t"
    "v_add_f32 v220, v220, v173\n\t"
    "v_mfma_f32_16x16x32_bf16 v[180:183], a[208:211], v[48:51], v[180:183]\n\t"
    "ds_read_b64_tr_b16 a[224:225], %[vb0] offset:1024\n\t"
    "v_add_f32 v220, v220, v174\n\t"
    "v_mfma_f32_16x16x32_bf16 v[184:187], a[208:211], v[64:67], v[200:203]\n\t"
    "v_exp_f32 v176, v176\n\t"
    "v_mfma_f32_16x16x32_bf16 v[184:187], a[212:215], v[68:71], v[184:187]\n\t"
    "ds_read_b64_tr_b16 a[226:227], %[vb0] offset:5120\n\t"
    "v_add_f32 v220, v220, v175\n\t"
    "v_mfma_f32_16x16x32_bf16 v[184:187], a[216:219], v[72:75], v[184:187]\n\t"
    "v_exp_f32 v177, v177\n\t"
    "v_mfma_f32_16x16x32_bf16 v[184:187], a[220:223], v[76:79], v[184:187]\n\t"
    "ds_read_b64_tr_b16 a[228:229], %[vb0] offset:9216\n\t"
    "v_cvt_pk_bf16_f32 v130, v176, v177\n\t"
    "v_mfma_f32_16x16x32_bf16 v[188:191], a[220:223], v[92:95], v[204:207]\n\t"
    "v_exp_f32 v178, v178\n\t"
    "v_mfma_f32_16x16x32_bf16 v[188:191], a[216:219], v[88:91], v[188:191]\n\t"
    "ds_read_b64_tr_b16 a[230:231], %[vb0] offset:13312\n\t"
    "v_mfma_f32_16x16x32_bf16 v[188:191], a[212:215], v[84:87], v[188:191]\n\t"
    "v_exp_f32 v179, v179\n\t"
    "v_mfma_f32_16x16x32_bf16 v[188:191], a[208:211], v[80:83], v[188:191]\n\t"
    "ds_read_b64_tr_b16 a[232:233], %[vb1] offset:1024\n\t"
    "v_cvt_pk_bf16_f32 v131, v178, v179\n\t"
    "v_mfma_f32_16x16x32_bf16 a[32:35], a[240:243], v[96:99], a[32:35]\n\t"
    "v_exp_f32 v180, v180\n\t"
    "v_mfma_f32_16x16x32_bf16 a[32:35], a[244:247], v[100:103], a[32:35]\n\t"
    "ds_read_b64_tr_b16 a[234:235], %[vb1] offset:5120\n\t"
    "v_mfma_f32_16x16x32_bf16 a[36:39], a[244:247], v[108:111], a[36:39]\n\t"
    "v_exp_f32 v181, v181\n\t"
    "v_mfma_f32_16x16x32_bf16 a[36:39], a[240:243], v[104:107], a[36:39]\n\t"
    "ds_read_b64_tr_b16 a[236:237], %[vb1] offset:9216\n\t"
    "v_cvt_pk_bf16_f32 v138, v180, v181\n\t"
    "v_mfma_f32_16x16x32_bf16 a[40:43], a[240:243], v[112:115], a[40:43]\n\t"
    "v_exp_f32 v182, v182\n\t"
    "v_mfma_f32_16x16x32_bf16 a[40:43], a[244:247], v[116:119], a[40:43]\n\t"
    "ds_read_b64_tr_b16 a[238:239], %[vb1] offset:13312\n\t"
    "v_mfma_f32_16x16x32_bf16 a[44:47], a[244:247], v[124:127], a[44:47]\n\t"
    "v_exp_f32 v183, v183\n\t"
    "v_mfma_f32_16x16x32_bf16 a[44:47], a[240:243], v[120:123], a[44:47]\n\t"
    "buffer_load_dwordx4 %[vv0], %[srdv], s40 offen lds\n\t"
    "v_cvt_pk_bf16_f32 v139, v182, v183\n\t"
    "v_mfma_f32_16x16x32_bf16 a[60:63], a[248:251], v[120:123], a[60:63]\n\t"
    "v_exp_f32 v184, v184\n\t"
    "v_mfma_f32_16x16x32_bf16 a[60:63], a[252:255], v[124:127], a[60:63]\n\t"
    "buffer_load_dword v224, %[xl], %[srdu], s46 offen\n\t"
    "v_add_f32 v216, v216, v184\n\t"
    "v_mfma_f32_16x16x32_bf16 a[56:59], a[252:255], v[116:119], a[56:59]\n\t"
    "v_exp_f32 v185, v185\n\t"
    "v_mfma_f32_16x16x32_bf16 a[56:59], a[248:251], v[112:115], a[56:59]\n\t"
    "buffer_load_dwordx4 %[vv1], %[srdv], s40 offen offset:1024 lds\n\t"
    "v_cvt_pk_bf16_f32 v146, v184, v185\n\t"
    "v_mfma_f32_16x16x32_bf16 a[52:55], a[248:251], v[104:107], a[52:55]\n\t"
    "v_exp_f32 v186, v186\n\t"
    "v_mfma_f32_16x16x32_bf16 a[52:55], a[252:255], v[108:111], a[52:55]\n\t"
    "v_add_f32 v216, v216, v185\n\t"
    "v_add_f32 v216, v216, v186\n\t"
    "v_mfma_f32_16x16x32_bf16 a[48:51], a[252:255], v[100:103], a[48:51]\n\t"
    "v_exp_f32 v187, v187\n\t"
    "v_mfma_f32_16x16x32_bf16 a[48:51], a[248:251], v[96:99], a[48:51]\n\t"
    "v_cvt_pk_bf16_f32 v147, v186, v187\n\t"
    "s_waitcnt lgkmcnt(0)\n\t"
    "v_mfma_f32_16x16x32_bf16 v[160:163], a[192:195], v[32:35], v[192:195]\n\t"
    "v_exp_f32 v188, v188\n\t"
    "v_mfma_f32_16x16x32_bf16 v[160:163], a[196:199], v[36:39], v[160:163]\n\t"
    "ds_read_b128 a[208:211], %[kb0] offset:28672\n\t"
    "v_add_f32 v216, v216, v187\n\t"
    "v_mfma_f32_16x16x32_bf16 v[160:163], a[200:203], v[40:43], v[160:163]\n\t"
    "v_exp_f32 v189, v189\n\t"
    "v_mfma_f32_16x16x32_bf16 v[160:163], a[204:207], v[44:47], v[160:163]\n\t"
    "ds_read_b128 a[212:215], %[kb1] offset:28672\n\t"
    "v_cvt_pk_bf16_f32 v154, v188, v189\n\t"
    "v_mfma_f32_16x16x32_bf16 v[164:167], a[204:207], v[60:63], v[196:199]\n\t"
    "v_exp_f32 v190, v190\n\t"
    "v_mfma_f32_16x16x32_bf16 v[164:167], a[200:203], v[56:59], v[164:167]\n\t"
    "ds_read_b128 a[216:219], %[kb2] offset:28672\n\t"
    "v_add_f32 v220, v220, v188\n\t"
    "v_mfma_f32_16x16x32_bf16 v[164:167], a[196:199], v[52:55], v[164:167]\n\t"
    "v_exp_f32 v191, v191\n\t"
    "v_mfma_f32_16x16x32_bf16 v[164:167], a[192:195], v[48:51], v[164:167]\n\t"
    "ds_read_b128 a[220:223], %[kb3] offset:28672\n\t"
    "v_cvt_pk_bf16_f32 v155, v190, v191\n\t"
    "v_mfma_f32_16x16x32_bf16 v[168:171], a[192:195], v[64:67], v[200:203]\n\t"
    "v_exp_f32 v160, v160\n\t"
    "v_mfma_f32_16x16x32_bf16 v[168:171], a[196:199], v[68:71], v[168:171]\n\t"
    "ds_read_b64_tr_b16 a[240:241], %[vb0] offset:1536\n\t"
    "v_add_f32 v220, v220, v189\n\t"
    "v_mfma_f32_16x16x32_bf16 v[168:171], a[200:203], v[72:75], v[168:171]\n\t"
    "v_exp_f32 v161, v161\n\t"
    "v_mfma_f32_16x16x32_bf16 v[168:171], a[204:207], v[76:79], v[168:171]\n\t"
    "ds_read_b64_tr_b16 a[242:243], %[vb0] offset:5632\n\t"
    "v_cvt_pk_bf16_f32 v132, v160, v161\n\t"
    "v_mfma_f32_16x16x32_bf16 v[172:175], a[204:207], v[92:95], v[204:207]\n\t"
    "v_exp_f32 v162, v162\n\t"
    "v_mfma_f32_16x16x32_bf16 v[172:175], a[200:203], v[88:91], v[172:175]\n\t"
    "ds_read_b64_tr_b16 a[244:245], %[vb0] offset:9728\n\t"
    "v_add_f32 v220, v220, v190\n\t"
    "v_mfma_f32_16x16x32_bf16 v[172:175], a[196:199], v[84:87], v[172:175]\n\t"
    "v_exp_f32 v163, v163\n\t"
    "v_mfma_f32_16x16x32_bf16 v[172:175], a[192:195], v[80:83], v[172:175]\n\t"
    "ds_read_b64_tr_b16 a[246:247], %[vb0] offset:13824\n\t"
    "v_cvt_pk_bf16_f32 v133, v162, v163\n\t"
    "v_mfma_f32_16x16x32_bf16 a[64:67], a[224:227], v[96:99], a[64:67]\n\t"
    "v_exp_f32 v164, v164\n\t"
    "v_mfma_f32_16x16x32_bf16 a[64:67], a[228:231], v[100:103], a[64:67]\n\t"
    "ds_read_b64_tr_b16 a[248:249], %[vb1] offset:1536\n\t"
    "v_add_f32 v220, v220, v191\n\t"
    "v_mfma_f32_16x16x32_bf16 a[68:71], a[228:231], v[108:111], a[68:71]\n\t"
    "v_exp_f32 v165, v165\n\t"
    "v_mfma_f32_16x16x32_bf16 a[68:71], a[224:227], v[104:107], a[68:71]\n\t"
    "ds_read_b64_tr_b16 a[250:251], %[vb1] offset:5632\n\t"
    "v_cvt_pk_bf16_f32 v140, v164, v165\n\t"
    "v_mfma_f32_16x16x32_bf16 a[72:75], a[224:227], v[112:115], a[72:75]\n\t"
    "v_exp_f32 v166, v166\n\t"
    "v_mfma_f32_16x16x32_bf16 a[72:75], a[228:231], v[116:119], a[72:75]\n\t"
    "ds_read_b64_tr_b16 a[252:253], %[vb1] offset:9728\n\t"
    "v_mfma_f32_16x16x32_bf16 a[76:79], a[228:231], v[124:127], a[76:79]\n\t"
    "v_exp_f32 v167, v167\n\t"
    "v_mfma_f32_16x16x32_bf16 a[76:79], a[224:227], v[120:123], a[76:79]\n\t"
    "ds_read_b64_tr_b16 a[254:255], %[vb1] offset:13824\n\t"
    "v_cvt_pk_bf16_f32 v141, v166, v167\n\t"
    "v_mfma_f32_16x16x32_bf16 a[92:95], a[232:235], v[120:123], a[92:95]\n\t"
    "v_exp_f32 v168, v168\n\t"
    "v_mfma_f32_16x16x32_bf16 a[92:95], a[236:239], v[124:127], a[92:95]\n\t"
    "buffer_load_dwordx4 %[vv0], %[srdv], s40 offen offset:2048 lds\n\t"
    "v_add_f32 v216, v216, v168\n\t"
    "v_mfma_f32_16x16x32_bf16 a[88:91], a[236:239], v[116:119], a[88:91]\n\t"
    "v_exp_f32 v169, v169\n\t"
    "v_mfma_f32_16x16x32_bf16 a[88:91], a[232:235], v[112:115], a[88:91]\n\t"
    "buffer_store_dwordx4 v[232:235], %[xl], %[srdp], s46 offen sc1\n\t"
    "v_cvt_pk_bf16_f32 v148, v168, v169\n\t"
    "v_mfma_f32_16x16x32_bf16 a[84:87], a[232:235], v[104:107], a[84:87]\n\t"
    "v_exp_f32 v170, v170\n\t"
    "v_mfma_f32_16x16x32_bf16 a[84:87], a[236:239], v[108:111], a[84:87]\n\t"
    "buffer_load_dwordx4 %[vv1], %[srdv], s40 offen offset:3072 lds\n\t"
    "v_add_f32 v216, v216, v169\n\t"
    "v_mfma_f32_16x16x32_bf16 a[80:83], a[236:239], v[100:103], a[80:83]\n\t"
    "v_exp_f32 v171, v171\n\t"
    "v_mfma_f32_16x16x32_bf16 a[80:83], a[232:235], v[96:99], a[80:83]\n\t"
    "v_add_f32 v216, v216, v170\n\t"
    "v_cvt_pk_bf16_f32 v149, v170, v171\n\t"
    "v_mfma_f32_16x16x32_bf16 v[212:215], v[236:239], v[104:107], v[212:215]\n\t"
    "v_exp_f32 v172, v172\n\t"
    "v_mfma_f32_16x16x32_bf16 v[212:215], v[236:239], v[108:111], v[212:215]\n\t"
    "s_waitcnt vmcnt(18) lgkmcnt(0)\n\t"
    "s_barrier\n\t"
    "v_mfma_f32_16x16x32_bf16 v[176:179], a[208:211], v[32:35], v[192:195]\n\t"
    "v_exp_f32 v173, v173\n\t"
    "v_mfma_f32_16x16x32_bf16 v[176:179], a[212:215], v[36:39], v[176:179]\n\t"
    "ds_read_b128 a[192:195], %[kb0] offset:32768\n\t"
    "s_add_u32 s40, s40, 16384\n\t"
    "v_mfma_f32_16x16x32_bf16 v[176:179], a[216:219], v[40:43], v[176:179]\n\t"
    "v_exp_f32 v174, v174\n\t"
    "v_mfma_f32_16x16x32_bf16 v[176:179], a[220:223], v[44:47], v[176:179]\n\t"
    "ds_read_b128 a[196:199], %[kb1] offset:32768\n\t"
    "s_add_u32 m0, %[m0b], 16384\n\t"
    "v_mfma_f32_16x16x32_bf16 v[180:183], a[220:223], v[60:63], v[196:199]\n\t"
    "v_exp_f32 v175, v175\n\t"
    "v_mfma_f32_16x16x32_bf16 v[180:183], a[216:219], v[56:59], v[180:183]\n\t"
    "ds_read_b128 a[200:203], %[kb2] offset:32768\n\t"
    "v_cvt_pk_bf16_f32 v156, v172, v173\n\t"
    "v_mfma_f32_16x16x32_bf16 v[180:183], a[212:215], v[52:55], v[180:183]\n\t"
    "ds_read_b128 a[204:207], %[kb3] offset:32768\n\t"
    "v_cvt_pk_bf16_f32 v157, v174, v175\n\t"
    "v_mfma_f32_16x16x32_bf16 v[180:183], a[208:211], v[48:51], v[180:183]\n\t"
    "ds_read_b64_tr_b16 a[224:225], %[vb0] offset:16384\n\t"
    "v_add_f32 v216, v216, v171\n\t"
    "v_mfma_f32_16x16x32_bf16 v[184:187], a[208:211], v[64:67], v[200:203]\n\t"
    "v_exp_f32 v176, v176\n\t"
    "v_mfma_f32_16x16x32_bf16 v[184:187], a[212:215], v[68:71], v[184:187]\n\t"
    "ds_read_b64_tr_b16 a[226:227], %[vb0] offset:20480\n\t"
    "v_add_f32 v220, v220, v172\n\t"
    "v_mfma_f32_16x16x32_bf16 v[184:187], a[216:219], v[72:75], v[184:187]\n\t"
    "v_exp_f32 v177, v177\n\t"
    "v_mfma_f32_16x16x32_bf16 v[184:187], a[220:223], v[76:79], v[184:187]\n\t"
    "ds_read_b64_tr_b16 a[228:229], %[vb0] offset:24576\n\t"
    "v_cvt_pk_bf16_f32 v134, v176, v177\n\t"
    "v_mfma_f32_16x16x32_bf16 v[188:191], a[220:223], v[92:95], v[204:207]\n\t"
    "v_exp_f32 v178, v178\n\t"
    "v_mfma_f32_16x16x32_bf16 v[188:191], a[216:219], v[88:91], v[188:191]\n\t"
    "ds_read_b64_tr_b16 a[230:231], %[vb0] offset:28672\n\t"
    "v_add_f32 v220, v220, v173\n\t"
    "v_mfma_f32_16x16x32_bf16 v[188:191], a[212:215], v[84:87], v[188:191]\n\t"
    "v_exp_f32 v179, v179\n\t"
    "v_mfma_f32_16x16x32_bf16 v[188:191], a[208:211], v[80:83], v[188:191]\n\t"
    "ds_read_b64_tr_b16 a[232:233], %[vb1] offset:16384\n\t"
    "v_cvt_pk_bf16_f32 v135, v178, v179\n\t"
    "v_mfma_f32_16x16x32_bf16 a[96:99], a[240:243], v[96:99], a[96:99]\n\t"
    "v_exp_f32 v180, v180\n\t"
    "v_mfma_f32_16x16x32_bf16 a[96:99], a[244:247], v[100:103], a[96:99]\n\t"
    "ds_read_b64_tr_b16 a[234:235], %[vb1] offset:20480\n\t"
    "v_add_f32 v220, v220, v174\n\t"
    "v_mfma_f32_16x16x32_bf16 a[100:103], a[244:247], v[108:111], a[100:103]\n\t"
    "v_exp_f32 v181, v181\n\t"
    "v_mfma_f32_16x16x32_bf16 a[100:103], a[240:243], v[104:107], a[100:103]\n\t"
    "ds_read_b64_tr_b16 a[236:237], %[vb1] offset:24576\n\t"
    "v_cvt_pk_bf16_f32 v142, v180, v181\n\t"
    "v_mfma_f32_16x16x32_bf16 a[104:107], a[240:243], v[112:115], a[104:107]\n\t"
    "v_exp_f32 v182, v182\n\t"
    "v_mfma_f32_16x16x32_bf16 a[104:107], a[244:247], v[116:119], a[104:107]\n\t"
    "ds_read_b64_tr_b16 a[238:239], %[vb1] offset:28672\n\t"
    "v_add_f32 v220, v220, v175\n\t"
    "v_mfma_f32_16x16x32_bf16 a[108:111], a[244:247], v[124:127], a[108:111]\n\t"
    "v_exp_f32 v183, v183\n\t"
    "v_mfma_f32_16x16x32_bf16 a[108:111], a[240:243], v[120:123], a[108:111]\n\t"
    "buffer_load_dwordx4 %[kv0], %[srdk1], s40 offen lds\n\t"
    "v_cvt_pk_bf16_f32 v143, v182, v183\n\t"
    "v_mfma_f32_16x16x32_bf16 a[124:127], a[248:251], v[120:123], a[124:127]\n\t"
    "v_exp_f32 v184, v184\n\t"
    "v_mfma_f32_16x16x32_bf16 a[124:127], a[252:255], v[124:127], a[124:127]\n\t"
    "v_add_f32 v216, v216, v184\n\t"
    "v_mfma_f32_16x16x32_bf16 a[120:123], a[252:255], v[116:119], a[120:123]\n\t"
    "v_exp_f32 v185, v185\n\t"
    "v_mfma_f32_16x16x32_bf16 a[120:123], a[248:251], v[112:115], a[120:123]\n\t"
    "buffer_load_dwordx4 %[kv1], %[srdk1], s40 offen offset:1024 lds\n\t"
    "v_cvt_pk_bf16_f32 v150, v184, v185\n\t"
    "v_mfma_f32_16x16x32_bf16 a[116:119], a[248:251], v[104:107], a[116:119]\n\t"
    "v_exp_f32 v186, v186\n\t"
    "v_mfma_f32_16x16x32_bf16 a[116:119], a[252:255], v[108:111], a[116:119]\n\t"
    "v_add_f32 v216, v216, v185\n\t"
    "v_add_f32 v216, v216, v186\n\t"
    "v_mfma_f32_16x16x32_bf16 a[112:115], a[252:255], v[100:103], a[112:115]\n\t"
    "v_exp_f32 v187, v187\n\t"
    "v_mfma_f32_16x16x32_bf16 a[112:115], a[248:251], v[96:99], a[112:115]\n\t"
    "v_cvt_pk_bf16_f32 v151, v186, v187\n\t"
    "s_waitcnt lgkmcnt(0)\n\t"
    "v_mfma_f32_16x16x32_bf16 v[160:163], a[192:195], v[32:35], v[192:195]\n\t"
    "v_exp_f32 v188, v188\n\t"
    "v_mfma_f32_16x16x32_bf16 v[160:163], a[196:199], v[36:39], v[160:163]\n\t"
    "ds_read_b128 a[208:211], %[kb0] offset:36864\n\t"
    "s_add_u32 s46, s46, 4096\n\t"
    "v_mfma_f32_16x16x32_bf16 v[160:163], a[200:203], v[40:43], v[160:163]\n\t"
    "v_exp_f32 v189, v189\n\t"
    "v_mfma_f32_16x16x32_bf16 v[160:163], a[204:207], v[44:47], v[160:163]\n\t"
    "ds_read_b128 a[212:215], %[kb1] offset:36864\n\t"
    "v_cvt_pk_bf16_f32 v158, v188, v189\n\t"
    "v_mfma_f32_16x16x32_bf16 v[164:167], a[204:207], v[60:63], v[196:199]\n\t"
    "v_exp_f32 v190, v190\n\t"
    "v_mfma_f32_16x16x32_bf16 v[164:167], a[200:203], v[56:59], v[164:167]\n\t"
    "ds_read_b128 a[216:219], %[kb2] offset:36864\n\t"
    "v_add_f32 v216, v216, v187\n\t"
    "v_mfma_f32_16x16x32_bf16 v[164:167], a[196:199], v[52:55], v[164:167]\n\t"
    "v_exp_f32 v191, v191\n\t"
    "v_mfma_f32_16x16x32_bf16 v[164:167], a[192:195], v[48:51], v[164:167]\n\t"
    "ds_read_b128 a[220:223], %[kb3] offset:36864\n\t"
    "v_cvt_pk_bf16_f32 v159, v190, v191\n\t"
    "v_mfma_f32_16x16x32_bf16 v[168:171], a[192:195], v[64:67], v[200:203]\n\t"
    "v_exp_f32 v160, v160\n\t"
    "v_mfma_f32_16x16x32_bf16 v[168:171], a[196:199], v[68:71], v[168:171]\n\t"
    "ds_read_b64_tr_b16 a[240:241], %[vb0] offset:16896\n\t"
    "v_add_f32 v220, v220, v188\n\t"
    "v_mfma_f32_16x16x32_bf16 v[168:171], a[200:203], v[72:75], v[168:171]\n\t"
    "v_exp_f32 v161, v161\n\t"
    "v_mfma_f32_16x16x32_bf16 v[168:171], a[204:207], v[76:79], v[168:171]\n\t"
    "ds_read_b64_tr_b16 a[242:243], %[vb0] offset:20992\n\t"
    "v_cvt_pk_bf16_f32 v96, v160, v161\n\t"
    "v_mfma_f32_16x16x32_bf16 v[172:175], a[204:207], v[92:95], v[204:207]\n\t"
    "v_exp_f32 v162, v162\n\t"
    "v_mfma_f32_16x16x32_bf16 v[172:175], a[200:203], v[88:91], v[172:175]\n\t"
    "ds_read_b64_tr_b16 a[244:245], %[vb0] offset:25088\n\t"
    "v_add_f32 v220, v220, v189\n\t"
    "v_mfma_f32_16x16x32_bf16 v[172:175], a[196:199], v[84:87], v[172:175]\n\t"
    "v_exp_f32 v163, v163\n\t"
    "v_mfma_f32_16x16x32_bf16 v[172:175], a[192:195], v[80:83], v[172:175]\n\t"
    "ds_read_b64_tr_b16 a[246:247], %[vb0] offset:29184\n\t"
    "v_cvt_pk_bf16_f32 v97, v162, v163\n\t"
    "v_mfma_f32_16x16x32_bf16 a[0:3], a[224:227], v[128:131], a[0:3]\n\t"
    "v_exp_f32 v164, v164\n\t"
    "v_mfma_f32_16x16x32_bf16 a[0:3], a[228:231], v[132:135], a[0:3]\n\t"
    "ds_read_b64_tr_b16 a[248:249], %[vb1] offset:16896\n\t"
    "v_add_f32 v220, v220, v190\n\t"
    "v_mfma_f32_16x16x32_bf16 a[4:7], a[228:231], v[140:143], a[4:7]\n\t"
    "v_exp_f32 v165, v165\n\t"
    "v_mfma_f32_16x16x32_bf16 a[4:7], a[224:227], v[136:139], a[4:7]\n\t"
    "ds_read_b64_tr_b16 a[250:251], %[vb1] offset:20992\n\t"
    "v_cvt_pk_bf16_f32 v104, v164, v165\n\t"
    "v_mfma_f32_16x16x32_bf16 a[8:11], a[224:227], v[144:147], a[8:11]\n\t"
    "v_exp_f32 v166, v166\n\t"
    "v_mfma_f32_16x16x32_bf16 a[8:11], a[228:231], v[148:151], a[8:11]\n\t"
    "ds_read_b64_tr_b16 a[252:253], %[vb1] offset:25088\n\t"
    "v_add_f32 v220, v220, v191\n\t"
    "v_mfma_f32_16x16x32_bf16 a[12:15], a[228:231], v[156:159], a[12:15]\n\t"
    "v_exp_f32 v167, v167\n\t"
    "v_mfma_f32_16x16x32_bf16 a[12:15], a[224:227], v[152:155], a[12:15]\n\t"
    "ds_read_b64_tr_b16 a[254:255], %[vb1] offset:29184\n\t"
    "v_cvt_pk_bf16_f32 v105, v166, v167\n\t"
    "v_mfma_f32_16x16x32_bf16 a[28:31], a[232:235], v[152:155], a[28:31]\n\t"
    "v_exp_f32 v168, v168\n\t"
    "v_mfma_f32_16x16x32_bf16 a[28:31], a[236:239], v[156:159], a[28:31]\n\t"
    "buffer_load_dwordx4 %[kv2], %[srdk1], s40 offen offset:2048 lds\n\t"
    "v_add_f32 v216, v216, v168\n\t"
    "v_mfma_f32_16x16x32_bf16 a[24:27], a[236:239], v[148:151], a[24:27]\n\t"
    "v_exp_f32 v169, v169\n\t"
    "v_mfma_f32_16x16x32_bf16 a[24:27], a[232:235], v[144:147], a[24:27]\n\t"
    "buffer_load_dword v227, %[fxv], %[srd], 0 offen sc1\n\t"
    "v_cvt_pk_bf16_f32 v112, v168, v169\n\t"
    "v_mfma_f32_16x16x32_bf16 a[20:23], a[232:235], v[136:139], a[20:23]\n\t"
    "v_exp_f32 v170, v170\n\t"
    "v_mfma_f32_16x16x32_bf16 a[20:23], a[236:239], v[140:143], a[20:23]\n\t"
    "buffer_load_dwordx4 %[kv3], %[srdk1], s40 offen offset:3072 lds\n\t"
    "v_add_f32 v216, v216, v169\n\t"
    "v_mfma_f32_16x16x32_bf16 a[16:19], a[236:239], v[132:135], a[16:19]\n\t"
    "v_exp_f32 v171, v171\n\t"
    "v_mfma_f32_16x16x32_bf16 a[16:19], a[232:235], v[128:131], a[16:19]\n\t"
    "v_add_f32 v216, v216, v170\n\t"
    "v_cvt_pk_bf16_f32 v113, v170, v171\n\t"
    "v_mfma_f32_16x16x32_bf16 v[208:211], v[236:239], v[128:131], v[208:211]\n\t"
    "v_exp_f32 v172, v172\n\t"
    "v_mfma_f32_16x16x32_bf16 v[208:211], v[236:239], v[132:135], v[208:211]\n\t"
    "v_add_f32 v216, v216, v171\n\t"
    "s_waitcnt lgkmcnt(0)\n\t"
    "v_mfma_f32_16x16x32_bf16 v[176:179], a[208:211], v[32:35], v[192:195]\n\t"
    "v_exp_f32 v173, v173\n\t"
    "v_mfma_f32_16x16x32_bf16 v[176:179], a[212:215], v[36:39], v[176:179]\n\t"
    "ds_read_b128 a[192:195], %[kb0] offset:40960\n\t"
    "s_add_u32 m0, %[m0b], 65536\n\t"
    "v_mfma_f32_16x16x32_bf16 v[176:179], a[216:219], v[40:43], v[176:179]\n\t"
    "v_exp_f32 v174, v174\n\t"
    "v_mfma_f32_16x16x32_bf16 v[176:179], a[220:223], v[44:47], v[176:179]\n\t"
    "ds_read_b128 a[196:199], %[kb1] offset:40960\n\t"
    "v_cvt_pk_bf16_f32 v120, v172, v173\n\t"
    "v_mfma_f32_16x16x32_bf16 v[180:183], a[220:223], v[60:63], v[196:199]\n\t"
    "v_exp_f32 v175, v175\n\t"
    "v_mfma_f32_16x16x32_bf16 v[180:183], a[216:219], v[56:59], v[180:183]\n\t"
    "ds_read_b128 a[200:203], %[kb2] offset:40960\n\t"
    "v_cvt_pk_bf16_f32 v121, v174, v175\n\t"
    "v_mfma_f32_16x16x32_bf16 v[180:183], a[212:215], v[52:55], v[180:183]\n\t"
    "ds_read_b128 a[204:207], %[kb3] offset:40960\n\t"
    "v_add_f32 v220, v220, v172\n\t"
    "v_mfma_f32_16x16x32_bf16 v[180:183], a[208:211], v[48:51], v[180:183]\n\t"
    "ds_read_b64_tr_b16 a[224:225], %[vb0] offset:17408\n\t"
    "v_add_f32 v220, v220, v173\n\t"
    "v_mfma_f32_16x16x32_bf16 v[184:187], a[208:211], v[64:67], v[200:203]\n\t"
    "v_exp_f32 v176, v176\n\t"
    "v_mfma_f32_16x16x32_bf16 v[184:187], a[212:215], v[68:71], v[184:187]\n\t"
    "ds_read_b64_tr_b16 a[226:227], %[vb0] offset:21504\n\t"
    "v_add_f32 v220, v220, v174\n\t"
    "v_mfma_f32_16x16x32_bf16 v[184:187], a[216:219], v[72:75], v[184:187]\n\t"
    "v_exp_f32 v177, v177\n\t"
    "v_mfma_f32_16x16x32_bf16 v[184:187], a[220:223], v[76:79], v[184:187]\n\t"
    "ds_read_b64_tr_b16 a[228:229], %[vb0] offset:25600\n\t"
    "v_cvt_pk_bf16_f32 v98, v176, v177\n\t"
    "v_mfma_f32_16x16x32_bf16 v[188:191], a[220:223], v[92:95], v[204:207]\n\t"
    "v_exp_f32 v178, v178\n\t"
    "v_mfma_f32_16x16x32_bf16 v[188:191], a[216:219], v[88:91], v[188:191]\n\t"
    "ds_read_b64_tr_b16 a[230:231], %[vb0] offset:29696\n\t"
    "v_add_f32 v220, v220, v175\n\t"
    "v_mfma_f32_16x16x32_bf16 v[188:191], a[212:215], v[84:87], v[188:191]\n\t"
    "v_exp_f32 v179, v179\n\t"
    "v_mfma_f32_16x16x32_bf16 v[188:191], a[208:211], v[80:83], v[188:191]\n\t"
    "ds_read_b64_tr_b16 a[232:233], %[vb1] offset:17408\n\t"
    "v_cvt_pk_bf16_f32 v99, v178, v179\n\t"
    "v_mfma_f32_16x16x32_bf16 a[32:35], a[240:243], v[128:131], a[32:35]\n\t"
    "v_exp_f32 v180, v180\n\t"
    "v_mfma_f32_16x16x32_bf16 a[32:35], a[244:247], v[132:135], a[32:35]\n\t"
    "ds_read_b64_tr_b16 a[234:235], %[vb1] offset:21504\n\t"
    "v_mfma_f32_16x16x32_bf16 a[36:39], a[244:247], v[140:143], a[36:39]\n\t"
    "v_exp_f32 v181, v181\n\t"
    "v_mfma_f32_16x16x32_bf16 a[36:39], a[240:243], v[136:139], a[36:39]\n\t"
    "ds_read_b64_tr_b16 a[236:237], %[vb1] offset:25600\n\t"
    "v_cvt_pk_bf16_f32 v106, v180, v181\n\t"
    "v_mfma_f32_16x16x32_bf16 a[40:43], a[240:243], v[144:147], a[40:43]\n\t"
    "v_exp_f32 v182, v182\n\t"
    "v_mfma_f32_16x16x32_bf16 a[40:43], a[244:247], v[148:151], a[40:43]\n\t"
    "ds_read_b64_tr_b16 a[238:239], %[vb1] offset:29696\n\t"
    "v_mfma_f32_16x16x32_bf16 a[44:47], a[244:247], v[156:159], a[44:47]\n\t"
    "v_exp_f32 v183, v183\n\t"
    "v_mfma_f32_16x16x32_bf16 a[44:47], a[240:243], v[152:155], a[44:47]\n\t"
    "buffer_load_dwordx4 %[vv0], %[srdv], s40 offen lds\n\t"
    "v_cvt_pk_bf16_f32 v107, v182, v183\n\t"
    "v_mfma_f32_16x16x32_bf16 a[60:63], a[248:251], v[152:155], a[60:63]\n\t"
    "v_exp_f32 v184, v184\n\t"
    "v_mfma_f32_16x16x32_bf16 a[60:63], a[252:255], v[156:159], a[60:63]\n\t"
    "buffer_load_dword v224, %[xl], %[srdu], s46 offen\n\t"
    "v_add_f32 v216, v216, v184\n\t"
    "v_mfma_f32_16x16x32_bf16 a[56:59], a[252:255], v[148:151], a[56:59]\n\t"
    "v_exp_f32 v185, v185\n\t"
    "v_mfma_f32_16x16x32_bf16 a[56:59], a[248:251], v[144:147], a[56:59]\n\t"
    "buffer_load_dwordx4 %[vv1], %[srdv], s40 offen offset:1024 lds\n\t"
    "v_cvt_pk_bf16_f32 v114, v184, v185\n\t"
    "v_mfma_f32_16x16x32_bf16 a[52:55], a[248:251], v[136:139], a[52:55]\n\t"
    "v_exp_f32 v186, v186\n\t"
    "v_mfma_f32_16x16x32_bf16 a[52:55], a[252:255], v[140:143], a[52:55]\n\t"
    "v_add_f32 v216, v216, v185\n\t"
    "v_add_f32 v216, v216, v186\n\t"
    "v_mfma_f32_16x16x32_bf16 a[48:51], a[252:255], v[132:135], a[48:51]\n\t"
    "v_exp_f32 v187, v187\n\t"
    "v_mfma_f32_16x16x32_bf16 a[48:51], a[248:251], v[128:131], a[48:51]\n\t"
    "v_cvt_pk_bf16_f32 v115, v186, v187\n\t"
    "s_waitcnt lgkmcnt(0)\n\t"
    "v_mfma_f32_16x16x32_bf16 v[160:163], a[192:195], v[32:35], v[192:195]\n\t"
    "v_exp_f32 v188, v188\n\t"
    "v_mfma_f32_16x16x32_bf16 v[160:163], a[196:199], v[36:39], v[160:163]\n\t"
    "ds_read_b128 a[208:211], %[kb0] offset:45056\n\t"
    "v_add_f32 v216, v216, v187\n\t"
    "v_mfma_f32_16x16x32_bf16 v[160:163], a[200:203], v[40:43], v[160:163]\n\t"
    "v_exp_f32 v189, v189\n\t"
    "v_mfma_f32_16x16x32_bf16 v[160:163], a[204:207], v[44:47], v[160:163]\n\t"
    "ds_read_b128 a[212:215], %[kb1] offset:45056\n\t"
    "v_cvt_pk_bf16_f32 v122, v188, v189\n\t"
    "v_mfma_f32_16x16x32_bf16 v[164:167], a[204:207], v[60:63], v[196:199]\n\t"
    "v_exp_f32 v190, v190\n\t"
    "v_mfma_f32_16x16x32_bf16 v[164:167], a[200:203], v[56:59], v[164:167]\n\t"
    "ds_read_b128 a[216:219], %[kb2] offset:45056\n\t"
    "v_add_f32 v220, v220, v188\n\t"
    "v_mfma_f32_16x16x32_bf16 v[164:167], a[196:199], v[52:55], v[164:167]\n\t"
    "v_exp_f32 v191, v191\n\t"
    "v_mfma_f32_16x16x32_bf16 v[164:167], a[192:195], v[48:51], v[164:167]\n\t"
    "ds_read_b128 a[220:223], %[kb3] offset:45056\n\t"
    "v_cvt_pk_bf16_f32 v123, v190, v191\n\t"
    "v_mfma_f32_16x16x32_bf16 v[168:171], a[192:195], v[64:67], v[200:203]\n\t"
    "v_exp_f32 v160, v160\n\t"
    "v_mfma_f32_16x16x32_bf16 v[168:171], a[196:199], v[68:71], v[168:171]\n\t"
    "ds_read_b64_tr_b16 a[240:241], %[vb0] offset:17920\n\t"
    "v_add_f32 v220, v220, v189\n\t"
    "v_mfma_f32_16x16x32_bf16 v[168:171], a[200:203], v[72:75], v[168:171]\n\t"
    "v_exp_f32 v161, v161\n\t"
    "v_mfma_f32_16x16x32_bf16 v[168:171], a[204:207], v[76:79], v[168:171]\n\t"
    "ds_read_b64_tr_b16 a[242:243], %[vb0] offset:22016\n\t"
    "v_cvt_pk_bf16_f32 v100, v160, v161\n\t"
    "v_mfma_f32_16x16x32_bf16 v[172:175], a[204:207], v[92:95], v[204:207]\n\t"
    "v_exp_f32 v162, v162\n\t"
    "v_mfma_f32_16x16x32_bf16 v[172:175], a[200:203], v[88:91], v[172:175]\n\t"
    "ds_read_b64_tr_b16 a[244:245], %[vb0] offset:26112\n\t"
    "v_add_f32 v220, v220, v190\n\t"
    "v_mfma_f32_16x16x32_bf16 v[172:175], a[196:199], v[84:87], v[172:175]\n\t"
    "v_exp_f32 v163, v163\n\t"
    "v_mfma_f32_16x16x32_bf16 v[172:175], a[192:195], v[80:83], v[172:175]\n\t"
    "ds_read_b64_tr_b16 a[246:247], %[vb0] offset:30208\n\t"
    "v_cvt_pk_bf16_f32 v101, v162, v163\n\t"
    "v_mfma_f32_16x16x32_bf16 a[64:67], a[224:227], v[128:131], a[64:67]\n\t"
    "v_exp_f32 v164, v164\n\t"
    "v_mfma_f32_16x16x32_bf16 a[64:67], a[228:231], v[132:135], a[64:67]\n\t"
    "ds_read_b64_tr_b16 a[248:249], %[vb1] offset:17920\n\t"
    "v_add_f32 v220, v220, v191\n\t"
    "v_mfma_f32_16x16x32_bf16 a[68:71], a[228:231], v[140:143], a[68:71]\n\t"
    "v_exp_f32 v165, v165\n\t"
    "v_mfma_f32_16x16x32_bf16 a[68:71], a[224:227], v[136:139], a[68:71]\n\t"
    "ds_read_b64_tr_b16 a[250:251], %[vb1] offset:22016\n\t"
    "v_cvt_pk_bf16_f32 v108, v164, v165\n\t"
    "v_mfma_f32_16x16x32_bf16 a[72:75], a[224:227], v[144:147], a[72:75]\n\t"
    "v_exp_f32 v166, v166\n\t"
    "v_mfma_f32_16x16x32_bf16 a[72:75], a[228:231], v[148:151], a[72:75]\n\t"
    "ds_read_b64_tr_b16 a[252:253], %[vb1] offset:26112\n\t"
    "v_mfma_f32_16x16x32_bf16 a[76:79], a[228:231], v[156:159], a[76:79]\n\t"
    "v_exp_f32 v167, v167\n\t"
    "v_mfma_f32_16x16x32_bf16 a[76:79], a[224:227], v[152:155], a[76:79]\n\t"
    "ds_read_b64_tr_b16 a[254:255], %[vb1] offset:30208\n\t"
    "v_cvt_pk_bf16_f32 v109, v166, v167\n\t"
    "v_mfma_f32_16x16x32_bf16 a[92:95], a[232:235], v[152:155], a[92:95]\n\t"
    "v_exp_f32 v168, v168\n\t"
    "v_mfma_f32_16x16x32_bf16 a[92:95], a[236:239], v[156:159], a[92:95]\n\t"
    "buffer_load_dwordx4 %[vv0], %[srdv], s40 offen offset:2048 lds\n\t"
    "v_add_f32 v216, v216, v168\n\t"
    "v_mfma_f32_16x16x32_bf16 a[88:91], a[236:239], v[148:151], a[88:91]\n\t"
    "v_exp_f32 v169, v169\n\t"
    "v_mfma_f32_16x16x32_bf16 a[88:91], a[232:235], v[144:147], a[88:91]\n\t"
    "buffer_store_dwordx4 v[232:235], %[xl], %[srdp], s46 offen sc1\n\t"
    "v_cvt_pk_bf16_f32 v116, v168, v169\n\t"
    "v_mfma_f32_16x16x32_bf16 a[84:87], a[232:235], v[136:139], a[84:87]\n\t"
    "v_exp_f32 v170, v170\n\t"
    "v_mfma_f32_16x16x32_bf16 a[84:87], a[236:239], v[140:143], a[84:87]\n\t"
    "buffer_load_dwordx4 %[vv1], %[srdv], s40 offen offset:3072 lds\n\t"
    "v_add_f32 v216, v216, v169\n\t"
    "v_mfma_f32_16x16x32_bf16 a[80:83], a[236:239], v[132:135], a[80:83]\n\t"
    "v_exp_f32 v171, v171\n\t"
    "v_mfma_f32_16x16x32_bf16 a[80:83], a[232:235], v[128:131], a[80:83]\n\t"
    "v_add_f32 v216, v216, v170\n\t"
    "v_cvt_pk_bf16_f32 v117, v170, v171\n\t"
    "v_mfma_f32_16x16x32_bf16 v[212:215], v[236:239], v[136:139], v[212:215]\n\t"
    "v_exp_f32 v172, v172\n\t"
    "v_mfma_f32_16x16x32_bf16 v[212:215], v[236:239], v[140:143], v[212:215]\n\t"
    "s_waitcnt vmcnt(18) lgkmcnt(0)\n\t"
    "s_barrier\n\t"
    "v_mfma_f32_16x16x32_bf16 v[176:179], a[208:211], v[32:35], v[192:195]\n\t"
    "v_exp_f32 v173, v173\n\t"
    "v_mfma_f32_16x16x32_bf16 v[176:179], a[212:215], v[36:39], v[176:179]\n\t"
    "ds_read_b128 a[192:195], %[kb0] offset:49152\n\t"
    "s_add_u32 s40, s40, 16384\n\t"
    "v_mfma_f32_16x16x32_bf16 v[176:179], a[216:219], v[40:43], v[176:179]\n\t"
    "v_exp_f32 v174, v174\n\t"
    "v_mfma_f32_16x16x32_bf16 v[176:179], a[220:223], v[44:47], v[176:179]\n\t"
    "ds_read_b128 a[196:199], %[kb1] offset:49152\n\t"
    "s_add_u32 m0, %[m0b], 32768\n\t"
    "v_mfma_f32_16x16x32_bf16 v[180:183], a[220:223], v[60:63], v[196:199]\n\t"
    "v_exp_f32 v175, v175\n\t"
    "v_mfma_f32_16x16x32_bf16 v[180:183], a[216:219], v[56:59], v[180:183]\n\t"
    "ds_read_b128 a[200:203], %[kb2] offset:49152\n\t"
    "v_cvt_pk_bf16_f32 v124, v172, v173\n\t"
    "v_mfma_f32_16x16x32_bf16 v[180:183], a[212:215], v[52:55], v[180:183]\n\t"
    "ds_read_b128 a[204:207], %[kb3] offset:49152\n\t"
    "v_cvt_pk_bf16_f32 v125, v174, v175\n\t"
    "v_mfma_f32_16x16x32_bf16 v[180:183], a[208:211], v[48:51], v[180:183]\n\t"
    "ds_read_b64_tr_b16 a[224:225], %[vb0] offset:32768\n\t"
    "v_add_f32 v216, v216, v171\n\t"
    "v_mfma_f32_16x16x32_bf16 v[184:187], a[208:211], v[64:67], v[200:203]\n\t"
    "v_exp_f32 v176, v176\n\t"
    "v_mfma_f32_16x16x32_bf16 v[184:187], a[212:215], v[68:71], v[184:187]\n\t"
    "ds_read_b64_tr_b16 a[226:227], %[vb0] offset:36864\n\t"
    "v_add_f32 v220, v220, v172\n\t"
    "v_mfma_f32_16x16x32_bf16 v[184:187], a[216:219], v[72:75], v[184:187]\n\t"
    "v_exp_f32 v177, v177\n\t"
    "v_mfma_f32_16x16x32_bf16 v[184:187], a[220:223], v[76:79], v[184:187]\n\t"
    "ds_read_b64_tr_b16 a[228:229], %[vb0] offset:40960\n\t"
    "v_cvt_pk_bf16_f32 v102, v176, v177\n\t"
    "v_mfma_f32_16x16x32_bf16 v[188:191], a[220:223], v[92:95], v[204:207]\n\t"
    "v_exp_f32 v178, v178\n\t"
    "v_mfma_f32_16x16x32_bf16 v[188:191], a[216:219], v[88:91], v[188:191]\n\t"
    "ds_read_b64_tr_b16 a[230:231], %[vb0] offset:45056\n\t"
    "v_add_f32 v220, v220, v173\n\t"
    "v_mfma_f32_16x16x32_bf16 v[188:191], a[212:215], v[84:87], v[188:191]\n\t"
    "v_exp_f32 v179, v179\n\t"
    "v_mfma_f32_16x16x32_bf16 v[188:191], a[208:211], v[80:83], v[188:191]\n\t"
    "ds_read_b64_tr_b16 a[232:233], %[vb1] offset:32768\n\t"
    "v_cvt_pk_bf16_f32 v103, v178, v179\n\t"
    "v_mfma_f32_16x16x32_bf16 a[96:99], a[240:243], v[128:131], a[96:99]\n\t"
    "v_exp_f32 v180, v180\n\t"
    "v_mfma_f32_16x16x32_bf16 a[96:99], a[244:247], v[132:135], a[96:99]\n\t"
    "ds_read_b64_tr_b16 a[234:235], %[vb1] offset:36864\n\t"
    "v_add_f32 v220, v220, v174\n\t"
    "v_mfma_f32_16x16x32_bf16 a[100:103], a[244:247], v[140:143], a[100:103]\n\t"
    "v_exp_f32 v181, v181\n\t"
    "v_mfma_f32_16x16x32_bf16 a[100:103], a[240:243], v[136:139], a[100:103]\n\t"
    "ds_read_b64_tr_b16 a[236:237], %[vb1] offset:40960\n\t"
    "v_cvt_pk_bf16_f32 v110, v180, v181\n\t"
    "v_mfma_f32_16x16x32_bf16 a[104:107], a[240:243], v[144:147], a[104:107]\n\t"
    "v_exp_f32 v182, v182\n\t"
    "v_mfma_f32_16x16x32_bf16 a[104:107], a[244:247], v[148:151], a[104:107]\n\t"
    "ds_read_b64_tr_b16 a[238:239], %[vb1] offset:45056\n\t"
    "v_add_f32 v220, v220, v175\n\t"
    "v_mfma_f32_16x16x32_bf16 a[108:111], a[244:247], v[156:159], a[108:111]\n\t"
    "v_exp_f32 v183, v183\n\t"
    "v_mfma_f32_16x16x32_bf16 a[108:111], a[240:243], v[152:155], a[108:111]\n\t"
    "buffer_load_dwordx4 %[kv0], %[srdk1], s40 offen lds\n\t"
    "v_cvt_pk_bf16_f32 v111, v182, v183\n\t"
    "v_mfma_f32_16x16x32_bf16 a[124:127], a[248:251], v[152:155], a[124:127]\n\t"
    "v_exp_f32 v184, v184\n\t"
    "v_mfma_f32_16x16x32_bf16 a[124:127], a[252:255], v[156:159], a[124:127]\n\t"
    "v_add_f32 v216, v216, v184\n\t"
    "v_mfma_f32_16x16x32_bf16 a[120:123], a[252:255], v[148:151], a[120:123]\n\t"
    "v_exp_f32 v185, v185\n\t"
    "v_mfma_f32_16x16x32_bf16 a[120:123], a[248:251], v[144:147], a[120:123]\n\t"
    "buffer_load_dwordx4 %[kv1], %[srdk1], s40 offen offset:1024 lds\n\t"
    "v_cvt_pk_bf16_f32 v118, v184, v185\n\t"
    "v_mfma_f32_16x16x32_bf16 a[116:119], a[248:251], v[136:139], a[116:119]\n\t"
    "v_exp_f32 v186, v186\n\t"
    "v_mfma_f32_16x16x32_bf16 a[116:119], a[252:255], v[140:143], a[116:119]\n\t"
    "v_add_f32 v216, v216, v185\n\t"
    "v_add_f32 v216, v216, v186\n\t"
    "v_mfma_f32_16x16x32_bf16 a[112:115], a[252:255], v[132:135], a[112:115]\n\t"
    "v_exp_f32 v187, v187\n\t"
    "v_mfma_f32_16x16x32_bf16 a[112:115], a[248:251], v[128:131], a[112:115]\n\t"
    "v_cvt_pk_bf16_f32 v119, v186, v187\n\t"
    "s_waitcnt lgkmcnt(0)\n\t"
    "v_mfma_f32_16x16x32_bf16 v[160:163], a[192:195], v[32:35], v[192:195]\n\t"
    "v_exp_f32 v188, v188\n\t"
    "v_mfma_f32_16x16x32_bf16 v[160:163], a[196:199], v[36:39], v[160:163]\n\t"
    "ds_read_b128 a[208:211], %[kb0] offset:53248\n\t"
    "s_add_u32 s46, s46, 4096\n\t"
    "v_mfma_f32_16x16x32_bf16 v[160:163], a[200:203], v[40:43], v[160:163]\n\t"
    "v_exp_f32 v189, v189\n\t"
    "v_mfma_f32_16x16x32_bf16 v[160:163], a[204:207], v[44:47], v[160:163]\n\t"
    "ds_read_b128 a[212:215], %[kb1] offset:53248\n\t"
    "v_cvt_pk_bf16_f32 v126, v188, v189\n\t"
    "v_mfma_f32_16x16x32_bf16 v[164:167], a[204:207], v[60:63], v[196:199]\n\t"
    "v_exp_f32 v190, v190\n\t"
    "v_mfma_f32_16x16x32_bf16 v[164:167], a[200:203], v[56:59], v[164:167]\n\t"
    "ds_read_b128 a[216:219], %[kb2] offset:53248\n\t"
    "v_add_f32 v216, v216, v187\n\t"
    "v_mfma_f32_16x16x32_bf16 v[164:167], a[196:199], v[52:55], v[164:167]\n\t"
    "v_exp_f32 v191, v191\n\t"
    "v_mfma_f32_16x16x32_bf16 v[164:167], a[192:195], v[48:51], v[164:167]\n\t"
    "ds_read_b128 a[220:223], %[kb3] offset:53248\n\t"
    "v_cvt_pk_bf16_f32 v127, v190, v191\n\t"
    "v_mfma_f32_16x16x32_bf16 v[168:171], a[192:195], v[64:67], v[200:203]\n\t"
    "v_exp_f32 v160, v160\n\t"
    "v_mfma_f32_16x16x32_bf16 v[168:171], a[196:199], v[68:71], v[168:171]\n\t"
    "ds_read_b64_tr_b16 a[240:241], %[vb0] offset:33280\n\t"
    "v_add_f32 v220, v220, v188\n\t"
    "v_mfma_f32_16x16x32_bf16 v[168:171], a[200:203], v[72:75], v[168:171]\n\t"
    "v_exp_f32 v161, v161\n\t"
    "v_mfma_f32_16x16x32_bf16 v[168:171], a[204:207], v[76:79], v[168:171]\n\t"
    "ds_read_b64_tr_b16 a[242:243], %[vb0] offset:37376\n\t"
    "v_cvt_pk_bf16_f32 v128, v160, v161\n\t"
    "v_mfma_f32_16x16x32_bf16 v[172:175], a[204:207], v[92:95], v[204:207]\n\t"
    "v_exp_f32 v162, v162\n\t"
    "v_mfma_f32_16x16x32_bf16 v[172:175], a[200:203], v[88:91], v[172:175]\n\t"
    "ds_read_b64_tr_b16 a[244:245], %[vb0] offset:41472\n\t"
    "v_add_f32 v220, v220, v189\n\t"
    "v_mfma_f32_16x16x32_bf16 v[172:175], a[196:199], v[84:87], v[172:175]\n\t"
    "v_exp_f32 v163, v163\n\t"
    "v_mfma_f32_16x16x32_bf16 v[172:175], a[192:195], v[80:83], v[172:175]\n\t"
    "ds_read_b64_tr_b16 a[246:247], %[vb0] offset:45568\n\t"
    "v_cvt_pk_bf16_f32 v129, v162, v163\n\t"
    "v_mfma_f32_16x16x32_bf16 a[0:3], a[224:227], v[96:99], a[0:3]\n\t"
    "v_exp_f32 v164, v164\n\t"
    "v_mfma_f32_16x16x32_bf16 a[0:3], a[228:231], v[100:103], a[0:3]\n\t"
    "ds_read_b64_tr_b16 a[248:249], %[vb1] offset:33280\n\t"
    "v_add_f32 v220, v220, v190\n\t"
    "v_mfma_f32_16x16x32_bf16 a[4:7], a[228:231], v[108:111], a[4:7]\n\t"
    "v_exp_f32 v165, v165\n\t"
    "v_mfma_f32_16x16x32_bf16 a[4:7], a[224:227], v[104:107], a[4:7]\n\t"
    "ds_read_b64_tr_b16 a[250:251], %[vb1] offset:37376\n\t"
    "v_cvt_pk_bf16_f32 v136, v164, v165\n\t"
    "v_mfma_f32_16x16x32_bf16 a[8:11], a[224:227], v[112:115], a[8:11]\n\t"
    "v_exp_f32 v166, v166\n\t"
    "v_mfma_f32_16x16x32_bf16 a[8:11], a[228:231], v[116:119], a[8:11]\n\t"
    "ds_read_b64_tr_b16 a[252:253], %[vb1] offset:41472\n\t"
    "v_add_f32 v220, v220, v191\n\t"
    "v_mfma_f32_16x16x32_bf16 a[12:15], a[228:231], v[124:127], a[12:15]\n\t"
    "v_exp_f32 v167, v167\n\t"
    "v_mfma_f32_16x16x32_bf16 a[12:15], a[224:227], v[120:123], a[12:15]\n\t"
    "ds_read_b64_tr_b16 a[254:255], %[vb1] offset:45568\n\t"
    "v_cvt_pk_bf16_f32 v137, v166, v167\n\t"
    "v_mfma_f32_16x16x32_bf16 a[28:31], a[232:235], v[120:123], a[28:31]\n\t"
    "v_exp_f32 v168, v168\n\t"
    "v_mfma_f32_16x16x32_bf16 a[28:31], a[236:239], v[124:127], a[28:31]\n\t"
    "buffer_load_dwordx4 %[kv2], %[srdk1], s40 offen offset:2048 lds\n\t"
    "v_add_f32 v216, v216, v168\n\t"
    "v_mfma_f32_16x16x32_bf16 a[24:27], a[236:239], v[116:119], a[24:27]\n\t"
    "v_exp_f32 v169, v169\n\t"
    "v_mfma_f32_16x16x32_bf16 a[24:27], a[232:235], v[112:115], a[24:27]\n\t"
    "v_cvt_pk_bf16_f32 v144, v168, v169\n\t"
    "v_add_f32 v216, v216, v169\n\t"
    "v_mfma_f32_16x16x32_bf16 a[20:23], a[232:235], v[104:107], a[20:23]\n\t"
    "v_exp_f32 v170, v170\n\t"
    "v_mfma_f32_16x16x32_bf16 a[20:23], a[236:239], v[108:111], a[20:23]\n\t"
    "buffer_load_dwordx4 %[kv3], %[srdk1], s40 offen offset:3072 lds\n\t"
    "v_add_f32 v216, v216, v170\n\t"
    "v_mfma_f32_16x16x32_bf16 a[16:19], a[236:239], v[100:103], a[16:19]\n\t"
    "v_exp_f32 v171, v171\n\t"
    "v_mfma_f32_16x16x32_bf16 a[16:19], a[232:235], v[96:99], a[16:19]\n\t"
    "v_cvt_pk_bf16_f32 v145, v170, v171\n\t"
    "v_add_f32 v216, v216, v171\n\t"
    "v_mfma_f32_16x16x32_bf16 v[208:211], v[236:239], v[96:99], v[208:211]\n\t"
    "v_exp_f32 v172, v172\n\t"
    "v_mfma_f32_16x16x32_bf16 v[208:211], v[236:239], v[100:103], v[208:211]\n\t"
    "v_add_f32 v220, v220, v172\n\t"
    "s_waitcnt lgkmcnt(0)\n\t"
    "v_mfma_f32_16x16x32_bf16 v[176:179], a[208:211], v[32:35], v[192:195]\n\t"
    "v_exp_f32 v173, v173\n\t"
    "v_mfma_f32_16x16x32_bf16 v[176:179], a[212:215], v[36:39], v[176:179]\n\t"
    "ds_read_b128 a[192:195], %[kb0] offset:57344\n\t"
    "s_add_u32 m0, %[m0b], 81920\n\t"
    "v_mfma_f32_16x16x32_bf16 v[176:179], a[216:219], v[40:43], v[176:179]\n\t"
    "v_exp_f32 v174, v174\n\t"
    "v_mfma_f32_16x16x32_bf16 v[176:179], a[220:223], v[44:47], v[176:179]\n\t"
    "ds_read_b128 a[196:199], %[kb1] offset:57344\n\t"
    "v_cvt_pk_bf16_f32 v152, v172, v173\n\t"
    "v_mfma_f32_16x16x32_bf16 v[180:183], a[220:223], v[60:63], v[196:199]\n\t"
    "v_exp_f32 v175, v175\n\t"
    "v_mfma_f32_16x16x32_bf16 v[180:183], a[216:219], v[56:59], v[180:183]\n\t"
    "ds_read_b128 a[200:203], %[kb2] offset:57344\n\t"
    "v_cvt_pk_bf16_f32 v153, v174, v175\n\t"
    "v_mfma_f32_16x16x32_bf16 v[180:183], a[212:215], v[52:55], v[180:183]\n\t"
    "ds_read_b128 a[204:207], %[kb3] offset:57344\n\t"
    "v_add_f32 v220, v220, v173\n\t"
    "v_mfma_f32_16x16x32_bf16 v[180:183], a[208:211], v[48:51], v[180:183]\n\t"
    "ds_read_b64_tr_b16 a[224:225], %[vb0] offset:33792\n\t"
    "v_add_f32 v220, v220, v174\n\t"
    "v_mfma_f32_16x16x32_bf16 v[184:187], a[208:211], v[64:67], v[200:203]\n\t"
    "v_exp_f32 v176, v176\n\t"
    "v_mfma_f32_16x16x32_bf16 v[184:187], a[212:215], v[68:71], v[184:187]\n\t"
    "ds_read_b64_tr_b16 a[226:227], %[vb0] offset:37888\n\t"
    "v_add_f32 v220, v220, v175\n\t"
    "v_mfma_f32_16x16x32_bf16 v[184:187], a[216:219], v[72:75], v[184:187]\n\t"
    "v_exp_f32 v177, v177\n\t"
    "v_mfma_f32_16x16x32_bf16 v[184:187], a[220:223], v[76:79], v[184:187]\n\t"
    "ds_read_b64_tr_b16 a[228:229], %[vb0] offset:41984\n\t"
    "v_cvt_pk_bf16_f32 v130, v176, v177\n\t"
    "v_mfma_f32_16x16x32_bf16 v[188:191], a[220:223], v[92:95], v[204:207]\n\t"
    "v_exp_f32 v178, v178\n\t"
    "v_mfma_f32_16x16x32_bf16 v[188:191], a[216:219], v[88:91], v[188:191]\n\t"
    "ds_read_b64_tr_b16 a[230:231], %[vb0] offset:46080\n\t"
    "v_mfma_f32_16x16x32_bf16 v[188:191], a[212:215], v[84:87], v[188:191]\n\t"
    "v_exp_f32 v179, v179\n\t"
    "v_mfma_f32_16x16x32_bf16 v[188:191], a[208:211], v[80:83], v[188:191]\n\t"
    "ds_read_b64_tr_b16 a[232:233], %[vb1] offset:33792\n\t"
    "v_cvt_pk_bf16_f32 v131, v178, v179\n\t"
    "v_mfma_f32_16x16x32_bf16 a[32:35], a[240:243], v[96:99], a[32:35]\n\t"
    "v_exp_f32 v180, v180\n\t"
    "v_mfma_f32_16x16x32_bf16 a[32:35], a[244:247], v[100:103], a[32:35]\n\t"
    "ds_read_b64_tr_b16 a[234:235], %[vb1] offset:37888\n\t"
    "v_mfma_f32_16x16x32_bf16 a[36:39], a[244:247], v[108:111], a[36:39]\n\t"
    "v_exp_f32 v181, v181\n\t"
    "v_mfma_f32_16x16x32_bf16 a[36:39], a[240:243], v[104:107], a[36:39]\n\t"
    "ds_read_b64_tr_b16 a[236:237], %[vb1] offset:41984\n\t"
    "v_cvt_pk_bf16_f32 v138, v180, v181\n\t"
    "v_mfma_f32_16x16x32_bf16 a[40:43], a[240:243], v[112:115], a[40:43]\n\t"
    "v_exp_f32 v182, v182\n\t"
    "v_mfma_f32_16x16x32_bf16 a[40:43], a[244:247], v[116:119], a[40:43]\n\t"
    "ds_read_b64_tr_b16 a[238:239], %[vb1] offset:46080\n\t"
    "v_mfma_f32_16x16x32_bf16 a[44:47], a[244:247], v[124:127], a[44:47]\n\t"
    "v_exp_f32 v183, v183\n\t"
    "v_mfma_f32_16x16x32_bf16 a[44:47], a[240:243], v[120:123], a[44:47]\n\t"
    "buffer_load_dwordx4 %[vv0], %[srdv], s40 offen lds\n\t"
    "v_cvt_pk_bf16_f32 v139, v182, v183\n\t"
    "v_mfma_f32_16x16x32_bf16 a[60:63], a[248:251], v[120:123], a[60:63]\n\t"
    "v_exp_f32 v184, v184\n\t"
    "v_mfma_f32_16x16x32_bf16 a[60:63], a[252:255], v[124:127], a[60:63]\n\t"
    "buffer_load_dword v224, %[xl], %[srdu], s46 offen\n\t"
    "v_add_f32 v216, v216, v184\n\t"
    "v_mfma_f32_16x16x32_bf16 a[56:59], a[252:255], v[116:119], a[56:59]\n\t"
    "v_exp_f32 v185, v185\n\t"
    "v_mfma_f32_16x16x32_bf16 a[56:59], a[248:251], v[112:115], a[56:59]\n\t"
    "buffer_load_dwordx4 %[vv1], %[srdv], s40 offen offset:1024 lds\n\t"
    "v_cvt_pk_bf16_f32 v146, v184, v185\n\t"
    "v_mfma_f32_16x16x32_bf16 a[52:55], a[248:251], v[104:107], a[52:55]\n\t"
    "v_exp_f32 v186, v186\n\t"
    "v_mfma_f32_16x16x32_bf16 a[52:55], a[252:255], v[108:111], a[52:55]\n\t"
    "v_add_f32 v216, v216, v185\n\t"
    "v_add_f32 v216, v216, v186\n\t"
    "v_mfma_f32_16x16x32_bf16 a[48:51], a[252:255], v[100:103], a[48:51]\n\t"
    "v_exp_f32 v187, v187\n\t"
    "v_mfma_f32_16x16x32_bf16 a[48:51], a[248:251], v[96:99], a[48:51]\n\t"
    "v_cvt_pk_bf16_f32 v147, v186, v187\n\t"
    "s_waitcnt lgkmcnt(0)\n\t"
    "v_mfma_f32_16x16x32_bf16 v[160:163], a[192:195], v[32:35], v[192:195]\n\t"
    "v_exp_f32 v188, v188\n\t"
    "v_mfma_f32_16x16x32_bf16 v[160:163], a[196:199], v[36:39], v[160:163]\n\t"
    "ds_read_b128 a[208:211], %[kb0] offset:61440\n\t"
    "v_add_f32 v216, v216, v187\n\t"
    "v_mfma_f32_16x16x32_bf16 v[160:163], a[200:203], v[40:43], v[160:163]\n\t"
    "v_exp_f32 v189, v189\n\t"
    "v_mfma_f32_16x16x32_bf16 v[160:163], a[204:207], v[44:47], v[160:163]\n\t"
    "ds_read_b128 a[212:215], %[kb1] offset:61440\n\t"
    "v_cvt_pk_bf16_f32 v154, v188, v189\n\t"
    "v_mfma_f32_16x16x32_bf16 v[164:167], a[204:207], v[60:63], v[196:199]\n\t"
    "v_exp_f32 v190, v190\n\t"
    "v_mfma_f32_16x16x32_bf16 v[164:167], a[200:203], v[56:59], v[164:167]\n\t"
    "ds_read_b128 a[216:219], %[kb2] offset:61440\n\t"
    "v_add_f32 v220, v220, v188\n\t"
    "v_mfma_f32_16x16x32_bf16 v[164:167], a[196:199], v[52:55], v[164:167]\n\t"
    "v_exp_f32 v191, v191\n\t"
    "v_mfma_f32_16x16x32_bf16 v[164:167], a[192:195], v[48:51], v[164:167]\n\t"
    "ds_read_b128 a[220:223], %[kb3] offset:61440\n\t"
    "v_cvt_pk_bf16_f32 v155, v190, v191\n\t"
    "v_mfma_f32_16x16x32_bf16 v[168:171], a[192:195], v[64:67], v[200:203]\n\t"
    "v_exp_f32 v160, v160\n\t"
    "v_mfma_f32_16x16x32_bf16 v[168:171], a[196:199], v[68:71], v[168:171]\n\t"
    "ds_read_b64_tr_b16 a[240:241], %[vb0] offset:34304\n\t"
    "v_add_f32 v220, v220, v189\n\t"
    "v_mfma_f32_16x16x32_bf16 v[168:171], a[200:203], v[72:75], v[168:171]\n\t"
    "v_exp_f32 v161, v161\n\t"
    "v_mfma_f32_16x16x32_bf16 v[168:171], a[204:207], v[76:79], v[168:171]\n\t"
    "ds_read_b64_tr_b16 a[242:243], %[vb0] offset:38400\n\t"
    "v_cvt_pk_bf16_f32 v132, v160, v161\n\t"
    "v_mfma_f32_16x16x32_bf16 v[172:175], a[204:207], v[92:95], v[204:207]\n\t"
    "v_exp_f32 v162, v162\n\t"
    "v_mfma_f32_16x16x32_bf16 v[172:175], a[200:203], v[88:91], v[172:175]\n\t"
    "ds_read_b64_tr_b16 a[244:245], %[vb0] offset:42496\n\t"
    "v_add_f32 v220, v220, v190\n\t"
    "v_mfma_f32_16x16x32_bf16 v[172:175], a[196:199], v[84:87], v[172:175]\n\t"
    "v_exp_f32 v163, v163\n\t"
    "v_mfma_f32_16x16x32_bf16 v[172:175], a[192:195], v[80:83], v[172:175]\n\t"
    "ds_read_b64_tr_b16 a[246:247], %[vb0] offset:46592\n\t"
    "v_cvt_pk_bf16_f32 v133, v162, v163\n\t"
    "v_mfma_f32_16x16x32_bf16 a[64:67], a[224:227], v[96:99], a[64:67]\n\t"
    "v_exp_f32 v164, v164\n\t"
    "v_mfma_f32_16x16x32_bf16 a[64:67], a[228:231], v[100:103], a[64:67]\n\t"
    "ds_read_b64_tr_b16 a[248:249], %[vb1] offset:34304\n\t"
    "v_add_f32 v220, v220, v191\n\t"
    "v_mfma_f32_16x16x32_bf16 a[68:71], a[228:231], v[108:111], a[68:71]\n\t"
    "v_exp_f32 v165, v165\n\t"
    "v_mfma_f32_16x16x32_bf16 a[68:71], a[224:227], v[104:107], a[68:71]\n\t"
    "ds_read_b64_tr_b16 a[250:251], %[vb1] offset:38400\n\t"
    "v_cvt_pk_bf16_f32 v140, v164, v165\n\t"
    "v_mfma_f32_16x16x32_bf16 a[72:75], a[224:227], v[112:115], a[72:75]\n\t"
    "v_exp_f32 v166, v166\n\t"
    "v_mfma_f32_16x16x32_bf16 a[72:75], a[228:231], v[116:119], a[72:75]\n\t"
    "ds_read_b64_tr_b16 a[252:253], %[vb1] offset:42496\n\t"
    "v_mfma_f32_16x16x32_bf16 a[76:79], a[228:231], v[124:127], a[76:79]\n\t"
    "v_exp_f32 v167, v167\n\t"
    "v_mfma_f32_16x16x32_bf16 a[76:79], a[224:227], v[120:123], a[76:79]\n\t"
    "ds_read_b64_tr_b16 a[254:255], %[vb1] offset:46592\n\t"
    "v_cvt_pk_bf16_f32 v141, v166, v167\n\t"
    "v_mfma_f32_16x16x32_bf16 a[92:95], a[232:235], v[120:123], a[92:95]\n\t"
    "v_exp_f32 v168, v168\n\t"
    "v_mfma_f32_16x16x32_bf16 a[92:95], a[236:239], v[124:127], a[92:95]\n\t"
    "buffer_load_dwordx4 %[vv0], %[srdv], s40 offen offset:2048 lds\n\t"
    "v_add_f32 v216, v216, v168\n\t"
    "v_mfma_f32_16x16x32_bf16 a[88:91], a[236:239], v[116:119], a[88:91]\n\t"
    "v_exp_f32 v169, v169\n\t"
    "v_mfma_f32_16x16x32_bf16 a[88:91], a[232:235], v[112:115], a[88:91]\n\t"
    "buffer_store_dwordx4 v[232:235], %[xl], %[srdp], s46 offen sc1\n\t"
    "v_cvt_pk_bf16_f32 v148, v168, v169\n\t"
    "v_mfma_f32_16x16x32_bf16 a[84:87], a[232:235], v[104:107], a[84:87]\n\t"
    "v_exp_f32 v170, v170\n\t"
    "v_mfma_f32_16x16x32_bf16 a[84:87], a[236:239], v[108:111], a[84:87]\n\t"
    "buffer_load_dwordx4 %[vv1], %[srdv], s40 offen offset:3072 lds\n\t"
    "v_add_f32 v216, v216, v169\n\t"
    "v_mfma_f32_16x16x32_bf16 a[80:83], a[236:239], v[100:103], a[80:83]\n\t"
    "v_exp_f32 v171, v171\n\t"
    "v_mfma_f32_16x16x32_bf16 a[80:83], a[232:235], v[96:99], a[80:83]\n\t"
    "v_add_f32 v216, v216, v170\n\t"
    "v_cvt_pk_bf16_f32 v149, v170, v171\n\t"
    "v_mfma_f32_16x16x32_bf16 v[212:215], v[236:239], v[104:107], v[212:215]\n\t"
    "v_exp_f32 v172, v172\n\t"
    "v_mfma_f32_16x16x32_bf16 v[212:215], v[236:239], v[108:111], v[212:215]\n\t"
    "s_waitcnt vmcnt(18) lgkmcnt(0)\n\t"
    "s_barrier\n\t"
    "v_mfma_f32_16x16x32_bf16 v[176:179], a[208:211], v[32:35], v[192:195]\n\t"
    "v_exp_f32 v173, v173\n\t"
    "v_mfma_f32_16x16x32_bf16 v[176:179], a[212:215], v[36:39], v[176:179]\n\t"
    "ds_read_b128 a[192:195], %[kb0] offset:0\n\t"
    "s_add_u32 s40, s40, 16384\n\t"
    "v_mfma_f32_16x16x32_bf16 v[176:179], a[216:219], v[40:43], v[176:179]\n\t"
    "v_exp_f32 v174, v174\n\t"
    "v_mfma_f32_16x16x32_bf16 v[176:179], a[220:223], v[44:47], v[176:179]\n\t"
    "ds_read_b128 a[196:199], %[kb1] offset:0\n\t"
    "s_add_u32 m0, %[m0b], 49152\n\t"
    "v_mfma_f32_16x16x32_bf16 v[180:183], a[220:223], v[60:63], v[196:199]\n\t"
    "v_exp_f32 v175, v175\n\t"
    "v_mfma_f32_16x16x32_bf16 v[180:183], a[216:219], v[56:59], v[180:183]\n\t"
    "ds_read_b128 a[200:203], %[kb2] offset:0\n\t"
    "s_cmp_lg_u32 s40, %[kvend]\n\t"
    "v_mfma_f32_16x16x32_bf16 v[180:183], a[212:215], v[52:55], v[180:183]\n\t"
    "ds_read_b128 a[204:207], %[kb3] offset:0\n\t"
    "v_cvt_pk_bf16_f32 v156, v172, v173\n\t"
    "v_mfma_f32_16x16x32_bf16 v[180:183], a[208:211], v[48:51], v[180:183]\n\t"
    "ds_read_b64_tr_b16 a[224:225], %[vb0] offset:49152\n\t"
    "v_cvt_pk_bf16_f32 v157, v174, v175\n\t"
    "v_mfma_f32_16x16x32_bf16 v[184:187], a[208:211], v[64:67], v[200:203]\n\t"
    "v_exp_f32 v176, v176\n\t"
    "v_mfma_f32_16x16x32_bf16 v[184:187], a[212:215], v[68:71], v[184:187]\n\t"
    "ds_read_b64_tr_b16 a[226:227], %[vb0] offset:53248\n\t"
    "v_add_f32 v216, v216, v171\n\t"
    "v_mfma_f32_16x16x32_bf16 v[184:187], a[216:219], v[72:75], v[184:187]\n\t"
    "v_exp_f32 v177, v177\n\t"
    "v_mfma_f32_16x16x32_bf16 v[184:187], a[220:223], v[76:79], v[184:187]\n\t"
    "ds_read_b64_tr_b16 a[228:229], %[vb0] offset:57344\n\t"
    "v_cvt_pk_bf16_f32 v134, v176, v177\n\t"
    "v_mfma_f32_16x16x32_bf16 v[188:191], a[220:223], v[92:95], v[204:207]\n\t"
    "v_exp_f32 v178, v178\n\t"
    "v_mfma_f32_16x16x32_bf16 v[188:191], a[216:219], v[88:91], v[188:191]\n\t"
    "ds_read_b64_tr_b16 a[230:231], %[vb0] offset:61440\n\t"
    "v_add_f32 v220, v220, v172\n\t"
    "v_mfma_f32_16x16x32_bf16 v[188:191], a[212:215], v[84:87], v[188:191]\n\t"
    "v_exp_f32 v179, v179\n\t"
    "v_mfma_f32_16x16x32_bf16 v[188:191], a[208:211], v[80:83], v[188:191]\n\t"
    "ds_read_b64_tr_b16 a[232:233], %[vb1] offset:49152\n\t"
    "v_cvt_pk_bf16_f32 v135, v178, v179\n\t"
    "v_mfma_f32_16x16x32_bf16 a[96:99], a[240:243], v[96:99], a[96:99]\n\t"
    "v_exp_f32 v180, v180\n\t"
    "v_mfma_f32_16x16x32_bf16 a[96:99], a[244:247], v[100:103], a[96:99]\n\t"
    "ds_read_b64_tr_b16 a[234:235], %[vb1] offset:53248\n\t"
    "v_add_f32 v220, v220, v173\n\t"
    "v_mfma_f32_16x16x32_bf16 a[100:103], a[244:247], v[108:111], a[100:103]\n\t"
    "v_exp_f32 v181, v181\n\t"
    "v_mfma_f32_16x16x32_bf16 a[100:103], a[240:243], v[104:107], a[100:103]\n\t"
    "ds_read_b64_tr_b16 a[236:237], %[vb1] offset:57344\n\t"
    "v_cvt_pk_bf16_f32 v142, v180, v181\n\t"
    "v_mfma_f32_16x16x32_bf16 a[104:107], a[240:243], v[112:115], a[104:107]\n\t"
    "v_exp_f32 v182, v182\n\t"
    "v_mfma_f32_16x16x32_bf16 a[104:107], a[244:247], v[116:119], a[104:107]\n\t"
    "ds_read_b64_tr_b16 a[238:239], %[vb1] offset:61440\n\t"
    "v_add_f32 v220, v220, v174\n\t"
    "v_mfma_f32_16x16x32_bf16 a[108:111], a[244:247], v[124:127], a[108:111]\n\t"
    "v_exp_f32 v183, v183\n\t"
    "v_mfma_f32_16x16x32_bf16 a[108:111], a[240:243], v[120:123], a[108:111]\n\t"
    "buffer_load_dwordx4 %[kv0], %[srdk1], s40 offen lds\n\t"
    "v_cvt_pk_bf16_f32 v143, v182, v183\n\t"
    "v_mfma_f32_16x16x32_bf16 a[124:127], a[248:251], v[120:123], a[124:127]\n\t"
    "v_exp_f32 v184, v184\n\t"
    "v_mfma_f32_16x16x32_bf16 a[124:127], a[252:255], v[124:127], a[124:127]\n\t"
    "v_add_f32 v220, v220, v175\n\t"
    "v_add_f32 v216, v216, v184\n\t"
    "v_mfma_f32_16x16x32_bf16 a[120:123], a[252:255], v[116:119], a[120:123]\n\t"
    "v_exp_f32 v185, v185\n\t"
    "v_mfma_f32_16x16x32_bf16 a[120:123], a[248:251], v[112:115], a[120:123]\n\t"
    "buffer_load_dwordx4 %[kv1], %[srdk1], s40 offen offset:1024 lds\n\t"
    "v_cvt_pk_bf16_f32 v150, v184, v185\n\t"
    "v_mfma_f32_16x16x32_bf16 a[116:119], a[248:251], v[104:107], a[116:119]\n\t"
    "v_exp_f32 v186, v186\n\t"
    "v_mfma_f32_16x16x32_bf16 a[116:119], a[252:255], v[108:111], a[116:119]\n\t"
    "v_add_f32 v216, v216, v185\n\t"
    "v_add_f32 v216, v216, v186\n\t"
    "v_mfma_f32_16x16x32_bf16 a[112:115], a[252:255], v[100:103], a[112:115]\n\t"
    "v_exp_f32 v187, v187\n\t"
    "v_mfma_f32_16x16x32_bf16 a[112:115], a[248:251], v[96:99], a[112:115]\n\t"
    "s_waitcnt lgkmcnt(0)\n\t"
    "s_cbranch_scc1 Lloop_%=\n\t"
    "s_cmp_eq_u64 %[ym], 0\n\t"
    "s_cbranch_scc0 Lyt_%=\n\t"
    "v_exp_f32 v188, v188\n\t"
    "ds_read_b64_tr_b16 a[240:241], %[vb0] offset:49664\n\t"
    "v_exp_f32 v189, v189\n\t"
    "ds_read_b64_tr_b16 a[242:243], %[vb0] offset:53760\n\t"
    "v_cvt_pk_bf16_f32 v151, v186, v187\n\t"
    "v_exp_f32 v190, v190\n\t"
    "ds_read_b64_tr_b16 a[244:245], %[vb0] offset:57856\n\t"
    "v_cvt_pk_bf16_f32 v158, v188, v189\n\t"
    "v_exp_f32 v191, v191\n\t"
    "ds_read_b64_tr_b16 a[246:247], %[vb0] offset:61952\n\t"
    "v_cvt_pk_bf16_f32 v159, v190, v191\n\t"
    "v_mfma_f32_16x16x32_bf16 v[208:211], v[236:239], v[128:131], v[208:211]\n\t"
    "ds_read_b64_tr_b16 a[248:249], %[vb1] offset:49664\n\t"
    "v_mfma_f32_16x16x32_bf16 v[208:211], v[236:239], v[132:135], v[208:211]\n\t"
    "ds_read_b64_tr_b16 a[250:251], %[vb1] offset:53760\n\t"
    "v_add_f32 v216, v216, v187\n\t"
    "ds_read_b64_tr_b16 a[252:253], %[vb1] offset:57856\n\t"
    "ds_read_b64_tr_b16 a[254:255], %[vb1] offset:61952\n\t"
    "v_mfma_f32_16x16x32_bf16 v[212:215], v[236:239], v[136:139], v[212:215]\n\t"
    "v_mfma_f32_16x16x32_bf16 v[212:215], v[236:239], v[140:143], v[212:215]\n\t"
    "v_add_f32 v220, v220, v188\n\t"
    "v_add_f32 v220, v220, v189\n\t"
    "v_add_f32 v220, v220, v190\n\t"
    "v_add_f32 v220, v220, v191\n\t"
    "v_mfma_f32_16x16x32_bf16 a[0:3], a[224:227], v[128:131], a[0:3]\n\t"
    "v_mfma_f32_16x16x32_bf16 a[0:3], a[228:231], v[132:135], a[0:3]\n\t"
    "v_mfma_f32_16x16x32_bf16 a[4:7], a[228:231], v[140:143], a[4:7]\n\t"
    "v_mfma_f32_16x16x32_bf16 a[4:7], a[224:227], v[136:139], a[4:7]\n\t"
    "v_mfma_f32_16x16x32_bf16 a[8:11], a[224:227], v[144:147], a[8:11]\n\t"
    "v_mov_b32 v234, v216\n\t"
    "v_mov_b32 v235, v220\n\t"
    "s_nop 0\n\t"
    "v_permlane32_swap_b32 v216, v234\n\t"
    "v_mfma_f32_16x16x32_bf16 a[8:11], a[228:231], v[148:151], a[8:11]\n\t"
    "v_permlane32_swap_b32 v220, v235\n\t"
    "v_add_f32 v216, v216, v234\n\t"
    "v_add_f32 v220, v220, v235\n\t"
    "v_mfma_f32_16x16x32_bf16 a[12:15], a[228:231], v[156:159], a[12:15]\n\t"
    "v_mov_b32 v234, v216\n\t"
    "v_mov_b32 v235, v220\n\t"
    "s_nop 0\n\t"
    "v_permlane16_swap_b32 v216, v234\n\t"
    "v_mfma_f32_16x16x32_bf16 a[12:15], a[224:227], v[152:155], a[12:15]\n\t"
    "v_permlane16_swap_b32 v220, v235\n\t"
    "v_add_f32 v216, v216, v234\n\t"
    "v_add_f32 v220, v220, v235\n\t"
    "v_mfma_f32_16x16x32_bf16 a[28:31], a[232:235], v[152:155], a[28:31]\n\t"
    "v_mov_b32 v209, v212\n\t"
    "v_mov_b32 v210, v216\n\t"
    "v_mov_b32 v211, v220\n\t"
    "v_mfma_f32_16x16x32_bf16 a[28:31], a[236:239], v[156:159], a[28:31]\n\t"
    "v_rcp_f32 v228, v208\n\t"
    "v_rcp_f32 v229, v209\n\t"
    "v_rcp_f32 v230, v210\n\t"
    "v_mfma_f32_16x16x32_bf16 a[24:27], a[236:239], v[148:151], a[24:27]\n\t"
    "s_add_u32 s42, %[os], 0\n\t"
    "s_add_u32 s43, %[os], 278528\n\t"
    "v_rcp_f32 v231, v211\n\t"
    "v_mfma_f32_16x16x32_bf16 a[24:27], a[232:235], v[144:147], a[24:27]\n\t"
    "s_add_u32 s44, %[os], 557056\n\t"
    "s_add_u32 s45, %[os], 835584\n\t"
    "v_mfma_f32_16x16x32_bf16 a[20:23], a[232:235], v[136:139], a[20:23]\n\t"
    "v_mfma_f32_16x16x32_bf16 a[20:23], a[236:239], v[140:143], a[20:23]\n\t"
    "v_mfma_f32_16x16x32_bf16 a[16:19], a[236:239], v[132:135], a[16:19]\n\t"
    "v_mfma_f32_16x16x32_bf16 a[16:19], a[232:235], v[128:131], a[16:19]\n\t"
    "s_waitcnt lgkmcnt(0)\n\t"
    "ds_read_b64_tr_b16 a[224:225], %[vb0] offset:50176\n\t"
    "ds_read_b64_tr_b16 a[226:227], %[vb0] offset:54272\n\t"
    "ds_read_b64_tr_b16 a[228:229], %[vb0] offset:58368\n\t"
    "ds_read_b64_tr_b16 a[230:231], %[vb0] offset:62464\n\t"
    "ds_read_b64_tr_b16 a[232:233], %[vb1] offset:50176\n\t"
    "ds_read_b64_tr_b16 a[234:235], %[vb1] offset:54272\n\t"
    "ds_read_b64_tr_b16 a[236:237], %[vb1] offset:58368\n\t"
    "ds_read_b64_tr_b16 a[238:239], %[vb1] offset:62464\n\t"
    "v_mfma_f32_16x16x32_bf16 a[32:35], a[240:243], v[128:131], a[32:35]\n\t"
    "v_accvgpr_read_b32 v120, a12\n\t"
    "v_accvgpr_read_b32 v121, a13\n\t"
    "v_accvgpr_read_b32 v122, a14\n\t"
    "v_accvgpr_read_b32 v123, a15\n\t"
    "v_accvgpr_read_b32 v124, a28\n\t"
    "v_accvgpr_read_b32 v125, a29\n\t"
    "v_mfma_f32_16x16x32_bf16 a[32:35], a[244:247], v[132:135], a[32:35]\n\t"
    "v_accvgpr_read_b32 v126, a30\n\t"
    "v_accvgpr_read_b32 v127, a31\n\t"
    "v_mul_f32 v120, v120, v231\n\t"
    "v_mul_f32 v121, v121, v231\n\t"
    "v_mul_f32 v122, v122, v231\n\t"
    "v_mul_f32 v123, v123, v231\n\t"
    "v_mfma_f32_16x16x32_bf16 a[36:39], a[244:247], v[140:143], a[36:39]\n\t"
    "v_mul_f32 v124, v124, v231\n\t"
    "v_mul_f32 v125, v125, v231\n\t"
    "v_mul_f32 v126, v126, v231\n\t"
    "v_mul_f32 v127, v127, v231\n\t"
    "v_cvt_pk_bf16_f32 v120, v120, v121\n\t"
    "v_mfma_f32_16x16x32_bf16 a[36:39], a[240:243], v[136:139], a[36:39]\n\t"
    "v_cvt_pk_bf16_f32 v121, v122, v123\n\t"
    "v_cvt_pk_bf16_f32 v122, v124, v125\n\t"
    "v_cvt_pk_bf16_f32 v123, v126, v127\n\t"
    "buffer_store_dwordx4 v[120:123], %[xl], %[srd], s45 offen sc1\n\t"
    "v_accvgpr_read_b32 v112, a8\n\t"
    "v_accvgpr_read_b32 v113, a9\n\t"
    "v_mfma_f32_16x16x32_bf16 a[40:43], a[240:243], v[144:147], a[40:43]\n\t"
    "v_accvgpr_read_b32 v114, a10\n\t"
    "v_accvgpr_read_b32 v115, a11\n\t"
    "v_accvgpr_read_b32 v116, a24\n\t"
    "v_accvgpr_read_b32 v117, a25\n\t"
    "v_accvgpr_read_b32 v118, a26\n\t"
    "v_mfma_f32_16x16x32_bf16 a[40:43], a[244:247], v[148:151], a[40:43]\n\t"
    "v_accvgpr_read_b32 v119, a27\n\t"
    "v_mul_f32 v112, v112, v230\n\t"
    "v_mul_f32 v113, v113, v230\n\t"
    "v_mul_f32 v114, v114, v230\n\t"
    "v_mul_f32 v115, v115, v230\n\t"
    "v_mul_f32 v116, v116, v230\n\t"
    "v_mfma_f32_16x16x32_bf16 a[44:47], a[244:247], v[156:159], a[44:47]\n\t"
    "v_mul_f32 v117, v117, v230\n\t"
    "v_mul_f32 v118, v118, v230\n\t"
    "v_mul_f32 v119, v119, v230\n\t"
    "v_cvt_pk_bf16_f32 v112, v112, v113\n\t"
    "v_cvt_pk_bf16_f32 v113, v114, v115\n\t"
    "v_cvt_pk_bf16_f32 v114, v116, v117\n\t"
    "v_mfma_f32_16x16x32_bf16 a[44:47], a[240:243], v[152:155], a[44:47]\n\t"
    "v_cvt_pk_bf16_f32 v115, v118, v119\n\t"
    "buffer_store_dwordx4 v[112:115], %[xl], %[srd], s44 offen sc1\n\t"
    "v_accvgpr_read_b32 v104, a4\n\t"
    "v_accvgpr_read_b32 v105, a5\n\t"
    "v_accvgpr_read_b32 v106, a6\n\t"
    "v_mfma_f32_16x16x32_bf16 a[60:63], a[248:251], v[152:155], a[60:63]\n\t"
    "v_accvgpr_read_b32 v107, a7\n\t"
    "v_accvgpr_read_b32 v108, a20\n\t"
    "v_accvgpr_read_b32 v109, a21\n\t"
    "v_accvgpr_read_b32 v110, a22\n\t"
    "v_accvgpr_read_b32 v111, a23\n\t"
    "v_mul_f32 v104, v104, v229\n\t"
    "v_mfma_f32_16x16x32_bf16 a[60:63], a[252:255], v[156:159], a[60:63]\n\t"
    "v_mul_f32 v105, v105, v229\n\t"
    "v_mul_f32 v106, v106, v229\n\t"
    "v_mul_f32 v107, v107, v229\n\t"
    "v_mul_f32 v108, v108, v229\n\t"
    "v_mul_f32 v109, v109, v229\n\t"
    "v_mfma_f32_16x16x32_bf16 a[56:59], a[252:255], v[148:151], a[56:59]\n\t"
    "v_mul_f32 v110, v110, v229\n\t"
    "v_mul_f32 v111, v111, v229\n\t"
    "v_cvt_pk_bf16_f32 v104, v104, v105\n\t"
    "v_cvt_pk_bf16_f32 v105, v106, v107\n\t"
    "v_cvt_pk_bf16_f32 v106, v108, v109\n\t"
    "v_cvt_pk_bf16_f32 v107, v110, v111\n\t"
    "v_mfma_f32_16x16x32_bf16 a[56:59], a[248:251], v[144:147], a[56:59]\n\t"
    "buffer_store_dwordx4 v[104:107], %[xl], %[srd], s43 offen sc1\n\t"
    "v_accvgpr_read_b32 v96, a0\n\t"
    "v_accvgpr_read_b32 v97, a1\n\t"
    "v_accvgpr_read_b32 v98, a2\n\t"
    "v_accvgpr_read_b32 v99, a3\n\t"
    "v_accvgpr_read_b32 v100, a16\n\t"
    "v_mfma_f32_16x16x32_bf16 a[52:55], a[248:251], v[136:139], a[52:55]\n\t"
    "v_accvgpr_read_b32 v101, a17\n\t"
    "v_accvgpr_read_b32 v102, a18\n\t"
    "v_accvgpr_read_b32 v103, a19\n\t"
    "v_mul_f32 v96, v96, v228\n\t"
    "v_mul_f32 v97, v97, v228\n\t"
    "v_mfma_f32_16x16x32_bf16 a[52:55], a[252:255], v[140:143], a[52:55]\n\t"
    "v_mul_f32 v98, v98, v228\n\t"
    "v_mul_f32 v99, v99, v228\n\t"
    "v_mul_f32 v100, v100, v228\n\t"
    "v_mul_f32 v101, v101, v228\n\t"
    "v_mul_f32 v102, v102, v228\n\t"
    "v_mul_f32 v103, v103, v228\n\t"
    "v_mfma_f32_16x16x32_bf16 a[48:51], a[252:255], v[132:135], a[48:51]\n\t"
    "v_cvt_pk_bf16_f32 v96, v96, v97\n\t"
    "v_cvt_pk_bf16_f32 v97, v98, v99\n\t"
    "v_cvt_pk_bf16_f32 v98, v100, v101\n\t"
    "v_cvt_pk_bf16_f32 v99, v102, v103\n\t"
    "buffer_store_dwordx4 v[96:99], %[xl], %[srd], s42 offen sc1\n\t"
    "v_mfma_f32_16x16x32_bf16 a[48:51], a[248:251], v[128:131], a[48:51]\n\t"
    "s_waitcnt lgkmcnt(0)\n\t"
    "ds_read_b64_tr_b16 a[240:241], %[vb0] offset:50688\n\t"
    "ds_read_b64_tr_b16 a[242:243], %[vb0] offset:54784\n\t"
    "ds_read_b64_tr_b16 a[244:245], %[vb0] offset:58880\n\t"
    "ds_read_b64_tr_b16 a[246:247], %[vb0] offset:62976\n\t"
    "ds_read_b64_tr_b16 a[248:249], %[vb1] offset:50688\n\t"
    "ds_read_b64_tr_b16 a[250:251], %[vb1] offset:54784\n\t"
    "ds_read_b64_tr_b16 a[252:253], %[vb1] offset:58880\n\t"
    "ds_read_b64_tr_b16 a[254:255], %[vb1] offset:62976\n\t"
    "v_mfma_f32_16x16x32_bf16 a[64:67], a[224:227], v[128:131], a[64:67]\n\t"
    "v_accvgpr_read_b32 v120, a44\n\t"
    "v_accvgpr_read_b32 v121, a45\n\t"
    "v_accvgpr_read_b32 v122, a46\n\t"
    "v_accvgpr_read_b32 v123, a47\n\t"
    "v_accvgpr_read_b32 v124, a60\n\t"
    "v_accvgpr_read_b32 v125, a61\n\t"
    "v_mfma_f32_16x16x32_bf16 a[64:67], a[228:231], v[132:135], a[64:67]\n\t"
    "v_accvgpr_read_b32 v126, a62\n\t"
    "v_accvgpr_read_b32 v127, a63\n\t"
    "v_mul_f32 v120, v120, v231\n\t"
    "v_mul_f32 v121, v121, v231\n\t"
    "v_mul_f32 v122, v122, v231\n\t"
    "v_mul_f32 v123, v123, v231\n\t"
    "v_mfma_f32_16x16x32_bf16 a[68:71], a[228:231], v[140:143], a[68:71]\n\t"
    "v_mul_f32 v124, v124, v231\n\t"
    "v_mul_f32 v125, v125, v231\n\t"
    "v_mul_f32 v126, v126, v231\n\t"
    "v_mul_f32 v127, v127, v231\n\t"
    "v_cvt_pk_bf16_f32 v120, v120, v121\n\t"
    "v_mfma_f32_16x16x32_bf16 a[68:71], a[224:227], v[136:139], a[68:71]\n\t"
    "v_cvt_pk_bf16_f32 v121, v122, v123\n\t"
    "v_cvt_pk_bf16_f32 v122, v124, v125\n\t"
    "v_cvt_pk_bf16_f32 v123, v126, v127\n\t"
    "buffer_store_dwordx4 v[120:123], %[xl], %[srd], s45 offen offset:1024 sc1\n\t"
    "v_accvgpr_read_b32 v112, a40\n\t"
    "v_accvgpr_read_b32 v113, a41\n\t"
    "v_mfma_f32_16x16x32_bf16 a[72:75], a[224:227], v[144:147], a[72:75]\n\t"
    "v_accvgpr_read_b32 v114, a42\n\t"
    "v_accvgpr_read_b32 v115, a43\n\t"
    "v_accvgpr_read_b32 v116, a56\n\t"
    "v_accvgpr_read_b32 v117, a57\n\t"
    "v_accvgpr_read_b32 v118, a58\n\t"
    "v_mfma_f32_16x16x32_bf16 a[72:75], a[228:231], v[148:151], a[72:75]\n\t"
    "v_accvgpr_read_b32 v119, a59\n\t"
    "v_mul_f32 v112, v112, v230\n\t"
    "v_mul_f32 v113, v113, v230\n\t"
    "v_mul_f32 v114, v114, v230\n\t"
    "v_mul_f32 v115, v115, v230\n\t"
    "v_mul_f32 v116, v116, v230\n\t"
    "v_mfma_f32_16x16x32_bf16 a[76:79], a[228:231], v[156:159], a[76:79]\n\t"
    "v_mul_f32 v117, v117, v230\n\t"
    "v_mul_f32 v118, v118, v230\n\t"
    "v_mul_f32 v119, v119, v230\n\t"
    "v_cvt_pk_bf16_f32 v112, v112, v113\n\t"
    "v_cvt_pk_bf16_f32 v113, v114, v115\n\t"
    "v_cvt_pk_bf16_f32 v114, v116, v117\n\t"
    "v_mfma_f32_16x16x32_bf16 a[76:79], a[224:227], v[152:155], a[76:79]\n\t"
    "v_cvt_pk_bf16_f32 v115, v118, v119\n\t"
    "buffer_store_dwordx4 v[112:115], %[xl], %[srd], s44 offen offset:1024 sc1\n\t"
    "v_accvgpr_read_b32 v104, a36\n\t"
    "v_accvgpr_read_b32 v105, a37\n\t"
    "v_accvgpr_read_b32 v106, a38\n\t"
    "v_mfma_f32_16x16x32_bf16 a[92:95], a[232:235], v[152:155], a[92:95]\n\t"
    "v_accvgpr_read_b32 v107, a39\n\t"
    "v_accvgpr_read_b32 v108, a52\n\t"
    "v_accvgpr_read_b32 v109, a53\n\t"
    "v_accvgpr_read_b32 v110, a54\n\t"
    "v_accvgpr_read_b32 v111, a55\n\t"
    "v_mul_f32 v104, v104, v229\n\t"
    "v_mfma_f32_16x16x32_bf16 a[92:95], a[236:239], v[156:159], a[92:95]\n\t"
    "v_mul_f32 v105, v105, v229\n\t"
    "v_mul_f32 v106, v106, v229\n\t"
    "v_mul_f32 v107, v107, v229\n\t"
    "v_mul_f32 v108, v108, v229\n\t"
    "v_mul_f32 v109, v109, v229\n\t"
    "v_mfma_f32_16x16x32_bf16 a[88:91], a[236:239], v[148:151], a[88:91]\n\t"
    "v_mul_f32 v110, v110, v229\n\t"
    "v_mul_f32 v111, v111, v229\n\t"
    "v_cvt_pk_bf16_f32 v104, v104, v105\n\t"
    "v_cvt_pk_bf16_f32 v105, v106, v107\n\t"
    "v_cvt_pk_bf16_f32 v106, v108, v109\n\t"
    "v_cvt_pk_bf16_f32 v107, v110, v111\n\t"
    "v_mfma_f32_16x16x32_bf16 a[88:91], a[232:235], v[144:147], a[88:91]\n\t"
    "buffer_store_dwordx4 v[104:107], %[xl], %[srd], s43 offen offset:1024 sc1\n\t"
    "v_accvgpr_read_b32 v96, a32\n\t"
    "v_accvgpr_read_b32 v97, a33\n\t"
    "v_accvgpr_read_b32 v98, a34\n\t"
    "v_accvgpr_read_b32 v99, a35\n\t"
    "v_accvgpr_read_b32 v100, a48\n\t"
    "v_mfma_f32_16x16x32_bf16 a[84:87], a[232:235], v[136:139], a[84:87]\n\t"
    "v_accvgpr_read_b32 v101, a49\n\t"
    "v_accvgpr_read_b32 v102, a50\n\t"
    "v_accvgpr_read_b32 v103, a51\n\t"
    "v_mul_f32 v96, v96, v228\n\t"
    "v_mul_f32 v97, v97, v228\n\t"
    "v_mfma_f32_16x16x32_bf16 a[84:87], a[236:239], v[140:143], a[84:87]\n\t"
    "v_mul_f32 v98, v98, v228\n\t"
    "v_mul_f32 v99, v99, v228\n\t"
    "v_mul_f32 v100, v100, v228\n\t"
    "v_mul_f32 v101, v101, v228\n\t"
    "v_mul_f32 v102, v102, v228\n\t"
    "v_mul_f32 v103, v103, v228\n\t"
    "v_mfma_f32_16x16x32_bf16 a[80:83], a[236:239], v[132:135], a[80:83]\n\t"
    "v_cvt_pk_bf16_f32 v96, v96, v97\n\t"
    "v_cvt_pk_bf16_f32 v97, v98, v99\n\t"
    "v_cvt_pk_bf16_f32 v98, v100, v101\n\t"
    "v_cvt_pk_bf16_f32 v99, v102, v103\n\t"
    "buffer_store_dwordx4 v[96:99], %[xl], %[srd], s42 offen offset:1024 sc1\n\t"
    "v_mfma_f32_16x16x32_bf16 a[80:83], a[232:235], v[128:131], a[80:83]\n\t"
    "s_waitcnt lgkmcnt(0)\n\t"
    "v_mfma_f32_16x16x32_bf16 a[96:99], a[240:243], v[128:131], a[96:99]\n\t"
    "v_accvgpr_read_b32 v120, a76\n\t"
    "v_accvgpr_read_b32 v121, a77\n\t"
    "v_accvgpr_read_b32 v122, a78\n\t"
    "v_accvgpr_read_b32 v123, a79\n\t"
    "v_accvgpr_read_b32 v124, a92\n\t"
    "v_accvgpr_read_b32 v125, a93\n\t"
    "v_mfma_f32_16x16x32_bf16 a[96:99], a[244:247], v[132:135], a[96:99]\n\t"
    "v_accvgpr_read_b32 v126, a94\n\t"
    "v_accvgpr_read_b32 v127, a95\n\t"
    "v_mul_f32 v120, v120, v231\n\t"
    "v_mul_f32 v121, v121, v231\n\t"
    "v_mul_f32 v122, v122, v231\n\t"
    "v_mul_f32 v123, v123, v231\n\t"
    "v_mfma_f32_16x16x32_bf16 a[100:103], a[244:247], v[140:143], a[100:103]\n\t"
    "v_mul_f32 v124, v124, v231\n\t"
    "v_mul_f32 v125, v125, v231\n\t"
    "v_mul_f32 v126, v126, v231\n\t"
    "v_mul_f32 v127, v127, v231\n\t"
    "v_cvt_pk_bf16_f32 v120, v120, v121\n\t"
    "v_mfma_f32_16x16x32_bf16 a[100:103], a[240:243], v[136:139], a[100:103]\n\t"
    "v_cvt_pk_bf16_f32 v121, v122, v123\n\t"
    "v_cvt_pk_bf16_f32 v122, v124, v125\n\t"
    "v_cvt_pk_bf16_f32 v123, v126, v127\n\t"
    "buffer_store_dwordx4 v[120:123], %[xl], %[srd], s45 offen offset:2048 sc1\n\t"
    "v_accvgpr_read_b32 v112, a72\n\t"
    "v_accvgpr_read_b32 v113, a73\n\t"
    "v_mfma_f32_16x16x32_bf16 a[104:107], a[240:243], v[144:147], a[104:107]\n\t"
    "v_accvgpr_read_b32 v114, a74\n\t"
    "v_accvgpr_read_b32 v115, a75\n\t"
    "v_accvgpr_read_b32 v116, a88\n\t"
    "v_accvgpr_read_b32 v117, a89\n\t"
    "v_accvgpr_read_b32 v118, a90\n\t"
    "v_mfma_f32_16x16x32_bf16 a[104:107], a[244:247], v[148:151], a[104:107]\n\t"
    "v_accvgpr_read_b32 v119, a91\n\t"
    "v_mul_f32 v112, v112, v230\n\t"
    "v_mul_f32 v113, v113, v230\n\t"
    "v_mul_f32 v114, v114, v230\n\t"
    "v_mul_f32 v115, v115, v230\n\t"
    "v_mul_f32 v116, v116, v230\n\t"
    "v_mfma_f32_16x16x32_bf16 a[108:111], a[244:247], v[156:159], a[108:111]\n\t"
    "v_mul_f32 v117, v117, v230\n\t"
    "v_mul_f32 v118, v118, v230\n\t"
    "v_mul_f32 v119, v119, v230\n\t"
    "v_cvt_pk_bf16_f32 v112, v112, v113\n\t"
    "v_cvt_pk_bf16_f32 v113, v114, v115\n\t"
    "v_cvt_pk_bf16_f32 v114, v116, v117\n\t"
    "v_mfma_f32_16x16x32_bf16 a[108:111], a[240:243], v[152:155], a[108:111]\n\t"
    "v_cvt_pk_bf16_f32 v115, v118, v119\n\t"
    "buffer_store_dwordx4 v[112:115], %[xl], %[srd], s44 offen offset:2048 sc1\n\t"
    "v_accvgpr_read_b32 v104, a68\n\t"
    "v_accvgpr_read_b32 v105, a69\n\t"
    "v_accvgpr_read_b32 v106, a70\n\t"
    "v_mfma_f32_16x16x32_bf16 a[124:127], a[248:251], v[152:155], a[124:127]\n\t"
    "v_accvgpr_read_b32 v107, a71\n\t"
    "v_accvgpr_read_b32 v108, a84\n\t"
    "v_accvgpr_read_b32 v109, a85\n\t"
    "v_accvgpr_read_b32 v110, a86\n\t"
    "v_accvgpr_read_b32 v111, a87\n\t"
    "v_mul_f32 v104, v104, v229\n\t"
    "v_mfma_f32_16x16x32_bf16 a[124:127], a[252:255], v[156:159], a[124:127]\n\t"
    "v_mul_f32 v105, v105, v229\n\t"
    "v_mul_f32 v106, v106, v229\n\t"
    "v_mul_f32 v107, v107, v229\n\t"
    "v_mul_f32 v108, v108, v229\n\t"
    "v_mul_f32 v109, v109, v229\n\t"
    "v_mfma_f32_16x16x32_bf16 a[120:123], a[252:255], v[148:151], a[120:123]\n\t"
    "v_mul_f32 v110, v110, v229\n\t"
    "v_mul_f32 v111, v111, v229\n\t"
    "v_cvt_pk_bf16_f32 v104, v104, v105\n\t"
    "v_cvt_pk_bf16_f32 v105, v106, v107\n\t"
    "v_cvt_pk_bf16_f32 v106, v108, v109\n\t"
    "v_cvt_pk_bf16_f32 v107, v110, v111\n\t"
    "v_mfma_f32_16x16x32_bf16 a[120:123], a[248:251], v[144:147], a[120:123]\n\t"
    "buffer_store_dwordx4 v[104:107], %[xl], %[srd], s43 offen offset:2048 sc1\n\t"
    "v_accvgpr_read_b32 v96, a64\n\t"
    "v_accvgpr_read_b32 v97, a65\n\t"
    "v_accvgpr_read_b32 v98, a66\n\t"
    "v_accvgpr_read_b32 v99, a67\n\t"
    "v_accvgpr_read_b32 v100, a80\n\t"
    "v_mfma_f32_16x16x32_bf16 a[116:119], a[248:251], v[136:139], a[116:119]\n\t"
    "v_accvgpr_read_b32 v101, a81\n\t"
    "v_accvgpr_read_b32 v102, a82\n\t"
    "v_accvgpr_read_b32 v103, a83\n\t"
    "v_mul_f32 v96, v96, v228\n\t"
    "v_mul_f32 v97, v97, v228\n\t"
    "v_mfma_f32_16x16x32_bf16 a[116:119], a[252:255], v[140:143], a[116:119]\n\t"
    "v_mul_f32 v98, v98, v228\n\t"
    "v_mul_f32 v99, v99, v228\n\t"
    "v_mul_f32 v100, v100, v228\n\t"
    "v_mul_f32 v101, v101, v228\n\t"
    "v_mul_f32 v102, v102, v228\n\t"
    "v_mul_f32 v103, v103, v228\n\t"
    "v_mfma_f32_16x16x32_bf16 a[112:115], a[252:255], v[132:135], a[112:115]\n\t"
    "v_cvt_pk_bf16_f32 v96, v96, v97\n\t"
    "v_cvt_pk_bf16_f32 v97, v98, v99\n\t"
    "v_cvt_pk_bf16_f32 v98, v100, v101\n\t"
    "v_cvt_pk_bf16_f32 v99, v102, v103\n\t"
    "buffer_store_dwordx4 v[96:99], %[xl], %[srd], s42 offen offset:2048 sc1\n\t"
    "v_mfma_f32_16x16x32_bf16 a[112:115], a[248:251], v[128:131], a[112:115]\n\t"
    "v_accvgpr_read_b32 v120, a108\n\t"
    "v_accvgpr_read_b32 v121, a109\n\t"
    "v_accvgpr_read_b32 v122, a110\n\t"
    "v_accvgpr_read_b32 v123, a111\n\t"
    "v_accvgpr_read_b32 v124, a124\n\t"
    "v_accvgpr_read_b32 v125, a125\n\t"
    "v_accvgpr_read_b32 v126, a126\n\t"
    "v_accvgpr_read_b32 v127, a127\n\t"
    "v_mul_f32 v120, v120, v231\n\t"
    "v_mul_f32 v121, v121, v231\n\t"
    "v_mul_f32 v122, v122, v231\n\t"
    "v_mul_f32 v123, v123, v231\n\t"
    "v_mul_f32 v124, v124, v231\n\t"
    "v_mul_f32 v125, v125, v231\n\t"
    "v_mul_f32 v126, v126, v231\n\t"
    "v_mul_f32 v127, v127, v231\n\t"
    "v_cvt_pk_bf16_f32 v120, v120, v121\n\t"
    "v_cvt_pk_bf16_f32 v121, v122, v123\n\t"
    "v_cvt_pk_bf16_f32 v122, v124, v125\n\t"
    "v_cvt_pk_bf16_f32 v123, v126, v127\n\t"
    "buffer_store_dwordx4 v[120:123], %[xl], %[srd], s45 offen offset:3072 sc1\n\t"
    "v_accvgpr_read_b32 v112, a104\n\t"
    "v_accvgpr_read_b32 v113, a105\n\t"
    "v_accvgpr_read_b32 v114, a106\n\t"
    "v_accvgpr_read_b32 v115, a107\n\t"
    "v_accvgpr_read_b32 v116, a120\n\t"
    "v_accvgpr_read_b32 v117, a121\n\t"
    "v_accvgpr_read_b32 v118, a122\n\t"
    "v_accvgpr_read_b32 v119, a123\n\t"
    "v_mul_f32 v112, v112, v230\n\t"
    "v_mul_f32 v113, v113, v230\n\t"
    "v_mul_f32 v114, v114, v230\n\t"
    "v_mul_f32 v115, v115, v230\n\t"
    "v_mul_f32 v116, v116, v230\n\t"
    "v_mul_f32 v117, v117, v230\n\t"
    "v_mul_f32 v118, v118, v230\n\t"
    "v_mul_f32 v119, v119, v230\n\t"
    "v_cvt_pk_bf16_f32 v112, v112, v113\n\t"
    "v_cvt_pk_bf16_f32 v113, v114, v115\n\t"
    "v_cvt_pk_bf16_f32 v114, v116, v117\n\t"
    "v_cvt_pk_bf16_f32 v115, v118, v119\n\t"
    "buffer_store_dwordx4 v[112:115], %[xl], %[srd], s44 offen offset:3072 sc1\n\t"
    "v_accvgpr_read_b32 v104, a100\n\t"
    "v_accvgpr_read_b32 v105, a101\n\t"
    "v_accvgpr_read_b32 v106, a102\n\t"
    "v_accvgpr_read_b32 v107, a103\n\t"
    "v_accvgpr_read_b32 v108, a116\n\t"
    "v_accvgpr_read_b32 v109, a117\n\t"
    "v_accvgpr_read_b32 v110, a118\n\t"
    "v_accvgpr_read_b32 v111, a119\n\t"
    "v_mul_f32 v104, v104, v229\n\t"
    "v_mul_f32 v105, v105, v229\n\t"
    "v_mul_f32 v106, v106, v229\n\t"
    "v_mul_f32 v107, v107, v229\n\t"
    "v_mul_f32 v108, v108, v229\n\t"
    "v_mul_f32 v109, v109, v229\n\t"
    "v_mul_f32 v110, v110, v229\n\t"
    "v_mul_f32 v111, v111, v229\n\t"
    "v_cvt_pk_bf16_f32 v104, v104, v105\n\t"
    "v_cvt_pk_bf16_f32 v105, v106, v107\n\t"
    "v_cvt_pk_bf16_f32 v106, v108, v109\n\t"
    "v_cvt_pk_bf16_f32 v107, v110, v111\n\t"
    "buffer_store_dwordx4 v[104:107], %[xl], %[srd], s43 offen offset:3072 sc1\n\t"
    "v_accvgpr_read_b32 v96, a96\n\t"
    "v_accvgpr_read_b32 v97, a97\n\t"
    "v_accvgpr_read_b32 v98, a98\n\t"
    "v_accvgpr_read_b32 v99, a99\n\t"
    "v_accvgpr_read_b32 v100, a112\n\t"
    "v_accvgpr_read_b32 v101, a113\n\t"
    "v_accvgpr_read_b32 v102, a114\n\t"
    "v_accvgpr_read_b32 v103, a115\n\t"
    "v_mul_f32 v96, v96, v228\n\t"
    "v_mul_f32 v97, v97, v228\n\t"
    "v_mul_f32 v98, v98, v228\n\t"
    "v_mul_f32 v99, v99, v228\n\t"
    "v_mul_f32 v100, v100, v228\n\t"
    "v_mul_f32 v101, v101, v228\n\t"
    "v_mul_f32 v102, v102, v228\n\t"
    "v_mul_f32 v103, v103, v228\n\t"
    "v_cvt_pk_bf16_f32 v96, v96, v97\n\t"
    "v_cvt_pk_bf16_f32 v97, v98, v99\n\t"
    "v_cvt_pk_bf16_f32 v98, v100, v101\n\t"
    "v_cvt_pk_bf16_f32 v99, v102, v103\n\t"
    "buffer_store_dwordx4 v[96:99], %[xl], %[srd], s42 offen offset:3072 sc1\n\t"
    "s_branch Lend_%=\n\t"
    "Lyt_%=:\n\t"
    "v_exp_f32 v188, v188\n\t"
    "ds_read_b64_tr_b16 a[240:241], %[vb0] offset:49664\n\t"
    "v_exp_f32 v189, v189\n\t"
    "ds_read_b64_tr_b16 a[242:243], %[vb0] offset:53760\n\t"
    "v_cvt_pk_bf16_f32 v151, v186, v187\n\t"
    "v_exp_f32 v190, v190\n\t"
    "ds_read_b64_tr_b16 a[244:245], %[vb0] offset:57856\n\t"
    "v_cvt_pk_bf16_f32 v158, v188, v189\n\t"
    "v_exp_f32 v191, v191\n\t"
    "ds_read_b64_tr_b16 a[246:247], %[vb0] offset:61952\n\t"
    "v_cvt_pk_bf16_f32 v159, v190, v191\n\t"
    "s_waitcnt vmcnt(19)\n\t"
    "v_cmp_ne_u32 vcc, 1, v227\n\t"
    "s_and_b64 vcc, vcc, %[ym]\n\t"
    "s_cbranch_vccnz Lfxs_%=\n\t"
    "Lfxk_%=:\n\t"
    "v_mfma_f32_16x16x32_bf16 v[208:211], v[236:239], v[128:131], v[208:211]\n\t"
    "ds_read_b64_tr_b16 a[248:249], %[vb1] offset:49664\n\t"
    "v_mfma_f32_16x16x32_bf16 v[208:211], v[236:239], v[132:135], v[208:211]\n\t"
    "ds_read_b64_tr_b16 a[250:251], %[vb1] offset:53760\n\t"
    "v_add_f32 v216, v216, v187\n\t"
    "ds_read_b64_tr_b16 a[252:253], %[vb1] offset:57856\n\t"
    "ds_read_b64_tr_b16 a[254:255], %[vb1] offset:61952\n\t"
    "v_mfma_f32_16x16x32_bf16 v[212:215], v[236:239], v[136:139], v[212:215]\n\t"
    "v_mfma_f32_16x16x32_bf16 v[212:215], v[236:239], v[140:143], v[212:215]\n\t"
    "v_add_f32 v220, v220, v188\n\t"
    "v_add_f32 v220, v220, v189\n\t"
    "v_add_f32 v220, v220, v190\n\t"
    "v_add_f32 v220, v220, v191\n\t"
    "v_mfma_f32_16x16x32_bf16 a[0:3], a[224:227], v[128:131], a[0:3]\n\t"
    "s_add_u32 m0, %[um0], 131072\n\t"
    "s_add_u32 s41, %[uws], 0\n\t"
    "v_mfma_f32_16x16x32_bf16 a[0:3], a[228:231], v[132:135], a[0:3]\n\t"
    "buffer_load_dwordx4 %[uwv], %[usrdw], s41 offen lds\n\t"
    "v_mfma_f32_16x16x32_bf16 a[4:7], a[228:231], v[140:143], a[4:7]\n\t"
    "s_add_u32 m0, %[um0], 132096\n\t"
    "s_add_u32 s41, %[uws], 73728\n\t"
    "v_mfma_f32_16x16x32_bf16 a[4:7], a[224:227], v[136:139], a[4:7]\n\t"
    "buffer_load_dwordx4 %[uwv], %[usrdw], s41 offen lds\n\t"
    "v_mfma_f32_16x16x32_bf16 a[8:11], a[224:227], v[144:147], a[8:11]\n\t"
    "s_add_u32 m0, %[um0], 133120\n\t"
    "s_add_u32 s41, %[uws], 147456\n\t"
    "v_mov_b32 v234, v216\n\t"
    "v_mov_b32 v235, v220\n\t"
    "s_nop 0\n\t"
    "v_permlane32_swap_b32 v216, v234\n\t"
    "v_mfma_f32_16x16x32_bf16 a[8:11], a[228:231], v[148:151], a[8:11]\n\t"
    "buffer_load_dwordx4 %[uwv], %[usrdw], s41 offen lds\n\t"
    "v_permlane32_swap_b32 v220, v235\n\t"
    "v_add_f32 v216, v216, v234\n\t"
    "v_add_f32 v220, v220, v235\n\t"
    "v_mfma_f32_16x16x32_bf16 a[12:15], a[228:231], v[156:159], a[12:15]\n\t"
    "s_add_u32 s41, %[uxs], 0\n\t"
    "v_mov_b32 v234, v216\n\t"
    "v_mov_b32 v235, v220\n\t"
    "s_nop 0\n\t"
    "v_permlane16_swap_b32 v216, v234\n\t"
    "v_mfma_f32_16x16x32_bf16 a[12:15], a[224:227], v[152:155], a[12:15]\n\t"
    "buffer_load_dwordx4 a[128:131], %[xl], %[srd], s41 offen sc1\n\t"
    "v_permlane16_swap_b32 v220, v235\n\t"
    "v_add_f32 v216, v216, v234\n\t"
    "v_add_f32 v220, v220, v235\n\t"
    "v_mfma_f32_16x16x32_bf16 a[28:31], a[232:235], v[152:155], a[28:31]\n\t"
    "v_mov_b32 v209, v212\n\t"
    "v_mov_b32 v210, v216\n\t"
    "v_mov_b32 v211, v220\n\t"
    "v_mfma_f32_16x16x32_bf16 a[28:31], a[236:239], v[156:159], a[28:31]\n\t"
    "buffer_load_dwordx4 a[144:147], %[xl], %[srd], s41 offen offset:1024 sc1\n\t"
    "v_rcp_f32 v228, v208\n\t"
    "v_rcp_f32 v229, v209\n\t"
    "v_rcp_f32 v230, v210\n\t"
    "v_mfma_f32_16x16x32_bf16 a[24:27], a[236:239], v[148:151], a[24:27]\n\t"
    "s_add_u32 s42, %[os], 0\n\t"
    "s_add_u32 s43, %[os], 278528\n\t"
    "v_rcp_f32 v231, v211\n\t"
    "v_mfma_f32_16x16x32_bf16 a[24:27], a[232:235], v[144:147], a[24:27]\n\t"
    "buffer_load_dwordx4 a[160:163], %[xl], %[srd], s41 offen offset:2048 sc1\n\t"
    "s_add_u32 s44, %[os], 557056\n\t"
    "s_add_u32 s45, %[os], 835584\n\t"
    "v_mfma_f32_16x16x32_bf16 a[20:23], a[232:235], v[136:139], a[20:23]\n\t"
    "s_add_u32 s41, %[uxs], 18432\n\t"
    "v_mfma_f32_16x16x32_bf16 a[20:23], a[236:239], v[140:143], a[20:23]\n\t"
    "buffer_load_dwordx4 a[132:135], %[xl], %[srd], s41 offen sc1\n\t"
    "v_mfma_f32_16x16x32_bf16 a[16:19], a[236:239], v[132:135], a[16:19]\n\t"
    "v_mfma_f32_16x16x32_bf16 a[16:19], a[232:235], v[128:131], a[16:19]\n\t"
    "buffer_load_dwordx4 a[148:151], %[xl], %[srd], s41 offen offset:1024 sc1\n\t"
    "s_waitcnt lgkmcnt(0)\n\t"
    "ds_read_b64_tr_b16 a[224:225], %[vb0] offset:50176\n\t"
    "ds_read_b64_tr_b16 a[226:227], %[vb0] offset:54272\n\t"
    "ds_read_b64_tr_b16 a[228:229], %[vb0] offset:58368\n\t"
    "ds_read_b64_tr_b16 a[230:231], %[vb0] offset:62464\n\t"
    "ds_read_b64_tr_b16 a[232:233], %[vb1] offset:50176\n\t"
    "ds_read_b64_tr_b16 a[234:235], %[vb1] offset:54272\n\t"
    "ds_read_b64_tr_b16 a[236:237], %[vb1] offset:58368\n\t"
    "ds_read_b64_tr_b16 a[238:239], %[vb1] offset:62464\n\t"
    "v_mfma_f32_16x16x32_bf16 a[32:35], a[240:243], v[128:131], a[32:35]\n\t"
    "v_accvgpr_read_b32 v120, a12\n\t"
    "v_accvgpr_read_b32 v121, a13\n\t"
    "v_accvgpr_read_b32 v122, a14\n\t"
    "v_accvgpr_read_b32 v123, a15\n\t"
    "v_accvgpr_read_b32 v124, a28\n\t"
    "v_accvgpr_read_b32 v125, a29\n\t"
    "v_mfma_f32_16x16x32_bf16 a[32:35], a[244:247], v[132:135], a[32:35]\n\t"
    "buffer_load_dwordx4 a[164:167], %[xl], %[srd], s41 offen offset:2048 sc1\n\t"
    "v_accvgpr_read_b32 v126, a30\n\t"
    "v_accvgpr_read_b32 v127, a31\n\t"
    "v_mul_f32 v120, v120, v231\n\t"
    "v_mul_f32 v121, v121, v231\n\t"
    "v_mul_f32 v122, v122, v231\n\t"
    "v_mul_f32 v123, v123, v231\n\t"
    "v_mfma_f32_16x16x32_bf16 a[36:39], a[244:247], v[140:143], a[36:39]\n\t"
    "s_add_u32 s41, %[uxs], 36864\n\t"
    "v_mul_f32 v124, v124, v231\n\t"
    "v_mul_f32 v125, v125, v231\n\t"
    "v_mul_f32 v126, v126, v231\n\t"
    "v_mul_f32 v127, v127, v231\n\t"
    "v_cvt_pk_bf16_f32 v120, v120, v121\n\t"
    "v_mfma_f32_16x16x32_bf16 a[36:39], a[240:243], v[136:139], a[36:39]\n\t"
    "buffer_load_dwordx4 a[136:139], %[xl], %[srd], s41 offen sc1\n\t"
    "v_cvt_pk_bf16_f32 v121, v122, v123\n\t"
    "v_cvt_pk_bf16_f32 v122, v124, v125\n\t"
    "v_cvt_pk_bf16_f32 v123, v126, v127\n\t"
    "s_waitcnt vmcnt(10)\n\t"
    "buffer_store_dwordx4 v[120:123], %[xl], %[srd], s45 offen sc1\n\t"
    "v_accvgpr_read_b32 v112, a8\n\t"
    "v_accvgpr_read_b32 v113, a9\n\t"
    "v_mfma_f32_16x16x32_bf16 a[40:43], a[240:243], v[144:147], a[40:43]\n\t"
    "v_accvgpr_read_b32 v114, a10\n\t"
    "v_accvgpr_read_b32 v115, a11\n\t"
    "v_accvgpr_read_b32 v116, a24\n\t"
    "v_accvgpr_read_b32 v117, a25\n\t"
    "v_accvgpr_read_b32 v118, a26\n\t"
    "v_mfma_f32_16x16x32_bf16 a[40:43], a[244:247], v[148:151], a[40:43]\n\t"
    "buffer_load_dwordx4 a[152:155], %[xl], %[srd], s41 offen offset:1024 sc1\n\t"
    "v_accvgpr_read_b32 v119, a27\n\t"
    "v_mul_f32 v112, v112, v230\n\t"
    "v_mul_f32 v113, v113, v230\n\t"
    "v_mul_f32 v114, v114, v230\n\t"
    "v_mul_f32 v115, v115, v230\n\t"
    "v_mul_f32 v116, v116, v230\n\t"
    "v_mfma_f32_16x16x32_bf16 a[44:47], a[244:247], v[156:159], a[44:47]\n\t"
    "v_mul_f32 v117, v117, v230\n\t"
    "v_mul_f32 v118, v118, v230\n\t"
    "v_mul_f32 v119, v119, v230\n\t"
    "v_cvt_pk_bf16_f32 v112, v112, v113\n\t"
    "v_cvt_pk_bf16_f32 v113, v114, v115\n\t"
    "v_cvt_pk_bf16_f32 v114, v116, v117\n\t"
    "v_mfma_f32_16x16x32_bf16 a[44:47], a[240:243], v[152:155], a[44:47]\n\t"
    "buffer_load_dwordx4 a[168:171], %[xl], %[srd], s41 offen offset:2048 sc1\n\t"
    "v_cvt_pk_bf16_f32 v115, v118, v119\n\t"
    "buffer_store_dwordx4 v[112:115], %[xl], %[srd], s44 offen sc1\n\t"
    "v_accvgpr_read_b32 v104, a4\n\t"
    "v_accvgpr_read_b32 v105, a5\n\t"
    "v_accvgpr_read_b32 v106, a6\n\t"
    "v_mfma_f32_16x16x32_bf16 a[60:63], a[248:251], v[152:155], a[60:63]\n\t"
    "s_add_u32 s41, %[uxs], 55296\n\t"
    "v_accvgpr_read_b32 v107, a7\n\t"
    "v_accvgpr_read_b32 v108, a20\n\t"
    "v_accvgpr_read_b32 v109, a21\n\t"
    "v_accvgpr_read_b32 v110, a22\n\t"
    "v_accvgpr_read_b32 v111, a23\n\t"
    "v_mul_f32 v104, v104, v229\n\t"
    "v_mfma_f32_16x16x32_bf16 a[60:63], a[252:255], v[156:159], a[60:63]\n\t"
    "buffer_load_dwordx4 a[140:143], %[xl], %[srd], s41 offen sc1\n\t"
    "v_mul_f32 v105, v105, v229\n\t"
    "v_mul_f32 v106, v106, v229\n\t"
    "v_mul_f32 v107, v107, v229\n\t"
    "v_mul_f32 v108, v108, v229\n\t"
    "v_mul_f32 v109, v109, v229\n\t"
    "v_mfma_f32_16x16x32_bf16 a[56:59], a[252:255], v[148:151], a[56:59]\n\t"
    "v_mul_f32 v110, v110, v229\n\t"
    "v_mul_f32 v111, v111, v229\n\t"
    "v_cvt_pk_bf16_f32 v104, v104, v105\n\t"
    "v_cvt_pk_bf16_f32 v105, v106, v107\n\t"
    "v_cvt_pk_bf16_f32 v106, v108, v109\n\t"
    "v_cvt_pk_bf16_f32 v107, v110, v111\n\t"
    "v_mfma_f32_16x16x32_bf16 a[56:59], a[248:251], v[144:147], a[56:59]\n\t"
    "buffer_load_dwordx4 a[156:159], %[xl], %[srd], s41 offen offset:1024 sc1\n\t"
    "buffer_store_dwordx4 v[104:107], %[xl], %[srd], s43 offen sc1\n\t"
    "v_accvgpr_read_b32 v96, a0\n\t"
    "v_accvgpr_read_b32 v97, a1\n\t"
    "v_accvgpr_read_b32 v98, a2\n\t"
    "v_accvgpr_read_b32 v99, a3\n\t"
    "v_accvgpr_read_b32 v100, a16\n\t"
    "v_mfma_f32_16x16x32_bf16 a[52:55], a[248:251], v[136:139], a[52:55]\n\t"
    "v_accvgpr_read_b32 v101, a17\n\t"
    "v_accvgpr_read_b32 v102, a18\n\t"
    "v_accvgpr_read_b32 v103, a19\n\t"
    "v_mul_f32 v96, v96, v228\n\t"
    "v_mul_f32 v97, v97, v228\n\t"
    "v_mfma_f32_16x16x32_bf16 a[52:55], a[252:255], v[140:143], a[52:55]\n\t"
    "buffer_load_dwordx4 a[172:175], %[xl], %[srd], s41 offen offset:2048 sc1\n\t"
    "v_mul_f32 v98, v98, v228\n\t"
    "v_mul_f32 v99, v99, v228\n\t"
    "v_mul_f32 v100, v100, v228\n\t"
    "v_mul_f32 v101, v101, v228\n\t"
    "v_mul_f32 v102, v102, v228\n\t"
    "v_mul_f32 v103, v103, v228\n\t"
    "v_mfma_f32_16x16x32_bf16 a[48:51], a[252:255], v[132:135], a[48:51]\n\t"
    "s_add_u32 m0, %[um0], 143360\n\t"
    "s_add_u32 s41, %[uws], 884736\n\t"
    "v_cvt_pk_bf16_f32 v96, v96, v97\n\t"
    "v_cvt_pk_bf16_f32 v97, v98, v99\n\t"
    "v_cvt_pk_bf16_f32 v98, v100, v101\n\t"
    "v_cvt_pk_bf16_f32 v99, v102, v103\n\t"
    "buffer_store_dwordx4 v[96:99], %[xl], %[srd], s42 offen sc1\n\t"
    "v_mfma_f32_16x16x32_bf16 a[48:51], a[248:251], v[128:131], a[48:51]\n\t"
    "buffer_load_dwordx4 %[uwv], %[usrdw], s41 offen lds\n\t"
    "s_waitcnt lgkmcnt(0)\n\t"
    "ds_read_b64_tr_b16 a[240:241], %[vb0] offset:50688\n\t"
    "ds_read_b64_tr_b16 a[242:243], %[vb0] offset:54784\n\t"
    "ds_read_b64_tr_b16 a[244:245], %[vb0] offset:58880\n\t"
    "ds_read_b64_tr_b16 a[246:247], %[vb0] offset:62976\n\t"
    "ds_read_b64_tr_b16 a[248:249], %[vb1] offset:50688\n\t"
    "ds_read_b64_tr_b16 a[250:251], %[vb1] offset:54784\n\t"
    "ds_read_b64_tr_b16 a[252:253], %[vb1] offset:58880\n\t"
    "ds_read_b64_tr_b16 a[254:255], %[vb1] offset:62976\n\t"
    "v_mfma_f32_16x16x32_bf16 a[64:67], a[224:227], v[128:131], a[64:67]\n\t"
    "s_add_u32 m0, %[um0], 144384\n\t"
    "s_add_u32 s41, %[uws], 958464\n\t"
    "v_accvgpr_read_b32 v120, a44\n\t"
    "v_accvgpr_read_b32 v121, a45\n\t"
    "v_accvgpr_read_b32 v122, a46\n\t"
    "v_accvgpr_read_b32 v123, a47\n\t"
    "v_accvgpr_read_b32 v124, a60\n\t"
    "v_accvgpr_read_b32 v125, a61\n\t"
    "v_mfma_f32_16x16x32_bf16 a[64:67], a[228:231], v[132:135], a[64:67]\n\t"
    "buffer_load_dwordx4 %[uwv], %[usrdw], s41 offen lds\n\t"
    "v_accvgpr_read_b32 v126, a62\n\t"
    "v_accvgpr_read_b32 v127, a63\n\t"
    "v_mul_f32 v120, v120, v231\n\t"
    "v_mul_f32 v121, v121, v231\n\t"
    "v_mul_f32 v122, v122, v231\n\t"
    "v_mul_f32 v123, v123, v231\n\t"
    "v_mfma_f32_16x16x32_bf16 a[68:71], a[228:231], v[140:143], a[68:71]\n\t"
    "s_add_u32 m0, %[um0], 145408\n\t"
    "s_add_u32 s41, %[uws], 1032192\n\t"
    "v_mul_f32 v124, v124, v231\n\t"
    "v_mul_f32 v125, v125, v231\n\t"
    "v_mul_f32 v126, v126, v231\n\t"
    "v_mul_f32 v127, v127, v231\n\t"
    "v_cvt_pk_bf16_f32 v120, v120, v121\n\t"
    "v_mfma_f32_16x16x32_bf16 a[68:71], a[224:227], v[136:139], a[68:71]\n\t"
    "buffer_load_dwordx4 %[uwv], %[usrdw], s41 offen lds\n\t"
    "v_cvt_pk_bf16_f32 v121, v122, v123\n\t"
    "v_cvt_pk_bf16_f32 v122, v124, v125\n\t"
    "v_cvt_pk_bf16_f32 v123, v126, v127\n\t"
    "buffer_store_dwordx4 v[120:123], %[xl], %[srd], s45 offen offset:1024 sc1\n\t"
    "v_accvgpr_read_b32 v112, a40\n\t"
    "v_accvgpr_read_b32 v113, a41\n\t"
    "v_mfma_f32_16x16x32_bf16 a[72:75], a[224:227], v[144:147], a[72:75]\n\t"
    "s_add_u32 s41, %[uxs], 3072\n\t"
    "v_accvgpr_read_b32 v114, a42\n\t"
    "v_accvgpr_read_b32 v115, a43\n\t"
    "v_accvgpr_read_b32 v116, a56\n\t"
    "v_accvgpr_read_b32 v117, a57\n\t"
    "v_accvgpr_read_b32 v118, a58\n\t"
    "v_mfma_f32_16x16x32_bf16 a[72:75], a[228:231], v[148:151], a[72:75]\n\t"
    "buffer_load_dwordx4 a[176:179], %[xl], %[srd], s41 offen sc1\n\t"
    "v_accvgpr_read_b32 v119, a59\n\t"
    "v_mul_f32 v112, v112, v230\n\t"
    "v_mul_f32 v113, v113, v230\n\t"
    "v_mul_f32 v114, v114, v230\n\t"
    "v_mul_f32 v115, v115, v230\n\t"
    "v_mul_f32 v116, v116, v230\n\t"
    "v_mfma_f32_16x16x32_bf16 a[76:79], a[228:231], v[156:159], a[76:79]\n\t"
    "v_mul_f32 v117, v117, v230\n\t"
    "v_mul_f32 v118, v118, v230\n\t"
    "v_mul_f32 v119, v119, v230\n\t"
    "v_cvt_pk_bf16_f32 v112, v112, v113\n\t"
    "v_cvt_pk_bf16_f32 v113, v114, v115\n\t"
    "v_cvt_pk_bf16_f32 v114, v116, v117\n\t"
    "v_mfma_f32_16x16x32_bf16 a[76:79], a[224:227], v[152:155], a[76:79]\n\t"
    "buffer_load_dwordx4 a[192:195], %[xl], %[srd], s41 offen offset:1024 sc1\n\t"
    "v_cvt_pk_bf16_f32 v115, v118, v119\n\t"
    "buffer_store_dwordx4 v[112:115], %[xl], %[srd], s44 offen offset:1024 sc1\n\t"
    "v_accvgpr_read_b32 v104, a36\n\t"
    "v_accvgpr_read_b32 v105, a37\n\t"
    "v_accvgpr_read_b32 v106, a38\n\t"
    "v_mfma_f32_16x16x32_bf16 a[92:95], a[232:235], v[152:155], a[92:95]\n\t"
    "v_accvgpr_read_b32 v107, a39\n\t"
    "v_accvgpr_read_b32 v108, a52\n\t"
    "v_accvgpr_read_b32 v109, a53\n\t"
    "v_accvgpr_read_b32 v110, a54\n\t"
    "v_accvgpr_read_b32 v111, a55\n\t"
    "v_mul_f32 v104, v104, v229\n\t"
    "v_mfma_f32_16x16x32_bf16 a[92:95], a[236:239], v[156:159], a[92:95]\n\t"
    "buffer_load_dwordx4 a[208:211], %[xl], %[srd], s41 offen offset:2048 sc1\n\t"
    "v_mul_f32 v105, v105, v229\n\t"
    "v_mul_f32 v106, v106, v229\n\t"
    "v_mul_f32 v107, v107, v229\n\t"
    "v_mul_f32 v108, v108, v229\n\t"
    "v_mul_f32 v109, v109, v229\n\t"
    "v_mfma_f32_16x16x32_bf16 a[88:91], a[236:239], v[148:151], a[88:91]\n\t"
    "s_add_u32 s41, %[uxs], 21504\n\t"
    "v_mul_f32 v110, v110, v229\n\t"
    "v_mul_f32 v111, v111, v229\n\t"
    "v_cvt_pk_bf16_f32 v104, v104, v105\n\t"
    "v_cvt_pk_bf16_f32 v105, v106, v107\n\t"
    "v_cvt_pk_bf16_f32 v106, v108, v109\n\t"
    "v_cvt_pk_bf16_f32 v107, v110, v111\n\t"
    "v_mfma_f32_16x16x32_bf16 a[88:91], a[232:235], v[144:147], a[88:91]\n\t"
    "buffer_load_dwordx4 a[180:183], %[xl], %[srd], s41 offen sc1\n\t"
    "buffer_store_dwordx4 v[104:107], %[xl], %[srd], s43 offen offset:1024 sc1\n\t"
    "v_accvgpr_read_b32 v96, a32\n\t"
    "v_accvgpr_read_b32 v97, a33\n\t"
    "v_accvgpr_read_b32 v98, a34\n\t"
    "v_accvgpr_read_b32 v99, a35\n\t"
    "v_accvgpr_read_b32 v100, a48\n\t"
    "v_mfma_f32_16x16x32_bf16 a[84:87], a[232:235], v[136:139], a[84:87]\n\t"
    "v_accvgpr_read_b32 v101, a49\n\t"
    "v_accvgpr_read_b32 v102, a50\n\t"
    "v_accvgpr_read_b32 v103, a51\n\t"
    "v_mul_f32 v96, v96, v228\n\t"
    "v_mul_f32 v97, v97, v228\n\t"
    "v_mfma_f32_16x16x32_bf16 a[84:87], a[236:239], v[140:143], a[84:87]\n\t"
    "buffer_load_dwordx4 a[196:199], %[xl], %[srd], s41 offen offset:1024 sc1\n\t"
    "v_mul_f32 v98, v98, v228\n\t"
    "v_mul_f32 v99, v99, v228\n\t"
    "v_mul_f32 v100, v100, v228\n\t"
    "v_mul_f32 v101, v101, v228\n\t"
    "v_mul_f32 v102, v102, v228\n\t"
    "v_mul_f32 v103, v103, v228\n\t"
    "v_mfma_f32_16x16x32_bf16 a[80:83], a[236:239], v[132:135], a[80:83]\n\t"
    "v_cvt_pk_bf16_f32 v96, v96, v97\n\t"
    "v_cvt_pk_bf16_f32 v97, v98, v99\n\t"
    "v_cvt_pk_bf16_f32 v98, v100, v101\n\t"
    "v_cvt_pk_bf16_f32 v99, v102, v103\n\t"
    "buffer_store_dwordx4 v[96:99], %[xl], %[srd], s42 offen offset:1024 sc1\n\t"
    "v_mfma_f32_16x16x32_bf16 a[80:83], a[232:235], v[128:131], a[80:83]\n\t"
    "buffer_load_dwordx4 a[212:215], %[xl], %[srd], s41 offen offset:2048 sc1\n\t"
    "s_waitcnt lgkmcnt(0)\n\t"
    "v_mfma_f32_16x16x32_bf16 a[96:99], a[240:243], v[128:131], a[96:99]\n\t"
    "s_add_u32 s41, %[uxs], 39936\n\t"
    "v_accvgpr_read_b32 v120, a76\n\t"
    "v_accvgpr_read_b32 v121, a77\n\t"
    "v_accvgpr_read_b32 v122, a78\n\t"
    "v_accvgpr_read_b32 v123, a79\n\t"
    "v_accvgpr_read_b32 v124, a92\n\t"
    "v_accvgpr_read_b32 v125, a93\n\t"
    "v_mfma_f32_16x16x32_bf16 a[96:99], a[244:247], v[132:135], a[96:99]\n\t"
    "buffer_load_dwordx4 a[184:187], %[xl], %[srd], s41 offen sc1\n\t"
    "v_accvgpr_read_b32 v126, a94\n\t"
    "v_accvgpr_read_b32 v127, a95\n\t"
    "v_mul_f32 v120, v120, v231\n\t"
    "v_mul_f32 v121, v121, v231\n\t"
    "v_mul_f32 v122, v122, v231\n\t"
    "v_mul_f32 v123, v123, v231\n\t"
    "v_mfma_f32_16x16x32_bf16 a[100:103], a[244:247], v[140:143], a[100:103]\n\t"
    "v_mul_f32 v124, v124, v231\n\t"
    "v_mul_f32 v125, v125, v231\n\t"
    "v_mul_f32 v126, v126, v231\n\t"
    "v_mul_f32 v127, v127, v231\n\t"
    "v_cvt_pk_bf16_f32 v120, v120, v121\n\t"
    "v_mfma_f32_16x16x32_bf16 a[100:103], a[240:243], v[136:139], a[100:103]\n\t"
    "buffer_load_dwordx4 a[200:203], %[xl], %[srd], s41 offen offset:1024 sc1\n\t"
    "v_cvt_pk_bf16_f32 v121, v122, v123\n\t"
    "v_cvt_pk_bf16_f32 v122, v124, v125\n\t"
    "v_cvt_pk_bf16_f32 v123, v126, v127\n\t"
    "buffer_store_dwordx4 v[120:123], %[xl], %[srd], s45 offen offset:2048 sc1\n\t"
    "v_accvgpr_read_b32 v112, a72\n\t"
    "v_accvgpr_read_b32 v113, a73\n\t"
    "v_mfma_f32_16x16x32_bf16 a[104:107], a[240:243], v[144:147], a[104:107]\n\t"
    "v_accvgpr_read_b32 v114, a74\n\t"
    "v_accvgpr_read_b32 v115, a75\n\t"
    "v_accvgpr_read_b32 v116, a88\n\t"
    "v_accvgpr_read_b32 v117, a89\n\t"
    "v_accvgpr_read_b32 v118, a90\n\t"
    "v_mfma_f32_16x16x32_bf16 a[104:107], a[244:247], v[148:151], a[104:107]\n\t"
    "buffer_load_dwordx4 a[216:219], %[xl], %[srd], s41 offen offset:2048 sc1\n\t"
    "v_accvgpr_read_b32 v119, a91\n\t"
    "v_mul_f32 v112, v112, v230\n\t"
    "v_mul_f32 v113, v113, v230\n\t"
    "v_mul_f32 v114, v114, v230\n\t"
    "v_mul_f32 v115, v115, v230\n\t"
    "v_mul_f32 v116, v116, v230\n\t"
    "v_mfma_f32_16x16x32_bf16 a[108:111], a[244:247], v[156:159], a[108:111]\n\t"
    "s_add_u32 s41, %[uxs], 58368\n\t"
    "v_mul_f32 v117, v117, v230\n\t"
    "v_mul_f32 v118, v118, v230\n\t"
    "v_mul_f32 v119, v119, v230\n\t"
    "v_cvt_pk_bf16_f32 v112, v112, v113\n\t"
    "v_cvt_pk_bf16_f32 v113, v114, v115\n\t"
    "v_cvt_pk_bf16_f32 v114, v116, v117\n\t"
    "v_mfma_f32_16x16x32_bf16 a[108:111], a[240:243], v[152:155], a[108:111]\n\t"
    "buffer_load_dwordx4 a[188:191], %[xl], %[srd], s41 offen sc1\n\t"
    "v_cvt_pk_bf16_f32 v115, v118, v119\n\t"
    "buffer_store_dwordx4 v[112:115], %[xl], %[srd], s44 offen offset:2048 sc1\n\t"
    "v_accvgpr_read_b32 v104, a68\n\t"
    "v_accvgpr_read_b32 v105, a69\n\t"
    "v_accvgpr_read_b32 v106, a70\n\t"
    "v_mfma_f32_16x16x32_bf16 a[124:127], a[248:251], v[152:155], a[124:127]\n\t"
    "v_accvgpr_read_b32 v107, a71\n\t"
    "v_accvgpr_read_b32 v108, a84\n\t"
    "v_accvgpr_read_b32 v109, a85\n\t"
    "v_accvgpr_read_b32 v110, a86\n\t"
    "v_accvgpr_read_b32 v111, a87\n\t"
    "v_mul_f32 v104, v104, v229\n\t"
    "v_mfma_f32_16x16x32_bf16 a[124:127], a[252:255], v[156:159], a[124:127]\n\t"
    "buffer_load_dwordx4 a[204:207], %[xl], %[srd], s41 offen offset:1024 sc1\n\t"
    "v_mul_f32 v105, v105, v229\n\t"
    "v_mul_f32 v106, v106, v229\n\t"
    "v_mul_f32 v107, v107, v229\n\t"
    "v_mul_f32 v108, v108, v229\n\t"
    "v_mul_f32 v109, v109, v229\n\t"
    "v_mfma_f32_16x16x32_bf16 a[120:123], a[252:255], v[148:151], a[120:123]\n\t"
    "v_mul_f32 v110, v110, v229\n\t"
    "v_mul_f32 v111, v111, v229\n\t"
    "v_cvt_pk_bf16_f32 v104, v104, v105\n\t"
    "v_cvt_pk_bf16_f32 v105, v106, v107\n\t"
    "v_cvt_pk_bf16_f32 v106, v108, v109\n\t"
    "v_cvt_pk_bf16_f32 v107, v110, v111\n\t"
    "v_mfma_f32_16x16x32_bf16 a[120:123], a[248:251], v[144:147], a[120:123]\n\t"
    "buffer_load_dwordx4 a[220:223], %[xl], %[srd], s41 offen offset:2048 sc1\n\t"
    "buffer_store_dwordx4 v[104:107], %[xl], %[srd], s43 offen offset:2048 sc1\n\t"
    "v_accvgpr_read_b32 v96, a64\n\t"
    "v_accvgpr_read_b32 v97, a65\n\t"
    "v_accvgpr_read_b32 v98, a66\n\t"
    "v_accvgpr_read_b32 v99, a67\n\t"
    "v_accvgpr_read_b32 v100, a80\n\t"
    "v_mfma_f32_16x16x32_bf16 a[116:119], a[248:251], v[136:139], a[116:119]\n\t"
    "v_accvgpr_read_b32 v101, a81\n\t"
    "v_accvgpr_read_b32 v102, a82\n\t"
    "v_accvgpr_read_b32 v103, a83\n\t"
    "v_mul_f32 v96, v96, v228\n\t"
    "v_mul_f32 v97, v97, v228\n\t"
    "v_mfma_f32_16x16x32_bf16 a[116:119], a[252:255], v[140:143], a[116:119]\n\t"
    "v_mul_f32 v98, v98, v228\n\t"
    "v_mul_f32 v99, v99, v228\n\t"
    "v_mul_f32 v100, v100, v228\n\t"
    "v_mul_f32 v101, v101, v228\n\t"
    "v_mul_f32 v102, v102, v228\n\t"
    "v_mul_f32 v103, v103, v228\n\t"
    "v_mfma_f32_16x16x32_bf16 a[112:115], a[252:255], v[132:135], a[112:115]\n\t"
    "v_cvt_pk_bf16_f32 v96, v96, v97\n\t"
    "v_cvt_pk_bf16_f32 v97, v98, v99\n\t"
    "v_cvt_pk_bf16_f32 v98, v100, v101\n\t"
    "v_cvt_pk_bf16_f32 v99, v102, v103\n\t"
    "buffer_store_dwordx4 v[96:99], %[xl], %[srd], s42 offen offset:2048 sc1\n\t"
    "v_mfma_f32_16x16x32_bf16 a[112:115], a[248:251], v[128:131], a[112:115]\n\t"
    "v_accvgpr_read_b32 v120, a108\n\t"
    "v_accvgpr_read_b32 v121, a109\n\t"
    "v_accvgpr_read_b32 v122, a110\n\t"
    "v_accvgpr_read_b32 v123, a111\n\t"
    "v_accvgpr_read_b32 v124, a124\n\t"
    "v_accvgpr_read_b32 v125, a125\n\t"
    "v_accvgpr_read_b32 v126, a126\n\t"
    "v_accvgpr_read_b32 v127, a127\n\t"
    "v_mul_f32 v120, v120, v231\n\t"
    "v_mul_f32 v121, v121, v231\n\t"
    "v_mul_f32 v122, v122, v231\n\t"
    "v_mul_f32 v123, v123, v231\n\t"
    "v_mul_f32 v124, v124, v231\n\t"
    "v_mul_f32 v125, v125, v231\n\t"
    "v_mul_f32 v126, v126, v231\n\t"
    "v_mul_f32 v127, v127, v231\n\t"
    "v_cvt_pk_bf16_f32 v120, v120, v121\n\t"
    "v_cvt_pk_bf16_f32 v121, v122, v123\n\t"
    "v_cvt_pk_bf16_f32 v122, v124, v125\n\t"
    "v_cvt_pk_bf16_f32 v123, v126, v127\n\t"
    "buffer_store_dwordx4 v[120:123], %[xl], %[srd], s45 offen offset:3072 sc1\n\t"
    "v_accvgpr_read_b32 v112, a104\n\t"
    "v_accvgpr_read_b32 v113, a105\n\t"
    "v_accvgpr_read_b32 v114, a106\n\t"
    "v_accvgpr_read_b32 v115, a107\n\t"
    "v_accvgpr_read_b32 v116, a120\n\t"
    "v_accvgpr_read_b32 v117, a121\n\t"
    "v_accvgpr_read_b32 v118, a122\n\t"
    "v_accvgpr_read_b32 v119, a123\n\t"
    "v_mul_f32 v112, v112, v230\n\t"
    "v_mul_f32 v113, v113, v230\n\t"
    "v_mul_f32 v114, v114, v230\n\t"
    "v_mul_f32 v115, v115, v230\n\t"
    "v_mul_f32 v116, v116, v230\n\t"
    "v_mul_f32 v117, v117, v230\n\t"
    "v_mul_f32 v118, v118, v230\n\t"
    "v_mul_f32 v119, v119, v230\n\t"
    "v_cvt_pk_bf16_f32 v112, v112, v113\n\t"
    "v_cvt_pk_bf16_f32 v113, v114, v115\n\t"
    "v_cvt_pk_bf16_f32 v114, v116, v117\n\t"
    "v_cvt_pk_bf16_f32 v115, v118, v119\n\t"
    "buffer_store_dwordx4 v[112:115], %[xl], %[srd], s44 offen offset:3072 sc1\n\t"
    "v_accvgpr_read_b32 v104, a100\n\t"
    "v_accvgpr_read_b32 v105, a101\n\t"
    "v_accvgpr_read_b32 v106, a102\n\t"
    "v_accvgpr_read_b32 v107, a103\n\t"
    "v_accvgpr_read_b32 v108, a116\n\t"
    "v_accvgpr_read_b32 v109, a117\n\t"
    "v_accvgpr_read_b32 v110, a118\n\t"
    "v_accvgpr_read_b32 v111, a119\n\t"
    "v_mul_f32 v104, v104, v229\n\t"
    "v_mul_f32 v105, v105, v229\n\t"
    "v_mul_f32 v106, v106, v229\n\t"
    "v_mul_f32 v107, v107, v229\n\t"
    "v_mul_f32 v108, v108, v229\n\t"
    "v_mul_f32 v109, v109, v229\n\t"
    "v_mul_f32 v110, v110, v229\n\t"
    "v_mul_f32 v111, v111, v229\n\t"
    "v_cvt_pk_bf16_f32 v104, v104, v105\n\t"
    "v_cvt_pk_bf16_f32 v105, v106, v107\n\t"
    "v_cvt_pk_bf16_f32 v106, v108, v109\n\t"
    "v_cvt_pk_bf16_f32 v107, v110, v111\n\t"
    "buffer_store_dwordx4 v[104:107], %[xl], %[srd], s43 offen offset:3072 sc1\n\t"
    "v_accvgpr_read_b32 v96, a96\n\t"
    "v_accvgpr_read_b32 v97, a97\n\t"
    "v_accvgpr_read_b32 v98, a98\n\t"
    "v_accvgpr_read_b32 v99, a99\n\t"
    "v_accvgpr_read_b32 v100, a112\n\t"
    "v_accvgpr_read_b32 v101, a113\n\t"
    "v_accvgpr_read_b32 v102, a114\n\t"
    "v_accvgpr_read_b32 v103, a115\n\t"
    "v_mul_f32 v96, v96, v228\n\t"
    "v_mul_f32 v97, v97, v228\n\t"
    "v_mul_f32 v98, v98, v228\n\t"
    "v_mul_f32 v99, v99, v228\n\t"
    "v_mul_f32 v100, v100, v228\n\t"
    "v_mul_f32 v101, v101, v228\n\t"
    "v_mul_f32 v102, v102, v228\n\t"
    "v_mul_f32 v103, v103, v228\n\t"
    "v_cvt_pk_bf16_f32 v96, v96, v97\n\t"
    "v_cvt_pk_bf16_f32 v97, v98, v99\n\t"
    "v_cvt_pk_bf16_f32 v98, v100, v101\n\t"
    "v_cvt_pk_bf16_f32 v99, v102, v103\n\t"
    "buffer_store_dwordx4 v[96:99], %[xl], %[srd], s42 offen offset:3072 sc1\n\t"
    "Lend_%=:\n\t"
    "s_nop 1\n\t"
      : "+{v[32:47]}"(q0), "+{v[48:63]}"(q1), "+{v[64:79]}"(q2), "+{v[80:95]}"(q3), "=&{v[208:211]}"(ls)
      : [kb0] "v"(kb[0]), [kb1] "v"(kb[1]), [kb2] "v"(kb[2]), [kb3] "v"(kb[3]), [kv0] "v"(kv[0]), [kv1] "v"(kv[1]), [kv2] "v"(kv[2]), [kv3] "v"(kv[3]),
        [vb0] "v"(vb[0]), [vb1] "v"(vb[1]), [vv0] "v"(vv[0]), [vv1] "v"(vv[1]), [wb] "v"(wb), [wv] "v"(8192u * r8 + sw), [xl] "v"(xl),
        [mg0] "v"(mg[0]), [mg1] "v"(mg[1]), [mg2] "v"(mg[2]), [mg3] "v"(mg[3]), [uwv] "v"(9216u * r8 + sw), [fxv] "v"(FLAG(FL_X + l)),
        [srdk] "s"(srdk), [srdk1] "s"(mk_srd((const char*)Kc + (size_t)u.h * 524288 + 16384, 524288u - 16384u)), [srdv] "s"(srdv), [srdw] "s"(srdw), [srd] "s"(srd), [srdp] "s"(srdp), [srdu] "s"(srdu),
        [usrdw] "s"(usrdw), [os] "s"(WS_A + ((16u * u.qblk + 4u * w) * 272u + head_ks(u.h)) * 1024u),
        [m0b] "s"(rfl(ldsb) + 4096u * w), [m0w] "s"(rfl(ldsb) + 16384u + 6144u * w), [kvs] "s"(kvs), [kvend] "s"(kvs + 34u * 16384u),
        [ws] "s"(24u * 8192u * w), [xs] "s"(WS_XQ + (16u * u.qblk + 4u * w) * 18432u), [um0] "s"(rfl(ldsb) + 3072u * w), [uws] "s"(24u * 9216u * w),
        [uxs] "s"(u.y ? WS_X1N + (16u * u.rb + 4u * w) * 18432u : OOB), [ym] "s"(mask64(u.y)), [fxo] "s"(u.y ? OOB : FLAG(FL_X + u.qblk * 8u + u.h - 24u)), [retry] "s"(retry)
      : "v96", "v97", "v98", "v99", "v100", "v101", "v102", "v103", "v104", "v105", "v106", "v107", "v108", "v109", "v110", "v111", "v112", "v113", "v114", "v115", "v116", "v117", "v118", "v119", "v120", "v121", "v122", "v123", "v124", "v125", "v126", "v127", "v128", "v129", "v130", "v131", "v132", "v133", "v134", "v135", "v136", "v137", "v138", "v139", "v140", "v141", "v142", "v143", "v144", "v145", "v146", "v147", "v148", "v149", "v150", "v151", "v152", "v153", "v154", "v155", "v156", "v157", "v158", "v159", "v160", "v161", "v162", "v163", "v164", "v165", "v166", "v167", "v168", "v169", "v170", "v171", "v172", "v173", "v174", "v175", "v176", "v177", "v178", "v179", "v180", "v181", "v182", "v183", "v184", "v185", "v186", "v187", "v188", "v189", "v190", "v191", "v192", "v193", "v194", "v195", "v196", "v197", "v198", "v199", "v200", "v201", "v202", "v203", "v204", "v205", "v206", "v207", "v212", "v213", "v214", "v215", "v216", "v217", "v218", "v219", "v220", "v221", "v222", "v223", "v224", "v225", "v226", "v227", "v228", "v229", "v230", "v231", "v232", "v233", "v234", "v235", "v236", "v237", "v238", "v239", "a0", "a1", "a2", "a3", "a4", "a5", "a6", "a7", "a8", "a9", "a10", "a11", "a12", "a13", "a14", "a15", "a16", "a17", "a18", "a19", "a20", "a21", "a22", "a23", "a24", "a25", "a26", "a27", "a28", "a29", "a30", "a31", "a32", "a33", "a34", "a35", "a36", "a37", "a38", "a39", "a40", "a41", "a42", "a43", "a44", "a45", "a46", "a47", "a48", "a49", "a50", "a51", "a52", "a53", "a54", "a55", "a56", "a57", "a58", "a59", "a60", "a61", "a62", "a63", "a64", "a65", "a66", "a67", "a68", "a69", "a70", "a71", "a72", "a73", "a74", "a75", "a76", "a77", "a78", "a79", "a80", "a81", "a82", "a83", "a84", "a85", "a86", "a87", "a88", "a89", "a90", "a91", "a92", "a93", "a94", "a95", "a96", "a97", "a98", "a99", "a100", "a101", "a102", "a103", "a104", "a105", "a106", "a107", "a108", "a109", "a110", "a111", "a112", "a113", "a114", "a115", "a116", "a117", "a118", "a119", "a120", "a121", "a122", "a123", "a124", "a125", "a126", "a127", "a128", "a129", "a130", "a131", "a132", "a133", "a134", "a135", "a136", "a137", "a138", "a139", "a140", "a141", "a142", "a143", "a144", "a145", "a146", "a147", "a148", "a149", "a150", "a151", "a152", "a153", "a154", "a155", "a156", "a157", "a158", "a159", "a160", "a161", "a162", "a163", "a164", "a165", "a166", "a167", "a168", "a169", "a170", "a171", "a172", "a173", "a174", "a175", "a176", "a177", "a178", "a179", "a180", "a181", "a182", "a183", "a184", "a185", "a186", "a187", "a188", "a189", "a190", "a191", "a192", "a193", "a194", "a195", "a196", "a197", "a198", "a199", "a200", "a201", "a202", "a203", "a204", "a205", "a206", "a207", "a208", "a209", "a210", "a211", "a212", "a213", "a214", "a215", "a216", "a217", "a218", "a219", "a220", "a221", "a222", "a223", "a224", "a225", "a226", "a227", "a228", "a229", "a230", "a231", "a232", "a233", "a234", "a235", "a236", "a237", "a238", "a239", "a240", "a241", "a242", "a243", "a244", "a245", "a246", "a247", "a248", "a249", "a250", "a251", "a252", "a253", "a254", "a255", "s40", "s41", "s42", "s43", "s44", "s45", "s46", "s47", "vcc", "scc", "memory");
    const bool bad = !(ls[0] <= 0x1p64f) | !(ls[1] <= 0x1p64f) | !(ls[2] <= 0x1p64f) | !(ls[3] <= 0x1p64f);
    volatile LAS unsigned* fl = (volatile LAS unsigned*)(lds + 163776);
    const unsigned anyb = __any((int)bad) ? 1u : 0u;
    if (l == 0) fl[w] = anyb;
    if (!u.y) asm volatile("s_waitcnt vmcnt(0)" ::: "memory");
    asm volatile("s_waitcnt lgkmcnt(0)\n\ts_barrier" ::: "memory");
    const unsigned tot = rfl(fl[0] | fl[1] | fl[2] | fl[3]);
    if (!tot || retry) break;
    asm volatile("s_waitcnt vmcnt(0)" ::: "memory");
    const char* Kh = (const char*)Kc + (size_t)u.h * 524288;
    for (int kb16 = 0; kb16 < 128; ++kb16) {
      bf16x8 kf[4];
#pragma unroll
      for (int d = 0; d < 4; ++d) kf[d] = *(const bf16x8*)(Kh + (kb16 * 16 + i16) * 256 + d * 64 + g * 16);
#pragma unroll
      for (int qb = 0; qb < 4; ++qb) {
        const f32x16 qq = qb == 0 ? q0 : qb == 1 ? q1 : qb == 2 ? q2 : q3;
        f32x4 s = {0.f, 0.f, 0.f, 0.f};
#pragma unroll
        for (int d = 0; d < 4; ++d) {
          const f32x4 qf = {qq[4 * d], qq[4 * d + 1], qq[4 * d + 2], qq[4 * d + 3]};
          s = __builtin_amdgcn_mfma_f32_16x16x32_bf16(kf[d], __builtin_bit_cast(bf16x8, qf), s, 0, 0, 0);
        }
        mg[qb] = fmaxf(fmaxf(mg[qb], fmaxf(s[0], s[1])), fmaxf(s[2], s[3]));
      }
    }
    __syncthreads();
  }
}

DI void tail_unit(char* __restrict__ ws, const u16* __restrict__ x, const u16* __restrict__ w3, const u16* __restrict__ wo, const u16* __restrict__ w4,
                  u16* __restrict__ out, const Unit u, char* lds) {
  unsigned tid = threadIdx.x;
  asm volatile("" : "+v"(tid));
  const unsigned l = tid & 63u, w = rfl(tid >> 6), g = l >> 4, i16 = l & 15u, q4 = (l >> 2) & 3u, p4 = l & 3u;
  const unsigned tile = u.tile, mi = u.mi, rt = tile / 3u, ct = tile - 3u * rt, qb = rt >> 1, mo = mi;
  constexpr unsigned nch = NYO + 11u;
  const unsigned fa = l < NYO ? FL_O + (NYO * mi + l) * 8u + qb : FL_R + tile * 4u + (l & 3u);
  const unsigned ldsb = (unsigned)(size_t)lds, xl = 16u * l, r8 = (l >> 2) & 7u, cs = 16u * (p4 ^ (2u * (g & 1u)));
  unsigned tr[2], wv[3];
#pragma unroll
  for (unsigned e = 0; e < 2; ++e) tr[e] = ldsb + 256u * (g & 1u) + 64u * q4 + 16u * ((2u * e + (p4 >> 1)) ^ (2u * (g & 1u))) + 8u * (p4 & 1u);
#pragma unroll
  for (unsigned i = 0; i < 3; ++i) {
    const unsigned st = 2u * i + (l >> 5) + ((mo & 2u) ? 0u : 3u);
    wv[i] = 2048u + 1152u * (32u * w + r8) + 64u * (st >= 6u ? st - 6u : st) + cs - 1024u * i;
  }
  const unsigned u0 = tr[0] + 1024u * (g >> 1), u1 = tr[1] + 1024u * (g >> 1), r0 = tr[0] + 3072u * (g >> 1), r1 = tr[1] + 3072u * (g >> 1);
  const unsigned mblk = 8u * rt + 2u * w + (mo & 1u), col = 192u * ct + 96u * (mo >> 1);
  const char* A = ws + WS_A + (size_t)4096u * (NYO + 11u) * mi;
  const srd_t srdx0 = mk_srd(A + (size_t)(mblk ^ 1u) * 278528u, nch * 4096u), srdx1 = mk_srd(A + (size_t)mblk * 278528u, nch * 4096u);
  const srd_t srd = mk_srd(ws, WS_END);
  const srd_t usrdw = mk_srd((const char*)w3 + u.cb * 384u, (unsigned)(D * F * 2) - u.cb * 384u), srdi = mk_srd(x, S * D * 2);
  const char* wa = (const char*)wo + (size_t)(NYO * mi) * 147456u + ct * 384u - 2048;
  const char* wb = (const char*)w4 + (size_t)(9u * mo) * 147456u + ct * 384u - 2048;
  const char* wc = (const char*)wo + (size_t)(24u + 2u * mo) * 147456u + ct * 384u - 2048;
  unsigned eo[4], lo[4];
#pragma unroll
  for (unsigned f = 0; f < 4; ++f) {
    const unsigned b = WS_EX + (tile * 4u + (mo ^ f)) * INBOX + 3072u * w;
    eo[f] = b + (f - 1u) * 12288u;
    lo[f] = f ? b + (f + 2u) * 12288u : OOB;
  }
  const unsigned ib = WS_EX + (tile * 4u + mo) * INBOX + 3072u * w;
  constexpr unsigned NS = 6;
  f32x4 oh[6], os[6]; u32x4 xv[3], x1v[3], pr[3][NS];
  asm volatile(
    "s_nop 4\n\t"
    "s_nop 0\n\t"
    "v_mov_b32 v244, 0xbeeff558\n\t"
    "v_mov_b32 v246, 1\n\t"
    "s_add_u32 s52, %[hs], 0\n\t"
    "s_add_u32 s53, %[hs], 278528\n\t"
    "s_add_u32 s54, %[hs], 557056\n\t"
    "s_add_u32 s55, %[hs], 835584\n\t"
    "s_waitcnt vmcnt(43)\n\t"
    "s_barrier\n\t"
    "ds_read_b64_tr_b16 v[192:193], %[ur20] offset:0\n\t"
    "ds_read_b64_tr_b16 v[194:195], %[ur20] offset:2048\n\t"
    "ds_read_b64_tr_b16 v[196:197], %[ur20] offset:4096\n\t"
    "ds_read_b64_tr_b16 v[198:199], %[ur20] offset:6144\n\t"
    "ds_read_b64_tr_b16 v[200:201], %[ur20] offset:8192\n\t"
    "ds_read_b64_tr_b16 v[202:203], %[ur20] offset:10240\n\t"
    "s_waitcnt vmcnt(28) lgkmcnt(0)\n\t"
    "v_mfma_f32_16x16x32_bf16 v[64:67], v[192:195], a[128:131], 0\n\t"
    "ds_read_b64_tr_b16 v[216:217], %[ur21] offset:0\n\t"
    "s_add_u32 m0, %[um0], 0\n\t"
    "s_add_u32 s41, %[uws], 1769472\n\t"
    "v_mfma_f32_16x16x32_bf16 v[64:67], v[196:199], a[144:147], v[64:67]\n\t"
    "ds_read_b64_tr_b16 v[218:219], %[ur21] offset:2048\n\t"
    "buffer_load_dwordx4 %[uwv], %[usrdw], s41 offen lds\n\t"
    "v_mfma_f32_16x16x32_bf16 v[64:67], v[200:203], a[160:163], v[64:67]\n\t"
    "ds_read_b64_tr_b16 v[220:221], %[ur21] offset:4096\n\t"
    "s_add_u32 m0, %[um0], 1024\n\t"
    "s_add_u32 s41, %[uws], 1843200\n\t"
    "v_mfma_f32_16x16x32_bf16 v[72:75], v[200:203], a[164:167], 0\n\t"
    "ds_read_b64_tr_b16 v[222:223], %[ur21] offset:6144\n\t"
    "buffer_load_dwordx4 %[uwv], %[usrdw], s41 offen lds\n\t"
    "v_mfma_f32_16x16x32_bf16 v[72:75], v[196:199], a[148:151], v[72:75]\n\t"
    "ds_read_b64_tr_b16 v[224:225], %[ur21] offset:8192\n\t"
    "s_add_u32 m0, %[um0], 2048\n\t"
    "s_add_u32 s41, %[uws], 1916928\n\t"
    "v_mfma_f32_16x16x32_bf16 v[72:75], v[192:195], a[132:135], v[72:75]\n\t"
    "ds_read_b64_tr_b16 v[226:227], %[ur21] offset:10240\n\t"
    "buffer_load_dwordx4 %[uwv], %[usrdw], s41 offen lds\n\t"
    "v_mfma_f32_16x16x32_bf16 v[80:83], v[192:195], a[136:139], 0\n\t"
    "s_add_u32 s41, %[uxs], 6144\n\t"
    "v_mfma_f32_16x16x32_bf16 v[80:83], v[196:199], a[152:155], v[80:83]\n\t"
    "buffer_load_dwordx4 a[0:3], %[xl], %[srd], s41 offen sc1\n\t"
    "v_mfma_f32_16x16x32_bf16 v[80:83], v[200:203], a[168:171], v[80:83]\n\t"
    "v_mfma_f32_16x16x32_bf16 v[88:91], v[200:203], a[172:175], 0\n\t"
    "v_mfma_f32_16x16x32_bf16 v[88:91], v[196:199], a[156:159], v[88:91]\n\t"
    "buffer_load_dwordx4 a[16:19], %[xl], %[srd], s41 offen offset:1024 sc1\n\t"
    "v_mfma_f32_16x16x32_bf16 v[88:91], v[192:195], a[140:143], v[88:91]\n\t"
    "s_waitcnt lgkmcnt(0)\n\t"
    "v_mfma_f32_16x16x32_bf16 v[68:71], v[216:219], a[128:131], 0\n\t"
    "ds_read_b64_tr_b16 v[192:193], %[ur20] offset:512\n\t"
    "buffer_load_dwordx4 a[32:35], %[xl], %[srd], s41 offen offset:2048 sc1\n\t"
    "v_mfma_f32_16x16x32_bf16 v[68:71], v[220:223], a[144:147], v[68:71]\n\t"
    "ds_read_b64_tr_b16 v[194:195], %[ur20] offset:2560\n\t"
    "s_add_u32 s41, %[uxs], 24576\n\t"
    "v_mfma_f32_16x16x32_bf16 v[68:71], v[224:227], a[160:163], v[68:71]\n\t"
    "ds_read_b64_tr_b16 v[196:197], %[ur20] offset:4608\n\t"
    "buffer_load_dwordx4 a[4:7], %[xl], %[srd], s41 offen sc1\n\t"
    "v_mfma_f32_16x16x32_bf16 v[76:79], v[224:227], a[164:167], 0\n\t"
    "ds_read_b64_tr_b16 v[198:199], %[ur20] offset:6656\n\t"
    "v_mfma_f32_16x16x32_bf16 v[76:79], v[220:223], a[148:151], v[76:79]\n\t"
    "ds_read_b64_tr_b16 v[200:201], %[ur20] offset:8704\n\t"
    "buffer_load_dwordx4 a[20:23], %[xl], %[srd], s41 offen offset:1024 sc1\n\t"
    "v_mfma_f32_16x16x32_bf16 v[76:79], v[216:219], a[132:135], v[76:79]\n\t"
    "ds_read_b64_tr_b16 v[202:203], %[ur20] offset:10752\n\t"
    "v_mfma_f32_16x16x32_bf16 v[84:87], v[216:219], a[136:139], 0\n\t"
    "v_mfma_f32_16x16x32_bf16 v[84:87], v[220:223], a[152:155], v[84:87]\n\t"
    "buffer_load_dwordx4 a[36:39], %[xl], %[srd], s41 offen offset:2048 sc1\n\t"
    "v_mfma_f32_16x16x32_bf16 v[84:87], v[224:227], a[168:171], v[84:87]\n\t"
    "s_add_u32 s41, %[uxs], 43008\n\t"
    "v_mfma_f32_16x16x32_bf16 v[92:95], v[224:227], a[172:175], 0\n\t"
    "buffer_load_dwordx4 a[8:11], %[xl], %[srd], s41 offen sc1\n\t"
    "v_mfma_f32_16x16x32_bf16 v[92:95], v[220:223], a[156:159], v[92:95]\n\t"
    "v_mfma_f32_16x16x32_bf16 v[92:95], v[216:219], a[140:143], v[92:95]\n\t"
    "buffer_load_dwordx4 a[24:27], %[xl], %[srd], s41 offen offset:1024 sc1\n\t"
    "s_waitcnt lgkmcnt(0)\n\t"
    "v_mfma_f32_16x16x32_bf16 v[96:99], v[192:195], a[128:131], 0\n\t"
    "ds_read_b64_tr_b16 v[216:217], %[ur21] offset:512\n\t"
    "v_mfma_f32_16x16x32_bf16 v[96:99], v[196:199], a[144:147], v[96:99]\n\t"
    "ds_read_b64_tr_b16 v[218:219], %[ur21] offset:2560\n\t"
    "buffer_load_dwordx4 a[40:43], %[xl], %[srd], s41 offen offset:2048 sc1\n\t"
    "v_mfma_f32_16x16x32_bf16 v[96:99], v[200:203], a[160:163], v[96:99]\n\t"
    "ds_read_b64_tr_b16 v[220:221], %[ur21] offset:4608\n\t"
    "v_mfma_f32_16x16x32_bf16 v[104:107], v[200:203], a[164:167], 0\n\t"
    "ds_read_b64_tr_b16 v[222:223], %[ur21] offset:6656\n\t"
    "s_add_u32 s41, %[uxs], 61440\n\t"
    "v_mfma_f32_16x16x32_bf16 v[104:107], v[196:199], a[148:151], v[104:107]\n\t"
    "ds_read_b64_tr_b16 v[224:225], %[ur21] offset:8704\n\t"
    "buffer_load_dwordx4 a[12:15], %[xl], %[srd], s41 offen sc1\n\t"
    "v_mfma_f32_16x16x32_bf16 v[104:107], v[192:195], a[132:135], v[104:107]\n\t"
    "ds_read_b64_tr_b16 v[226:227], %[ur21] offset:10752\n\t"
    "v_mfma_f32_16x16x32_bf16 v[112:115], v[192:195], a[136:139], 0\n\t"
    "buffer_load_dwordx4 a[28:31], %[xl], %[srd], s41 offen offset:1024 sc1\n\t"
    "v_mfma_f32_16x16x32_bf16 v[112:115], v[196:199], a[152:155], v[112:115]\n\t"
    "v_mfma_f32_16x16x32_bf16 v[112:115], v[200:203], a[168:171], v[112:115]\n\t"
    "buffer_load_dwordx4 a[44:47], %[xl], %[srd], s41 offen offset:2048 sc1\n\t"
    "v_mfma_f32_16x16x32_bf16 v[120:123], v[200:203], a[172:175], 0\n\t"
    "v_mfma_f32_16x16x32_bf16 v[120:123], v[196:199], a[156:159], v[120:123]\n\t"
    "v_mfma_f32_16x16x32_bf16 v[120:123], v[192:195], a[140:143], v[120:123]\n\t"
    "s_waitcnt lgkmcnt(0)\n\t"
    "s_waitcnt vmcnt(21)\n\t"
    "s_barrier\n\t"
    "v_mfma_f32_16x16x32_bf16 v[100:103], v[216:219], a[128:131], 0\n\t"
    "ds_read_b64_tr_b16 v[192:193], %[ur20] offset:12288\n\t"
    "v_mfma_f32_16x16x32_bf16 v[100:103], v[220:223], a[144:147], v[100:103]\n\t"
    "ds_read_b64_tr_b16 v[194:195], %[ur20] offset:14336\n\t"
    "v_mfma_f32_16x16x32_bf16 v[100:103], v[224:227], a[160:163], v[100:103]\n\t"
    "ds_read_b64_tr_b16 v[196:197], %[ur20] offset:16384\n\t"
    "v_mfma_f32_16x16x32_bf16 v[108:111], v[224:227], a[164:167], 0\n\t"
    "ds_read_b64_tr_b16 v[198:199], %[ur20] offset:18432\n\t"
    "v_mfma_f32_16x16x32_bf16 v[108:111], v[220:223], a[148:151], v[108:111]\n\t"
    "ds_read_b64_tr_b16 v[200:201], %[ur20] offset:20480\n\t"
    "v_mfma_f32_16x16x32_bf16 v[108:111], v[216:219], a[132:135], v[108:111]\n\t"
    "ds_read_b64_tr_b16 v[202:203], %[ur20] offset:22528\n\t"
    "v_mfma_f32_16x16x32_bf16 v[116:119], v[216:219], a[136:139], 0\n\t"
    "v_mfma_f32_16x16x32_bf16 v[116:119], v[220:223], a[152:155], v[116:119]\n\t"
    "v_mfma_f32_16x16x32_bf16 v[116:119], v[224:227], a[168:171], v[116:119]\n\t"
    "v_mfma_f32_16x16x32_bf16 v[124:127], v[224:227], a[172:175], 0\n\t"
    "v_mfma_f32_16x16x32_bf16 v[124:127], v[220:223], a[156:159], v[124:127]\n\t"
    "v_mfma_f32_16x16x32_bf16 v[124:127], v[216:219], a[140:143], v[124:127]\n\t"
    "s_waitcnt lgkmcnt(0)\n\t"
    "v_mfma_f32_16x16x32_bf16 v[64:67], v[192:195], a[176:179], v[64:67]\n\t"
    "ds_read_b64_tr_b16 v[216:217], %[ur21] offset:12288\n\t"
    "s_add_u32 m0, %[um0], 12288\n\t"
    "s_add_u32 s41, %[uws], 2654208\n\t"
    "v_mfma_f32_16x16x32_bf16 v[64:67], v[196:199], a[192:195], v[64:67]\n\t"
    "ds_read_b64_tr_b16 v[218:219], %[ur21] offset:14336\n\t"
    "buffer_load_dwordx4 %[uwv], %[usrdw], s41 offen lds\n\t"
    "v_mfma_f32_16x16x32_bf16 v[64:67], v[200:203], a[208:211], v[64:67]\n\t"
    "ds_read_b64_tr_b16 v[220:221], %[ur21] offset:16384\n\t"
    "s_add_u32 m0, %[um0], 13312\n\t"
    "s_add_u32 s41, %[uws], 2727936\n\t"
    "v_mfma_f32_16x16x32_bf16 v[72:75], v[200:203], a[212:215], v[72:75]\n\t"
    "ds_read_b64_tr_b16 v[222:223], %[ur21] offset:18432\n\t"
    "buffer_load_dwordx4 %[uwv], %[usrdw], s41 offen lds\n\t"
    "v_mfma_f32_16x16x32_bf16 v[72:75], v[196:199], a[196:199], v[72:75]\n\t"
    "ds_read_b64_tr_b16 v[224:225], %[ur21] offset:20480\n\t"
    "s_add_u32 m0, %[um0], 14336\n\t"
    "s_add_u32 s41, %[uws], 2801664\n\t"
    "v_mfma_f32_16x16x32_bf16 v[72:75], v[192:195], a[180:183], v[72:75]\n\t"
    "ds_read_b64_tr_b16 v[226:227], %[ur21] offset:22528\n\t"
    "buffer_load_dwordx4 %[uwv], %[usrdw], s41 offen lds\n\t"
    "v_mfma_f32_16x16x32_bf16 v[80:83], v[192:195], a[184:187], v[80:83]\n\t"
    "s_add_u32 s41, %[uxs], 9216\n\t"
    "v_mfma_f32_16x16x32_bf16 v[80:83], v[196:199], a[200:203], v[80:83]\n\t"
    "buffer_load_dwordx4 a[48:51], %[xl], %[srd], s41 offen sc1\n\t"
    "v_mfma_f32_16x16x32_bf16 v[80:83], v[200:203], a[216:219], v[80:83]\n\t"
    "v_mfma_f32_16x16x32_bf16 v[88:91], v[200:203], a[220:223], v[88:91]\n\t"
    "v_mfma_f32_16x16x32_bf16 v[88:91], v[196:199], a[204:207], v[88:91]\n\t"
    "buffer_load_dwordx4 a[64:67], %[xl], %[srd], s41 offen offset:1024 sc1\n\t"
    "v_mfma_f32_16x16x32_bf16 v[88:91], v[192:195], a[188:191], v[88:91]\n\t"
    "s_waitcnt lgkmcnt(0)\n\t"
    "v_mfma_f32_16x16x32_bf16 v[68:71], v[216:219], a[176:179], v[68:71]\n\t"
    "ds_read_b64_tr_b16 v[192:193], %[ur20] offset:12800\n\t"
    "buffer_load_dwordx4 a[80:83], %[xl], %[srd], s41 offen offset:2048 sc1\n\t"
    "v_mfma_f32_16x16x32_bf16 v[68:71], v[220:223], a[192:195], v[68:71]\n\t"
    "ds_read_b64_tr_b16 v[194:195], %[ur20] offset:14848\n\t"
    "s_add_u32 s41, %[uxs], 27648\n\t"
    "v_mfma_f32_16x16x32_bf16 v[68:71], v[224:227], a[208:211], v[68:71]\n\t"
    "ds_read_b64_tr_b16 v[196:197], %[ur20] offset:16896\n\t"
    "buffer_load_dwordx4 a[52:55], %[xl], %[srd], s41 offen sc1\n\t"
    "v_mfma_f32_16x16x32_bf16 v[76:79], v[224:227], a[212:215], v[76:79]\n\t"
    "ds_read_b64_tr_b16 v[198:199], %[ur20] offset:18944\n\t"
    "v_mfma_f32_16x16x32_bf16 v[76:79], v[220:223], a[196:199], v[76:79]\n\t"
    "ds_read_b64_tr_b16 v[200:201], %[ur20] offset:20992\n\t"
    "buffer_load_dwordx4 a[68:71], %[xl], %[srd], s41 offen offset:1024 sc1\n\t"
    "v_mfma_f32_16x16x32_bf16 v[76:79], v[216:219], a[180:183], v[76:79]\n\t"
    "ds_read_b64_tr_b16 v[202:203], %[ur20] offset:23040\n\t"
    "v_mfma_f32_16x16x32_bf16 v[84:87], v[216:219], a[184:187], v[84:87]\n\t"
    "v_mfma_f32_16x16x32_bf16 v[84:87], v[220:223], a[200:203], v[84:87]\n\t"
    "buffer_load_dwordx4 a[84:87], %[xl], %[srd], s41 offen offset:2048 sc1\n\t"
    "v_mfma_f32_16x16x32_bf16 v[84:87], v[224:227], a[216:219], v[84:87]\n\t"
    "s_add_u32 s41, %[uxs], 46080\n\t"
    "v_mfma_f32_16x16x32_bf16 v[92:95], v[224:227], a[220:223], v[92:95]\n\t"
    "buffer_load_dwordx4 a[56:59], %[xl], %[srd], s41 offen sc1\n\t"
    "v_mfma_f32_16x16x32_bf16 v[92:95], v[220:223], a[204:207], v[92:95]\n\t"
    "v_mfma_f32_16x16x32_bf16 v[92:95], v[216:219], a[188:191], v[92:95]\n\t"
    "buffer_load_dwordx4 a[72:75], %[xl], %[srd], s41 offen offset:1024 sc1\n\t"
    "s_waitcnt lgkmcnt(0)\n\t"
    "v_mfma_f32_16x16x32_bf16 v[96:99], v[192:195], a[176:179], v[96:99]\n\t"
    "ds_read_b64_tr_b16 v[216:217], %[ur21] offset:12800\n\t"
    "v_mfma_f32_16x16x32_bf16 v[96:99], v[196:199], a[192:195], v[96:99]\n\t"
    "ds_read_b64_tr_b16 v[218:219], %[ur21] offset:14848\n\t"
    "buffer_load_dwordx4 a[88:91], %[xl], %[srd], s41 offen offset:2048 sc1\n\t"
    "v_mfma_f32_16x16x32_bf16 v[96:99], v[200:203], a[208:211], v[96:99]\n\t"
    "ds_read_b64_tr_b16 v[220:221], %[ur21] offset:16896\n\t"
    "v_mfma_f32_16x16x32_bf16 v[104:107], v[200:203], a[212:215], v[104:107]\n\t"
    "ds_read_b64_tr_b16 v[222:223], %[ur21] offset:18944\n\t"
    "s_add_u32 s41, %[uxs], 64512\n\t"
    "v_mfma_f32_16x16x32_bf16 v[104:107], v[196:199], a[196:199], v[104:107]\n\t"
    "ds_read_b64_tr_b16 v[224:225], %[ur21] offset:20992\n\t"
    "buffer_load_dwordx4 a[60:63], %[xl], %[srd], s41 offen sc1\n\t"
    "v_mfma_f32_16x16x32_bf16 v[104:107], v[192:195], a[180:183], v[104:107]\n\t"
    "ds_read_b64_tr_b16 v[226:227], %[ur21] offset:23040\n\t"
    "v_mfma_f32_16x16x32_bf16 v[112:115], v[192:195], a[184:187], v[112:115]\n\t"
    "buffer_load_dwordx4 a[76:79], %[xl], %[srd], s41 offen offset:1024 sc1\n\t"
    "v_mfma_f32_16x16x32_bf16 v[112:115], v[196:199], a[200:203], v[112:115]\n\t"
    "v_mfma_f32_16x16x32_bf16 v[112:115], v[200:203], a[216:219], v[112:115]\n\t"
    "buffer_load_dwordx4 a[92:95], %[xl], %[srd], s41 offen offset:2048 sc1\n\t"
    "v_mfma_f32_16x16x32_bf16 v[120:123], v[200:203], a[220:223], v[120:123]\n\t"
    "v_mfma_f32_16x16x32_bf16 v[120:123], v[196:199], a[204:207], v[120:123]\n\t"
    "v_mfma_f32_16x16x32_bf16 v[120:123], v[192:195], a[188:191], v[120:123]\n\t"
    "s_waitcnt lgkmcnt(0)\n\t"
    "s_waitcnt vmcnt(15)\n\t"
    "s_barrier\n\t"
    "v_mfma_f32_16x16x32_bf16 v[100:103], v[216:219], a[176:179], v[100:103]\n\t"
    "ds_read_b64_tr_b16 v[192:193], %[ur00] offset:0\n\t"
    "v_mfma_f32_16x16x32_bf16 v[100:103], v[220:223], a[192:195], v[100:103]\n\t"
    "ds_read_b64_tr_b16 v[194:195], %[ur00] offset:2048\n\t"
    "buffer_store_dword v246, %[fov], %[srd], 0 offen sc1\n\t"
    "v_mfma_f32_16x16x32_bf16 v[100:103], v[224:227], a[208:211], v[100:103]\n\t"
    "ds_read_b64_tr_b16 v[196:197], %[ur00] offset:4096\n\t"
    "v_mfma_f32_16x16x32_bf16 v[108:111], v[224:227], a[212:215], v[108:111]\n\t"
    "ds_read_b64_tr_b16 v[198:199], %[ur00] offset:6144\n\t"
    "v_mfma_f32_16x16x32_bf16 v[108:111], v[220:223], a[196:199], v[108:111]\n\t"
    "ds_read_b64_tr_b16 v[200:201], %[ur00] offset:8192\n\t"
    "v_mfma_f32_16x16x32_bf16 v[108:111], v[216:219], a[180:183], v[108:111]\n\t"
    "ds_read_b64_tr_b16 v[202:203], %[ur00] offset:10240\n\t"
    "v_mfma_f32_16x16x32_bf16 v[116:119], v[216:219], a[184:187], v[116:119]\n\t"
    "v_mfma_f32_16x16x32_bf16 v[116:119], v[220:223], a[200:203], v[116:119]\n\t"
    "v_mfma_f32_16x16x32_bf16 v[116:119], v[224:227], a[216:219], v[116:119]\n\t"
    "v_mfma_f32_16x16x32_bf16 v[124:127], v[224:227], a[220:223], v[124:127]\n\t"
    "v_mfma_f32_16x16x32_bf16 v[124:127], v[220:223], a[204:207], v[124:127]\n\t"
    "v_mfma_f32_16x16x32_bf16 v[124:127], v[216:219], a[188:191], v[124:127]\n\t"
    "s_waitcnt lgkmcnt(0)\n\t"
    "v_mfma_f32_16x16x32_bf16 v[64:67], v[192:195], a[0:3], v[64:67]\n\t"
    "ds_read_b64_tr_b16 v[216:217], %[ur01] offset:0\n\t"
    "s_add_u32 m0, %[um0], 24576\n\t"
    "s_add_u32 s41, %[uws], 3538944\n\t"
    "v_mfma_f32_16x16x32_bf16 v[64:67], v[196:199], a[16:19], v[64:67]\n\t"
    "ds_read_b64_tr_b16 v[218:219], %[ur01] offset:2048\n\t"
    "buffer_load_dwordx4 %[uwv], %[usrdw], s41 offen lds\n\t"
    "v_mfma_f32_16x16x32_bf16 v[64:67], v[200:203], a[32:35], v[64:67]\n\t"
    "ds_read_b64_tr_b16 v[220:221], %[ur01] offset:4096\n\t"
    "s_add_u32 m0, %[um0], 25600\n\t"
    "s_add_u32 s41, %[uws], 3612672\n\t"
    "v_mfma_f32_16x16x32_bf16 v[72:75], v[200:203], a[36:39], v[72:75]\n\t"
    "ds_read_b64_tr_b16 v[222:223], %[ur01] offset:6144\n\t"
    "buffer_load_dwordx4 %[uwv], %[usrdw], s41 offen lds\n\t"
    "v_mfma_f32_16x16x32_bf16 v[72:75], v[196:199], a[20:23], v[72:75]\n\t"
    "ds_read_b64_tr_b16 v[224:225], %[ur01] offset:8192\n\t"
    "s_add_u32 m0, %[um0], 26624\n\t"
    "s_add_u32 s41, %[uws], 3686400\n\t"
    "v_mfma_f32_16x16x32_bf16 v[72:75], v[192:195], a[4:7], v[72:75]\n\t"
    "ds_read_b64_tr_b16 v[226:227], %[ur01] offset:10240\n\t"
    "buffer_load_dwordx4 %[uwv], %[usrdw], s41 offen lds\n\t"
    "v_mfma_f32_16x16x32_bf16 v[80:83], v[192:195], a[8:11], v[80:83]\n\t"
    "s_add_u32 s41, %[uxs], 12288\n\t"
    "v_mfma_f32_16x16x32_bf16 v[80:83], v[196:199], a[24:27], v[80:83]\n\t"
    "buffer_load_dwordx4 a[96:99], %[xl], %[srd], s41 offen sc1\n\t"
    "v_mfma_f32_16x16x32_bf16 v[80:83], v[200:203], a[40:43], v[80:83]\n\t"
    "v_mfma_f32_16x16x32_bf16 v[88:91], v[200:203], a[44:47], v[88:91]\n\t"
    "v_mfma_f32_16x16x32_bf16 v[88:91], v[196:199], a[28:31], v[88:91]\n\t"
    "buffer_load_dwordx4 a[112:115], %[xl], %[srd], s41 offen offset:1024 sc1\n\t"
    "v_mfma_f32_16x16x32_bf16 v[88:91], v[192:195], a[12:15], v[88:91]\n\t"
    "s_waitcnt lgkmcnt(0)\n\t"
    "v_mfma_f32_16x16x32_bf16 v[68:71], v[216:219], a[0:3], v[68:71]\n\t"
    "ds_read_b64_tr_b16 v[192:193], %[ur00] offset:512\n\t"
    "buffer_load_dwordx4 a[224:227], %[xl], %[srd], s41 offen offset:2048 sc1\n\t"
    "v_mfma_f32_16x16x32_bf16 v[68:71], v[220:223], a[16:19], v[68:71]\n\t"
    "ds_read_b64_tr_b16 v[194:195], %[ur00] offset:2560\n\t"
    "s_add_u32 s41, %[uxs], 30720\n\t"
    "v_mfma_f32_16x16x32_bf16 v[68:71], v[224:227], a[32:35], v[68:71]\n\t"
    "ds_read_b64_tr_b16 v[196:197], %[ur00] offset:4608\n\t"
    "buffer_load_dwordx4 a[100:103], %[xl], %[srd], s41 offen sc1\n\t"
    "v_mfma_f32_16x16x32_bf16 v[76:79], v[224:227], a[36:39], v[76:79]\n\t"
    "ds_read_b64_tr_b16 v[198:199], %[ur00] offset:6656\n\t"
    "v_mfma_f32_16x16x32_bf16 v[76:79], v[220:223], a[20:23], v[76:79]\n\t"
    "ds_read_b64_tr_b16 v[200:201], %[ur00] offset:8704\n\t"
    "buffer_load_dwordx4 a[116:119], %[xl], %[srd], s41 offen offset:1024 sc1\n\t"
    "v_mfma_f32_16x16x32_bf16 v[76:79], v[216:219], a[4:7], v[76:79]\n\t"
    "ds_read_b64_tr_b16 v[202:203], %[ur00] offset:10752\n\t"
    "v_mfma_f32_16x16x32_bf16 v[84:87], v[216:219], a[8:11], v[84:87]\n\t"
    "v_mfma_f32_16x16x32_bf16 v[84:87], v[220:223], a[24:27], v[84:87]\n\t"
    "buffer_load_dwordx4 a[228:231], %[xl], %[srd], s41 offen offset:2048 sc1\n\t"
    "v_mfma_f32_16x16x32_bf16 v[84:87], v[224:227], a[40:43], v[84:87]\n\t"
    "s_add_u32 s41, %[uxs], 49152\n\t"
    "v_mfma_f32_16x16x32_bf16 v[92:95], v[224:227], a[44:47], v[92:95]\n\t"
    "buffer_load_dwordx4 a[104:107], %[xl], %[srd], s41 offen sc1\n\t"
    "v_mfma_f32_16x16x32_bf16 v[92:95], v[220:223], a[28:31], v[92:95]\n\t"
    "v_mfma_f32_16x16x32_bf16 v[92:95], v[216:219], a[12:15], v[92:95]\n\t"
    "buffer_load_dwordx4 a[120:123], %[xl], %[srd], s41 offen offset:1024 sc1\n\t"
    "s_waitcnt lgkmcnt(0)\n\t"
    "v_mfma_f32_16x16x32_bf16 v[96:99], v[192:195], a[0:3], v[96:99]\n\t"
    "ds_read_b64_tr_b16 v[216:217], %[ur01] offset:512\n\t"
    "v_mfma_f32_16x16x32_bf16 v[96:99], v[196:199], a[16:19], v[96:99]\n\t"
    "ds_read_b64_tr_b16 v[218:219], %[ur01] offset:2560\n\t"
    "buffer_load_dwordx4 a[232:235], %[xl], %[srd], s41 offen offset:2048 sc1\n\t"
    "v_mfma_f32_16x16x32_bf16 v[96:99], v[200:203], a[32:35], v[96:99]\n\t"
    "ds_read_b64_tr_b16 v[220:221], %[ur01] offset:4608\n\t"
    "v_mfma_f32_16x16x32_bf16 v[104:107], v[200:203], a[36:39], v[104:107]\n\t"
    "ds_read_b64_tr_b16 v[222:223], %[ur01] offset:6656\n\t"
    "s_add_u32 s41, %[uxs], 67584\n\t"
    "v_mfma_f32_16x16x32_bf16 v[104:107], v[196:199], a[20:23], v[104:107]\n\t"
    "ds_read_b64_tr_b16 v[224:225], %[ur01] offset:8704\n\t"
    "buffer_load_dwordx4 a[108:111], %[xl], %[srd], s41 offen sc1\n\t"
    "v_mfma_f32_16x16x32_bf16 v[104:107], v[192:195], a[4:7], v[104:107]\n\t"
    "ds_read_b64_tr_b16 v[226:227], %[ur01] offset:10752\n\t"
    "v_mfma_f32_16x16x32_bf16 v[112:115], v[192:195], a[8:11], v[112:115]\n\t"
    "buffer_load_dwordx4 a[124:127], %[xl], %[srd], s41 offen offset:1024 sc1\n\t"
    "v_mfma_f32_16x16x32_bf16 v[112:115], v[196:199], a[24:27], v[112:115]\n\t"
    "v_mfma_f32_16x16x32_bf16 v[112:115], v[200:203], a[40:43], v[112:115]\n\t"
    "buffer_load_dwordx4 a[236:239], %[xl], %[srd], s41 offen offset:2048 sc1\n\t"
    "v_mfma_f32_16x16x32_bf16 v[120:123], v[200:203], a[44:47], v[120:123]\n\t"
    "v_mfma_f32_16x16x32_bf16 v[120:123], v[196:199], a[28:31], v[120:123]\n\t"
    "v_mfma_f32_16x16x32_bf16 v[120:123], v[192:195], a[12:15], v[120:123]\n\t"
    "s_waitcnt lgkmcnt(0)\n\t"
    "s_waitcnt vmcnt(16)\n\t"
    "s_barrier\n\t"
    "v_mfma_f32_16x16x32_bf16 v[100:103], v[216:219], a[0:3], v[100:103]\n\t"
    "ds_read_b64_tr_b16 v[192:193], %[ur00] offset:12288\n\t"
    "v_mfma_f32_16x16x32_bf16 v[100:103], v[220:223], a[16:19], v[100:103]\n\t"
    "ds_read_b64_tr_b16 v[194:195], %[ur00] offset:14336\n\t"
    "v_mfma_f32_16x16x32_bf16 v[100:103], v[224:227], a[32:35], v[100:103]\n\t"
    "ds_read_b64_tr_b16 v[196:197], %[ur00] offset:16384\n\t"
    "v_mfma_f32_16x16x32_bf16 v[108:111], v[224:227], a[36:39], v[108:111]\n\t"
    "ds_read_b64_tr_b16 v[198:199], %[ur00] offset:18432\n\t"
    "v_mfma_f32_16x16x32_bf16 v[108:111], v[220:223], a[20:23], v[108:111]\n\t"
    "ds_read_b64_tr_b16 v[200:201], %[ur00] offset:20480\n\t"
    "v_mfma_f32_16x16x32_bf16 v[108:111], v[216:219], a[4:7], v[108:111]\n\t"
    "ds_read_b64_tr_b16 v[202:203], %[ur00] offset:22528\n\t"
    "v_mfma_f32_16x16x32_bf16 v[116:119], v[216:219], a[8:11], v[116:119]\n\t"
    "v_mfma_f32_16x16x32_bf16 v[116:119], v[220:223], a[24:27], v[116:119]\n\t"
    "v_mfma_f32_16x16x32_bf16 v[116:119], v[224:227], a[40:43], v[116:119]\n\t"
    "v_mfma_f32_16x16x32_bf16 v[124:127], v[224:227], a[44:47], v[124:127]\n\t"
    "v_mfma_f32_16x16x32_bf16 v[124:127], v[220:223], a[28:31], v[124:127]\n\t"
    "v_mfma_f32_16x16x32_bf16 v[124:127], v[216:219], a[12:15], v[124:127]\n\t"
    "s_waitcnt lgkmcnt(0)\n\t"
    "v_mfma_f32_16x16x32_bf16 v[64:67], v[192:195], a[48:51], v[64:67]\n\t"
    "ds_read_b64_tr_b16 v[216:217], %[ur01] offset:12288\n\t"
    "s_add_u32 m0, %[um0], 36864\n\t"
    "s_add_u32 s41, %[uws], 4423680\n\t"
    "v_mfma_f32_16x16x32_bf16 v[64:67], v[196:199], a[64:67], v[64:67]\n\t"
    "ds_read_b64_tr_b16 v[218:219], %[ur01] offset:14336\n\t"
    "buffer_load_dwordx4 %[uwv], %[usrdw], s41 offen lds\n\t"
    "v_mfma_f32_16x16x32_bf16 v[64:67], v[200:203], a[80:83], v[64:67]\n\t"
    "ds_read_b64_tr_b16 v[220:221], %[ur01] offset:16384\n\t"
    "s_add_u32 m0, %[um0], 37888\n\t"
    "s_add_u32 s41, %[uws], 4497408\n\t"
    "v_mfma_f32_16x16x32_bf16 v[72:75], v[200:203], a[84:87], v[72:75]\n\t"
    "ds_read_b64_tr_b16 v[222:223], %[ur01] offset:18432\n\t"
    "buffer_load_dwordx4 %[uwv], %[usrdw], s41 offen lds\n\t"
    "v_mfma_f32_16x16x32_bf16 v[72:75], v[196:199], a[68:71], v[72:75]\n\t"
    "ds_read_b64_tr_b16 v[224:225], %[ur01] offset:20480\n\t"
    "s_add_u32 m0, %[um0], 38912\n\t"
    "s_add_u32 s41, %[uws], 4571136\n\t"
    "v_mfma_f32_16x16x32_bf16 v[72:75], v[192:195], a[52:55], v[72:75]\n\t"
    "ds_read_b64_tr_b16 v[226:227], %[ur01] offset:22528\n\t"
    "buffer_load_dwordx4 %[uwv], %[usrdw], s41 offen lds\n\t"
    "v_mfma_f32_16x16x32_bf16 v[80:83], v[192:195], a[56:59], v[80:83]\n\t"
    "s_add_u32 s41, %[uxs], 15360\n\t"
    "v_mfma_f32_16x16x32_bf16 v[80:83], v[196:199], a[72:75], v[80:83]\n\t"
    "buffer_load_dwordx4 a[240:243], %[xl], %[srd], s41 offen sc1\n\t"
    "v_mfma_f32_16x16x32_bf16 v[80:83], v[200:203], a[88:91], v[80:83]\n\t"
    "v_mfma_f32_16x16x32_bf16 v[88:91], v[200:203], a[92:95], v[88:91]\n\t"
    "v_mfma_f32_16x16x32_bf16 v[88:91], v[196:199], a[76:79], v[88:91]\n\t"
    "buffer_load_dwordx4 v[32:35], %[xl], %[srd], s41 offen offset:1024 sc1\n\t"
    "v_mfma_f32_16x16x32_bf16 v[88:91], v[192:195], a[60:63], v[88:91]\n\t"
    "s_waitcnt lgkmcnt(0)\n\t"
    "v_mfma_f32_16x16x32_bf16 v[68:71], v[216:219], a[48:51], v[68:71]\n\t"
    "ds_read_b64_tr_b16 v[192:193], %[ur00] offset:12800\n\t"
    "buffer_load_dwordx4 v[48:51], %[xl], %[srd], s41 offen offset:2048 sc1\n\t"
    "v_mfma_f32_16x16x32_bf16 v[68:71], v[220:223], a[64:67], v[68:71]\n\t"
    "ds_read_b64_tr_b16 v[194:195], %[ur00] offset:14848\n\t"
    "s_add_u32 s41, %[uxs], 33792\n\t"
    "v_mfma_f32_16x16x32_bf16 v[68:71], v[224:227], a[80:83], v[68:71]\n\t"
    "ds_read_b64_tr_b16 v[196:197], %[ur00] offset:16896\n\t"
    "buffer_load_dwordx4 a[244:247], %[xl], %[srd], s41 offen sc1\n\t"
    "v_mfma_f32_16x16x32_bf16 v[76:79], v[224:227], a[84:87], v[76:79]\n\t"
    "ds_read_b64_tr_b16 v[198:199], %[ur00] offset:18944\n\t"
    "v_mfma_f32_16x16x32_bf16 v[76:79], v[220:223], a[68:71], v[76:79]\n\t"
    "ds_read_b64_tr_b16 v[200:201], %[ur00] offset:20992\n\t"
    "buffer_load_dwordx4 v[36:39], %[xl], %[srd], s41 offen offset:1024 sc1\n\t"
    "v_mfma_f32_16x16x32_bf16 v[76:79], v[216:219], a[52:55], v[76:79]\n\t"
    "ds_read_b64_tr_b16 v[202:203], %[ur00] offset:23040\n\t"
    "v_mfma_f32_16x16x32_bf16 v[84:87], v[216:219], a[56:59], v[84:87]\n\t"
    "v_mfma_f32_16x16x32_bf16 v[84:87], v[220:223], a[72:75], v[84:87]\n\t"
    "buffer_load_dwordx4 v[52:55], %[xl], %[srd], s41 offen offset:2048 sc1\n\t"
    "v_mfma_f32_16x16x32_bf16 v[84:87], v[224:227], a[88:91], v[84:87]\n\t"
    "s_add_u32 s41, %[uxs], 52224\n\t"
    "v_mfma_f32_16x16x32_bf16 v[92:95], v[224:227], a[92:95], v[92:95]\n\t"
    "buffer_load_dwordx4 a[248:251], %[xl], %[srd], s41 offen sc1\n\t"
    "v_mfma_f32_16x16x32_bf16 v[92:95], v[220:223], a[76:79], v[92:95]\n\t"
    "v_mfma_f32_16x16x32_bf16 v[92:95], v[216:219], a[60:63], v[92:95]\n\t"
    "buffer_load_dwordx4 v[40:43], %[xl], %[srd], s41 offen offset:1024 sc1\n\t"
    "s_waitcnt lgkmcnt(0)\n\t"
    "v_mfma_f32_16x16x32_bf16 v[96:99], v[192:195], a[48:51], v[96:99]\n\t"
    "ds_read_b64_tr_b16 v[216:217], %[ur01] offset:12800\n\t"
    "v_mfma_f32_16x16x32_bf16 v[96:99], v[196:199], a[64:67], v[96:99]\n\t"
    "ds_read_b64_tr_b16 v[218:219], %[ur01] offset:14848\n\t"
    "buffer_load_dwordx4 v[56:59], %[xl], %[srd], s41 offen offset:2048 sc1\n\t"
    "v_mfma_f32_16x16x32_bf16 v[96:99], v[200:203], a[80:83], v[96:99]\n\t"
    "ds_read_b64_tr_b16 v[220:221], %[ur01] offset:16896\n\t"
    "v_mfma_f32_16x16x32_bf16 v[104:107], v[200:203], a[84:87], v[104:107]\n\t"
    "ds_read_b64_tr_b16 v[222:223], %[ur01] offset:18944\n\t"
    "s_add_u32 s41, %[uxs], 70656\n\t"
    "v_mfma_f32_16x16x32_bf16 v[104:107], v[196:199], a[68:71], v[104:107]\n\t"
    "ds_read_b64_tr_b16 v[224:225], %[ur01] offset:20992\n\t"
    "buffer_load_dwordx4 a[252:255], %[xl], %[srd], s41 offen sc1\n\t"
    "v_mfma_f32_16x16x32_bf16 v[104:107], v[192:195], a[52:55], v[104:107]\n\t"
    "ds_read_b64_tr_b16 v[226:227], %[ur01] offset:23040\n\t"
    "v_mfma_f32_16x16x32_bf16 v[112:115], v[192:195], a[56:59], v[112:115]\n\t"
    "buffer_load_dwordx4 v[44:47], %[xl], %[srd], s41 offen offset:1024 sc1\n\t"
    "v_mfma_f32_16x16x32_bf16 v[112:115], v[196:199], a[72:75], v[112:115]\n\t"
    "v_mfma_f32_16x16x32_bf16 v[112:115], v[200:203], a[88:91], v[112:115]\n\t"
    "buffer_load_dwordx4 v[60:63], %[xl], %[srd], s41 offen offset:2048 sc1\n\t"
    "v_mfma_f32_16x16x32_bf16 v[120:123], v[200:203], a[92:95], v[120:123]\n\t"
    "v_mfma_f32_16x16x32_bf16 v[120:123], v[196:199], a[76:79], v[120:123]\n\t"
    "v_mfma_f32_16x16x32_bf16 v[120:123], v[192:195], a[60:63], v[120:123]\n\t"
    "s_waitcnt lgkmcnt(0)\n\t"
    "s_waitcnt vmcnt(15)\n\t"
    "s_barrier\n\t"
    "v_mfma_f32_16x16x32_bf16 v[100:103], v[216:219], a[48:51], v[100:103]\n\t"
    "ds_read_b64_tr_b16 v[192:193], %[ur00] offset:24576\n\t"
    "v_mfma_f32_16x16x32_bf16 v[100:103], v[220:223], a[64:67], v[100:103]\n\t"
    "ds_read_b64_tr_b16 v[194:195], %[ur00] offset:26624\n\t"
    "v_mfma_f32_16x16x32_bf16 v[100:103], v[224:227], a[80:83], v[100:103]\n\t"
    "ds_read_b64_tr_b16 v[196:197], %[ur00] offset:28672\n\t"
    "v_mfma_f32_16x16x32_bf16 v[108:111], v[224:227], a[84:87], v[108:111]\n\t"
    "ds_read_b64_tr_b16 v[198:199], %[ur00] offset:30720\n\t"
    "v_mfma_f32_16x16x32_bf16 v[108:111], v[220:223], a[68:71], v[108:111]\n\t"
    "ds_read_b64_tr_b16 v[200:201], %[ur00] offset:32768\n\t"
    "v_mfma_f32_16x16x32_bf16 v[108:111], v[216:219], a[52:55], v[108:111]\n\t"
    "ds_read_b64_tr_b16 v[202:203], %[ur00] offset:34816\n\t"
    "v_mfma_f32_16x16x32_bf16 v[116:119], v[216:219], a[56:59], v[116:119]\n\t"
    "v_mfma_f32_16x16x32_bf16 v[116:119], v[220:223], a[72:75], v[116:119]\n\t"
    "v_mfma_f32_16x16x32_bf16 v[116:119], v[224:227], a[88:91], v[116:119]\n\t"
    "v_mfma_f32_16x16x32_bf16 v[124:127], v[224:227], a[92:95], v[124:127]\n\t"
    "v_mfma_f32_16x16x32_bf16 v[124:127], v[220:223], a[76:79], v[124:127]\n\t"
    "v_mfma_f32_16x16x32_bf16 v[124:127], v[216:219], a[60:63], v[124:127]\n\t"
    "s_waitcnt lgkmcnt(0)\n\t"
    "v_mfma_f32_16x16x32_bf16 v[64:67], v[192:195], a[96:99], v[64:67]\n\t"
    "ds_read_b64_tr_b16 v[216:217], %[ur01] offset:24576\n\t"
    "s_add_u32 m0, %[um0], 49152\n\t"
    "s_add_u32 s41, %[uws], 128\n\t"
    "v_mfma_f32_16x16x32_bf16 v[64:67], v[196:199], a[112:115], v[64:67]\n\t"
    "ds_read_b64_tr_b16 v[218:219], %[ur01] offset:26624\n\t"
    "buffer_load_dwordx4 %[uwv], %[usrdw], s41 offen lds\n\t"
    "v_mfma_f32_16x16x32_bf16 v[64:67], v[200:203], a[224:227], v[64:67]\n\t"
    "ds_read_b64_tr_b16 v[220:221], %[ur01] offset:28672\n\t"
    "v_mfma_f32_16x16x32_bf16 v[72:75], v[200:203], a[228:231], v[72:75]\n\t"
    "ds_read_b64_tr_b16 v[222:223], %[ur01] offset:30720\n\t"
    "v_mfma_f32_16x16x32_bf16 v[72:75], v[196:199], a[116:119], v[72:75]\n\t"
    "ds_read_b64_tr_b16 v[224:225], %[ur01] offset:32768\n\t"
    "s_add_u32 m0, %[um0], 50176\n\t"
    "s_add_u32 s41, %[uws], 73856\n\t"
    "v_mfma_f32_16x16x32_bf16 v[72:75], v[192:195], a[100:103], v[72:75]\n\t"
    "ds_read_b64_tr_b16 v[226:227], %[ur01] offset:34816\n\t"
    "buffer_load_dwordx4 %[uwv], %[usrdw], s41 offen lds\n\t"
    "v_mfma_f32_16x16x32_bf16 v[80:83], v[192:195], a[104:107], v[80:83]\n\t"
    "v_mfma_f32_16x16x32_bf16 v[80:83], v[196:199], a[120:123], v[80:83]\n\t"
    "v_mfma_f32_16x16x32_bf16 v[80:83], v[200:203], a[232:235], v[80:83]\n\t"
    "s_add_u32 m0, %[um0], 51200\n\t"
    "s_add_u32 s41, %[uws], 147584\n\t"
    "v_mfma_f32_16x16x32_bf16 v[88:91], v[200:203], a[236:239], v[88:91]\n\t"
    "buffer_load_dwordx4 %[uwv], %[usrdw], s41 offen lds\n\t"
    "v_mfma_f32_16x16x32_bf16 v[88:91], v[196:199], a[124:127], v[88:91]\n\t"
    "v_mfma_f32_16x16x32_bf16 v[88:91], v[192:195], a[108:111], v[88:91]\n\t"
    "s_waitcnt lgkmcnt(0)\n\t"
    "v_mfma_f32_16x16x32_bf16 v[68:71], v[216:219], a[96:99], v[68:71]\n\t"
    "ds_read_b64_tr_b16 v[192:193], %[ur00] offset:25088\n\t"
    "v_mfma_f32_16x16x32_bf16 v[68:71], v[220:223], a[112:115], v[68:71]\n\t"
    "ds_read_b64_tr_b16 v[194:195], %[ur00] offset:27136\n\t"
    "s_add_u32 m0, %[um0], 61440\n\t"
    "s_add_u32 s41, %[uws], 884864\n\t"
    "v_mfma_f32_16x16x32_bf16 v[68:71], v[224:227], a[224:227], v[68:71]\n\t"
    "ds_read_b64_tr_b16 v[196:197], %[ur00] offset:29184\n\t"
    "buffer_load_dwordx4 %[uwv], %[usrdw], s41 offen lds\n\t"
    "v_mfma_f32_16x16x32_bf16 v[76:79], v[224:227], a[228:231], v[76:79]\n\t"
    "ds_read_b64_tr_b16 v[198:199], %[ur00] offset:31232\n\t"
    "v_mfma_f32_16x16x32_bf16 v[76:79], v[220:223], a[116:119], v[76:79]\n\t"
    "ds_read_b64_tr_b16 v[200:201], %[ur00] offset:33280\n\t"
    "v_mfma_f32_16x16x32_bf16 v[76:79], v[216:219], a[100:103], v[76:79]\n\t"
    "ds_read_b64_tr_b16 v[202:203], %[ur00] offset:35328\n\t"
    "s_add_u32 m0, %[um0], 62464\n\t"
    "s_add_u32 s41, %[uws], 958592\n\t"
    "v_mfma_f32_16x16x32_bf16 v[84:87], v[216:219], a[104:107], v[84:87]\n\t"
    "buffer_load_dwordx4 %[uwv], %[usrdw], s41 offen lds\n\t"
    "v_mfma_f32_16x16x32_bf16 v[84:87], v[220:223], a[120:123], v[84:87]\n\t"
    "v_mfma_f32_16x16x32_bf16 v[84:87], v[224:227], a[232:235], v[84:87]\n\t"
    "v_mfma_f32_16x16x32_bf16 v[92:95], v[224:227], a[236:239], v[92:95]\n\t"
    "v_mfma_f32_16x16x32_bf16 v[92:95], v[220:223], a[124:127], v[92:95]\n\t"
    "s_add_u32 m0, %[um0], 63488\n\t"
    "s_add_u32 s41, %[uws], 1032320\n\t"
    "v_mfma_f32_16x16x32_bf16 v[92:95], v[216:219], a[108:111], v[92:95]\n\t"
    "buffer_load_dwordx4 %[uwv], %[usrdw], s41 offen lds\n\t"
    "s_waitcnt lgkmcnt(0)\n\t"
    "v_mfma_f32_16x16x32_bf16 v[96:99], v[192:195], a[96:99], v[96:99]\n\t"
    "ds_read_b64_tr_b16 v[216:217], %[ur01] offset:25088\n\t"
    "v_mfma_f32_16x16x32_bf16 v[96:99], v[196:199], a[112:115], v[96:99]\n\t"
    "ds_read_b64_tr_b16 v[218:219], %[ur01] offset:27136\n\t"
    "v_mfma_f32_16x16x32_bf16 v[96:99], v[200:203], a[224:227], v[96:99]\n\t"
    "ds_read_b64_tr_b16 v[220:221], %[ur01] offset:29184\n\t"
    "s_add_u32 m0, %[um0], 73728\n\t"
    "s_add_u32 s41, %[uws], 1769600\n\t"
    "v_mfma_f32_16x16x32_bf16 v[104:107], v[200:203], a[228:231], v[104:107]\n\t"
    "ds_read_b64_tr_b16 v[222:223], %[ur01] offset:31232\n\t"
    "buffer_load_dwordx4 %[uwv], %[usrdw], s41 offen lds\n\t"
    "v_mfma_f32_16x16x32_bf16 v[104:107], v[196:199], a[116:119], v[104:107]\n\t"
    "ds_read_b64_tr_b16 v[224:225], %[ur01] offset:33280\n\t"
    "v_mfma_f32_16x16x32_bf16 v[104:107], v[192:195], a[100:103], v[104:107]\n\t"
    "ds_read_b64_tr_b16 v[226:227], %[ur01] offset:35328\n\t"
    "v_mfma_f32_16x16x32_bf16 v[112:115], v[192:195], a[104:107], v[112:115]\n\t"
    "v_mfma_f32_16x16x32_bf16 v[112:115], v[196:199], a[120:123], v[112:115]\n\t"
    "s_add_u32 m0, %[um0], 74752\n\t"
    "s_add_u32 s41, %[uws], 1843328\n\t"
    "v_mfma_f32_16x16x32_bf16 v[112:115], v[200:203], a[232:235], v[112:115]\n\t"
    "buffer_load_dwordx4 %[uwv], %[usrdw], s41 offen lds\n\t"
    "v_mfma_f32_16x16x32_bf16 v[120:123], v[200:203], a[236:239], v[120:123]\n\t"
    "v_mfma_f32_16x16x32_bf16 v[120:123], v[196:199], a[124:127], v[120:123]\n\t"
    "v_mfma_f32_16x16x32_bf16 v[120:123], v[192:195], a[108:111], v[120:123]\n\t"
    "s_add_u32 m0, %[um0], 75776\n\t"
    "s_add_u32 s41, %[uws], 1917056\n\t"
    "s_waitcnt lgkmcnt(0)\n\t"
    "s_waitcnt vmcnt(8)\n\t"
    "s_barrier\n\t"
    "v_mfma_f32_16x16x32_bf16 v[100:103], v[216:219], a[96:99], v[100:103]\n\t"
    "ds_read_b64_tr_b16 v[192:193], %[ur00] offset:36864\n\t"
    "buffer_load_dwordx4 %[uwv], %[usrdw], s41 offen lds\n\t"
    "v_mfma_f32_16x16x32_bf16 v[100:103], v[220:223], a[112:115], v[100:103]\n\t"
    "ds_read_b64_tr_b16 v[194:195], %[ur00] offset:38912\n\t"
    "v_mfma_f32_16x16x32_bf16 v[100:103], v[224:227], a[224:227], v[100:103]\n\t"
    "ds_read_b64_tr_b16 v[196:197], %[ur00] offset:40960\n\t"
    "v_mfma_f32_16x16x32_bf16 v[108:111], v[224:227], a[228:231], v[108:111]\n\t"
    "ds_read_b64_tr_b16 v[198:199], %[ur00] offset:43008\n\t"
    "v_mfma_f32_16x16x32_bf16 v[108:111], v[220:223], a[116:119], v[108:111]\n\t"
    "ds_read_b64_tr_b16 v[200:201], %[ur00] offset:45056\n\t"
    "s_add_u32 m0, %[um0], 86016\n\t"
    "s_add_u32 s41, %[uws], 2654336\n\t"
    "v_mfma_f32_16x16x32_bf16 v[108:111], v[216:219], a[100:103], v[108:111]\n\t"
    "ds_read_b64_tr_b16 v[202:203], %[ur00] offset:47104\n\t"
    "buffer_load_dwordx4 %[uwv], %[usrdw], s41 offen lds\n\t"
    "v_mfma_f32_16x16x32_bf16 v[116:119], v[216:219], a[104:107], v[116:119]\n\t"
    "v_mfma_f32_16x16x32_bf16 v[116:119], v[220:223], a[120:123], v[116:119]\n\t"
    "v_mfma_f32_16x16x32_bf16 v[116:119], v[224:227], a[232:235], v[116:119]\n\t"
    "s_add_u32 m0, %[um0], 87040\n\t"
    "s_add_u32 s41, %[uws], 2728064\n\t"
    "v_mfma_f32_16x16x32_bf16 v[124:127], v[224:227], a[236:239], v[124:127]\n\t"
    "buffer_load_dwordx4 %[uwv], %[usrdw], s41 offen lds\n\t"
    "v_mfma_f32_16x16x32_bf16 v[124:127], v[220:223], a[124:127], v[124:127]\n\t"
    "v_mfma_f32_16x16x32_bf16 v[124:127], v[216:219], a[108:111], v[124:127]\n\t"
    "s_waitcnt lgkmcnt(0)\n\t"
    "v_mfma_f32_16x16x32_bf16 v[64:67], v[192:195], a[240:243], v[64:67]\n\t"
    "ds_read_b64_tr_b16 v[216:217], %[ur01] offset:36864\n\t"
    "s_add_u32 m0, %[um0], 88064\n\t"
    "s_add_u32 s41, %[uws], 2801792\n\t"
    "v_mfma_f32_16x16x32_bf16 v[64:67], v[196:199], v[32:35], v[64:67]\n\t"
    "ds_read_b64_tr_b16 v[218:219], %[ur01] offset:38912\n\t"
    "buffer_load_dwordx4 %[uwv], %[usrdw], s41 offen lds\n\t"
    "v_mfma_f32_16x16x32_bf16 v[64:67], v[200:203], v[48:51], v[64:67]\n\t"
    "ds_read_b64_tr_b16 v[220:221], %[ur01] offset:40960\n\t"
    "v_mfma_f32_16x16x32_bf16 v[72:75], v[200:203], v[52:55], v[72:75]\n\t"
    "ds_read_b64_tr_b16 v[222:223], %[ur01] offset:43008\n\t"
    "v_mfma_f32_16x16x32_bf16 v[72:75], v[196:199], v[36:39], v[72:75]\n\t"
    "ds_read_b64_tr_b16 v[224:225], %[ur01] offset:45056\n\t"
    "v_mfma_f32_16x16x32_bf16 v[72:75], v[192:195], a[244:247], v[72:75]\n\t"
    "ds_read_b64_tr_b16 v[226:227], %[ur01] offset:47104\n\t"
    "s_add_u32 m0, %[um0], 98304\n\t"
    "s_add_u32 s41, %[uws], 3539072\n\t"
    "v_mfma_f32_16x16x32_bf16 v[80:83], v[192:195], a[248:251], v[80:83]\n\t"
    "buffer_load_dwordx4 %[uwv], %[usrdw], s41 offen lds\n\t"
    "v_mfma_f32_16x16x32_bf16 v[80:83], v[196:199], v[40:43], v[80:83]\n\t"
    "v_mfma_f32_16x16x32_bf16 v[80:83], v[200:203], v[56:59], v[80:83]\n\t"
    "v_mfma_f32_16x16x32_bf16 v[88:91], v[200:203], v[60:63], v[88:91]\n\t"
    "s_add_u32 m0, %[um0], 99328\n\t"
    "s_add_u32 s41, %[uws], 3612800\n\t"
    "v_mfma_f32_16x16x32_bf16 v[88:91], v[196:199], v[44:47], v[88:91]\n\t"
    "buffer_load_dwordx4 %[uwv], %[usrdw], s41 offen lds\n\t"
    "v_mfma_f32_16x16x32_bf16 v[88:91], v[192:195], a[252:255], v[88:91]\n\t"
    "s_waitcnt lgkmcnt(0)\n\t"
    "v_mfma_f32_16x16x32_bf16 v[68:71], v[216:219], a[240:243], v[68:71]\n\t"
    "ds_read_b64_tr_b16 v[192:193], %[ur00] offset:37376\n\t"
    "v_mfma_f32_16x16x32_bf16 v[68:71], v[220:223], v[32:35], v[68:71]\n\t"
    "ds_read_b64_tr_b16 v[194:195], %[ur00] offset:39424\n\t"
    "v_mfma_f32_16x16x32_bf16 v[68:71], v[224:227], v[48:51], v[68:71]\n\t"
    "ds_read_b64_tr_b16 v[196:197], %[ur00] offset:41472\n\t"
    "s_add_u32 m0, %[um0], 100352\n\t"
    "s_add_u32 s41, %[uws], 3686528\n\t"
    "v_mfma_f32_16x16x32_bf16 v[76:79], v[224:227], v[52:55], v[76:79]\n\t"
    "ds_read_b64_tr_b16 v[198:199], %[ur00] offset:43520\n\t"
    "buffer_load_dwordx4 %[uwv], %[usrdw], s41 offen lds\n\t"
    "v_mfma_f32_16x16x32_bf16 v[76:79], v[220:223], v[36:39], v[76:79]\n\t"
    "ds_read_b64_tr_b16 v[200:201], %[ur00] offset:45568\n\t"
    "v_mfma_f32_16x16x32_bf16 v[76:79], v[216:219], a[244:247], v[76:79]\n\t"
    "ds_read_b64_tr_b16 v[202:203], %[ur00] offset:47616\n\t"
    "v_mfma_f32_16x16x32_bf16 v[84:87], v[216:219], a[248:251], v[84:87]\n\t"
    "s_add_u32 m0, %[um0], 110592\n\t"
    "s_add_u32 s41, %[uws], 4423808\n\t"
    "v_mfma_f32_16x16x32_bf16 v[84:87], v[220:223], v[40:43], v[84:87]\n\t"
    "buffer_load_dwordx4 %[uwv], %[usrdw], s41 offen lds\n\t"
    "v_mfma_f32_16x16x32_bf16 v[84:87], v[224:227], v[56:59], v[84:87]\n\t"
    "v_mfma_f32_16x16x32_bf16 v[92:95], v[224:227], v[60:63], v[92:95]\n\t"
    "v_mfma_f32_16x16x32_bf16 v[92:95], v[220:223], v[44:47], v[92:95]\n\t"
    "v_mfma_f32_16x16x32_bf16 v[92:95], v[216:219], a[252:255], v[92:95]\n\t"
    "s_add_u32 m0, %[um0], 111616\n\t"
    "s_add_u32 s41, %[uws], 4497536\n\t"
    "s_waitcnt lgkmcnt(0)\n\t"
    "v_mfma_f32_16x16x32_bf16 v[96:99], v[192:195], a[240:243], v[96:99]\n\t"
    "ds_read_b64_tr_b16 v[216:217], %[ur01] offset:37376\n\t"
    "buffer_load_dwordx4 %[uwv], %[usrdw], s41 offen lds\n\t"
    "v_mfma_f32_16x16x32_bf16 v[96:99], v[196:199], v[32:35], v[96:99]\n\t"
    "ds_read_b64_tr_b16 v[218:219], %[ur01] offset:39424\n\t"
    "v_mfma_f32_16x16x32_bf16 v[96:99], v[200:203], v[48:51], v[96:99]\n\t"
    "ds_read_b64_tr_b16 v[220:221], %[ur01] offset:41472\n\t"
    "v_mfma_f32_16x16x32_bf16 v[104:107], v[200:203], v[52:55], v[104:107]\n\t"
    "ds_read_b64_tr_b16 v[222:223], %[ur01] offset:43520\n\t"
    "s_add_u32 m0, %[um0], 112640\n\t"
    "s_add_u32 s41, %[uws], 4571264\n\t"
    "v_mfma_f32_16x16x32_bf16 v[104:107], v[196:199], v[36:39], v[104:107]\n\t"
    "ds_read_b64_tr_b16 v[224:225], %[ur01] offset:45568\n\t"
    "buffer_load_dwordx4 %[uwv], %[usrdw], s41 offen lds\n\t"
    "v_mfma_f32_16x16x32_bf16 v[104:107], v[192:195], a[244:247], v[104:107]\n\t"
    "ds_read_b64_tr_b16 v[226:227], %[ur01] offset:47616\n\t"
    "v_mfma_f32_16x16x32_bf16 v[112:115], v[192:195], a[248:251], v[112:115]\n\t"
    "v_mfma_f32_16x16x32_bf16 v[112:115], v[196:199], v[40:43], v[112:115]\n\t"
    "v_mfma_f32_16x16x32_bf16 v[112:115], v[200:203], v[56:59], v[112:115]\n\t"
    "v_mfma_f32_16x16x32_bf16 v[120:123], v[200:203], v[60:63], v[120:123]\n\t"
    "v_mfma_f32_16x16x32_bf16 v[120:123], v[196:199], v[44:47], v[120:123]\n\t"
    "v_mfma_f32_16x16x32_bf16 v[120:123], v[192:195], a[252:255], v[120:123]\n\t"
    "s_waitcnt lgkmcnt(0)\n\t"
    "s_waitcnt vmcnt(12)\n\t"
    "s_barrier\n\t"
    "v_mfma_f32_16x16x32_bf16 v[100:103], v[216:219], a[240:243], v[100:103]\n\t"
    "ds_read_b64_tr_b16 v[192:193], %[ur00] offset:49152\n\t"
    "ds_read_b64_tr_b16 v[194:195], %[ur00] offset:51200\n\t"
    "v_mfma_f32_16x16x32_bf16 v[100:103], v[220:223], v[32:35], v[100:103]\n\t"
    "ds_read_b64_tr_b16 v[196:197], %[ur00] offset:53248\n\t"
    "ds_read_b64_tr_b16 v[198:199], %[ur00] offset:55296\n\t"
    "v_mfma_f32_16x16x32_bf16 v[100:103], v[224:227], v[48:51], v[100:103]\n\t"
    "ds_read_b64_tr_b16 v[200:201], %[ur00] offset:57344\n\t"
    "ds_read_b64_tr_b16 v[202:203], %[ur00] offset:59392\n\t"
    "v_mfma_f32_16x16x32_bf16 v[108:111], v[224:227], v[52:55], v[108:111]\n\t"
    "ds_read_b64_tr_b16 v[204:205], %[ur00] offset:61440\n\t"
    "ds_read_b64_tr_b16 v[206:207], %[ur00] offset:63488\n\t"
    "v_mfma_f32_16x16x32_bf16 v[108:111], v[220:223], v[36:39], v[108:111]\n\t"
    "ds_read_b64_tr_b16 v[208:209], %[ur10] offset:0\n\t"
    "ds_read_b64_tr_b16 v[210:211], %[ur10] offset:2048\n\t"
    "v_mfma_f32_16x16x32_bf16 v[108:111], v[216:219], a[244:247], v[108:111]\n\t"
    "ds_read_b64_tr_b16 v[212:213], %[ur10] offset:4096\n\t"
    "ds_read_b64_tr_b16 v[214:215], %[ur10] offset:6144\n\t"
    "v_mfma_f32_16x16x32_bf16 v[116:119], v[216:219], a[248:251], v[116:119]\n\t"
    "v_mfma_f32_16x16x32_bf16 v[116:119], v[220:223], v[40:43], v[116:119]\n\t"
    "v_mfma_f32_16x16x32_bf16 v[116:119], v[224:227], v[56:59], v[116:119]\n\t"
    "v_mfma_f32_16x16x32_bf16 v[124:127], v[224:227], v[60:63], v[124:127]\n\t"
    "v_mfma_f32_16x16x32_bf16 v[124:127], v[220:223], v[44:47], v[124:127]\n\t"
    "v_mfma_f32_16x16x32_bf16 v[124:127], v[216:219], a[252:255], v[124:127]\n\t"
    "s_waitcnt lgkmcnt(0)\n\t"
    "v_mfma_f32_16x16x32_bf16 v[128:131], v[192:195], a[128:131], 0\n\t"
    "ds_read_b64_tr_b16 v[216:217], %[ur01] offset:49152\n\t"
    "v_mfma_f32_16x16x32_bf16 v[128:131], v[196:199], a[144:147], v[128:131]\n\t"
    "ds_read_b64_tr_b16 v[218:219], %[ur01] offset:51200\n\t"
    "v_mfma_f32_16x16x32_bf16 v[128:131], v[200:203], a[160:163], v[128:131]\n\t"
    "v_mfma_f32_16x16x32_bf16 v[128:131], v[204:207], a[176:179], v[128:131]\n\t"
    "ds_read_b64_tr_b16 v[220:221], %[ur01] offset:53248\n\t"
    "v_fmaak_f32 v240, v64, v64, 0x409d30fc\n\t"
    "v_fmaak_f32 v241, v65, v65, 0x409d30fc\n\t"
    "v_mfma_f32_16x16x32_bf16 v[128:131], v[208:211], a[192:195], v[128:131]\n\t"
    "ds_read_b64_tr_b16 v[222:223], %[ur01] offset:55296\n\t"
    "s_add_u32 m0, %[um0], 131072\n\t"
    "s_add_u32 s41, %[uws], 256\n\t"
    "v_mfma_f32_16x16x32_bf16 v[128:131], v[212:215], a[208:211], v[128:131]\n\t"
    "buffer_load_dwordx4 %[uwv], %[usrdw], s41 offen lds\n\t"
    "v_fmaak_f32 v242, v66, v66, 0x409d30fc\n\t"
    "v_mfma_f32_16x16x32_bf16 v[136:139], v[212:215], a[212:215], 0\n\t"
    "ds_read_b64_tr_b16 v[224:225], %[ur01] offset:57344\n\t"
    "v_fmaak_f32 v243, v67, v67, 0x409d30fc\n\t"
    "v_mul_f32 v240, v240, v64\n\t"
    "v_mfma_f32_16x16x32_bf16 v[136:139], v[208:211], a[196:199], v[136:139]\n\t"
    "ds_read_b64_tr_b16 v[226:227], %[ur01] offset:59392\n\t"
    "v_mul_f32 v241, v241, v65\n\t"
    "v_mfma_f32_16x16x32_bf16 v[136:139], v[204:207], a[180:183], v[136:139]\n\t"
    "v_mul_f32 v242, v242, v66\n\t"
    "v_mul_f32 v243, v243, v67\n\t"
    "v_mfma_f32_16x16x32_bf16 v[136:139], v[200:203], a[164:167], v[136:139]\n\t"
    "ds_read_b64_tr_b16 v[228:229], %[ur01] offset:61440\n\t"
    "v_exp_f32 v240, v240\n\t"
    "v_mfma_f32_16x16x32_bf16 v[136:139], v[196:199], a[148:151], v[136:139]\n\t"
    "v_exp_f32 v241, v241\n\t"
    "v_mfma_f32_16x16x32_bf16 v[136:139], v[192:195], a[132:135], v[136:139]\n\t"
    "ds_read_b64_tr_b16 v[230:231], %[ur01] offset:63488\n\t"
    "v_exp_f32 v242, v242\n\t"
    "v_mfma_f32_16x16x32_bf16 v[144:147], v[192:195], a[136:139], 0\n\t"
    "ds_read_b64_tr_b16 v[232:233], %[ur11] offset:0\n\t"
    "v_exp_f32 v243, v243\n\t"
    "v_fma_f32 v240, v240, v244, v244\n\t"
    "v_mfma_f32_16x16x32_bf16 v[144:147], v[196:199], a[152:155], v[144:147]\n\t"
    "v_fma_f32 v241, v241, v244, v244\n\t"
    "v_fma_f32 v242, v242, v244, v244\n\t"
    "v_fma_f32 v243, v243, v244, v244\n\t"
    "v_mfma_f32_16x16x32_bf16 v[144:147], v[200:203], a[168:171], v[144:147]\n\t"
    "ds_read_b64_tr_b16 v[234:235], %[ur11] offset:2048\n\t"
    "s_add_u32 m0, %[um0], 132096\n\t"
    "s_add_u32 s41, %[uws], 73984\n\t"
    "v_mfma_f32_16x16x32_bf16 v[144:147], v[204:207], a[184:187], v[144:147]\n\t"
    "ds_read_b64_tr_b16 v[236:237], %[ur11] offset:4096\n\t"
    "buffer_load_dwordx4 %[uwv], %[usrdw], s41 offen lds\n\t"
    "v_mfma_f32_16x16x32_bf16 v[144:147], v[208:211], a[200:203], v[144:147]\n\t"
    "v_rcp_f32 v240, v240\n\t"
    "v_mfma_f32_16x16x32_bf16 v[144:147], v[212:215], a[216:219], v[144:147]\n\t"
    "ds_read_b64_tr_b16 v[238:239], %[ur11] offset:6144\n\t"
    "v_rcp_f32 v241, v241\n\t"
    "v_mfma_f32_16x16x32_bf16 v[152:155], v[212:215], a[220:223], 0\n\t"
    "v_rcp_f32 v242, v242\n\t"
    "v_mfma_f32_16x16x32_bf16 v[152:155], v[208:211], a[204:207], v[152:155]\n\t"
    "v_rcp_f32 v243, v243\n\t"
    "v_mul_f32 v64, v64, v240\n\t"
    "v_mul_f32 v65, v65, v241\n\t"
    "v_mfma_f32_16x16x32_bf16 v[152:155], v[204:207], a[188:191], v[152:155]\n\t"
    "v_mul_f32 v66, v66, v242\n\t"
    "v_mul_f32 v67, v67, v243\n\t"
    "v_mfma_f32_16x16x32_bf16 v[152:155], v[200:203], a[172:175], v[152:155]\n\t"
    "v_cvt_pk_bf16_f32 v64, v64, v65\n\t"
    "v_cvt_pk_bf16_f32 v65, v66, v67\n\t"
    "v_fmaak_f32 v240, v68, v68, 0x409d30fc\n\t"
    "v_mfma_f32_16x16x32_bf16 v[152:155], v[196:199], a[156:159], v[152:155]\n\t"
    "v_fmaak_f32 v241, v69, v69, 0x409d30fc\n\t"
    "v_fmaak_f32 v242, v70, v70, 0x409d30fc\n\t"
    "v_fmaak_f32 v243, v71, v71, 0x409d30fc\n\t"
    "v_mfma_f32_16x16x32_bf16 v[152:155], v[192:195], a[140:143], v[152:155]\n\t"
    "v_mul_f32 v240, v240, v68\n\t"
    "s_waitcnt lgkmcnt(0)\n\t"
    "v_mfma_f32_16x16x32_bf16 v[132:135], v[216:219], a[128:131], 0\n\t"
    "ds_read_b64_tr_b16 v[192:193], %[ur00] offset:49664\n\t"
    "s_add_u32 m0, %[um0], 133120\n\t"
    "s_add_u32 s41, %[uws], 147712\n\t"
    "v_mfma_f32_16x16x32_bf16 v[132:135], v[220:223], a[144:147], v[132:135]\n\t"
    "ds_read_b64_tr_b16 v[194:195], %[ur00] offset:51712\n\t"
    "buffer_load_dwordx4 %[uwv], %[usrdw], s41 offen lds\n\t"
    "v_mul_f32 v241, v241, v69\n\t"
    "v_mfma_f32_16x16x32_bf16 v[132:135], v[224:227], a[160:163], v[132:135]\n\t"
    "v_mul_f32 v242, v242, v70\n\t"
    "v_mul_f32 v243, v243, v71\n\t"
    "v_mfma_f32_16x16x32_bf16 v[132:135], v[228:231], a[176:179], v[132:135]\n\t"
    "ds_read_b64_tr_b16 v[196:197], %[ur00] offset:53760\n\t"
    "v_exp_f32 v240, v240\n\t"
    "v_mfma_f32_16x16x32_bf16 v[132:135], v[232:235], a[192:195], v[132:135]\n\t"
    "ds_read_b64_tr_b16 v[198:199], %[ur00] offset:55808\n\t"
    "v_exp_f32 v241, v241\n\t"
    "v_mfma_f32_16x16x32_bf16 v[132:135], v[236:239], a[208:211], v[132:135]\n\t"
    "v_exp_f32 v242, v242\n\t"
    "v_mfma_f32_16x16x32_bf16 v[140:143], v[236:239], a[212:215], 0\n\t"
    "ds_read_b64_tr_b16 v[200:201], %[ur00] offset:57856\n\t"
    "v_exp_f32 v243, v243\n\t"
    "v_mfma_f32_16x16x32_bf16 v[140:143], v[232:235], a[196:199], v[140:143]\n\t"
    "ds_read_b64_tr_b16 v[202:203], %[ur00] offset:59904\n\t"
    "v_fma_f32 v240, v240, v244, v244\n\t"
    "v_fma_f32 v241, v241, v244, v244\n\t"
    "v_mfma_f32_16x16x32_bf16 v[140:143], v[228:231], a[180:183], v[140:143]\n\t"
    "v_fma_f32 v242, v242, v244, v244\n\t"
    "v_fma_f32 v243, v243, v244, v244\n\t"
    "v_mfma_f32_16x16x32_bf16 v[140:143], v[224:227], a[164:167], v[140:143]\n\t"
    "ds_read_b64_tr_b16 v[204:205], %[ur00] offset:61952\n\t"
    "v_rcp_f32 v240, v240\n\t"
    "v_mfma_f32_16x16x32_bf16 v[140:143], v[220:223], a[148:151], v[140:143]\n\t"
    "v_rcp_f32 v241, v241\n\t"
    "v_mfma_f32_16x16x32_bf16 v[140:143], v[216:219], a[132:135], v[140:143]\n\t"
    "ds_read_b64_tr_b16 v[206:207], %[ur00] offset:64000\n\t"
    "s_add_u32 m0, %[um0], 143360\n\t"
    "s_add_u32 s41, %[uws], 884992\n\t"
    "v_mfma_f32_16x16x32_bf16 v[148:151], v[216:219], a[136:139], 0\n\t"
    "ds_read_b64_tr_b16 v[208:209], %[ur10] offset:512\n\t"
    "buffer_load_dwordx4 %[uwv], %[usrdw], s41 offen lds\n\t"
    "v_mfma_f32_16x16x32_bf16 v[148:151], v[220:223], a[152:155], v[148:151]\n\t"
    "v_rcp_f32 v242, v242\n\t"
    "v_rcp_f32 v243, v243\n\t"
    "v_mfma_f32_16x16x32_bf16 v[148:151], v[224:227], a[168:171], v[148:151]\n\t"
    "ds_read_b64_tr_b16 v[210:211], %[ur10] offset:2560\n\t"
    "v_mul_f32 v68, v68, v240\n\t"
    "v_mul_f32 v69, v69, v241\n\t"
    "v_mfma_f32_16x16x32_bf16 v[148:151], v[228:231], a[184:187], v[148:151]\n\t"
    "ds_read_b64_tr_b16 v[212:213], %[ur10] offset:4608\n\t"
    "v_mul_f32 v70, v70, v242\n\t"
    "v_mfma_f32_16x16x32_bf16 v[148:151], v[232:235], a[200:203], v[148:151]\n\t"
    "v_mul_f32 v71, v71, v243\n\t"
    "v_cvt_pk_bf16_f32 v66, v68, v69\n\t"
    "v_cvt_pk_bf16_f32 v67, v70, v71\n\t"
    "v_mfma_f32_16x16x32_bf16 v[148:151], v[236:239], a[216:219], v[148:151]\n\t"
    "ds_read_b64_tr_b16 v[214:215], %[ur10] offset:6656\n\t"
    "v_fmaak_f32 v240, v72, v72, 0x409d30fc\n\t"
    "v_mfma_f32_16x16x32_bf16 v[156:159], v[236:239], a[220:223], 0\n\t"
    "buffer_store_dwordx4 v[64:67], %[xl], %[srd], s52 offen sc1\n\t"
    "v_fmaak_f32 v241, v73, v73, 0x409d30fc\n\t"
    "v_fmaak_f32 v242, v74, v74, 0x409d30fc\n\t"
    "v_mfma_f32_16x16x32_bf16 v[156:159], v[232:235], a[204:207], v[156:159]\n\t"
    "v_fmaak_f32 v243, v75, v75, 0x409d30fc\n\t"
    "v_mul_f32 v240, v240, v72\n\t"
    "v_mul_f32 v241, v241, v73\n\t"
    "v_mfma_f32_16x16x32_bf16 v[156:159], v[228:231], a[188:191], v[156:159]\n\t"
    "v_mul_f32 v242, v242, v74\n\t"
    "v_mul_f32 v243, v243, v75\n\t"
    "v_mfma_f32_16x16x32_bf16 v[156:159], v[224:227], a[172:175], v[156:159]\n\t"
    "s_add_u32 m0, %[um0], 144384\n\t"
    "s_add_u32 s41, %[uws], 958720\n\t"
    "v_mfma_f32_16x16x32_bf16 v[156:159], v[220:223], a[156:159], v[156:159]\n\t"
    "buffer_load_dwordx4 %[uwv], %[usrdw], s41 offen lds\n\t"
    "v_exp_f32 v240, v240\n\t"
    "v_mfma_f32_16x16x32_bf16 v[156:159], v[216:219], a[140:143], v[156:159]\n\t"
    "v_exp_f32 v241, v241\n\t"
    "s_waitcnt lgkmcnt(0)\n\t"
    "v_mfma_f32_16x16x32_bf16 v[160:163], v[192:195], a[128:131], 0\n\t"
    "ds_read_b64_tr_b16 v[216:217], %[ur01] offset:49664\n\t"
    "v_exp_f32 v242, v242\n\t"
    "v_mfma_f32_16x16x32_bf16 v[160:163], v[196:199], a[144:147], v[160:163]\n\t"
    "ds_read_b64_tr_b16 v[218:219], %[ur01] offset:51712\n\t"
    "v_exp_f32 v243, v243\n\t"
    "v_mfma_f32_16x16x32_bf16 v[160:163], v[200:203], a[160:163], v[160:163]\n\t"
    "v_fma_f32 v240, v240, v244, v244\n\t"
    "v_fma_f32 v241, v241, v244, v244\n\t"
    "v_fma_f32 v242, v242, v244, v244\n\t"
    "v_mfma_f32_16x16x32_bf16 v[160:163], v[204:207], a[176:179], v[160:163]\n\t"
    "ds_read_b64_tr_b16 v[220:221], %[ur01] offset:53760\n\t"
    "v_fma_f32 v243, v243, v244, v244\n\t"
    "v_mfma_f32_16x16x32_bf16 v[160:163], v[208:211], a[192:195], v[160:163]\n\t"
    "ds_read_b64_tr_b16 v[222:223], %[ur01] offset:55808\n\t"
    "v_rcp_f32 v240, v240\n\t"
    "v_mfma_f32_16x16x32_bf16 v[160:163], v[212:215], a[208:211], v[160:163]\n\t"
    "v_rcp_f32 v241, v241\n\t"
    "v_mfma_f32_16x16x32_bf16 v[168:171], v[212:215], a[212:215], 0\n\t"
    "ds_read_b64_tr_b16 v[224:225], %[ur01] offset:57856\n\t"
    "v_rcp_f32 v242, v242\n\t"
    "v_mfma_f32_16x16x32_bf16 v[168:171], v[208:211], a[196:199], v[168:171]\n\t"
    "ds_read_b64_tr_b16 v[226:227], %[ur01] offset:59904\n\t"
    "s_add_u32 m0, %[um0], 145408\n\t"
    "s_add_u32 s41, %[uws], 1032448\n\t"
    "v_mfma_f32_16x16x32_bf16 v[168:171], v[204:207], a[180:183], v[168:171]\n\t"
    "buffer_load_dwordx4 %[uwv], %[usrdw], s41 offen lds\n\t"
    "v_rcp_f32 v243, v243\n\t"
    "v_mfma_f32_16x16x32_bf16 v[168:171], v[200:203], a[164:167], v[168:171]\n\t"
    "ds_read_b64_tr_b16 v[228:229], %[ur01] offset:61952\n\t"
    "v_mul_f32 v72, v72, v240\n\t"
    "v_mul_f32 v73, v73, v241\n\t"
    "v_mfma_f32_16x16x32_bf16 v[168:171], v[196:199], a[148:151], v[168:171]\n\t"
    "v_mul_f32 v74, v74, v242\n\t"
    "v_mul_f32 v75, v75, v243\n\t"
    "v_mfma_f32_16x16x32_bf16 v[168:171], v[192:195], a[132:135], v[168:171]\n\t"
    "ds_read_b64_tr_b16 v[230:231], %[ur01] offset:64000\n\t"
    "v_cvt_pk_bf16_f32 v72, v72, v73\n\t"
    "v_cvt_pk_bf16_f32 v73, v74, v75\n\t"
    "v_mfma_f32_16x16x32_bf16 v[176:179], v[192:195], a[136:139], 0\n\t"
    "ds_read_b64_tr_b16 v[232:233], %[ur11] offset:512\n\t"
    "v_fmaak_f32 v240, v76, v76, 0x409d30fc\n\t"
    "v_fmaak_f32 v241, v77, v77, 0x409d30fc\n\t"
    "v_mfma_f32_16x16x32_bf16 v[176:179], v[196:199], a[152:155], v[176:179]\n\t"
    "v_fmaak_f32 v242, v78, v78, 0x409d30fc\n\t"
    "v_fmaak_f32 v243, v79, v79, 0x409d30fc\n\t"
    "v_mfma_f32_16x16x32_bf16 v[176:179], v[200:203], a[168:171], v[176:179]\n\t"
    "ds_read_b64_tr_b16 v[234:235], %[ur11] offset:2560\n\t"
    "v_mul_f32 v240, v240, v76\n\t"
    "v_mul_f32 v241, v241, v77\n\t"
    "v_mfma_f32_16x16x32_bf16 v[176:179], v[204:207], a[184:187], v[176:179]\n\t"
    "ds_read_b64_tr_b16 v[236:237], %[ur11] offset:4608\n\t"
    "v_mul_f32 v242, v242, v78\n\t"
    "v_mul_f32 v243, v243, v79\n\t"
    "v_mfma_f32_16x16x32_bf16 v[176:179], v[208:211], a[200:203], v[176:179]\n\t"
    "v_exp_f32 v240, v240\n\t"
    "v_mfma_f32_16x16x32_bf16 v[176:179], v[212:215], a[216:219], v[176:179]\n\t"
    "ds_read_b64_tr_b16 v[238:239], %[ur11] offset:6656\n\t"
    "v_exp_f32 v241, v241\n\t"
    "v_mfma_f32_16x16x32_bf16 v[184:187], v[212:215], a[220:223], 0\n\t"
    "s_add_u32 m0, %[um0], 0\n\t"
    "s_add_u32 s41, %[uws], 1769728\n\t"
    "v_mfma_f32_16x16x32_bf16 v[184:187], v[208:211], a[204:207], v[184:187]\n\t"
    "buffer_load_dwordx4 %[uwv], %[usrdw], s41 offen lds\n\t"
    "v_exp_f32 v242, v242\n\t"
    "v_mfma_f32_16x16x32_bf16 v[184:187], v[204:207], a[188:191], v[184:187]\n\t"
    "v_exp_f32 v243, v243\n\t"
    "v_fma_f32 v240, v240, v244, v244\n\t"
    "v_mfma_f32_16x16x32_bf16 v[184:187], v[200:203], a[172:175], v[184:187]\n\t"
    "v_fma_f32 v241, v241, v244, v244\n\t"
    "v_fma_f32 v242, v242, v244, v244\n\t"
    "v_fma_f32 v243, v243, v244, v244\n\t"
    "v_mfma_f32_16x16x32_bf16 v[184:187], v[196:199], a[156:159], v[184:187]\n\t"
    "v_rcp_f32 v240, v240\n\t"
    "v_mfma_f32_16x16x32_bf16 v[184:187], v[192:195], a[140:143], v[184:187]\n\t"
    "v_rcp_f32 v241, v241\n\t"
    "s_waitcnt lgkmcnt(0)\n\t"
    "s_waitcnt vmcnt(14)\n\t"
    "s_barrier\n\t"
    "v_mfma_f32_16x16x32_bf16 v[164:167], v[216:219], a[128:131], 0\n\t"
    "ds_read_b64_tr_b16 v[192:193], %[ur10] offset:8192\n\t"
    "v_rcp_f32 v242, v242\n\t"
    "v_mfma_f32_16x16x32_bf16 v[164:167], v[220:223], a[144:147], v[164:167]\n\t"
    "ds_read_b64_tr_b16 v[194:195], %[ur10] offset:10240\n\t"
    "v_mfma_f32_16x16x32_bf16 v[164:167], v[224:227], a[160:163], v[164:167]\n\t"
    "v_rcp_f32 v243, v243\n\t"
    "v_mul_f32 v76, v76, v240\n\t"
    "v_mul_f32 v77, v77, v241\n\t"
    "v_mfma_f32_16x16x32_bf16 v[164:167], v[228:231], a[176:179], v[164:167]\n\t"
    "ds_read_b64_tr_b16 v[196:197], %[ur10] offset:12288\n\t"
    "v_mul_f32 v78, v78, v242\n\t"
    "v_mul_f32 v79, v79, v243\n\t"
    "v_mfma_f32_16x16x32_bf16 v[164:167], v[232:235], a[192:195], v[164:167]\n\t"
    "ds_read_b64_tr_b16 v[198:199], %[ur10] offset:14336\n\t"
    "s_add_u32 m0, %[um0], 1024\n\t"
    "s_add_u32 s41, %[uws], 1843456\n\t"
    "v_mfma_f32_16x16x32_bf16 v[164:167], v[236:239], a[208:211], v[164:167]\n\t"
    "buffer_load_dwordx4 %[uwv], %[usrdw], s41 offen lds\n\t"
    "v_cvt_pk_bf16_f32 v74, v76, v77\n\t"
    "v_mfma_f32_16x16x32_bf16 v[172:175], v[236:239], a[212:215], 0\n\t"
    "ds_read_b64_tr_b16 v[200:201], %[ur10] offset:16384\n\t"
    "v_cvt_pk_bf16_f32 v75, v78, v79\n\t"
    "v_mfma_f32_16x16x32_bf16 v[172:175], v[232:235], a[196:199], v[172:175]\n\t"
    "ds_read_b64_tr_b16 v[202:203], %[ur10] offset:18432\n\t"
    "buffer_store_dwordx4 v[72:75], %[xl], %[srd], s53 offen sc1\n\t"
    "v_fmaak_f32 v240, v80, v80, 0x409d30fc\n\t"
    "v_fmaak_f32 v241, v81, v81, 0x409d30fc\n\t"
    "v_mfma_f32_16x16x32_bf16 v[172:175], v[228:231], a[180:183], v[172:175]\n\t"
    "v_fmaak_f32 v242, v82, v82, 0x409d30fc\n\t"
    "v_fmaak_f32 v243, v83, v83, 0x409d30fc\n\t"
    "v_mfma_f32_16x16x32_bf16 v[172:175], v[224:227], a[164:167], v[172:175]\n\t"
    "ds_read_b64_tr_b16 v[204:205], %[ur10] offset:20480\n\t"
    "v_mul_f32 v240, v240, v80\n\t"
    "v_mul_f32 v241, v241, v81\n\t"
    "v_mfma_f32_16x16x32_bf16 v[172:175], v[220:223], a[148:151], v[172:175]\n\t"
    "v_mul_f32 v242, v242, v82\n\t"
    "v_mul_f32 v243, v243, v83\n\t"
    "v_mfma_f32_16x16x32_bf16 v[172:175], v[216:219], a[132:135], v[172:175]\n\t"
    "ds_read_b64_tr_b16 v[206:207], %[ur10] offset:22528\n\t"
    "v_exp_f32 v240, v240\n\t"
    "v_mfma_f32_16x16x32_bf16 v[180:183], v[216:219], a[136:139], 0\n\t"
    "ds_read_b64_tr_b16 v[208:209], %[ur10] offset:24576\n\t"
    "v_exp_f32 v241, v241\n\t"
    "v_mfma_f32_16x16x32_bf16 v[180:183], v[220:223], a[152:155], v[180:183]\n\t"
    "v_exp_f32 v242, v242\n\t"
    "v_mfma_f32_16x16x32_bf16 v[180:183], v[224:227], a[168:171], v[180:183]\n\t"
    "ds_read_b64_tr_b16 v[210:211], %[ur10] offset:26624\n\t"
    "s_add_u32 m0, %[um0], 2048\n\t"
    "s_add_u32 s41, %[uws], 1917184\n\t"
    "v_mfma_f32_16x16x32_bf16 v[180:183], v[228:231], a[184:187], v[180:183]\n\t"
    "ds_read_b64_tr_b16 v[212:213], %[ur10] offset:28672\n\t"
    "buffer_load_dwordx4 %[uwv], %[usrdw], s41 offen lds\n\t"
    "v_mfma_f32_16x16x32_bf16 v[180:183], v[232:235], a[200:203], v[180:183]\n\t"
    "v_exp_f32 v243, v243\n\t"
    "v_fma_f32 v240, v240, v244, v244\n\t"
    "v_fma_f32 v241, v241, v244, v244\n\t"
    "v_mfma_f32_16x16x32_bf16 v[180:183], v[236:239], a[216:219], v[180:183]\n\t"
    "ds_read_b64_tr_b16 v[214:215], %[ur10] offset:30720\n\t"
    "v_fma_f32 v242, v242, v244, v244\n\t"
    "v_mfma_f32_16x16x32_bf16 v[188:191], v[236:239], a[220:223], 0\n\t"
    "v_fma_f32 v243, v243, v244, v244\n\t"
    "v_rcp_f32 v240, v240\n\t"
    "v_mfma_f32_16x16x32_bf16 v[188:191], v[232:235], a[204:207], v[188:191]\n\t"
    "v_rcp_f32 v241, v241\n\t"
    "v_mfma_f32_16x16x32_bf16 v[188:191], v[228:231], a[188:191], v[188:191]\n\t"
    "v_rcp_f32 v242, v242\n\t"
    "v_mfma_f32_16x16x32_bf16 v[188:191], v[224:227], a[172:175], v[188:191]\n\t"
    "v_rcp_f32 v243, v243\n\t"
    "v_mul_f32 v80, v80, v240\n\t"
    "v_mul_f32 v81, v81, v241\n\t"
    "v_mfma_f32_16x16x32_bf16 v[188:191], v[220:223], a[156:159], v[188:191]\n\t"
    "v_mul_f32 v82, v82, v242\n\t"
    "v_mul_f32 v83, v83, v243\n\t"
    "v_cvt_pk_bf16_f32 v80, v80, v81\n\t"
    "v_mfma_f32_16x16x32_bf16 v[188:191], v[216:219], a[140:143], v[188:191]\n\t"
    "v_cvt_pk_bf16_f32 v81, v82, v83\n\t"
    "s_waitcnt lgkmcnt(0)\n\t"
    "v_mfma_f32_16x16x32_bf16 v[128:131], v[192:195], a[0:3], v[128:131]\n\t"
    "ds_read_b64_tr_b16 v[216:217], %[ur11] offset:8192\n\t"
    "v_fmaak_f32 v240, v84, v84, 0x409d30fc\n\t"
    "v_fmaak_f32 v241, v85, v85, 0x409d30fc\n\t"
    "v_mfma_f32_16x16x32_bf16 v[128:131], v[196:199], a[16:19], v[128:131]\n\t"
    "ds_read_b64_tr_b16 v[218:219], %[ur11] offset:10240\n\t"
    "s_add_u32 m0, %[um0], 12288\n\t"
    "s_add_u32 s41, %[uws], 2654464\n\t"
    "v_mfma_f32_16x16x32_bf16 v[128:131], v[200:203], a[32:35], v[128:131]\n\t"
    "buffer_load_dwordx4 %[uwv], %[usrdw], s41 offen lds\n\t"
    "v_fmaak_f32 v242, v86, v86, 0x409d30fc\n\t"
    "v_mfma_f32_16x16x32_bf16 v[128:131], v[204:207], a[48:51], v[128:131]\n\t"
    "ds_read_b64_tr_b16 v[220:221], %[ur11] offset:12288\n\t"
    "v_fmaak_f32 v243, v87, v87, 0x409d30fc\n\t"
    "v_mul_f32 v240, v240, v84\n\t"
    "v_mfma_f32_16x16x32_bf16 v[128:131], v[208:211], a[64:67], v[128:131]\n\t"
    "ds_read_b64_tr_b16 v[222:223], %[ur11] offset:14336\n\t"
    "v_mul_f32 v241, v241, v85\n\t"
    "v_mul_f32 v242, v242, v86\n\t"
    "v_mfma_f32_16x16x32_bf16 v[128:131], v[212:215], a[80:83], v[128:131]\n\t"
    "v_mul_f32 v243, v243, v87\n\t"
    "v_exp_f32 v240, v240\n\t"
    "v_mfma_f32_16x16x32_bf16 v[136:139], v[212:215], a[84:87], v[136:139]\n\t"
    "ds_read_b64_tr_b16 v[224:225], %[ur11] offset:16384\n\t"
    "v_mfma_f32_16x16x32_bf16 v[136:139], v[208:211], a[68:71], v[136:139]\n\t"
    "ds_read_b64_tr_b16 v[226:227], %[ur11] offset:18432\n\t"
    "v_exp_f32 v241, v241\n\t"
    "v_mfma_f32_16x16x32_bf16 v[136:139], v[204:207], a[52:55], v[136:139]\n\t"
    "v_exp_f32 v242, v242\n\t"
    "v_exp_f32 v243, v243\n\t"
    "v_mfma_f32_16x16x32_bf16 v[136:139], v[200:203], a[36:39], v[136:139]\n\t"
    "ds_read_b64_tr_b16 v[228:229], %[ur11] offset:20480\n\t"
    "v_fma_f32 v240, v240, v244, v244\n\t"
    "v_mfma_f32_16x16x32_bf16 v[136:139], v[196:199], a[20:23], v[136:139]\n\t"
    "v_fma_f32 v241, v241, v244, v244\n\t"
    "v_fma_f32 v242, v242, v244, v244\n\t"
    "v_fma_f32 v243, v243, v244, v244\n\t"
    "v_mfma_f32_16x16x32_bf16 v[136:139], v[192:195], a[4:7], v[136:139]\n\t"
    "ds_read_b64_tr_b16 v[230:231], %[ur11] offset:22528\n\t"
    "s_add_u32 m0, %[um0], 13312\n\t"
    "s_add_u32 s41, %[uws], 2728192\n\t"
    "v_mfma_f32_16x16x32_bf16 v[144:147], v[192:195], a[8:11], v[144:147]\n\t"
    "ds_read_b64_tr_b16 v[232:233], %[ur11] offset:24576\n\t"
    "buffer_load_dwordx4 %[uwv], %[usrdw], s41 offen lds\n\t"
    "v_mfma_f32_16x16x32_bf16 v[144:147], v[196:199], a[24:27], v[144:147]\n\t"
    "v_rcp_f32 v240, v240\n\t"
    "v_mfma_f32_16x16x32_bf16 v[144:147], v[200:203], a[40:43], v[144:147]\n\t"
    "ds_read_b64_tr_b16 v[234:235], %[ur11] offset:26624\n\t"
    "v_rcp_f32 v241, v241\n\t"
    "v_mfma_f32_16x16x32_bf16 v[144:147], v[204:207], a[56:59], v[144:147]\n\t"
    "ds_read_b64_tr_b16 v[236:237], %[ur11] offset:28672\n\t"
    "v_rcp_f32 v242, v242\n\t"
    "v_mfma_f32_16x16x32_bf16 v[144:147], v[208:211], a[72:75], v[144:147]\n\t"
    "v_rcp_f32 v243, v243\n\t"
    "v_mul_f32 v84, v84, v240\n\t"
    "v_mfma_f32_16x16x32_bf16 v[144:147], v[212:215], a[88:91], v[144:147]\n\t"
    "ds_read_b64_tr_b16 v[238:239], %[ur11] offset:30720\n\t"
    "v_mul_f32 v85, v85, v241\n\t"
    "v_mul_f32 v86, v86, v242\n\t"
    "v_mfma_f32_16x16x32_bf16 v[152:155], v[212:215], a[92:95], v[152:155]\n\t"
    "v_mul_f32 v87, v87, v243\n\t"
    "v_cvt_pk_bf16_f32 v82, v84, v85\n\t"
    "v_mfma_f32_16x16x32_bf16 v[152:155], v[208:211], a[76:79], v[152:155]\n\t"
    "v_cvt_pk_bf16_f32 v83, v86, v87\n\t"
    "v_fmaak_f32 v240, v88, v88, 0x409d30fc\n\t"
    "v_mfma_f32_16x16x32_bf16 v[152:155], v[204:207], a[60:63], v[152:155]\n\t"
    "buffer_store_dwordx4 v[80:83], %[xl], %[srd], s54 offen sc1\n\t"
    "v_fmaak_f32 v241, v89, v89, 0x409d30fc\n\t"
    "v_fmaak_f32 v242, v90, v90, 0x409d30fc\n\t"
    "v_fmaak_f32 v243, v91, v91, 0x409d30fc\n\t"
    "v_mfma_f32_16x16x32_bf16 v[152:155], v[200:203], a[44:47], v[152:155]\n\t"
    "s_add_u32 m0, %[um0], 14336\n\t"
    "s_add_u32 s41, %[uws], 2801920\n\t"
    "v_mfma_f32_16x16x32_bf16 v[152:155], v[196:199], a[28:31], v[152:155]\n\t"
    "buffer_load_dwordx4 %[uwv], %[usrdw], s41 offen lds\n\t"
    "v_mul_f32 v240, v240, v88\n\t"
    "v_mul_f32 v241, v241, v89\n\t"
    "v_mfma_f32_16x16x32_bf16 v[152:155], v[192:195], a[12:15], v[152:155]\n\t"
    "v_mul_f32 v242, v242, v90\n\t"
    "v_mul_f32 v243, v243, v91\n\t"
    "s_waitcnt lgkmcnt(0)\n\t"
    "v_mfma_f32_16x16x32_bf16 v[132:135], v[216:219], a[0:3], v[132:135]\n\t"
    "ds_read_b64_tr_b16 v[192:193], %[ur10] offset:8704\n\t"
    "v_mfma_f32_16x16x32_bf16 v[132:135], v[220:223], a[16:19], v[132:135]\n\t"
    "ds_read_b64_tr_b16 v[194:195], %[ur10] offset:10752\n\t"
    "v_exp_f32 v240, v240\n\t"
    "v_mfma_f32_16x16x32_bf16 v[132:135], v[224:227], a[32:35], v[132:135]\n\t"
    "v_exp_f32 v241, v241\n\t"
    "v_exp_f32 v242, v242\n\t"
    "v_mfma_f32_16x16x32_bf16 v[132:135], v[228:231], a[48:51], v[132:135]\n\t"
    "ds_read_b64_tr_b16 v[196:197], %[ur10] offset:12800\n\t"
    "v_exp_f32 v243, v243\n\t"
    "v_mfma_f32_16x16x32_bf16 v[132:135], v[232:235], a[64:67], v[132:135]\n\t"
    "ds_read_b64_tr_b16 v[198:199], %[ur10] offset:14848\n\t"
    "v_fma_f32 v240, v240, v244, v244\n\t"
    "v_mfma_f32_16x16x32_bf16 v[132:135], v[236:239], a[80:83], v[132:135]\n\t"
    "v_fma_f32 v241, v241, v244, v244\n\t"
    "v_fma_f32 v242, v242, v244, v244\n\t"
    "v_fma_f32 v243, v243, v244, v244\n\t"
    "v_mfma_f32_16x16x32_bf16 v[140:143], v[236:239], a[84:87], v[140:143]\n\t"
    "ds_read_b64_tr_b16 v[200:201], %[ur10] offset:16896\n\t"
    "v_rcp_f32 v240, v240\n\t"
    "v_mfma_f32_16x16x32_bf16 v[140:143], v[232:235], a[68:71], v[140:143]\n\t"
    "ds_read_b64_tr_b16 v[202:203], %[ur10] offset:18944\n\t"
    "v_mfma_f32_16x16x32_bf16 v[140:143], v[228:231], a[52:55], v[140:143]\n\t"
    "s_add_u32 m0, %[um0], 24576\n\t"
    "s_add_u32 s41, %[uws], 3539200\n\t"
    "v_rcp_f32 v241, v241\n\t"
    "v_mfma_f32_16x16x32_bf16 v[140:143], v[224:227], a[36:39], v[140:143]\n\t"
    "ds_read_b64_tr_b16 v[204:205], %[ur10] offset:20992\n\t"
    "buffer_load_dwordx4 %[uwv], %[usrdw], s41 offen lds\n\t"
    "v_mfma_f32_16x16x32_bf16 v[140:143], v[220:223], a[20:23], v[140:143]\n\t"
    "v_rcp_f32 v242, v242\n\t"
    "v_mfma_f32_16x16x32_bf16 v[140:143], v[216:219], a[4:7], v[140:143]\n\t"
    "ds_read_b64_tr_b16 v[206:207], %[ur10] offset:23040\n\t"
    "v_rcp_f32 v243, v243\n\t"
    "v_mul_f32 v88, v88, v240\n\t"
    "v_mfma_f32_16x16x32_bf16 v[148:151], v[216:219], a[8:11], v[148:151]\n\t"
    "ds_read_b64_tr_b16 v[208:209], %[ur10] offset:25088\n\t"
    "v_mul_f32 v89, v89, v241\n\t"
    "v_mul_f32 v90, v90, v242\n\t"
    "v_mfma_f32_16x16x32_bf16 v[148:151], v[220:223], a[24:27], v[148:151]\n\t"
    "v_mul_f32 v91, v91, v243\n\t"
    "v_cvt_pk_bf16_f32 v88, v88, v89\n\t"
    "v_mfma_f32_16x16x32_bf16 v[148:151], v[224:227], a[40:43], v[148:151]\n\t"
    "ds_read_b64_tr_b16 v[210:211], %[ur10] offset:27136\n\t"
    "v_cvt_pk_bf16_f32 v89, v90, v91\n\t"
    "v_fmaak_f32 v240, v92, v92, 0x409d30fc\n\t"
    "v_mfma_f32_16x16x32_bf16 v[148:151], v[228:231], a[56:59], v[148:151]\n\t"
    "ds_read_b64_tr_b16 v[212:213], %[ur10] offset:29184\n\t"
    "v_fmaak_f32 v241, v93, v93, 0x409d30fc\n\t"
    "v_fmaak_f32 v242, v94, v94, 0x409d30fc\n\t"
    "v_mfma_f32_16x16x32_bf16 v[148:151], v[232:235], a[72:75], v[148:151]\n\t"
    "v_fmaak_f32 v243, v95, v95, 0x409d30fc\n\t"
    "v_mul_f32 v240, v240, v92\n\t"
    "v_mfma_f32_16x16x32_bf16 v[148:151], v[236:239], a[88:91], v[148:151]\n\t"
    "ds_read_b64_tr_b16 v[214:215], %[ur10] offset:31232\n\t"
    "v_mul_f32 v241, v241, v93\n\t"
    "v_mul_f32 v242, v242, v94\n\t"
    "v_mfma_f32_16x16x32_bf16 v[156:159], v[236:239], a[92:95], v[156:159]\n\t"
    "s_add_u32 m0, %[um0], 25600\n\t"
    "s_add_u32 s41, %[uws], 3612928\n\t"
    "v_mul_f32 v243, v243, v95\n\t"
    "v_mfma_f32_16x16x32_bf16 v[156:159], v[232:235], a[76:79], v[156:159]\n\t"
    "buffer_load_dwordx4 %[uwv], %[usrdw], s41 offen lds\n\t"
    "v_mfma_f32_16x16x32_bf16 v[156:159], v[228:231], a[60:63], v[156:159]\n\t"
    "v_exp_f32 v240, v240\n\t"
    "v_exp_f32 v241, v241\n\t"
    "v_mfma_f32_16x16x32_bf16 v[156:159], v[224:227], a[44:47], v[156:159]\n\t"
    "v_exp_f32 v242, v242\n\t"
    "v_mfma_f32_16x16x32_bf16 v[156:159], v[220:223], a[28:31], v[156:159]\n\t"
    "v_exp_f32 v243, v243\n\t"
    "v_fma_f32 v240, v240, v244, v244\n\t"
    "v_mfma_f32_16x16x32_bf16 v[156:159], v[216:219], a[12:15], v[156:159]\n\t"
    "v_fma_f32 v241, v241, v244, v244\n\t"
    "v_fma_f32 v242, v242, v244, v244\n\t"
    "s_waitcnt lgkmcnt(0)\n\t"
    "v_mfma_f32_16x16x32_bf16 v[160:163], v[192:195], a[0:3], v[160:163]\n\t"
    "ds_read_b64_tr_b16 v[216:217], %[ur11] offset:8704\n\t"
    "v_fma_f32 v243, v243, v244, v244\n\t"
    "v_mfma_f32_16x16x32_bf16 v[160:163], v[196:199], a[16:19], v[160:163]\n\t"
    "ds_read_b64_tr_b16 v[218:219], %[ur11] offset:10752\n\t"
    "v_rcp_f32 v240, v240\n\t"
    "v_mfma_f32_16x16x32_bf16 v[160:163], v[200:203], a[32:35], v[160:163]\n\t"
    "v_rcp_f32 v241, v241\n\t"
    "v_mfma_f32_16x16x32_bf16 v[160:163], v[204:207], a[48:51], v[160:163]\n\t"
    "ds_read_b64_tr_b16 v[220:221], %[ur11] offset:12800\n\t"
    "v_rcp_f32 v242, v242\n\t"
    "v_mfma_f32_16x16x32_bf16 v[160:163], v[208:211], a[64:67], v[160:163]\n\t"
    "ds_read_b64_tr_b16 v[222:223], %[ur11] offset:14848\n\t"
    "s_add_u32 m0, %[um0], 26624\n\t"
    "s_add_u32 s41, %[uws], 3686656\n\t"
    "v_mfma_f32_16x16x32_bf16 v[160:163], v[212:215], a[80:83], v[160:163]\n\t"
    "buffer_load_dwordx4 %[uwv], %[usrdw], s41 offen lds\n\t"
    "v_rcp_f32 v243, v243\n\t"
    "v_mfma_f32_16x16x32_bf16 v[168:171], v[212:215], a[84:87], v[168:171]\n\t"
    "ds_read_b64_tr_b16 v[224:225], %[ur11] offset:16896\n\t"
    "v_mul_f32 v92, v92, v240\n\t"
    "v_mul_f32 v93, v93, v241\n\t"
    "v_mfma_f32_16x16x32_bf16 v[168:171], v[208:211], a[68:71], v[168:171]\n\t"
    "ds_read_b64_tr_b16 v[226:227], %[ur11] offset:18944\n\t"
    "v_mul_f32 v94, v94, v242\n\t"
    "v_mul_f32 v95, v95, v243\n\t"
    "v_mfma_f32_16x16x32_bf16 v[168:171], v[204:207], a[52:55], v[168:171]\n\t"
    "v_cvt_pk_bf16_f32 v90, v92, v93\n\t"
    "v_cvt_pk_bf16_f32 v91, v94, v95\n\t"
    "v_mfma_f32_16x16x32_bf16 v[168:171], v[200:203], a[36:39], v[168:171]\n\t"
    "ds_read_b64_tr_b16 v[228:229], %[ur11] offset:20992\n\t"
    "v_fmaak_f32 v240, v96, v96, 0x409d30fc\n\t"
    "v_mfma_f32_16x16x32_bf16 v[168:171], v[196:199], a[20:23], v[168:171]\n\t"
    "buffer_store_dwordx4 v[88:91], %[xl], %[srd], s55 offen sc1\n\t"
    "v_fmaak_f32 v241, v97, v97, 0x409d30fc\n\t"
    "v_fmaak_f32 v242, v98, v98, 0x409d30fc\n\t"
    "v_fmaak_f32 v243, v99, v99, 0x409d30fc\n\t"
    "v_mfma_f32_16x16x32_bf16 v[168:171], v[192:195], a[4:7], v[168:171]\n\t"
    "ds_read_b64_tr_b16 v[230:231], %[ur11] offset:23040\n\t"
    "v_mul_f32 v240, v240, v96\n\t"
    "v_mfma_f32_16x16x32_bf16 v[176:179], v[192:195], a[8:11], v[176:179]\n\t"
    "ds_read_b64_tr_b16 v[232:233], %[ur11] offset:25088\n\t"
    "v_mul_f32 v241, v241, v97\n\t"
    "v_mul_f32 v242, v242, v98\n\t"
    "v_mfma_f32_16x16x32_bf16 v[176:179], v[196:199], a[24:27], v[176:179]\n\t"
    "v_mul_f32 v243, v243, v99\n\t"
    "v_exp_f32 v240, v240\n\t"
    "v_mfma_f32_16x16x32_bf16 v[176:179], v[200:203], a[40:43], v[176:179]\n\t"
    "ds_read_b64_tr_b16 v[234:235], %[ur11] offset:27136\n\t"
    "v_mfma_f32_16x16x32_bf16 v[176:179], v[204:207], a[56:59], v[176:179]\n\t"
    "ds_read_b64_tr_b16 v[236:237], %[ur11] offset:29184\n\t"
    "s_add_u32 m0, %[um0], 36864\n\t"
    "s_add_u32 s41, %[uws], 4423936\n\t"
    "v_mfma_f32_16x16x32_bf16 v[176:179], v[208:211], a[72:75], v[176:179]\n\t"
    "buffer_load_dwordx4 %[uwv], %[usrdw], s41 offen lds\n\t"
    "v_exp_f32 v241, v241\n\t"
    "v_mfma_f32_16x16x32_bf16 v[176:179], v[212:215], a[88:91], v[176:179]\n\t"
    "ds_read_b64_tr_b16 v[238:239], %[ur11] offset:31232\n\t"
    "v_exp_f32 v242, v242\n\t"
    "v_mfma_f32_16x16x32_bf16 v[184:187], v[212:215], a[92:95], v[184:187]\n\t"
    "v_exp_f32 v243, v243\n\t"
    "v_fma_f32 v240, v240, v244, v244\n\t"
    "v_mfma_f32_16x16x32_bf16 v[184:187], v[208:211], a[76:79], v[184:187]\n\t"
    "v_fma_f32 v241, v241, v244, v244\n\t"
    "v_fma_f32 v242, v242, v244, v244\n\t"
    "v_fma_f32 v243, v243, v244, v244\n\t"
    "v_mfma_f32_16x16x32_bf16 v[184:187], v[204:207], a[60:63], v[184:187]\n\t"
    "v_rcp_f32 v240, v240\n\t"
    "v_mfma_f32_16x16x32_bf16 v[184:187], v[200:203], a[44:47], v[184:187]\n\t"
    "v_rcp_f32 v241, v241\n\t"
    "v_mfma_f32_16x16x32_bf16 v[184:187], v[196:199], a[28:31], v[184:187]\n\t"
    "v_rcp_f32 v242, v242\n\t"
    "v_rcp_f32 v243, v243\n\t"
    "v_mfma_f32_16x16x32_bf16 v[184:187], v[192:195], a[12:15], v[184:187]\n\t"
    "v_mul_f32 v96, v96, v240\n\t"
    "v_mul_f32 v97, v97, v241\n\t"
    "s_waitcnt lgkmcnt(0)\n\t"
    "s_waitcnt vmcnt(20)\n\t"
    "s_barrier\n\t"
    "v_mfma_f32_16x16x32_bf16 v[164:167], v[216:219], a[0:3], v[164:167]\n\t"
    "ds_read_b64_tr_b16 v[192:193], %[ur10] offset:32768\n\t"
    "v_mul_f32 v98, v98, v242\n\t"
    "v_mfma_f32_16x16x32_bf16 v[164:167], v[220:223], a[16:19], v[164:167]\n\t"
    "ds_read_b64_tr_b16 v[194:195], %[ur10] offset:34816\n\t"
    "s_add_u32 m0, %[um0], 37888\n\t"
    "s_add_u32 s41, %[uws], 4497664\n\t"
    "v_mfma_f32_16x16x32_bf16 v[164:167], v[224:227], a[32:35], v[164:167]\n\t"
    "buffer_load_dwordx4 %[uwv], %[usrdw], s41 offen lds\n\t"
    "v_mul_f32 v99, v99, v243\n\t"
    "v_cvt_pk_bf16_f32 v96, v96, v97\n\t"
    "v_mfma_f32_16x16x32_bf16 v[164:167], v[228:231], a[48:51], v[164:167]\n\t"
    "ds_read_b64_tr_b16 v[196:197], %[ur10] offset:36864\n\t"
    "v_cvt_pk_bf16_f32 v97, v98, v99\n\t"
    "v_mfma_f32_16x16x32_bf16 v[164:167], v[232:235], a[64:67], v[164:167]\n\t"
    "ds_read_b64_tr_b16 v[198:199], %[ur10] offset:38912\n\t"
    "v_fmaak_f32 v240, v100, v100, 0x409d30fc\n\t"
    "v_fmaak_f32 v241, v101, v101, 0x409d30fc\n\t"
    "v_mfma_f32_16x16x32_bf16 v[164:167], v[236:239], a[80:83], v[164:167]\n\t"
    "v_fmaak_f32 v242, v102, v102, 0x409d30fc\n\t"
    "v_fmaak_f32 v243, v103, v103, 0x409d30fc\n\t"
    "v_mul_f32 v240, v240, v100\n\t"
    "v_mfma_f32_16x16x32_bf16 v[172:175], v[236:239], a[84:87], v[172:175]\n\t"
    "ds_read_b64_tr_b16 v[200:201], %[ur10] offset:40960\n\t"
    "v_mul_f32 v241, v241, v101\n\t"
    "v_mfma_f32_16x16x32_bf16 v[172:175], v[232:235], a[68:71], v[172:175]\n\t"
    "ds_read_b64_tr_b16 v[202:203], %[ur10] offset:43008\n\t"
    "v_mul_f32 v242, v242, v102\n\t"
    "v_mul_f32 v243, v243, v103\n\t"
    "v_mfma_f32_16x16x32_bf16 v[172:175], v[228:231], a[52:55], v[172:175]\n\t"
    "v_exp_f32 v240, v240\n\t"
    "v_mfma_f32_16x16x32_bf16 v[172:175], v[224:227], a[36:39], v[172:175]\n\t"
    "ds_read_b64_tr_b16 v[204:205], %[ur10] offset:45056\n\t"
    "v_exp_f32 v241, v241\n\t"
    "v_mfma_f32_16x16x32_bf16 v[172:175], v[220:223], a[20:23], v[172:175]\n\t"
    "v_exp_f32 v242, v242\n\t"
    "v_mfma_f32_16x16x32_bf16 v[172:175], v[216:219], a[4:7], v[172:175]\n\t"
    "ds_read_b64_tr_b16 v[206:207], %[ur10] offset:47104\n\t"
    "s_add_u32 m0, %[um0], 38912\n\t"
    "s_add_u32 s41, %[uws], 4571392\n\t"
    "v_mfma_f32_16x16x32_bf16 v[180:183], v[216:219], a[8:11], v[180:183]\n\t"
    "ds_read_b64_tr_b16 v[208:209], %[ur10] offset:49152\n\t"
    "buffer_load_dwordx4 %[uwv], %[usrdw], s41 offen lds\n\t"
    "v_mfma_f32_16x16x32_bf16 v[180:183], v[220:223], a[24:27], v[180:183]\n\t"
    "v_exp_f32 v243, v243\n\t"
    "v_fma_f32 v240, v240, v244, v244\n\t"
    "v_fma_f32 v241, v241, v244, v244\n\t"
    "v_mfma_f32_16x16x32_bf16 v[180:183], v[224:227], a[40:43], v[180:183]\n\t"
    "ds_read_b64_tr_b16 v[210:211], %[ur10] offset:51200\n\t"
    "v_fma_f32 v242, v242, v244, v244\n\t"
    "v_fma_f32 v243, v243, v244, v244\n\t"
    "v_mfma_f32_16x16x32_bf16 v[180:183], v[228:231], a[56:59], v[180:183]\n\t"
    "ds_read_b64_tr_b16 v[212:213], %[ur10] offset:53248\n\t"
    "v_mfma_f32_16x16x32_bf16 v[180:183], v[232:235], a[72:75], v[180:183]\n\t"
    "v_rcp_f32 v240, v240\n\t"
    "v_rcp_f32 v241, v241\n\t"
    "v_mfma_f32_16x16x32_bf16 v[180:183], v[236:239], a[88:91], v[180:183]\n\t"
    "ds_read_b64_tr_b16 v[214:215], %[ur10] offset:55296\n\t"
    "v_rcp_f32 v242, v242\n\t"
    "v_mfma_f32_16x16x32_bf16 v[188:191], v[236:239], a[92:95], v[188:191]\n\t"
    "v_rcp_f32 v243, v243\n\t"
    "v_mfma_f32_16x16x32_bf16 v[188:191], v[232:235], a[76:79], v[188:191]\n\t"
    "v_mul_f32 v100, v100, v240\n\t"
    "v_mul_f32 v101, v101, v241\n\t"
    "v_mul_f32 v102, v102, v242\n\t"
    "v_mfma_f32_16x16x32_bf16 v[188:191], v[228:231], a[60:63], v[188:191]\n\t"
    "v_mul_f32 v103, v103, v243\n\t"
    "v_cvt_pk_bf16_f32 v98, v100, v101\n\t"
    "v_cvt_pk_bf16_f32 v99, v102, v103\n\t"
    "v_mfma_f32_16x16x32_bf16 v[188:191], v[224:227], a[44:47], v[188:191]\n\t"
    "v_fmaak_f32 v240, v104, v104, 0x409d30fc\n\t"
    "v_fmaak_f32 v241, v105, v105, 0x409d30fc\n\t"
    "v_mfma_f32_16x16x32_bf16 v[188:191], v[220:223], a[28:31], v[188:191]\n\t"
    "buffer_store_dwordx4 v[96:99], %[xl], %[srd], s52 offen offset:1024 sc1\n\t"
    "v_fmaak_f32 v242, v106, v106, 0x409d30fc\n\t"
    "v_fmaak_f32 v243, v107, v107, 0x409d30fc\n\t"
    "v_mfma_f32_16x16x32_bf16 v[188:191], v[216:219], a[12:15], v[188:191]\n\t"
    "v_mul_f32 v240, v240, v104\n\t"
    "v_mul_f32 v241, v241, v105\n\t"
    "s_waitcnt lgkmcnt(0)\n\t"
    "v_mfma_f32_16x16x32_bf16 v[128:131], v[192:195], a[96:99], v[128:131]\n\t"
    "ds_read_b64_tr_b16 v[216:217], %[ur11] offset:32768\n\t"
    "v_mul_f32 v242, v242, v106\n\t"
    "v_mul_f32 v243, v243, v107\n\t"
    "v_mfma_f32_16x16x32_bf16 v[128:131], v[196:199], a[112:115], v[128:131]\n\t"
    "ds_read_b64_tr_b16 v[218:219], %[ur11] offset:34816\n\t"
    "v_mfma_f32_16x16x32_bf16 v[128:131], v[200:203], a[224:227], v[128:131]\n\t"
    "v_exp_f32 v240, v240\n\t"
    "v_exp_f32 v241, v241\n\t"
    "v_mfma_f32_16x16x32_bf16 v[128:131], v[204:207], a[240:243], v[128:131]\n\t"
    "ds_read_b64_tr_b16 v[220:221], %[ur11] offset:36864\n\t"
    "v_exp_f32 v242, v242\n\t"
    "v_mfma_f32_16x16x32_bf16 v[128:131], v[208:211], v[32:35], v[128:131]\n\t"
    "ds_read_b64_tr_b16 v[222:223], %[ur11] offset:38912\n\t"
    "v_mfma_f32_16x16x32_bf16 v[128:131], v[212:215], v[48:51], v[128:131]\n\t"
    "v_exp_f32 v243, v243\n\t"
    "v_fma_f32 v240, v240, v244, v244\n\t"
    "v_fma_f32 v241, v241, v244, v244\n\t"
    "v_mfma_f32_16x16x32_bf16 v[136:139], v[212:215], v[52:55], v[136:139]\n\t"
    "ds_read_b64_tr_b16 v[224:225], %[ur11] offset:40960\n\t"
    "v_fma_f32 v242, v242, v244, v244\n\t"
    "v_fma_f32 v243, v243, v244, v244\n\t"
    "v_mfma_f32_16x16x32_bf16 v[136:139], v[208:211], v[36:39], v[136:139]\n\t"
    "ds_read_b64_tr_b16 v[226:227], %[ur11] offset:43008\n\t"
    "v_mfma_f32_16x16x32_bf16 v[136:139], v[204:207], a[244:247], v[136:139]\n\t"
    "v_rcp_f32 v240, v240\n\t"
    "v_rcp_f32 v241, v241\n\t"
    "v_mfma_f32_16x16x32_bf16 v[136:139], v[200:203], a[228:231], v[136:139]\n\t"
    "ds_read_b64_tr_b16 v[228:229], %[ur11] offset:45056\n\t"
    "v_rcp_f32 v242, v242\n\t"
    "v_mfma_f32_16x16x32_bf16 v[136:139], v[196:199], a[116:119], v[136:139]\n\t"
    "v_rcp_f32 v243, v243\n\t"
    "v_mfma_f32_16x16x32_bf16 v[136:139], v[192:195], a[100:103], v[136:139]\n\t"
    "ds_read_b64_tr_b16 v[230:231], %[ur11] offset:47104\n\t"
    "v_mul_f32 v104, v104, v240\n\t"
    "v_mul_f32 v105, v105, v241\n\t"
    "v_mfma_f32_16x16x32_bf16 v[144:147], v[192:195], a[104:107], v[144:147]\n\t"
    "ds_read_b64_tr_b16 v[232:233], %[ur11] offset:49152\n\t"
    "v_mul_f32 v106, v106, v242\n\t"
    "v_mul_f32 v107, v107, v243\n\t"
    "v_mfma_f32_16x16x32_bf16 v[144:147], v[196:199], a[120:123], v[144:147]\n\t"
    "v_cvt_pk_bf16_f32 v104, v104, v105\n\t"
    "v_cvt_pk_bf16_f32 v105, v106, v107\n\t"
    "v_mfma_f32_16x16x32_bf16 v[144:147], v[200:203], a[232:235], v[144:147]\n\t"
    "ds_read_b64_tr_b16 v[234:235], %[ur11] offset:51200\n\t"
    "v_fmaak_f32 v240, v108, v108, 0x409d30fc\n\t"
    "v_fmaak_f32 v241, v109, v109, 0x409d30fc\n\t"
    "v_mfma_f32_16x16x32_bf16 v[144:147], v[204:207], a[248:251], v[144:147]\n\t"
    "ds_read_b64_tr_b16 v[236:237], %[ur11] offset:53248\n\t"
    "v_fmaak_f32 v242, v110, v110, 0x409d30fc\n\t"
    "v_fmaak_f32 v243, v111, v111, 0x409d30fc\n\t"
    "v_mfma_f32_16x16x32_bf16 v[144:147], v[208:211], v[40:43], v[144:147]\n\t"
    "v_mul_f32 v240, v240, v108\n\t"
    "v_mul_f32 v241, v241, v109\n\t"
    "v_mfma_f32_16x16x32_bf16 v[144:147], v[212:215], v[56:59], v[144:147]\n\t"
    "ds_read_b64_tr_b16 v[238:239], %[ur11] offset:55296\n\t"
    "v_mul_f32 v242, v242, v110\n\t"
    "v_mul_f32 v243, v243, v111\n\t"
    "v_mfma_f32_16x16x32_bf16 v[152:155], v[212:215], v[60:63], v[152:155]\n\t"
    "v_exp_f32 v240, v240\n\t"
    "v_mfma_f32_16x16x32_bf16 v[152:155], v[208:211], v[44:47], v[152:155]\n\t"
    "v_exp_f32 v241, v241\n\t"
    "v_exp_f32 v242, v242\n\t"
    "v_mfma_f32_16x16x32_bf16 v[152:155], v[204:207], a[252:255], v[152:155]\n\t"
    "v_exp_f32 v243, v243\n\t"
    "v_mfma_f32_16x16x32_bf16 v[152:155], v[200:203], a[236:239], v[152:155]\n\t"
    "v_fma_f32 v240, v240, v244, v244\n\t"
    "v_fma_f32 v241, v241, v244, v244\n\t"
    "v_fma_f32 v242, v242, v244, v244\n\t"
    "v_mfma_f32_16x16x32_bf16 v[152:155], v[196:199], a[124:127], v[152:155]\n\t"
    "v_fma_f32 v243, v243, v244, v244\n\t"
    "v_rcp_f32 v240, v240\n\t"
    "v_mfma_f32_16x16x32_bf16 v[152:155], v[192:195], a[108:111], v[152:155]\n\t"
    "s_waitcnt lgkmcnt(0)\n\t"
    "v_mfma_f32_16x16x32_bf16 v[132:135], v[216:219], a[96:99], v[132:135]\n\t"
    "ds_read_b64_tr_b16 v[192:193], %[ur10] offset:33280\n\t"
    "v_rcp_f32 v241, v241\n\t"
    "v_mfma_f32_16x16x32_bf16 v[132:135], v[220:223], a[112:115], v[132:135]\n\t"
    "ds_read_b64_tr_b16 v[194:195], %[ur10] offset:35328\n\t"
    "v_rcp_f32 v242, v242\n\t"
    "v_mfma_f32_16x16x32_bf16 v[132:135], v[224:227], a[224:227], v[132:135]\n\t"
    "v_rcp_f32 v243, v243\n\t"
    "v_mul_f32 v108, v108, v240\n\t"
    "v_mfma_f32_16x16x32_bf16 v[132:135], v[228:231], a[240:243], v[132:135]\n\t"
    "ds_read_b64_tr_b16 v[196:197], %[ur10] offset:37376\n\t"
    "v_mul_f32 v109, v109, v241\n\t"
    "v_mul_f32 v110, v110, v242\n\t"
    "v_mfma_f32_16x16x32_bf16 v[132:135], v[232:235], v[32:35], v[132:135]\n\t"
    "ds_read_b64_tr_b16 v[198:199], %[ur10] offset:39424\n\t"
    "v_mul_f32 v111, v111, v243\n\t"
    "v_cvt_pk_bf16_f32 v106, v108, v109\n\t"
    "v_mfma_f32_16x16x32_bf16 v[132:135], v[236:239], v[48:51], v[132:135]\n\t"
    "v_cvt_pk_bf16_f32 v107, v110, v111\n\t"
    "v_mfma_f32_16x16x32_bf16 v[140:143], v[236:239], v[52:55], v[140:143]\n\t"
    "ds_read_b64_tr_b16 v[200:201], %[ur10] offset:41472\n\t"
    "buffer_store_dwordx4 v[104:107], %[xl], %[srd], s53 offen offset:1024 sc1\n\t"
    "v_fmaak_f32 v240, v112, v112, 0x409d30fc\n\t"
    "v_fmaak_f32 v241, v113, v113, 0x409d30fc\n\t"
    "v_mfma_f32_16x16x32_bf16 v[140:143], v[232:235], v[36:39], v[140:143]\n\t"
    "ds_read_b64_tr_b16 v[202:203], %[ur10] offset:43520\n\t"
    "v_fmaak_f32 v242, v114, v114, 0x409d30fc\n\t"
    "v_fmaak_f32 v243, v115, v115, 0x409d30fc\n\t"
    "v_mfma_f32_16x16x32_bf16 v[140:143], v[228:231], a[244:247], v[140:143]\n\t"
    "v_mul_f32 v240, v240, v112\n\t"
    "v_mul_f32 v241, v241, v113\n\t"
    "v_mfma_f32_16x16x32_bf16 v[140:143], v[224:227], a[228:231], v[140:143]\n\t"
    "ds_read_b64_tr_b16 v[204:205], %[ur10] offset:45568\n\t"
    "v_mul_f32 v242, v242, v114\n\t"
    "v_mul_f32 v243, v243, v115\n\t"
    "v_mfma_f32_16x16x32_bf16 v[140:143], v[220:223], a[116:119], v[140:143]\n\t"
    "v_exp_f32 v240, v240\n\t"
    "v_mfma_f32_16x16x32_bf16 v[140:143], v[216:219], a[100:103], v[140:143]\n\t"
    "ds_read_b64_tr_b16 v[206:207], %[ur10] offset:47616\n\t"
    "v_exp_f32 v241, v241\n\t"
    "v_mfma_f32_16x16x32_bf16 v[148:151], v[216:219], a[104:107], v[148:151]\n\t"
    "ds_read_b64_tr_b16 v[208:209], %[ur10] offset:49664\n\t"
    "v_exp_f32 v242, v242\n\t"
    "v_mfma_f32_16x16x32_bf16 v[148:151], v[220:223], a[120:123], v[148:151]\n\t"
    "v_exp_f32 v243, v243\n\t"
    "v_fma_f32 v240, v240, v244, v244\n\t"
    "v_mfma_f32_16x16x32_bf16 v[148:151], v[224:227], a[232:235], v[148:151]\n\t"
    "ds_read_b64_tr_b16 v[210:211], %[ur10] offset:51712\n\t"
    "v_fma_f32 v241, v241, v244, v244\n\t"
    "v_mfma_f32_16x16x32_bf16 v[148:151], v[228:231], a[248:251], v[148:151]\n\t"
    "ds_read_b64_tr_b16 v[212:213], %[ur10] offset:53760\n\t"
    "v_fma_f32 v242, v242, v244, v244\n\t"
    "v_fma_f32 v243, v243, v244, v244\n\t"
    "v_mfma_f32_16x16x32_bf16 v[148:151], v[232:235], v[40:43], v[148:151]\n\t"
    "v_rcp_f32 v240, v240\n\t"
    "v_mfma_f32_16x16x32_bf16 v[148:151], v[236:239], v[56:59], v[148:151]\n\t"
    "ds_read_b64_tr_b16 v[214:215], %[ur10] offset:55808\n\t"
    "v_rcp_f32 v241, v241\n\t"
    "v_mfma_f32_16x16x32_bf16 v[156:159], v[236:239], v[60:63], v[156:159]\n\t"
    "v_rcp_f32 v242, v242\n\t"
    "v_mfma_f32_16x16x32_bf16 v[156:159], v[232:235], v[44:47], v[156:159]\n\t"
    "v_rcp_f32 v243, v243\n\t"
    "v_mul_f32 v112, v112, v240\n\t"
    "v_mul_f32 v113, v113, v241\n\t"
    "v_mfma_f32_16x16x32_bf16 v[156:159], v[228:231], a[252:255], v[156:159]\n\t"
    "v_mul_f32 v114, v114, v242\n\t"
    "v_mul_f32 v115, v115, v243\n\t"
    "v_cvt_pk_bf16_f32 v112, v112, v113\n\t"
    "v_mfma_f32_16x16x32_bf16 v[156:159], v[224:227], a[236:239], v[156:159]\n\t"
    "v_cvt_pk_bf16_f32 v113, v114, v115\n\t"
    "v_fmaak_f32 v240, v116, v116, 0x409d30fc\n\t"
    "v_mfma_f32_16x16x32_bf16 v[156:159], v[220:223], a[124:127], v[156:159]\n\t"
    "v_fmaak_f32 v241, v117, v117, 0x409d30fc\n\t"
    "v_fmaak_f32 v242, v118, v118, 0x409d30fc\n\t"
    "v_fmaak_f32 v243, v119, v119, 0x409d30fc\n\t"
    "v_mfma_f32_16x16x32_bf16 v[156:159], v[216:219], a[108:111], v[156:159]\n\t"
    "v_mul_f32 v240, v240, v116\n\t"
    "v_mul_f32 v241, v241, v117\n\t"
    "s_waitcnt lgkmcnt(0)\n\t"
    "v_mfma_f32_16x16x32_bf16 v[160:163], v[192:195], a[96:99], v[160:163]\n\t"
    "ds_read_b64_tr_b16 v[216:217], %[ur11] offset:33280\n\t"
    "v_mul_f32 v242, v242, v118\n\t"
    "v_mfma_f32_16x16x32_bf16 v[160:163], v[196:199], a[112:115], v[160:163]\n\t"
    "ds_read_b64_tr_b16 v[218:219], %[ur11] offset:35328\n\t"
    "v_mul_f32 v243, v243, v119\n\t"
    "v_mfma_f32_16x16x32_bf16 v[160:163], v[200:203], a[224:227], v[160:163]\n\t"
    "v_exp_f32 v240, v240\n\t"
    "v_exp_f32 v241, v241\n\t"
    "v_mfma_f32_16x16x32_bf16 v[160:163], v[204:207], a[240:243], v[160:163]\n\t"
    "ds_read_b64_tr_b16 v[220:221], %[ur11] offset:37376\n\t"
    "v_mfma_f32_16x16x32_bf16 v[160:163], v[208:211], v[32:35], v[160:163]\n\t"
    "ds_read_b64_tr_b16 v[222:223], %[ur11] offset:39424\n\t"
    "v_exp_f32 v242, v242\n\t"
    "v_mfma_f32_16x16x32_bf16 v[160:163], v[212:215], v[48:51], v[160:163]\n\t"
    "v_exp_f32 v243, v243\n\t"
    "v_fma_f32 v240, v240, v244, v244\n\t"
    "v_fma_f32 v241, v241, v244, v244\n\t"
    "v_mfma_f32_16x16x32_bf16 v[168:171], v[212:215], v[52:55], v[168:171]\n\t"
    "ds_read_b64_tr_b16 v[224:225], %[ur11] offset:41472\n\t"
    "v_fma_f32 v242, v242, v244, v244\n\t"
    "v_mfma_f32_16x16x32_bf16 v[168:171], v[208:211], v[36:39], v[168:171]\n\t"
    "ds_read_b64_tr_b16 v[226:227], %[ur11] offset:43520\n\t"
    "v_fma_f32 v243, v243, v244, v244\n\t"
    "v_mfma_f32_16x16x32_bf16 v[168:171], v[204:207], a[244:247], v[168:171]\n\t"
    "v_rcp_f32 v240, v240\n\t"
    "v_rcp_f32 v241, v241\n\t"
    "v_mfma_f32_16x16x32_bf16 v[168:171], v[200:203], a[228:231], v[168:171]\n\t"
    "ds_read_b64_tr_b16 v[228:229], %[ur11] offset:45568\n\t"
    "v_mfma_f32_16x16x32_bf16 v[168:171], v[196:199], a[116:119], v[168:171]\n\t"
    "v_rcp_f32 v242, v242\n\t"
    "v_rcp_f32 v243, v243\n\t"
    "v_mfma_f32_16x16x32_bf16 v[168:171], v[192:195], a[100:103], v[168:171]\n\t"
    "ds_read_b64_tr_b16 v[230:231], %[ur11] offset:47616\n\t"
    "v_mul_f32 v116, v116, v240\n\t"
    "v_mul_f32 v117, v117, v241\n\t"
    "v_mfma_f32_16x16x32_bf16 v[176:179], v[192:195], a[104:107], v[176:179]\n\t"
    "ds_read_b64_tr_b16 v[232:233], %[ur11] offset:49664\n\t"
    "v_mul_f32 v118, v118, v242\n\t"
    "v_mfma_f32_16x16x32_bf16 v[176:179], v[196:199], a[120:123], v[176:179]\n\t"
    "v_mul_f32 v119, v119, v243\n\t"
    "v_cvt_pk_bf16_f32 v114, v116, v117\n\t"
    "v_cvt_pk_bf16_f32 v115, v118, v119\n\t"
    "v_mfma_f32_16x16x32_bf16 v[176:179], v[200:203], a[232:235], v[176:179]\n\t"
    "ds_read_b64_tr_b16 v[234:235], %[ur11] offset:51712\n\t"
    "v_fmaak_f32 v240, v120, v120, 0x409d30fc\n\t"
    "v_mfma_f32_16x16x32_bf16 v[176:179], v[204:207], a[248:251], v[176:179]\n\t"
    "ds_read_b64_tr_b16 v[236:237], %[ur11] offset:53760\n\t"
    "buffer_store_dwordx4 v[112:115], %[xl], %[srd], s54 offen offset:1024 sc1\n\t"
    "v_fmaak_f32 v241, v121, v121, 0x409d30fc\n\t"
    "v_fmaak_f32 v242, v122, v122, 0x409d30fc\n\t"
    "v_mfma_f32_16x16x32_bf16 v[176:179], v[208:211], v[40:43], v[176:179]\n\t"
    "v_fmaak_f32 v243, v123, v123, 0x409d30fc\n\t"
    "v_mul_f32 v240, v240, v120\n\t"
    "v_mfma_f32_16x16x32_bf16 v[176:179], v[212:215], v[56:59], v[176:179]\n\t"
    "ds_read_b64_tr_b16 v[238:239], %[ur11] offset:55808\n\t"
    "v_mul_f32 v241, v241, v121\n\t"
    "v_mul_f32 v242, v242, v122\n\t"
    "v_mfma_f32_16x16x32_bf16 v[184:187], v[212:215], v[60:63], v[184:187]\n\t"
    "v_mul_f32 v243, v243, v123\n\t"
    "v_exp_f32 v240, v240\n\t"
    "v_mfma_f32_16x16x32_bf16 v[184:187], v[208:211], v[44:47], v[184:187]\n\t"
    "v_exp_f32 v241, v241\n\t"
    "v_mfma_f32_16x16x32_bf16 v[184:187], v[204:207], a[252:255], v[184:187]\n\t"
    "v_exp_f32 v242, v242\n\t"
    "v_mfma_f32_16x16x32_bf16 v[184:187], v[200:203], a[236:239], v[184:187]\n\t"
    "v_exp_f32 v243, v243\n\t"
    "v_fma_f32 v240, v240, v244, v244\n\t"
    "v_fma_f32 v241, v241, v244, v244\n\t"
    "v_mfma_f32_16x16x32_bf16 v[184:187], v[196:199], a[124:127], v[184:187]\n\t"
    "v_fma_f32 v242, v242, v244, v244\n\t"
    "v_fma_f32 v243, v243, v244, v244\n\t"
    "v_mfma_f32_16x16x32_bf16 v[184:187], v[192:195], a[108:111], v[184:187]\n\t"
    "v_rcp_f32 v240, v240\n\t"
    "s_waitcnt lgkmcnt(0)\n\t"
    "s_waitcnt vmcnt(3)\n\t"
    "s_barrier\n\t"
    "v_mfma_f32_16x16x32_bf16 v[164:167], v[216:219], a[96:99], v[164:167]\n\t"
    "ds_read_b64_tr_b16 v[192:193], %[ur20] offset:0\n\t"
    "v_rcp_f32 v241, v241\n\t"
    "v_mfma_f32_16x16x32_bf16 v[164:167], v[220:223], a[112:115], v[164:167]\n\t"
    "ds_read_b64_tr_b16 v[194:195], %[ur20] offset:2048\n\t"
    "v_mfma_f32_16x16x32_bf16 v[164:167], v[224:227], a[224:227], v[164:167]\n\t"
    "v_rcp_f32 v242, v242\n\t"
    "v_rcp_f32 v243, v243\n\t"
    "v_mfma_f32_16x16x32_bf16 v[164:167], v[228:231], a[240:243], v[164:167]\n\t"
    "ds_read_b64_tr_b16 v[196:197], %[ur20] offset:4096\n\t"
    "v_mul_f32 v120, v120, v240\n\t"
    "v_mul_f32 v121, v121, v241\n\t"
    "v_mfma_f32_16x16x32_bf16 v[164:167], v[232:235], v[32:35], v[164:167]\n\t"
    "ds_read_b64_tr_b16 v[198:199], %[ur20] offset:6144\n\t"
    "v_mul_f32 v122, v122, v242\n\t"
    "v_mfma_f32_16x16x32_bf16 v[164:167], v[236:239], v[48:51], v[164:167]\n\t"
    "v_mul_f32 v123, v123, v243\n\t"
    "v_cvt_pk_bf16_f32 v120, v120, v121\n\t"
    "v_cvt_pk_bf16_f32 v121, v122, v123\n\t"
    "v_mfma_f32_16x16x32_bf16 v[172:175], v[236:239], v[52:55], v[172:175]\n\t"
    "ds_read_b64_tr_b16 v[200:201], %[ur20] offset:8192\n\t"
    "v_fmaak_f32 v240, v124, v124, 0x409d30fc\n\t"
    "v_fmaak_f32 v241, v125, v125, 0x409d30fc\n\t"
    "v_mfma_f32_16x16x32_bf16 v[172:175], v[232:235], v[36:39], v[172:175]\n\t"
    "ds_read_b64_tr_b16 v[202:203], %[ur20] offset:10240\n\t"
    "v_fmaak_f32 v242, v126, v126, 0x409d30fc\n\t"
    "v_mfma_f32_16x16x32_bf16 v[172:175], v[228:231], a[244:247], v[172:175]\n\t"
    "v_fmaak_f32 v243, v127, v127, 0x409d30fc\n\t"
    "v_mul_f32 v240, v240, v124\n\t"
    "v_mul_f32 v241, v241, v125\n\t"
    "v_mfma_f32_16x16x32_bf16 v[172:175], v[224:227], a[228:231], v[172:175]\n\t"
    "ds_read_b64_tr_b16 v[204:205], %[ur20] offset:12288\n\t"
    "v_mul_f32 v242, v242, v126\n\t"
    "v_mul_f32 v243, v243, v127\n\t"
    "v_mfma_f32_16x16x32_bf16 v[172:175], v[220:223], a[116:119], v[172:175]\n\t"
    "v_exp_f32 v240, v240\n\t"
    "v_mfma_f32_16x16x32_bf16 v[172:175], v[216:219], a[100:103], v[172:175]\n\t"
    "ds_read_b64_tr_b16 v[206:207], %[ur20] offset:14336\n\t"
    "v_exp_f32 v241, v241\n\t"
    "v_mfma_f32_16x16x32_bf16 v[180:183], v[216:219], a[104:107], v[180:183]\n\t"
    "ds_read_b64_tr_b16 v[208:209], %[ur20] offset:16384\n\t"
    "v_exp_f32 v242, v242\n\t"
    "v_mfma_f32_16x16x32_bf16 v[180:183], v[220:223], a[120:123], v[180:183]\n\t"
    "v_exp_f32 v243, v243\n\t"
    "v_fma_f32 v240, v240, v244, v244\n\t"
    "v_mfma_f32_16x16x32_bf16 v[180:183], v[224:227], a[232:235], v[180:183]\n\t"
    "ds_read_b64_tr_b16 v[210:211], %[ur20] offset:18432\n\t"
    "v_fma_f32 v241, v241, v244, v244\n\t"
    "v_mfma_f32_16x16x32_bf16 v[180:183], v[228:231], a[248:251], v[180:183]\n\t"
    "ds_read_b64_tr_b16 v[212:213], %[ur20] offset:20480\n\t"
    "v_fma_f32 v242, v242, v244, v244\n\t"
    "v_fma_f32 v243, v243, v244, v244\n\t"
    "v_mfma_f32_16x16x32_bf16 v[180:183], v[232:235], v[40:43], v[180:183]\n\t"
    "v_rcp_f32 v240, v240\n\t"
    "v_mfma_f32_16x16x32_bf16 v[180:183], v[236:239], v[56:59], v[180:183]\n\t"
    "ds_read_b64_tr_b16 v[214:215], %[ur20] offset:22528\n\t"
    "v_rcp_f32 v241, v241\n\t"
    "v_mfma_f32_16x16x32_bf16 v[188:191], v[236:239], v[60:63], v[188:191]\n\t"
    "v_rcp_f32 v242, v242\n\t"
    "v_mfma_f32_16x16x32_bf16 v[188:191], v[232:235], v[44:47], v[188:191]\n\t"
    "v_rcp_f32 v243, v243\n\t"
    "v_mul_f32 v124, v124, v240\n\t"
    "v_mul_f32 v125, v125, v241\n\t"
    "v_mfma_f32_16x16x32_bf16 v[188:191], v[228:231], a[252:255], v[188:191]\n\t"
    "v_mul_f32 v126, v126, v242\n\t"
    "v_mul_f32 v127, v127, v243\n\t"
    "v_mfma_f32_16x16x32_bf16 v[188:191], v[224:227], a[236:239], v[188:191]\n\t"
    "v_cvt_pk_bf16_f32 v122, v124, v125\n\t"
    "v_cvt_pk_bf16_f32 v123, v126, v127\n\t"
    "v_mfma_f32_16x16x32_bf16 v[188:191], v[220:223], a[124:127], v[188:191]\n\t"
    "buffer_store_dwordx4 v[120:123], %[xl], %[srd], s55 offen offset:1024 sc1\n\t"
    "v_mfma_f32_16x16x32_bf16 v[188:191], v[216:219], a[108:111], v[188:191]\n\t"
    "s_waitcnt lgkmcnt(0)\n\t"
    "v_mfma_f32_16x16x32_bf16 v[64:67], v[192:195], a[128:131], 0\n\t"
    "ds_read_b64_tr_b16 v[216:217], %[ur21] offset:0\n\t"
    "v_mfma_f32_16x16x32_bf16 v[64:67], v[196:199], a[144:147], v[64:67]\n\t"
    "ds_read_b64_tr_b16 v[218:219], %[ur21] offset:2048\n\t"
    "v_mfma_f32_16x16x32_bf16 v[64:67], v[200:203], a[160:163], v[64:67]\n\t"
    "v_mfma_f32_16x16x32_bf16 v[64:67], v[204:207], a[176:179], v[64:67]\n\t"
    "ds_read_b64_tr_b16 v[220:221], %[ur21] offset:4096\n\t"
    "v_fmaak_f32 v240, v128, v128, 0x409d30fc\n\t"
    "v_fmaak_f32 v241, v129, v129, 0x409d30fc\n\t"
    "v_mfma_f32_16x16x32_bf16 v[64:67], v[208:211], a[192:195], v[64:67]\n\t"
    "ds_read_b64_tr_b16 v[222:223], %[ur21] offset:6144\n\t"
    "buffer_load_dword v245, %[fa], %[srd], 0 offen sc1\n\t"
    "v_mfma_f32_16x16x32_bf16 v[64:67], v[212:215], a[208:211], v[64:67]\n\t"
    "v_fmaak_f32 v242, v130, v130, 0x409d30fc\n\t"
    "v_fmaak_f32 v243, v131, v131, 0x409d30fc\n\t"
    "v_mul_f32 v240, v240, v128\n\t"
    "v_mfma_f32_16x16x32_bf16 v[72:75], v[212:215], a[212:215], 0\n\t"
    "ds_read_b64_tr_b16 v[224:225], %[ur21] offset:8192\n\t"
    "v_mul_f32 v241, v241, v129\n\t"
    "v_mul_f32 v242, v242, v130\n\t"
    "v_mfma_f32_16x16x32_bf16 v[72:75], v[208:211], a[196:199], v[72:75]\n\t"
    "ds_read_b64_tr_b16 v[226:227], %[ur21] offset:10240\n\t"
    "v_mul_f32 v243, v243, v131\n\t"
    "v_mfma_f32_16x16x32_bf16 v[72:75], v[204:207], a[180:183], v[72:75]\n\t"
    "v_exp_f32 v240, v240\n\t"
    "v_mfma_f32_16x16x32_bf16 v[72:75], v[200:203], a[164:167], v[72:75]\n\t"
    "ds_read_b64_tr_b16 v[228:229], %[ur21] offset:12288\n\t"
    "v_exp_f32 v241, v241\n\t"
    "v_mfma_f32_16x16x32_bf16 v[72:75], v[196:199], a[148:151], v[72:75]\n\t"
    "v_exp_f32 v242, v242\n\t"
    "v_exp_f32 v243, v243\n\t"
    "v_mfma_f32_16x16x32_bf16 v[72:75], v[192:195], a[132:135], v[72:75]\n\t"
    "ds_read_b64_tr_b16 v[230:231], %[ur21] offset:14336\n\t"
    "v_fma_f32 v240, v240, v244, v244\n\t"
    "v_fma_f32 v241, v241, v244, v244\n\t"
    "v_mfma_f32_16x16x32_bf16 v[80:83], v[192:195], a[136:139], 0\n\t"
    "ds_read_b64_tr_b16 v[232:233], %[ur21] offset:16384\n\t"
    "v_fma_f32 v242, v242, v244, v244\n\t"
    "v_mfma_f32_16x16x32_bf16 v[80:83], v[196:199], a[152:155], v[80:83]\n\t"
    "v_fma_f32 v243, v243, v244, v244\n\t"
    "v_rcp_f32 v240, v240\n\t"
    "v_mfma_f32_16x16x32_bf16 v[80:83], v[200:203], a[168:171], v[80:83]\n\t"
    "ds_read_b64_tr_b16 v[234:235], %[ur21] offset:18432\n\t"
    "v_rcp_f32 v241, v241\n\t"
    "v_mfma_f32_16x16x32_bf16 v[80:83], v[204:207], a[184:187], v[80:83]\n\t"
    "ds_read_b64_tr_b16 v[236:237], %[ur21] offset:20480\n\t"
    "v_mfma_f32_16x16x32_bf16 v[80:83], v[208:211], a[200:203], v[80:83]\n\t"
    "v_rcp_f32 v242, v242\n\t"
    "v_rcp_f32 v243, v243\n\t"
    "v_mfma_f32_16x16x32_bf16 v[80:83], v[212:215], a[216:219], v[80:83]\n\t"
    "ds_read_b64_tr_b16 v[238:239], %[ur21] offset:22528\n\t"
    "v_mul_f32 v128, v128, v240\n\t"
    "v_mul_f32 v129, v129, v241\n\t"
    "v_mfma_f32_16x16x32_bf16 v[88:91], v[212:215], a[220:223], 0\n\t"
    "v_mul_f32 v130, v130, v242\n\t"
    "v_mul_f32 v131, v131, v243\n\t"
    "v_cvt_pk_bf16_f32 v128, v128, v129\n\t"
    "v_mfma_f32_16x16x32_bf16 v[88:91], v[208:211], a[204:207], v[88:91]\n\t"
    "v_cvt_pk_bf16_f32 v129, v130, v131\n\t"
    "v_fmaak_f32 v240, v132, v132, 0x409d30fc\n\t"
    "v_mfma_f32_16x16x32_bf16 v[88:91], v[204:207], a[188:191], v[88:91]\n\t"
    "v_fmaak_f32 v241, v133, v133, 0x409d30fc\n\t"
    "v_fmaak_f32 v242, v134, v134, 0x409d30fc\n\t"
    "v_fmaak_f32 v243, v135, v135, 0x409d30fc\n\t"
    "v_mfma_f32_16x16x32_bf16 v[88:91], v[200:203], a[172:175], v[88:91]\n\t"
    "v_mul_f32 v240, v240, v132\n\t"
    "v_mul_f32 v241, v241, v133\n\t"
    "v_mul_f32 v242, v242, v134\n\t"
    "v_mfma_f32_16x16x32_bf16 v[88:91], v[196:199], a[156:159], v[88:91]\n\t"
    "v_mul_f32 v243, v243, v135\n\t"
    "v_mfma_f32_16x16x32_bf16 v[88:91], v[192:195], a[140:143], v[88:91]\n\t"
    "v_exp_f32 v240, v240\n\t"
    "s_waitcnt lgkmcnt(0)\n\t"
    "v_mfma_f32_16x16x32_bf16 v[68:71], v[216:219], a[128:131], 0\n\t"
    "ds_read_b64_tr_b16 v[192:193], %[ur20] offset:512\n\t"
    "v_exp_f32 v241, v241\n\t"
    "v_mfma_f32_16x16x32_bf16 v[68:71], v[220:223], a[144:147], v[68:71]\n\t"
    "ds_read_b64_tr_b16 v[194:195], %[ur20] offset:2560\n\t"
    "v_exp_f32 v242, v242\n\t"
    "v_mfma_f32_16x16x32_bf16 v[68:71], v[224:227], a[160:163], v[68:71]\n\t"
    "v_exp_f32 v243, v243\n\t"
    "v_fma_f32 v240, v240, v244, v244\n\t"
    "v_mfma_f32_16x16x32_bf16 v[68:71], v[228:231], a[176:179], v[68:71]\n\t"
    "ds_read_b64_tr_b16 v[196:197], %[ur20] offset:4608\n\t"
    "v_fma_f32 v241, v241, v244, v244\n\t"
    "v_fma_f32 v242, v242, v244, v244\n\t"
    "v_mfma_f32_16x16x32_bf16 v[68:71], v[232:235], a[192:195], v[68:71]\n\t"
    "ds_read_b64_tr_b16 v[198:199], %[ur20] offset:6656\n\t"
    "v_fma_f32 v243, v243, v244, v244\n\t"
    "v_mfma_f32_16x16x32_bf16 v[68:71], v[236:239], a[208:211], v[68:71]\n\t"
    "v_rcp_f32 v240, v240\n\t"
    "v_rcp_f32 v241, v241\n\t"
    "v_mfma_f32_16x16x32_bf16 v[76:79], v[236:239], a[212:215], 0\n\t"
    "ds_read_b64_tr_b16 v[200:201], %[ur20] offset:8704\n\t"
    "v_mfma_f32_16x16x32_bf16 v[76:79], v[232:235], a[196:199], v[76:79]\n\t"
    "ds_read_b64_tr_b16 v[202:203], %[ur20] offset:10752\n\t"
    "v_rcp_f32 v242, v242\n\t"
    "v_mfma_f32_16x16x32_bf16 v[76:79], v[228:231], a[180:183], v[76:79]\n\t"
    "v_rcp_f32 v243, v243\n\t"
    "v_mul_f32 v132, v132, v240\n\t"
    "v_mul_f32 v133, v133, v241\n\t"
    "v_mfma_f32_16x16x32_bf16 v[76:79], v[224:227], a[164:167], v[76:79]\n\t"
    "ds_read_b64_tr_b16 v[204:205], %[ur20] offset:12800\n\t"
    "v_mul_f32 v134, v134, v242\n\t"
    "v_mfma_f32_16x16x32_bf16 v[76:79], v[220:223], a[148:151], v[76:79]\n\t"
    "v_mul_f32 v135, v135, v243\n\t"
    "v_cvt_pk_bf16_f32 v130, v132, v133\n\t"
    "v_cvt_pk_bf16_f32 v131, v134, v135\n\t"
    "v_mfma_f32_16x16x32_bf16 v[76:79], v[216:219], a[132:135], v[76:79]\n\t"
    "ds_read_b64_tr_b16 v[206:207], %[ur20] offset:14848\n\t"
    "v_fmaak_f32 v240, v136, v136, 0x409d30fc\n\t"
    "v_mfma_f32_16x16x32_bf16 v[84:87], v[216:219], a[136:139], 0\n\t"
    "ds_read_b64_tr_b16 v[208:209], %[ur20] offset:16896\n\t"
    "buffer_store_dwordx4 v[128:131], %[xl], %[srd], s52 offen offset:2048 sc1\n\t"
    "v_fmaak_f32 v241, v137, v137, 0x409d30fc\n\t"
    "v_fmaak_f32 v242, v138, v138, 0x409d30fc\n\t"
    "v_mfma_f32_16x16x32_bf16 v[84:87], v[220:223], a[152:155], v[84:87]\n\t"
    "v_fmaak_f32 v243, v139, v139, 0x409d30fc\n\t"
    "v_mul_f32 v240, v240, v136\n\t"
    "v_mfma_f32_16x16x32_bf16 v[84:87], v[224:227], a[168:171], v[84:87]\n\t"
    "ds_read_b64_tr_b16 v[210:211], %[ur20] offset:18944\n\t"
    "v_mul_f32 v241, v241, v137\n\t"
    "v_mul_f32 v242, v242, v138\n\t"
    "v_mfma_f32_16x16x32_bf16 v[84:87], v[228:231], a[184:187], v[84:87]\n\t"
    "ds_read_b64_tr_b16 v[212:213], %[ur20] offset:20992\n\t"
    "v_mul_f32 v243, v243, v139\n\t"
    "v_mfma_f32_16x16x32_bf16 v[84:87], v[232:235], a[200:203], v[84:87]\n\t"
    "v_exp_f32 v240, v240\n\t"
    "v_exp_f32 v241, v241\n\t"
    "v_mfma_f32_16x16x32_bf16 v[84:87], v[236:239], a[216:219], v[84:87]\n\t"
    "ds_read_b64_tr_b16 v[214:215], %[ur20] offset:23040\n\t"
    "v_mfma_f32_16x16x32_bf16 v[92:95], v[236:239], a[220:223], 0\n\t"
    "v_exp_f32 v242, v242\n\t"
    "v_exp_f32 v243, v243\n\t"
    "v_mfma_f32_16x16x32_bf16 v[92:95], v[232:235], a[204:207], v[92:95]\n\t"
    "v_fma_f32 v240, v240, v244, v244\n\t"
    "v_fma_f32 v241, v241, v244, v244\n\t"
    "v_fma_f32 v242, v242, v244, v244\n\t"
    "v_mfma_f32_16x16x32_bf16 v[92:95], v[228:231], a[188:191], v[92:95]\n\t"
    "v_fma_f32 v243, v243, v244, v244\n\t"
    "v_mfma_f32_16x16x32_bf16 v[92:95], v[224:227], a[172:175], v[92:95]\n\t"
    "v_rcp_f32 v240, v240\n\t"
    "v_rcp_f32 v241, v241\n\t"
    "v_mfma_f32_16x16x32_bf16 v[92:95], v[220:223], a[156:159], v[92:95]\n\t"
    "v_rcp_f32 v242, v242\n\t"
    "v_mfma_f32_16x16x32_bf16 v[92:95], v[216:219], a[140:143], v[92:95]\n\t"
    "v_rcp_f32 v243, v243\n\t"
    "v_mul_f32 v136, v136, v240\n\t"
    "s_waitcnt lgkmcnt(0)\n\t"
    "v_mfma_f32_16x16x32_bf16 v[96:99], v[192:195], a[128:131], 0\n\t"
    "ds_read_b64_tr_b16 v[216:217], %[ur21] offset:512\n\t"
    "v_mul_f32 v137, v137, v241\n\t"
    "v_mfma_f32_16x16x32_bf16 v[96:99], v[196:199], a[144:147], v[96:99]\n\t"
    "ds_read_b64_tr_b16 v[218:219], %[ur21] offset:2560\n\t"
    "v_mul_f32 v138, v138, v242\n\t"
    "v_mul_f32 v139, v139, v243\n\t"
    "v_mfma_f32_16x16x32_bf16 v[96:99], v[200:203], a[160:163], v[96:99]\n\t"
    "v_cvt_pk_bf16_f32 v136, v136, v137\n\t"
    "v_cvt_pk_bf16_f32 v137, v138, v139\n\t"
    "v_fmaak_f32 v240, v140, v140, 0x409d30fc\n\t"
    "v_mfma_f32_16x16x32_bf16 v[96:99], v[204:207], a[176:179], v[96:99]\n\t"
    "ds_read_b64_tr_b16 v[220:221], %[ur21] offset:4608\n\t"
    "v_fmaak_f32 v241, v141, v141, 0x409d30fc\n\t"
    "v_mfma_f32_16x16x32_bf16 v[96:99], v[208:211], a[192:195], v[96:99]\n\t"
    "ds_read_b64_tr_b16 v[222:223], %[ur21] offset:6656\n\t"
    "v_fmaak_f32 v242, v142, v142, 0x409d30fc\n\t"
    "v_fmaak_f32 v243, v143, v143, 0x409d30fc\n\t"
    "v_mfma_f32_16x16x32_bf16 v[96:99], v[212:215], a[208:211], v[96:99]\n\t"
    "v_mul_f32 v240, v240, v140\n\t"
    "v_mul_f32 v241, v241, v141\n\t"
    "v_mul_f32 v242, v242, v142\n\t"
    "v_mfma_f32_16x16x32_bf16 v[104:107], v[212:215], a[212:215], 0\n\t"
    "ds_read_b64_tr_b16 v[224:225], %[ur21] offset:8704\n\t"
    "v_mul_f32 v243, v243, v143\n\t"
    "v_mfma_f32_16x16x32_bf16 v[104:107], v[208:211], a[196:199], v[104:107]\n\t"
    "ds_read_b64_tr_b16 v[226:227], %[ur21] offset:10752\n\t"
    "v_exp_f32 v240, v240\n\t"
    "v_mfma_f32_16x16x32_bf16 v[104:107], v[204:207], a[180:183], v[104:107]\n\t"
    "v_exp_f32 v241, v241\n\t"
    "v_mfma_f32_16x16x32_bf16 v[104:107], v[200:203], a[164:167], v[104:107]\n\t"
    "ds_read_b64_tr_b16 v[228:229], %[ur21] offset:12800\n\t"
    "v_exp_f32 v242, v242\n\t"
    "v_mfma_f32_16x16x32_bf16 v[104:107], v[196:199], a[148:151], v[104:107]\n\t"
    "v_exp_f32 v243, v243\n\t"
    "v_fma_f32 v240, v240, v244, v244\n\t"
    "v_fma_f32 v241, v241, v244, v244\n\t"
    "v_mfma_f32_16x16x32_bf16 v[104:107], v[192:195], a[132:135], v[104:107]\n\t"
    "ds_read_b64_tr_b16 v[230:231], %[ur21] offset:14848\n\t"
    "v_fma_f32 v242, v242, v244, v244\n\t"
    "v_mfma_f32_16x16x32_bf16 v[112:115], v[192:195], a[136:139], 0\n\t"
    "ds_read_b64_tr_b16 v[232:233], %[ur21] offset:16896\n\t"
    "v_fma_f32 v243, v243, v244, v244\n\t"
    "v_mfma_f32_16x16x32_bf16 v[112:115], v[196:199], a[152:155], v[112:115]\n\t"
    "v_rcp_f32 v240, v240\n\t"
    "v_rcp_f32 v241, v241\n\t"
    "v_mfma_f32_16x16x32_bf16 v[112:115], v[200:203], a[168:171], v[112:115]\n\t"
    "ds_read_b64_tr_b16 v[234:235], %[ur21] offset:18944\n\t"
    "v_mfma_f32_16x16x32_bf16 v[112:115], v[204:207], a[184:187], v[112:115]\n\t"
    "ds_read_b64_tr_b16 v[236:237], %[ur21] offset:20992\n\t"
    "v_rcp_f32 v242, v242\n\t"
    "v_mfma_f32_16x16x32_bf16 v[112:115], v[208:211], a[200:203], v[112:115]\n\t"
    "v_rcp_f32 v243, v243\n\t"
    "v_mul_f32 v140, v140, v240\n\t"
    "v_mul_f32 v141, v141, v241\n\t"
    "v_mfma_f32_16x16x32_bf16 v[112:115], v[212:215], a[216:219], v[112:115]\n\t"
    "ds_read_b64_tr_b16 v[238:239], %[ur21] offset:23040\n\t"
    "v_mul_f32 v142, v142, v242\n\t"
    "v_mul_f32 v143, v143, v243\n\t"
    "v_mfma_f32_16x16x32_bf16 v[120:123], v[212:215], a[220:223], 0\n\t"
    "v_cvt_pk_bf16_f32 v138, v140, v141\n\t"
    "v_cvt_pk_bf16_f32 v139, v142, v143\n\t"
    "v_mfma_f32_16x16x32_bf16 v[120:123], v[208:211], a[204:207], v[120:123]\n\t"
    "v_fmaak_f32 v240, v144, v144, 0x409d30fc\n\t"
    "v_fmaak_f32 v241, v145, v145, 0x409d30fc\n\t"
    "v_mfma_f32_16x16x32_bf16 v[120:123], v[204:207], a[188:191], v[120:123]\n\t"
    "buffer_store_dwordx4 v[136:139], %[xl], %[srd], s53 offen offset:2048 sc1\n\t"
    "v_fmaak_f32 v242, v146, v146, 0x409d30fc\n\t"
    "v_fmaak_f32 v243, v147, v147, 0x409d30fc\n\t"
    "v_mul_f32 v240, v240, v144\n\t"
    "v_mfma_f32_16x16x32_bf16 v[120:123], v[200:203], a[172:175], v[120:123]\n\t"
    "v_mul_f32 v241, v241, v145\n\t"
    "v_mul_f32 v242, v242, v146\n\t"
    "v_mul_f32 v243, v243, v147\n\t"
    "v_mfma_f32_16x16x32_bf16 v[120:123], v[196:199], a[156:159], v[120:123]\n\t"
    "v_exp_f32 v240, v240\n\t"
    "v_mfma_f32_16x16x32_bf16 v[120:123], v[192:195], a[140:143], v[120:123]\n\t"
    "v_exp_f32 v241, v241\n\t"
    "s_waitcnt lgkmcnt(0)\n\t"
    "v_mfma_f32_16x16x32_bf16 v[100:103], v[216:219], a[128:131], 0\n\t"
    "ds_read_b64_tr_b16 v[192:193], %[ur00] offset:0\n\t"
    "v_exp_f32 v242, v242\n\t"
    "v_mfma_f32_16x16x32_bf16 v[100:103], v[220:223], a[144:147], v[100:103]\n\t"
    "ds_read_b64_tr_b16 v[194:195], %[ur00] offset:2048\n\t"
    "v_mfma_f32_16x16x32_bf16 v[100:103], v[224:227], a[160:163], v[100:103]\n\t"
    "v_exp_f32 v243, v243\n\t"
    "v_fma_f32 v240, v240, v244, v244\n\t"
    "v_fma_f32 v241, v241, v244, v244\n\t"
    "v_mfma_f32_16x16x32_bf16 v[100:103], v[228:231], a[176:179], v[100:103]\n\t"
    "ds_read_b64_tr_b16 v[196:197], %[ur00] offset:4096\n\t"
    "v_fma_f32 v242, v242, v244, v244\n\t"
    "v_fma_f32 v243, v243, v244, v244\n\t"
    "v_mfma_f32_16x16x32_bf16 v[100:103], v[232:235], a[192:195], v[100:103]\n\t"
    "ds_read_b64_tr_b16 v[198:199], %[ur00] offset:6144\n\t"
    "v_rcp_f32 v240, v240\n\t"
    "v_mfma_f32_16x16x32_bf16 v[100:103], v[236:239], a[208:211], v[100:103]\n\t"
    "v_rcp_f32 v241, v241\n\t"
    "v_mfma_f32_16x16x32_bf16 v[108:111], v[236:239], a[212:215], 0\n\t"
    "ds_read_b64_tr_b16 v[200:201], %[ur00] offset:8192\n\t"
    "v_rcp_f32 v242, v242\n\t"
    "v_mfma_f32_16x16x32_bf16 v[108:111], v[232:235], a[196:199], v[108:111]\n\t"
    "ds_read_b64_tr_b16 v[202:203], %[ur00] offset:10240\n\t"
    "v_rcp_f32 v243, v243\n\t"
    "v_mfma_f32_16x16x32_bf16 v[108:111], v[228:231], a[180:183], v[108:111]\n\t"
    "v_mul_f32 v144, v144, v240\n\t"
    "v_mul_f32 v145, v145, v241\n\t"
    "v_mul_f32 v146, v146, v242\n\t"
    "v_mfma_f32_16x16x32_bf16 v[108:111], v[224:227], a[164:167], v[108:111]\n\t"
    "ds_read_b64_tr_b16 v[204:205], %[ur00] offset:12288\n\t"
    "v_mul_f32 v147, v147, v243\n\t"
    "v_mfma_f32_16x16x32_bf16 v[108:111], v[220:223], a[148:151], v[108:111]\n\t"
    "v_cvt_pk_bf16_f32 v144, v144, v145\n\t"
    "v_cvt_pk_bf16_f32 v145, v146, v147\n\t"
    "v_fmaak_f32 v240, v148, v148, 0x409d30fc\n\t"
    "v_mfma_f32_16x16x32_bf16 v[108:111], v[216:219], a[132:135], v[108:111]\n\t"
    "ds_read_b64_tr_b16 v[206:207], %[ur00] offset:14336\n\t"
    "v_fmaak_f32 v241, v149, v149, 0x409d30fc\n\t"
    "v_fmaak_f32 v242, v150, v150, 0x409d30fc\n\t"
    "v_mfma_f32_16x16x32_bf16 v[116:119], v[216:219], a[136:139], 0\n\t"
    "ds_read_b64_tr_b16 v[208:209], %[ur00] offset:16384\n\t"
    "v_fmaak_f32 v243, v151, v151, 0x409d30fc\n\t"
    "v_mfma_f32_16x16x32_bf16 v[116:119], v[220:223], a[152:155], v[116:119]\n\t"
    "v_mul_f32 v240, v240, v148\n\t"
    "v_mul_f32 v241, v241, v149\n\t"
    "v_mul_f32 v242, v242, v150\n\t"
    "v_mfma_f32_16x16x32_bf16 v[116:119], v[224:227], a[168:171], v[116:119]\n\t"
    "ds_read_b64_tr_b16 v[210:211], %[ur00] offset:18432\n\t"
    "v_mul_f32 v243, v243, v151\n\t"
    "v_mfma_f32_16x16x32_bf16 v[116:119], v[228:231], a[184:187], v[116:119]\n\t"
    "ds_read_b64_tr_b16 v[212:213], %[ur00] offset:20480\n\t"
    "v_exp_f32 v240, v240\n\t"
    "v_mfma_f32_16x16x32_bf16 v[116:119], v[232:235], a[200:203], v[116:119]\n\t"
    "v_exp_f32 v241, v241\n\t"
    "v_mfma_f32_16x16x32_bf16 v[116:119], v[236:239], a[216:219], v[116:119]\n\t"
    "ds_read_b64_tr_b16 v[214:215], %[ur00] offset:22528\n\t"
    "v_exp_f32 v242, v242\n\t"
    "v_mfma_f32_16x16x32_bf16 v[124:127], v[236:239], a[220:223], 0\n\t"
    "v_exp_f32 v243, v243\n\t"
    "v_fma_f32 v240, v240, v244, v244\n\t"
    "v_fma_f32 v241, v241, v244, v244\n\t"
    "v_mfma_f32_16x16x32_bf16 v[124:127], v[232:235], a[204:207], v[124:127]\n\t"
    "v_fma_f32 v242, v242, v244, v244\n\t"
    "v_fma_f32 v243, v243, v244, v244\n\t"
    "v_mfma_f32_16x16x32_bf16 v[124:127], v[228:231], a[188:191], v[124:127]\n\t"
    "v_rcp_f32 v240, v240\n\t"
    "v_mfma_f32_16x16x32_bf16 v[124:127], v[224:227], a[172:175], v[124:127]\n\t"
    "v_rcp_f32 v241, v241\n\t"
    "v_rcp_f32 v242, v242\n\t"
    "v_mfma_f32_16x16x32_bf16 v[124:127], v[220:223], a[156:159], v[124:127]\n\t"
    "v_rcp_f32 v243, v243\n\t"
    "v_mul_f32 v148, v148, v240\n\t"
    "v_mfma_f32_16x16x32_bf16 v[124:127], v[216:219], a[140:143], v[124:127]\n\t"
    "v_mul_f32 v149, v149, v241\n\t"
    "s_waitcnt lgkmcnt(0)\n\t"
    "s_barrier\n\t"
    "s_waitcnt vmcnt(2)\n\t"
    "v_cmp_ne_u32 vcc, 1, v245\n\t"
    "s_cbranch_vccnz Los_%=\n\t"
    "Lok_%=:\n\t"
    "s_mul_i32 s40, %[rot1], 36\n\t"
    "s_mov_b32 s41, %[rot1]\n\t"
    "s_mov_b32 s58, 0\n\t"
    "s_mov_b32 s59, %[chk]\n\t"
    "s_mov_b32 s60, 0\n\t"
    "s_mul_i32 s61, %[rot2], 36\n\t"
    "s_add_u32 s62, %[rot2], %[chk]\n\t"
    "s_add_u32 s63, %[chk], 36864\n\t"
    "s_mov_b64 s[48:49], %[wa]\n\t"
    "s_mov_b32 s50, %[wlen]\n\t"
    "s_mov_b32 s51, 0x20000\n\t"
    "s_mov_b32 s42, %[chk]\n\t"
    "s_mov_b64 s[56:57], %[wb]\n\t"
    "s_mov_b32 s47, %[xmid]\n\t"
    "v_mov_b32 v247, %[fv]\n\t"
    "v_mfma_f32_16x16x32_bf16 v[64:67], v[192:195], a[0:3], v[64:67]\n\t"
    "ds_read_b64_tr_b16 v[216:217], %[ur01] offset:0\n\t"
    "v_mul_f32 v150, v150, v242\n\t"
    "v_mul_f32 v151, v151, v243\n\t"
    "v_mfma_f32_16x16x32_bf16 v[64:67], v[196:199], a[16:19], v[64:67]\n\t"
    "ds_read_b64_tr_b16 v[218:219], %[ur01] offset:2048\n\t"
    "v_cvt_pk_bf16_f32 v146, v148, v149\n\t"
    "v_cvt_pk_bf16_f32 v147, v150, v151\n\t"
    "v_mfma_f32_16x16x32_bf16 v[64:67], v[200:203], a[32:35], v[64:67]\n\t"
    "s_add_u32 m0, %[m0b], 49152\n\t"
    "s_add_u32 s46, s40, 0\n\t"
    "v_mfma_f32_16x16x32_bf16 v[64:67], v[204:207], a[48:51], v[64:67]\n\t"
    "ds_read_b64_tr_b16 v[220:221], %[ur01] offset:4096\n\t"
    "buffer_load_dwordx4 %[wv0], s[48:51], s46 offen lds\n\t"
    "buffer_store_dwordx4 v[144:147], %[xl], %[srd], s54 offen offset:2048 sc1\n\t"
    "v_mfma_f32_16x16x32_bf16 v[64:67], v[208:211], a[64:67], v[64:67]\n\t"
    "ds_read_b64_tr_b16 v[222:223], %[ur01] offset:6144\n\t"
    "v_fmaak_f32 v240, v152, v152, 0x409d30fc\n\t"
    "v_fmaak_f32 v241, v153, v153, 0x409d30fc\n\t"
    "v_mfma_f32_16x16x32_bf16 v[64:67], v[212:215], a[80:83], v[64:67]\n\t"
    "v_fmaak_f32 v242, v154, v154, 0x409d30fc\n\t"
    "v_fmaak_f32 v243, v155, v155, 0x409d30fc\n\t"
    "v_mul_f32 v240, v240, v152\n\t"
    "v_mfma_f32_16x16x32_bf16 v[72:75], v[212:215], a[84:87], v[72:75]\n\t"
    "ds_read_b64_tr_b16 v[224:225], %[ur01] offset:8192\n\t"
    "v_mul_f32 v241, v241, v153\n\t"
    "v_mfma_f32_16x16x32_bf16 v[72:75], v[208:211], a[68:71], v[72:75]\n\t"
    "ds_read_b64_tr_b16 v[226:227], %[ur01] offset:10240\n\t"
    "buffer_load_dwordx4 %[wv1], s[48:51], s46 offen offset:1024 lds\n\t"
    "v_mul_f32 v242, v242, v154\n\t"
    "v_mfma_f32_16x16x32_bf16 v[72:75], v[204:207], a[52:55], v[72:75]\n\t"
    "v_mul_f32 v243, v243, v155\n\t"
    "v_exp_f32 v240, v240\n\t"
    "v_mfma_f32_16x16x32_bf16 v[72:75], v[200:203], a[36:39], v[72:75]\n\t"
    "ds_read_b64_tr_b16 v[228:229], %[ur01] offset:12288\n\t"
    "v_exp_f32 v241, v241\n\t"
    "v_mfma_f32_16x16x32_bf16 v[72:75], v[196:199], a[20:23], v[72:75]\n\t"
    "v_exp_f32 v242, v242\n\t"
    "v_mfma_f32_16x16x32_bf16 v[72:75], v[192:195], a[4:7], v[72:75]\n\t"
    "ds_read_b64_tr_b16 v[230:231], %[ur01] offset:14336\n\t"
    "v_exp_f32 v243, v243\n\t"
    "v_mfma_f32_16x16x32_bf16 v[80:83], v[192:195], a[8:11], v[80:83]\n\t"
    "ds_read_b64_tr_b16 v[232:233], %[ur01] offset:16384\n\t"
    "buffer_load_dwordx4 %[wv2], s[48:51], s46 offen offset:2048 lds\n\t"
    "v_fma_f32 v240, v240, v244, v244\n\t"
    "v_mfma_f32_16x16x32_bf16 v[80:83], v[196:199], a[24:27], v[80:83]\n\t"
    "v_fma_f32 v241, v241, v244, v244\n\t"
    "v_fma_f32 v242, v242, v244, v244\n\t"
    "v_fma_f32 v243, v243, v244, v244\n\t"
    "v_mfma_f32_16x16x32_bf16 v[80:83], v[200:203], a[40:43], v[80:83]\n\t"
    "ds_read_b64_tr_b16 v[234:235], %[ur01] offset:18432\n\t"
    "v_mfma_f32_16x16x32_bf16 v[80:83], v[204:207], a[56:59], v[80:83]\n\t"
    "ds_read_b64_tr_b16 v[236:237], %[ur01] offset:20480\n\t"
    "s_add_u32 m0, %[m0b], 52224\n\t"
    "s_add_u32 s46, s40, 9216\n\t"
    "v_mfma_f32_16x16x32_bf16 v[80:83], v[208:211], a[72:75], v[80:83]\n\t"
    "buffer_load_dwordx4 %[wv0], s[48:51], s46 offen lds\n\t"
    "v_rcp_f32 v240, v240\n\t"
    "v_mfma_f32_16x16x32_bf16 v[80:83], v[212:215], a[88:91], v[80:83]\n\t"
    "ds_read_b64_tr_b16 v[238:239], %[ur01] offset:22528\n\t"
    "v_rcp_f32 v241, v241\n\t"
    "v_mfma_f32_16x16x32_bf16 v[88:91], v[212:215], a[92:95], v[88:91]\n\t"
    "v_rcp_f32 v242, v242\n\t"
    "v_mfma_f32_16x16x32_bf16 v[88:91], v[208:211], a[76:79], v[88:91]\n\t"
    "v_rcp_f32 v243, v243\n\t"
    "v_mul_f32 v152, v152, v240\n\t"
    "v_mul_f32 v153, v153, v241\n\t"
    "v_mfma_f32_16x16x32_bf16 v[88:91], v[204:207], a[60:63], v[88:91]\n\t"
    "v_mul_f32 v154, v154, v242\n\t"
    "v_mul_f32 v155, v155, v243\n\t"
    "v_cvt_pk_bf16_f32 v152, v152, v153\n\t"
    "v_mfma_f32_16x16x32_bf16 v[88:91], v[200:203], a[44:47], v[88:91]\n\t"
    "buffer_load_dwordx4 %[wv1], s[48:51], s46 offen offset:1024 lds\n\t"
    "v_cvt_pk_bf16_f32 v153, v154, v155\n\t"
    "v_mfma_f32_16x16x32_bf16 v[88:91], v[196:199], a[28:31], v[88:91]\n\t"
    "v_fmaak_f32 v240, v156, v156, 0x409d30fc\n\t"
    "v_fmaak_f32 v241, v157, v157, 0x409d30fc\n\t"
    "v_fmaak_f32 v242, v158, v158, 0x409d30fc\n\t"
    "v_mfma_f32_16x16x32_bf16 v[88:91], v[192:195], a[12:15], v[88:91]\n\t"
    "v_fmaak_f32 v243, v159, v159, 0x409d30fc\n\t"
    "v_mul_f32 v240, v240, v156\n\t"
    "s_waitcnt lgkmcnt(0)\n\t"
    "v_mfma_f32_16x16x32_bf16 v[68:71], v[216:219], a[0:3], v[68:71]\n\t"
    "ds_read_b64_tr_b16 v[192:193], %[ur00] offset:512\n\t"
    "v_mul_f32 v241, v241, v157\n\t"
    "v_mfma_f32_16x16x32_bf16 v[68:71], v[220:223], a[16:19], v[68:71]\n\t"
    "ds_read_b64_tr_b16 v[194:195], %[ur00] offset:2560\n\t"
    "v_mul_f32 v242, v242, v158\n\t"
    "v_mul_f32 v243, v243, v159\n\t"
    "v_mfma_f32_16x16x32_bf16 v[68:71], v[224:227], a[32:35], v[68:71]\n\t"
    "buffer_load_dwordx4 %[wv2], s[48:51], s46 offen offset:2048 lds\n\t"
    "v_exp_f32 v240, v240\n\t"
    "v_mfma_f32_16x16x32_bf16 v[68:71], v[228:231], a[48:51], v[68:71]\n\t"
    "ds_read_b64_tr_b16 v[196:197], %[ur00] offset:4608\n\t"
    "v_exp_f32 v241, v241\n\t"
    "v_mfma_f32_16x16x32_bf16 v[68:71], v[232:235], a[64:67], v[68:71]\n\t"
    "ds_read_b64_tr_b16 v[198:199], %[ur00] offset:6656\n\t"
    "v_mfma_f32_16x16x32_bf16 v[68:71], v[236:239], a[80:83], v[68:71]\n\t"
    "s_add_u32 m0, %[m0b], 55296\n\t"
    "s_add_u32 s46, s40, 18432\n\t"
    "v_exp_f32 v242, v242\n\t"
    "v_mfma_f32_16x16x32_bf16 v[76:79], v[236:239], a[84:87], v[76:79]\n\t"
    "ds_read_b64_tr_b16 v[200:201], %[ur00] offset:8704\n\t"
    "buffer_load_dwordx4 %[wv0], s[48:51], s46 offen lds\n\t"
    "v_mfma_f32_16x16x32_bf16 v[76:79], v[232:235], a[68:71], v[76:79]\n\t"
    "ds_read_b64_tr_b16 v[202:203], %[ur00] offset:10752\n\t"
    "v_exp_f32 v243, v243\n\t"
    "v_fma_f32 v240, v240, v244, v244\n\t"
    "v_mfma_f32_16x16x32_bf16 v[76:79], v[228:231], a[52:55], v[76:79]\n\t"
    "v_fma_f32 v241, v241, v244, v244\n\t"
    "v_fma_f32 v242, v242, v244, v244\n\t"
    "v_mfma_f32_16x16x32_bf16 v[76:79], v[224:227], a[36:39], v[76:79]\n\t"
    "ds_read_b64_tr_b16 v[204:205], %[ur00] offset:12800\n\t"
    "v_fma_f32 v243, v243, v244, v244\n\t"
    "v_mfma_f32_16x16x32_bf16 v[76:79], v[220:223], a[20:23], v[76:79]\n\t"
    "v_rcp_f32 v240, v240\n\t"
    "v_rcp_f32 v241, v241\n\t"
    "v_mfma_f32_16x16x32_bf16 v[76:79], v[216:219], a[4:7], v[76:79]\n\t"
    "ds_read_b64_tr_b16 v[206:207], %[ur00] offset:14848\n\t"
    "buffer_load_dwordx4 %[wv1], s[48:51], s46 offen offset:1024 lds\n\t"
    "v_mfma_f32_16x16x32_bf16 v[84:87], v[216:219], a[8:11], v[84:87]\n\t"
    "ds_read_b64_tr_b16 v[208:209], %[ur00] offset:16896\n\t"
    "v_rcp_f32 v242, v242\n\t"
    "v_mfma_f32_16x16x32_bf16 v[84:87], v[220:223], a[24:27], v[84:87]\n\t"
    "v_rcp_f32 v243, v243\n\t"
    "v_mul_f32 v156, v156, v240\n\t"
    "v_mfma_f32_16x16x32_bf16 v[84:87], v[224:227], a[40:43], v[84:87]\n\t"
    "ds_read_b64_tr_b16 v[210:211], %[ur00] offset:18944\n\t"
    "v_mul_f32 v157, v157, v241\n\t"
    "v_mul_f32 v158, v158, v242\n\t"
    "v_mfma_f32_16x16x32_bf16 v[84:87], v[228:231], a[56:59], v[84:87]\n\t"
    "ds_read_b64_tr_b16 v[212:213], %[ur00] offset:20992\n\t"
    "buffer_load_dwordx4 %[wv2], s[48:51], s46 offen offset:2048 lds\n\t"
    "v_mfma_f32_16x16x32_bf16 v[84:87], v[232:235], a[72:75], v[84:87]\n\t"
    "v_mul_f32 v159, v159, v243\n\t"
    "v_cvt_pk_bf16_f32 v154, v156, v157\n\t"
    "v_cvt_pk_bf16_f32 v155, v158, v159\n\t"
    "v_mfma_f32_16x16x32_bf16 v[84:87], v[236:239], a[88:91], v[84:87]\n\t"
    "ds_read_b64_tr_b16 v[214:215], %[ur00] offset:23040\n\t"
    "v_fmaak_f32 v240, v160, v160, 0x409d30fc\n\t"
    "v_mfma_f32_16x16x32_bf16 v[92:95], v[236:239], a[92:95], v[92:95]\n\t"
    "buffer_store_dwordx4 v[152:155], %[xl], %[srd], s55 offen offset:2048 sc1\n\t"
    "v_fmaak_f32 v241, v161, v161, 0x409d30fc\n\t"
    "v_fmaak_f32 v242, v162, v162, 0x409d30fc\n\t"
    "v_fmaak_f32 v243, v163, v163, 0x409d30fc\n\t"
    "v_mfma_f32_16x16x32_bf16 v[92:95], v[232:235], a[76:79], v[92:95]\n\t"
    "s_add_u32 m0, %[m0b], 58368\n\t"
    "s_add_u32 s46, s40, 27648\n\t"
    "v_mfma_f32_16x16x32_bf16 v[92:95], v[228:231], a[60:63], v[92:95]\n\t"
    "buffer_load_dwordx4 %[wv0], s[48:51], s46 offen lds\n\t"
    "v_mul_f32 v240, v240, v160\n\t"
    "v_mul_f32 v241, v241, v161\n\t"
    "v_mfma_f32_16x16x32_bf16 v[92:95], v[224:227], a[44:47], v[92:95]\n\t"
    "v_mul_f32 v242, v242, v162\n\t"
    "v_mul_f32 v243, v243, v163\n\t"
    "v_mfma_f32_16x16x32_bf16 v[92:95], v[220:223], a[28:31], v[92:95]\n\t"
    "v_exp_f32 v240, v240\n\t"
    "v_mfma_f32_16x16x32_bf16 v[92:95], v[216:219], a[12:15], v[92:95]\n\t"
    "v_exp_f32 v241, v241\n\t"
    "s_waitcnt lgkmcnt(0)\n\t"
    "v_mfma_f32_16x16x32_bf16 v[96:99], v[192:195], a[0:3], v[96:99]\n\t"
    "ds_read_b64_tr_b16 v[216:217], %[ur01] offset:512\n\t"
    "v_exp_f32 v242, v242\n\t"
    "v_mfma_f32_16x16x32_bf16 v[96:99], v[196:199], a[16:19], v[96:99]\n\t"
    "ds_read_b64_tr_b16 v[218:219], %[ur01] offset:2560\n\t"
    "buffer_load_dwordx4 %[wv1], s[48:51], s46 offen offset:1024 lds\n\t"
    "v_exp_f32 v243, v243\n\t"
    "v_mfma_f32_16x16x32_bf16 v[96:99], v[200:203], a[32:35], v[96:99]\n\t"
    "v_fma_f32 v240, v240, v244, v244\n\t"
    "v_fma_f32 v241, v241, v244, v244\n\t"
    "v_mfma_f32_16x16x32_bf16 v[96:99], v[204:207], a[48:51], v[96:99]\n\t"
    "ds_read_b64_tr_b16 v[220:221], %[ur01] offset:4608\n\t"
    "v_fma_f32 v242, v242, v244, v244\n\t"
    "v_fma_f32 v243, v243, v244, v244\n\t"
    "v_mfma_f32_16x16x32_bf16 v[96:99], v[208:211], a[64:67], v[96:99]\n\t"
    "ds_read_b64_tr_b16 v[222:223], %[ur01] offset:6656\n\t"
    "v_rcp_f32 v240, v240\n\t"
    "v_mfma_f32_16x16x32_bf16 v[96:99], v[212:215], a[80:83], v[96:99]\n\t"
    "buffer_load_dwordx4 %[wv2], s[48:51], s46 offen offset:2048 lds\n\t"
    "v_mfma_f32_16x16x32_bf16 v[104:107], v[212:215], a[84:87], v[104:107]\n\t"
    "ds_read_b64_tr_b16 v[224:225], %[ur01] offset:8704\n\t"
    "v_rcp_f32 v241, v241\n\t"
    "v_mfma_f32_16x16x32_bf16 v[104:107], v[208:211], a[68:71], v[104:107]\n\t"
    "ds_read_b64_tr_b16 v[226:227], %[ur01] offset:10752\n\t"
    "v_rcp_f32 v242, v242\n\t"
    "v_mfma_f32_16x16x32_bf16 v[104:107], v[204:207], a[52:55], v[104:107]\n\t"
    "v_rcp_f32 v243, v243\n\t"
    "v_mul_f32 v160, v160, v240\n\t"
    "v_mul_f32 v161, v161, v241\n\t"
    "v_mfma_f32_16x16x32_bf16 v[104:107], v[200:203], a[36:39], v[104:107]\n\t"
    "ds_read_b64_tr_b16 v[228:229], %[ur01] offset:12800\n\t"
    "v_mul_f32 v162, v162, v242\n\t"
    "v_mfma_f32_16x16x32_bf16 v[104:107], v[196:199], a[20:23], v[104:107]\n\t"
    "buffer_load_dwordx4 a[128:131], %[xl], %[srdx0], s41 offen sc1\n\t"
    "v_mul_f32 v163, v163, v243\n\t"
    "v_cvt_pk_bf16_f32 v160, v160, v161\n\t"
    "v_mfma_f32_16x16x32_bf16 v[104:107], v[192:195], a[4:7], v[104:107]\n\t"
    "ds_read_b64_tr_b16 v[230:231], %[ur01] offset:14848\n\t"
    "v_cvt_pk_bf16_f32 v161, v162, v163\n\t"
    "v_fmaak_f32 v240, v164, v164, 0x409d30fc\n\t"
    "v_mfma_f32_16x16x32_bf16 v[112:115], v[192:195], a[8:11], v[112:115]\n\t"
    "ds_read_b64_tr_b16 v[232:233], %[ur01] offset:16896\n\t"
    "v_fmaak_f32 v241, v165, v165, 0x409d30fc\n\t"
    "v_fmaak_f32 v242, v166, v166, 0x409d30fc\n\t"
    "v_mfma_f32_16x16x32_bf16 v[112:115], v[196:199], a[24:27], v[112:115]\n\t"
    "v_fmaak_f32 v243, v167, v167, 0x409d30fc\n\t"
    "v_mul_f32 v240, v240, v164\n\t"
    "v_mfma_f32_16x16x32_bf16 v[112:115], v[200:203], a[40:43], v[112:115]\n\t"
    "ds_read_b64_tr_b16 v[234:235], %[ur01] offset:18944\n\t"
    "buffer_load_dwordx4 a[132:135], %[xl], %[srdx0], s41 offen offset:1024 sc1\n\t"
    "v_mul_f32 v241, v241, v165\n\t"
    "v_mfma_f32_16x16x32_bf16 v[112:115], v[204:207], a[56:59], v[112:115]\n\t"
    "ds_read_b64_tr_b16 v[236:237], %[ur01] offset:20992\n\t"
    "v_mul_f32 v242, v242, v166\n\t"
    "v_mul_f32 v243, v243, v167\n\t"
    "v_mfma_f32_16x16x32_bf16 v[112:115], v[208:211], a[72:75], v[112:115]\n\t"
    "v_exp_f32 v240, v240\n\t"
    "v_mfma_f32_16x16x32_bf16 v[112:115], v[212:215], a[88:91], v[112:115]\n\t"
    "ds_read_b64_tr_b16 v[238:239], %[ur01] offset:23040\n\t"
    "v_exp_f32 v241, v241\n\t"
    "v_mfma_f32_16x16x32_bf16 v[120:123], v[212:215], a[92:95], v[120:123]\n\t"
    "v_exp_f32 v242, v242\n\t"
    "v_mfma_f32_16x16x32_bf16 v[120:123], v[208:211], a[76:79], v[120:123]\n\t"
    "buffer_load_dwordx4 a[136:139], %[xl], %[srdx0], s41 offen offset:2048 sc1\n\t"
    "v_exp_f32 v243, v243\n\t"
    "v_fma_f32 v240, v240, v244, v244\n\t"
    "v_mfma_f32_16x16x32_bf16 v[120:123], v[204:207], a[60:63], v[120:123]\n\t"
    "v_fma_f32 v241, v241, v244, v244\n\t"
    "v_fma_f32 v242, v242, v244, v244\n\t"
    "v_mfma_f32_16x16x32_bf16 v[120:123], v[200:203], a[44:47], v[120:123]\n\t"
    "v_fma_f32 v243, v243, v244, v244\n\t"
    "v_rcp_f32 v240, v240\n\t"
    "v_mfma_f32_16x16x32_bf16 v[120:123], v[196:199], a[28:31], v[120:123]\n\t"
    "v_rcp_f32 v241, v241\n\t"
    "v_mfma_f32_16x16x32_bf16 v[120:123], v[192:195], a[12:15], v[120:123]\n\t"
    "v_rcp_f32 v242, v242\n\t"
    "s_waitcnt lgkmcnt(0)\n\t"
    "v_mfma_f32_16x16x32_bf16 v[100:103], v[216:219], a[0:3], v[100:103]\n\t"
    "ds_read_b64_tr_b16 v[192:193], %[ur00] offset:24576\n\t"
    "buffer_load_dwordx4 a[140:143], %[xl], %[srdx0], s41 offen offset:3072 sc1\n\t"
    "v_mfma_f32_16x16x32_bf16 v[100:103], v[220:223], a[16:19], v[100:103]\n\t"
    "ds_read_b64_tr_b16 v[194:195], %[ur00] offset:26624\n\t"
    "v_rcp_f32 v243, v243\n\t"
    "v_mul_f32 v164, v164, v240\n\t"
    "v_mfma_f32_16x16x32_bf16 v[100:103], v[224:227], a[32:35], v[100:103]\n\t"
    "v_mul_f32 v165, v165, v241\n\t"
    "v_mul_f32 v166, v166, v242\n\t"
    "v_mul_f32 v167, v167, v243\n\t"
    "v_mfma_f32_16x16x32_bf16 v[100:103], v[228:231], a[48:51], v[100:103]\n\t"
    "ds_read_b64_tr_b16 v[196:197], %[ur00] offset:28672\n\t"
    "v_cvt_pk_bf16_f32 v162, v164, v165\n\t"
    "v_mfma_f32_16x16x32_bf16 v[100:103], v[232:235], a[64:67], v[100:103]\n\t"
    "ds_read_b64_tr_b16 v[198:199], %[ur00] offset:30720\n\t"
    "buffer_load_dwordx4 a[144:147], %[xl], %[srdx1], s41 offen sc1\n\t"
    "v_cvt_pk_bf16_f32 v163, v166, v167\n\t"
    "v_mfma_f32_16x16x32_bf16 v[100:103], v[236:239], a[80:83], v[100:103]\n\t"
    "v_fmaak_f32 v240, v168, v168, 0x409d30fc\n\t"
    "v_fmaak_f32 v241, v169, v169, 0x409d30fc\n\t"
    "v_mfma_f32_16x16x32_bf16 v[108:111], v[236:239], a[84:87], v[108:111]\n\t"
    "ds_read_b64_tr_b16 v[200:201], %[ur00] offset:32768\n\t"
    "buffer_store_dwordx4 v[160:163], %[xl], %[srd], s52 offen offset:3072 sc1\n\t"
    "v_fmaak_f32 v242, v170, v170, 0x409d30fc\n\t"
    "v_fmaak_f32 v243, v171, v171, 0x409d30fc\n\t"
    "v_mfma_f32_16x16x32_bf16 v[108:111], v[232:235], a[68:71], v[108:111]\n\t"
    "ds_read_b64_tr_b16 v[202:203], %[ur00] offset:34816\n\t"
    "v_mul_f32 v240, v240, v168\n\t"
    "v_mfma_f32_16x16x32_bf16 v[108:111], v[228:231], a[52:55], v[108:111]\n\t"
    "v_mul_f32 v241, v241, v169\n\t"
    "v_mul_f32 v242, v242, v170\n\t"
    "v_mul_f32 v243, v243, v171\n\t"
    "v_mfma_f32_16x16x32_bf16 v[108:111], v[224:227], a[36:39], v[108:111]\n\t"
    "ds_read_b64_tr_b16 v[204:205], %[ur00] offset:36864\n\t"
    "buffer_load_dwordx4 a[148:151], %[xl], %[srdx1], s41 offen offset:1024 sc1\n\t"
    "v_mfma_f32_16x16x32_bf16 v[108:111], v[220:223], a[20:23], v[108:111]\n\t"
    "v_exp_f32 v240, v240\n\t"
    "v_mfma_f32_16x16x32_bf16 v[108:111], v[216:219], a[4:7], v[108:111]\n\t"
    "ds_read_b64_tr_b16 v[206:207], %[ur00] offset:38912\n\t"
    "v_exp_f32 v241, v241\n\t"
    "v_mfma_f32_16x16x32_bf16 v[116:119], v[216:219], a[8:11], v[116:119]\n\t"
    "ds_read_b64_tr_b16 v[208:209], %[ur00] offset:40960\n\t"
    "v_exp_f32 v242, v242\n\t"
    "v_mfma_f32_16x16x32_bf16 v[116:119], v[220:223], a[24:27], v[116:119]\n\t"
    "buffer_load_dwordx4 a[152:155], %[xl], %[srdx1], s41 offen offset:2048 sc1\n\t"
    "v_exp_f32 v243, v243\n\t"
    "v_fma_f32 v240, v240, v244, v244\n\t"
    "v_mfma_f32_16x16x32_bf16 v[116:119], v[224:227], a[40:43], v[116:119]\n\t"
    "ds_read_b64_tr_b16 v[210:211], %[ur00] offset:43008\n\t"
    "v_fma_f32 v241, v241, v244, v244\n\t"
    "v_mfma_f32_16x16x32_bf16 v[116:119], v[228:231], a[56:59], v[116:119]\n\t"
    "ds_read_b64_tr_b16 v[212:213], %[ur00] offset:45056\n\t"
    "v_fma_f32 v242, v242, v244, v244\n\t"
    "v_fma_f32 v243, v243, v244, v244\n\t"
    "v_mfma_f32_16x16x32_bf16 v[116:119], v[232:235], a[72:75], v[116:119]\n\t"
    "v_rcp_f32 v240, v240\n\t"
    "v_mfma_f32_16x16x32_bf16 v[116:119], v[236:239], a[88:91], v[116:119]\n\t"
    "ds_read_b64_tr_b16 v[214:215], %[ur00] offset:47104\n\t"
    "v_rcp_f32 v241, v241\n\t"
    "v_mfma_f32_16x16x32_bf16 v[124:127], v[236:239], a[92:95], v[124:127]\n\t"
    "buffer_load_dwordx4 a[156:159], %[xl], %[srdx1], s41 offen offset:3072 sc1\n\t"
    "v_rcp_f32 v242, v242\n\t"
    "v_mfma_f32_16x16x32_bf16 v[124:127], v[232:235], a[76:79], v[124:127]\n\t"
    "s_add_u32 s40, s40, 147456\n\t"
    "s_add_u32 s41, s41, 4096\n\t"
    "s_add_u32 s58, s58, 4096\n\t"
    "s_cmp_eq_u32 s41, s59\n\t"
    "s_cselect_b32 s41, s60, s41\n\t"
    "s_cselect_b32 s40, 0, s40\n\t"
    "v_mfma_f32_16x16x32_bf16 v[124:127], v[228:231], a[60:63], v[124:127]\n\t"
    "v_mfma_f32_16x16x32_bf16 v[124:127], v[224:227], a[44:47], v[124:127]\n\t"
    "v_rcp_f32 v243, v243\n\t"
    "v_mfma_f32_16x16x32_bf16 v[124:127], v[220:223], a[28:31], v[124:127]\n\t"
    "s_add_u32 m0, %[m0b], 98304\n\t"
    "s_add_u32 s46, s40, 0\n\t"
    "v_mul_f32 v168, v168, v240\n\t"
    "v_mfma_f32_16x16x32_bf16 v[124:127], v[216:219], a[12:15], v[124:127]\n\t"
    "buffer_load_dwordx4 %[wv0], s[48:51], s46 offen lds\n\t"
    "v_mul_f32 v169, v169, v241\n\t"
    "s_waitcnt lgkmcnt(0)\n\t"
    "v_mfma_f32_16x16x32_bf16 v[64:67], v[192:195], a[96:99], v[64:67]\n\t"
    "ds_read_b64_tr_b16 v[216:217], %[ur01] offset:24576\n\t"
    "v_mul_f32 v170, v170, v242\n\t"
    "v_mul_f32 v171, v171, v243\n\t"
    "v_mfma_f32_16x16x32_bf16 v[64:67], v[196:199], a[112:115], v[64:67]\n\t"
    "ds_read_b64_tr_b16 v[218:219], %[ur01] offset:26624\n\t"
    "v_cvt_pk_bf16_f32 v168, v168, v169\n\t"
    "v_mfma_f32_16x16x32_bf16 v[64:67], v[200:203], a[224:227], v[64:67]\n\t"
    "buffer_load_dwordx2 a[64:65], %[xo], %[srdi], 0 offen\n\t"
    "v_cvt_pk_bf16_f32 v169, v170, v171\n\t"
    "v_fmaak_f32 v240, v172, v172, 0x409d30fc\n\t"
    "v_mfma_f32_16x16x32_bf16 v[64:67], v[204:207], a[240:243], v[64:67]\n\t"
    "ds_read_b64_tr_b16 v[220:221], %[ur01] offset:28672\n\t"
    "buffer_load_dwordx4 %[wv1], s[48:51], s46 offen offset:1024 lds\n\t"
    "v_fmaak_f32 v241, v173, v173, 0x409d30fc\n\t"
    "v_mfma_f32_16x16x32_bf16 v[64:67], v[208:211], v[32:35], v[64:67]\n\t"
    "ds_read_b64_tr_b16 v[222:223], %[ur01] offset:30720\n\t"
    "v_fmaak_f32 v242, v174, v174, 0x409d30fc\n\t"
    "v_fmaak_f32 v243, v175, v175, 0x409d30fc\n\t"
    "v_mfma_f32_16x16x32_bf16 v[64:67], v[212:215], v[48:51], v[64:67]\n\t"
    "v_mul_f32 v240, v240, v172\n\t"
    "v_mul_f32 v241, v241, v173\n\t"
    "v_mfma_f32_16x16x32_bf16 v[72:75], v[212:215], v[52:55], v[72:75]\n\t"
    "ds_read_b64_tr_b16 v[224:225], %[ur01] offset:32768\n\t"
    "v_mul_f32 v242, v242, v174\n\t"
    "v_mul_f32 v243, v243, v175\n\t"
    "v_mfma_f32_16x16x32_bf16 v[72:75], v[208:211], v[36:39], v[72:75]\n\t"
    "ds_read_b64_tr_b16 v[226:227], %[ur01] offset:34816\n\t"
    "v_exp_f32 v240, v240\n\t"
    "v_mfma_f32_16x16x32_bf16 v[72:75], v[204:207], a[244:247], v[72:75]\n\t"
    "buffer_load_dwordx4 %[wv2], s[48:51], s46 offen offset:2048 lds\n\t"
    "v_mfma_f32_16x16x32_bf16 v[72:75], v[200:203], a[228:231], v[72:75]\n\t"
    "ds_read_b64_tr_b16 v[228:229], %[ur01] offset:36864\n\t"
    "v_exp_f32 v241, v241\n\t"
    "v_mfma_f32_16x16x32_bf16 v[72:75], v[196:199], a[116:119], v[72:75]\n\t"
    "v_exp_f32 v242, v242\n\t"
    "v_exp_f32 v243, v243\n\t"
    "v_mfma_f32_16x16x32_bf16 v[72:75], v[192:195], a[100:103], v[72:75]\n\t"
    "ds_read_b64_tr_b16 v[230:231], %[ur01] offset:38912\n\t"
    "buffer_load_dwordx2 a[66:67], %[xo], %[srdi], 0 offen offset:32\n\t"
    "s_add_u32 m0, %[m0b], 101376\n\t"
    "s_add_u32 s46, s40, 9216\n\t"
    "v_mfma_f32_16x16x32_bf16 v[80:83], v[192:195], a[104:107], v[80:83]\n\t"
    "ds_read_b64_tr_b16 v[232:233], %[ur01] offset:40960\n\t"
    "buffer_load_dwordx4 %[wv0], s[48:51], s46 offen lds\n\t"
    "v_mfma_f32_16x16x32_bf16 v[80:83], v[196:199], a[120:123], v[80:83]\n\t"
    "v_fma_f32 v240, v240, v244, v244\n\t"
    "v_fma_f32 v241, v241, v244, v244\n\t"
    "v_mfma_f32_16x16x32_bf16 v[80:83], v[200:203], a[232:235], v[80:83]\n\t"
    "ds_read_b64_tr_b16 v[234:235], %[ur01] offset:43008\n\t"
    "v_fma_f32 v242, v242, v244, v244\n\t"
    "v_fma_f32 v243, v243, v244, v244\n\t"
    "v_mfma_f32_16x16x32_bf16 v[80:83], v[204:207], a[248:251], v[80:83]\n\t"
    "ds_read_b64_tr_b16 v[236:237], %[ur01] offset:45056\n\t"
    "v_mfma_f32_16x16x32_bf16 v[80:83], v[208:211], v[40:43], v[80:83]\n\t"
    "v_rcp_f32 v240, v240\n\t"
    "v_rcp_f32 v241, v241\n\t"
    "v_mfma_f32_16x16x32_bf16 v[80:83], v[212:215], v[56:59], v[80:83]\n\t"
    "ds_read_b64_tr_b16 v[238:239], %[ur01] offset:47104\n\t"
    "buffer_load_dwordx4 %[wv1], s[48:51], s46 offen offset:1024 lds\n\t"
    "v_mfma_f32_16x16x32_bf16 v[88:91], v[212:215], v[60:63], v[88:91]\n\t"
    "v_rcp_f32 v242, v242\n\t"
    "v_rcp_f32 v243, v243\n\t"
    "v_mfma_f32_16x16x32_bf16 v[88:91], v[208:211], v[44:47], v[88:91]\n\t"
    "v_mul_f32 v172, v172, v240\n\t"
    "v_mul_f32 v173, v173, v241\n\t"
    "v_mfma_f32_16x16x32_bf16 v[88:91], v[204:207], a[252:255], v[88:91]\n\t"
    "buffer_load_dwordx2 a[68:69], %[xo], %[srdi], 0 offen offset:64\n\t"
    "v_mul_f32 v174, v174, v242\n\t"
    "v_mul_f32 v175, v175, v243\n\t"
    "v_mfma_f32_16x16x32_bf16 v[88:91], v[200:203], a[236:239], v[88:91]\n\t"
    "v_cvt_pk_bf16_f32 v170, v172, v173\n\t"
    "v_cvt_pk_bf16_f32 v171, v174, v175\n\t"
    "v_mfma_f32_16x16x32_bf16 v[88:91], v[196:199], a[124:127], v[88:91]\n\t"
    "buffer_load_dwordx4 %[wv2], s[48:51], s46 offen offset:2048 lds\n\t"
    "buffer_store_dwordx4 v[168:171], %[xl], %[srd], s53 offen offset:3072 sc1\n\t"
    "v_fmaak_f32 v240, v176, v176, 0x409d30fc\n\t"
    "v_fmaak_f32 v241, v177, v177, 0x409d30fc\n\t"
    "v_mfma_f32_16x16x32_bf16 v[88:91], v[192:195], a[108:111], v[88:91]\n\t"
    "v_fmaak_f32 v242, v178, v178, 0x409d30fc\n\t"
    "s_waitcnt lgkmcnt(0)\n\t"
    "v_mfma_f32_16x16x32_bf16 v[68:71], v[216:219], a[96:99], v[68:71]\n\t"
    "ds_read_b64_tr_b16 v[192:193], %[ur00] offset:25088\n\t"
    "v_fmaak_f32 v243, v179, v179, 0x409d30fc\n\t"
    "v_mul_f32 v240, v240, v176\n\t"
    "v_mfma_f32_16x16x32_bf16 v[68:71], v[220:223], a[112:115], v[68:71]\n\t"
    "ds_read_b64_tr_b16 v[194:195], %[ur00] offset:27136\n\t"
    "s_add_u32 m0, %[m0b], 104448\n\t"
    "s_add_u32 s46, s40, 18432\n\t"
    "v_mfma_f32_16x16x32_bf16 v[68:71], v[224:227], a[224:227], v[68:71]\n\t"
    "buffer_load_dwordx4 %[wv0], s[48:51], s46 offen lds\n\t"
    "v_mul_f32 v241, v241, v177\n\t"
    "v_mfma_f32_16x16x32_bf16 v[68:71], v[228:231], a[240:243], v[68:71]\n\t"
    "ds_read_b64_tr_b16 v[196:197], %[ur00] offset:29184\n\t"
    "v_mul_f32 v242, v242, v178\n\t"
    "v_mul_f32 v243, v243, v179\n\t"
    "v_mfma_f32_16x16x32_bf16 v[68:71], v[232:235], v[32:35], v[68:71]\n\t"
    "ds_read_b64_tr_b16 v[198:199], %[ur00] offset:31232\n\t"
    "v_exp_f32 v240, v240\n\t"
    "v_mfma_f32_16x16x32_bf16 v[68:71], v[236:239], v[48:51], v[68:71]\n\t"
    "buffer_load_dwordx2 a[70:71], %[xo], %[srdi], 0 offen offset:96\n\t"
    "v_exp_f32 v241, v241\n\t"
    "v_mfma_f32_16x16x32_bf16 v[76:79], v[236:239], v[52:55], v[76:79]\n\t"
    "ds_read_b64_tr_b16 v[200:201], %[ur00] offset:33280\n\t"
    "v_mfma_f32_16x16x32_bf16 v[76:79], v[232:235], v[36:39], v[76:79]\n\t"
    "ds_read_b64_tr_b16 v[202:203], %[ur00] offset:35328\n\t"
    "buffer_load_dwordx4 %[wv1], s[48:51], s46 offen offset:1024 lds\n\t"
    "v_exp_f32 v242, v242\n\t"
    "v_mfma_f32_16x16x32_bf16 v[76:79], v[228:231], a[244:247], v[76:79]\n\t"
    "v_exp_f32 v243, v243\n\t"
    "v_fma_f32 v240, v240, v244, v244\n\t"
    "v_mfma_f32_16x16x32_bf16 v[76:79], v[224:227], a[228:231], v[76:79]\n\t"
    "ds_read_b64_tr_b16 v[204:205], %[ur00] offset:37376\n\t"
    "v_fma_f32 v241, v241, v244, v244\n\t"
    "v_fma_f32 v242, v242, v244, v244\n\t"
    "v_mfma_f32_16x16x32_bf16 v[76:79], v[220:223], a[116:119], v[76:79]\n\t"
    "v_fma_f32 v243, v243, v244, v244\n\t"
    "v_mfma_f32_16x16x32_bf16 v[76:79], v[216:219], a[100:103], v[76:79]\n\t"
    "ds_read_b64_tr_b16 v[206:207], %[ur00] offset:39424\n\t"
    "buffer_load_dwordx4 %[wv2], s[48:51], s46 offen offset:2048 lds\n\t"
    "v_rcp_f32 v240, v240\n\t"
    "v_mfma_f32_16x16x32_bf16 v[84:87], v[216:219], a[104:107], v[84:87]\n\t"
    "ds_read_b64_tr_b16 v[208:209], %[ur00] offset:41472\n\t"
    "v_rcp_f32 v241, v241\n\t"
    "v_mfma_f32_16x16x32_bf16 v[84:87], v[220:223], a[120:123], v[84:87]\n\t"
    "v_rcp_f32 v242, v242\n\t"
    "v_mfma_f32_16x16x32_bf16 v[84:87], v[224:227], a[232:235], v[84:87]\n\t"
    "ds_read_b64_tr_b16 v[210:211], %[ur00] offset:43520\n\t"
    "buffer_load_dwordx2 a[72:73], %[xo], %[srdi], 0 offen offset:128\n\t"
    "v_mfma_f32_16x16x32_bf16 v[84:87], v[228:231], a[248:251], v[84:87]\n\t"
    "ds_read_b64_tr_b16 v[212:213], %[ur00] offset:45568\n\t"
    "s_add_u32 m0, %[m0b], 107520\n\t"
    "s_add_u32 s46, s40, 27648\n\t"
    "v_mfma_f32_16x16x32_bf16 v[84:87], v[232:235], v[40:43], v[84:87]\n\t"
    "buffer_load_dwordx4 %[wv0], s[48:51], s46 offen lds\n\t"
    "v_rcp_f32 v243, v243\n\t"
    "v_mul_f32 v176, v176, v240\n\t"
    "v_mfma_f32_16x16x32_bf16 v[84:87], v[236:239], v[56:59], v[84:87]\n\t"
    "ds_read_b64_tr_b16 v[214:215], %[ur00] offset:47616\n\t"
    "v_mul_f32 v177, v177, v241\n\t"
    "v_mfma_f32_16x16x32_bf16 v[92:95], v[236:239], v[60:63], v[92:95]\n\t"
    "v_mul_f32 v178, v178, v242\n\t"
    "v_mul_f32 v179, v179, v243\n\t"
    "v_cvt_pk_bf16_f32 v176, v176, v177\n\t"
    "v_mfma_f32_16x16x32_bf16 v[92:95], v[232:235], v[44:47], v[92:95]\n\t"
    "v_cvt_pk_bf16_f32 v177, v178, v179\n\t"
    "v_fmaak_f32 v240, v180, v180, 0x409d30fc\n\t"
    "v_fmaak_f32 v241, v181, v181, 0x409d30fc\n\t"
    "v_mfma_f32_16x16x32_bf16 v[92:95], v[228:231], a[252:255], v[92:95]\n\t"
    "v_fmaak_f32 v242, v182, v182, 0x409d30fc\n\t"
    "v_fmaak_f32 v243, v183, v183, 0x409d30fc\n\t"
    "v_mul_f32 v240, v240, v180\n\t"
    "v_mfma_f32_16x16x32_bf16 v[92:95], v[224:227], a[236:239], v[92:95]\n\t"
    "buffer_load_dwordx4 %[wv1], s[48:51], s46 offen offset:1024 lds\n\t"
    "v_mul_f32 v241, v241, v181\n\t"
    "v_mfma_f32_16x16x32_bf16 v[92:95], v[220:223], a[124:127], v[92:95]\n\t"
    "v_mul_f32 v242, v242, v182\n\t"
    "v_mul_f32 v243, v243, v183\n\t"
    "v_mfma_f32_16x16x32_bf16 v[92:95], v[216:219], a[108:111], v[92:95]\n\t"
    "buffer_load_dwordx2 a[74:75], %[xo], %[srdi], 0 offen offset:160\n\t"
    "v_exp_f32 v240, v240\n\t"
    "s_waitcnt lgkmcnt(0)\n\t"
    "v_mfma_f32_16x16x32_bf16 v[96:99], v[192:195], a[96:99], v[96:99]\n\t"
    "ds_read_b64_tr_b16 v[216:217], %[ur01] offset:25088\n\t"
    "v_mfma_f32_16x16x32_bf16 v[96:99], v[196:199], a[112:115], v[96:99]\n\t"
    "ds_read_b64_tr_b16 v[218:219], %[ur01] offset:27136\n\t"
    "buffer_load_dwordx4 %[wv2], s[48:51], s46 offen offset:2048 lds\n\t"
    "v_exp_f32 v241, v241\n\t"
    "v_mfma_f32_16x16x32_bf16 v[96:99], v[200:203], a[224:227], v[96:99]\n\t"
    "v_exp_f32 v242, v242\n\t"
    "v_mfma_f32_16x16x32_bf16 v[96:99], v[204:207], a[240:243], v[96:99]\n\t"
    "ds_read_b64_tr_b16 v[220:221], %[ur01] offset:29184\n\t"
    "v_exp_f32 v243, v243\n\t"
    "v_fma_f32 v240, v240, v244, v244\n\t"
    "v_mfma_f32_16x16x32_bf16 v[96:99], v[208:211], v[32:35], v[96:99]\n\t"
    "ds_read_b64_tr_b16 v[222:223], %[ur01] offset:31232\n\t"
    "v_fma_f32 v241, v241, v244, v244\n\t"
    "v_mfma_f32_16x16x32_bf16 v[96:99], v[212:215], v[48:51], v[96:99]\n\t"
    "v_fma_f32 v242, v242, v244, v244\n\t"
    "v_fma_f32 v243, v243, v244, v244\n\t"
    "v_mfma_f32_16x16x32_bf16 v[104:107], v[212:215], v[52:55], v[104:107]\n\t"
    "ds_read_b64_tr_b16 v[224:225], %[ur01] offset:33280\n\t"
    "buffer_load_dwordx4 a[160:163], %[xl], %[srdx0], s41 offen sc1\n\t"
    "v_rcp_f32 v240, v240\n\t"
    "v_mfma_f32_16x16x32_bf16 v[104:107], v[208:211], v[36:39], v[104:107]\n\t"
    "ds_read_b64_tr_b16 v[226:227], %[ur01] offset:35328\n\t"
    "v_mfma_f32_16x16x32_bf16 v[104:107], v[204:207], a[244:247], v[104:107]\n\t"
    "buffer_load_dwordx4 a[76:79], %[xl], %[srd], %[x1o] offen sc1\n\t"
    "v_rcp_f32 v241, v241\n\t"
    "v_mfma_f32_16x16x32_bf16 v[104:107], v[200:203], a[228:231], v[104:107]\n\t"
    "ds_read_b64_tr_b16 v[228:229], %[ur01] offset:37376\n\t"
    "v_rcp_f32 v242, v242\n\t"
    "v_mfma_f32_16x16x32_bf16 v[104:107], v[196:199], a[116:119], v[104:107]\n\t"
    "buffer_load_dwordx4 a[164:167], %[xl], %[srdx0], s41 offen offset:1024 sc1\n\t"
    "v_rcp_f32 v243, v243\n\t"
    "v_mul_f32 v180, v180, v240\n\t"
    "v_mfma_f32_16x16x32_bf16 v[104:107], v[192:195], a[100:103], v[104:107]\n\t"
    "ds_read_b64_tr_b16 v[230:231], %[ur01] offset:39424\n\t"
    "v_mul_f32 v181, v181, v241\n\t"
    "v_mfma_f32_16x16x32_bf16 v[112:115], v[192:195], a[104:107], v[112:115]\n\t"
    "ds_read_b64_tr_b16 v[232:233], %[ur01] offset:41472\n\t"
    "v_mul_f32 v182, v182, v242\n\t"
    "v_mul_f32 v183, v183, v243\n\t"
    "v_mfma_f32_16x16x32_bf16 v[112:115], v[196:199], a[120:123], v[112:115]\n\t"
    "v_cvt_pk_bf16_f32 v178, v180, v181\n\t"
    "v_cvt_pk_bf16_f32 v179, v182, v183\n\t"
    "v_mfma_f32_16x16x32_bf16 v[112:115], v[200:203], a[232:235], v[112:115]\n\t"
    "ds_read_b64_tr_b16 v[234:235], %[ur01] offset:43520\n\t"
    "buffer_store_dwordx4 v[176:179], %[xl], %[srd], s54 offen offset:3072 sc1\n\t"
    "v_fmaak_f32 v240, v184, v184, 0x409d30fc\n\t"
    "v_fmaak_f32 v241, v185, v185, 0x409d30fc\n\t"
    "v_mfma_f32_16x16x32_bf16 v[112:115], v[204:207], a[248:251], v[112:115]\n\t"
    "ds_read_b64_tr_b16 v[236:237], %[ur01] offset:45568\n\t"
    "buffer_load_dwordx4 a[168:171], %[xl], %[srdx0], s41 offen offset:2048 sc1\n\t"
    "v_mfma_f32_16x16x32_bf16 v[112:115], v[208:211], v[40:43], v[112:115]\n\t"
    "v_fmaak_f32 v242, v186, v186, 0x409d30fc\n\t"
    "v_fmaak_f32 v243, v187, v187, 0x409d30fc\n\t"
    "v_mul_f32 v240, v240, v184\n\t"
    "v_mfma_f32_16x16x32_bf16 v[112:115], v[212:215], v[56:59], v[112:115]\n\t"
    "ds_read_b64_tr_b16 v[238:239], %[ur01] offset:47616\n\t"
    "buffer_load_dwordx4 a[80:83], %[xl], %[srd], %[x1o] offen offset:1024 sc1\n\t"
    "v_mul_f32 v241, v241, v185\n\t"
    "v_mfma_f32_16x16x32_bf16 v[120:123], v[212:215], v[60:63], v[120:123]\n\t"
    "v_mul_f32 v242, v242, v186\n\t"
    "v_mul_f32 v243, v243, v187\n\t"
    "v_mfma_f32_16x16x32_bf16 v[120:123], v[208:211], v[44:47], v[120:123]\n\t"
    "v_exp_f32 v240, v240\n\t"
    "v_mfma_f32_16x16x32_bf16 v[120:123], v[204:207], a[252:255], v[120:123]\n\t"
    "buffer_load_dwordx4 a[172:175], %[xl], %[srdx0], s41 offen offset:3072 sc1\n\t"
    "v_exp_f32 v241, v241\n\t"
    "v_mfma_f32_16x16x32_bf16 v[120:123], v[200:203], a[236:239], v[120:123]\n\t"
    "v_exp_f32 v242, v242\n\t"
    "v_exp_f32 v243, v243\n\t"
    "v_mfma_f32_16x16x32_bf16 v[120:123], v[196:199], a[124:127], v[120:123]\n\t"
    "v_fma_f32 v240, v240, v244, v244\n\t"
    "v_fma_f32 v241, v241, v244, v244\n\t"
    "v_mfma_f32_16x16x32_bf16 v[120:123], v[192:195], a[108:111], v[120:123]\n\t"
    "v_fma_f32 v242, v242, v244, v244\n\t"
    "v_fma_f32 v243, v243, v244, v244\n\t"
    "s_waitcnt lgkmcnt(0)\n\t"
    "v_mfma_f32_16x16x32_bf16 v[100:103], v[216:219], a[96:99], v[100:103]\n\t"
    "buffer_load_dwordx4 a[176:179], %[xl], %[srdx1], s41 offen sc1\n\t"
    "v_rcp_f32 v240, v240\n\t"
    "v_mfma_f32_16x16x32_bf16 v[100:103], v[220:223], a[112:115], v[100:103]\n\t"
    "v_rcp_f32 v241, v241\n\t"
    "v_mfma_f32_16x16x32_bf16 v[100:103], v[224:227], a[224:227], v[100:103]\n\t"
    "buffer_load_dwordx4 a[84:87], %[xl], %[srd], %[x1o] offen offset:2048 sc1\n\t"
    "v_rcp_f32 v242, v242\n\t"
    "v_mfma_f32_16x16x32_bf16 v[100:103], v[228:231], a[240:243], v[100:103]\n\t"
    "v_rcp_f32 v243, v243\n\t"
    "v_mul_f32 v184, v184, v240\n\t"
    "v_mfma_f32_16x16x32_bf16 v[100:103], v[232:235], v[32:35], v[100:103]\n\t"
    "v_mul_f32 v185, v185, v241\n\t"
    "v_mul_f32 v186, v186, v242\n\t"
    "v_mul_f32 v187, v187, v243\n\t"
    "v_mfma_f32_16x16x32_bf16 v[100:103], v[236:239], v[48:51], v[100:103]\n\t"
    "buffer_load_dwordx4 a[180:183], %[xl], %[srdx1], s41 offen offset:1024 sc1\n\t"
    "v_cvt_pk_bf16_f32 v184, v184, v185\n\t"
    "v_mfma_f32_16x16x32_bf16 v[108:111], v[236:239], v[52:55], v[108:111]\n\t"
    "v_cvt_pk_bf16_f32 v185, v186, v187\n\t"
    "v_fmaak_f32 v240, v188, v188, 0x409d30fc\n\t"
    "v_fmaak_f32 v241, v189, v189, 0x409d30fc\n\t"
    "v_mfma_f32_16x16x32_bf16 v[108:111], v[232:235], v[36:39], v[108:111]\n\t"
    "v_fmaak_f32 v242, v190, v190, 0x409d30fc\n\t"
    "v_fmaak_f32 v243, v191, v191, 0x409d30fc\n\t"
    "v_mul_f32 v240, v240, v188\n\t"
    "v_mfma_f32_16x16x32_bf16 v[108:111], v[228:231], a[244:247], v[108:111]\n\t"
    "v_mul_f32 v241, v241, v189\n\t"
    "v_mul_f32 v242, v242, v190\n\t"
    "v_mul_f32 v243, v243, v191\n\t"
    "v_mfma_f32_16x16x32_bf16 v[108:111], v[224:227], a[228:231], v[108:111]\n\t"
    "buffer_load_dwordx4 a[184:187], %[xl], %[srdx1], s41 offen offset:2048 sc1\n\t"
    "v_mfma_f32_16x16x32_bf16 v[108:111], v[220:223], a[116:119], v[108:111]\n\t"
    "v_exp_f32 v240, v240\n\t"
    "v_exp_f32 v241, v241\n\t"
    "v_mfma_f32_16x16x32_bf16 v[108:111], v[216:219], a[100:103], v[108:111]\n\t"
    "v_exp_f32 v242, v242\n\t"
    "v_mfma_f32_16x16x32_bf16 v[116:119], v[216:219], a[104:107], v[116:119]\n\t"
    "v_exp_f32 v243, v243\n\t"
    "v_fma_f32 v240, v240, v244, v244\n\t"
    "v_mfma_f32_16x16x32_bf16 v[116:119], v[220:223], a[120:123], v[116:119]\n\t"
    "v_fma_f32 v241, v241, v244, v244\n\t"
    "v_fma_f32 v242, v242, v244, v244\n\t"
    "v_fma_f32 v243, v243, v244, v244\n\t"
    "v_mfma_f32_16x16x32_bf16 v[116:119], v[224:227], a[232:235], v[116:119]\n\t"
    "buffer_load_dwordx4 a[188:191], %[xl], %[srdx1], s41 offen offset:3072 sc1\n\t"
    "v_rcp_f32 v240, v240\n\t"
    "v_mfma_f32_16x16x32_bf16 v[116:119], v[228:231], a[248:251], v[116:119]\n\t"
    "s_add_u32 s40, s40, 147456\n\t"
    "s_add_u32 s41, s41, 4096\n\t"
    "s_add_u32 s58, s58, 4096\n\t"
    "s_cmp_eq_u32 s41, s59\n\t"
    "s_cselect_b32 s41, s60, s41\n\t"
    "s_cselect_b32 s40, 0, s40\n\t"
    "v_mfma_f32_16x16x32_bf16 v[116:119], v[232:235], v[40:43], v[116:119]\n\t"
    "v_mfma_f32_16x16x32_bf16 v[116:119], v[236:239], v[56:59], v[116:119]\n\t"
    "v_rcp_f32 v241, v241\n\t"
    "v_mfma_f32_16x16x32_bf16 v[124:127], v[236:239], v[60:63], v[124:127]\n\t"
    "v_rcp_f32 v242, v242\n\t"
    "v_mfma_f32_16x16x32_bf16 v[124:127], v[232:235], v[44:47], v[124:127]\n\t"
    "v_rcp_f32 v243, v243\n\t"
    "v_mul_f32 v188, v188, v240\n\t"
    "v_mul_f32 v189, v189, v241\n\t"
    "v_mfma_f32_16x16x32_bf16 v[124:127], v[228:231], a[252:255], v[124:127]\n\t"
    "v_mul_f32 v190, v190, v242\n\t"
    "v_mul_f32 v191, v191, v243\n\t"
    "v_mfma_f32_16x16x32_bf16 v[124:127], v[224:227], a[236:239], v[124:127]\n\t"
    "v_cvt_pk_bf16_f32 v186, v188, v189\n\t"
    "v_cvt_pk_bf16_f32 v187, v190, v191\n\t"
    "v_mfma_f32_16x16x32_bf16 v[124:127], v[220:223], a[124:127], v[124:127]\n\t"
    "buffer_store_dwordx4 v[184:187], %[xl], %[srd], s55 offen offset:3072 sc1\n\t"
    "v_mfma_f32_16x16x32_bf16 v[124:127], v[216:219], a[108:111], v[124:127]\n\t"
    "s_add_u32 s52, s52, 4096\n\t"
    "s_add_u32 s53, s53, 4096\n\t"
    "s_add_u32 s54, s54, 4096\n\t"
    "s_add_u32 s55, s55, 4096\n\t"
    "s_waitcnt vmcnt(46)\n\t"
    "v_fmaak_f32 v240, v64, v64, 0x409d30fc\n\t"
    "v_fmaak_f32 v241, v65, v65, 0x409d30fc\n\t"
    "v_fmaak_f32 v242, v66, v66, 0x409d30fc\n\t"
    "v_fmaak_f32 v243, v67, v67, 0x409d30fc\n\t"
    "v_mul_f32 v240, v240, v64\n\t"
    "v_mul_f32 v241, v241, v65\n\t"
    "v_mul_f32 v242, v242, v66\n\t"
    "v_mul_f32 v243, v243, v67\n\t"
    "v_exp_f32 v240, v240\n\t"
    "v_exp_f32 v241, v241\n\t"
    "v_exp_f32 v242, v242\n\t"
    "v_exp_f32 v243, v243\n\t"
    "v_fma_f32 v240, v240, v244, v244\n\t"
    "v_fma_f32 v241, v241, v244, v244\n\t"
    "v_fma_f32 v242, v242, v244, v244\n\t"
    "v_fma_f32 v243, v243, v244, v244\n\t"
    "v_rcp_f32 v240, v240\n\t"
    "v_rcp_f32 v241, v241\n\t"
    "v_rcp_f32 v242, v242\n\t"
    "v_rcp_f32 v243, v243\n\t"
    "v_mul_f32 v64, v64, v240\n\t"
    "v_mul_f32 v65, v65, v241\n\t"
    "v_mul_f32 v66, v66, v242\n\t"
    "v_mul_f32 v67, v67, v243\n\t"
    "v_cvt_pk_bf16_f32 v64, v64, v65\n\t"
    "v_cvt_pk_bf16_f32 v65, v66, v67\n\t"
    "v_fmaak_f32 v240, v68, v68, 0x409d30fc\n\t"
    "v_fmaak_f32 v241, v69, v69, 0x409d30fc\n\t"
    "v_fmaak_f32 v242, v70, v70, 0x409d30fc\n\t"
    "v_fmaak_f32 v243, v71, v71, 0x409d30fc\n\t"
    "v_mul_f32 v240, v240, v68\n\t"
    "v_mul_f32 v241, v241, v69\n\t"
    "v_mul_f32 v242, v242, v70\n\t"
    "v_mul_f32 v243, v243, v71\n\t"
    "v_exp_f32 v240, v240\n\t"
    "v_exp_f32 v241, v241\n\t"
    "v_exp_f32 v242, v242\n\t"
    "v_exp_f32 v243, v243\n\t"
    "v_fma_f32 v240, v240, v244, v244\n\t"
    "v_fma_f32 v241, v241, v244, v244\n\t"
    "v_fma_f32 v242, v242, v244, v244\n\t"
    "v_fma_f32 v243, v243, v244, v244\n\t"
    "v_rcp_f32 v240, v240\n\t"
    "v_rcp_f32 v241, v241\n\t"
    "v_rcp_f32 v242, v242\n\t"
    "v_rcp_f32 v243, v243\n\t"
    "v_mul_f32 v68, v68, v240\n\t"
    "v_mul_f32 v69, v69, v241\n\t"
    "v_mul_f32 v70, v70, v242\n\t"
    "v_mul_f32 v71, v71, v243\n\t"
    "v_cvt_pk_bf16_f32 v66, v68, v69\n\t"
    "v_cvt_pk_bf16_f32 v67, v70, v71\n\t"
    "buffer_store_dwordx4 v[64:67], %[xl], %[srd], s52 offen sc1\n\t"
    "v_fmaak_f32 v240, v72, v72, 0x409d30fc\n\t"
    "v_fmaak_f32 v241, v73, v73, 0x409d30fc\n\t"
    "v_fmaak_f32 v242, v74, v74, 0x409d30fc\n\t"
    "v_fmaak_f32 v243, v75, v75, 0x409d30fc\n\t"
    "v_mul_f32 v240, v240, v72\n\t"
    "v_mul_f32 v241, v241, v73\n\t"
    "v_mul_f32 v242, v242, v74\n\t"
    "v_mul_f32 v243, v243, v75\n\t"
    "v_exp_f32 v240, v240\n\t"
    "v_exp_f32 v241, v241\n\t"
    "v_exp_f32 v242, v242\n\t"
    "v_exp_f32 v243, v243\n\t"
    "v_fma_f32 v240, v240, v244, v244\n\t"
    "v_fma_f32 v241, v241, v244, v244\n\t"
    "v_fma_f32 v242, v242, v244, v244\n\t"
    "v_fma_f32 v243, v243, v244, v244\n\t"
    "v_rcp_f32 v240, v240\n\t"
    "v_rcp_f32 v241, v241\n\t"
    "v_rcp_f32 v242, v242\n\t"
    "v_rcp_f32 v243, v243\n\t"
    "v_mul_f32 v72, v72, v240\n\t"
    "v_mul_f32 v73, v73, v241\n\t"
    "v_mul_f32 v74, v74, v242\n\t"
    "v_mul_f32 v75, v75, v243\n\t"
    "v_cvt_pk_bf16_f32 v72, v72, v73\n\t"
    "v_cvt_pk_bf16_f32 v73, v74, v75\n\t"
    "v_fmaak_f32 v240, v76, v76, 0x409d30fc\n\t"
    "v_fmaak_f32 v241, v77, v77, 0x409d30fc\n\t"
    "v_fmaak_f32 v242, v78, v78, 0x409d30fc\n\t"
    "v_fmaak_f32 v243, v79, v79, 0x409d30fc\n\t"
    "v_mul_f32 v240, v240, v76\n\t"
    "v_mul_f32 v241, v241, v77\n\t"
    "v_mul_f32 v242, v242, v78\n\t"
    "v_mul_f32 v243, v243, v79\n\t"
    "v_exp_f32 v240, v240\n\t"
    "v_exp_f32 v241, v241\n\t"
    "v_exp_f32 v242, v242\n\t"
    "v_exp_f32 v243, v243\n\t"
    "v_fma_f32 v240, v240, v244, v244\n\t"
    "v_fma_f32 v241, v241, v244, v244\n\t"
    "v_fma_f32 v242, v242, v244, v244\n\t"
    "v_fma_f32 v243, v243, v244, v244\n\t"
    "v_rcp_f32 v240, v240\n\t"
    "v_rcp_f32 v241, v241\n\t"
    "v_rcp_f32 v242, v242\n\t"
    "v_rcp_f32 v243, v243\n\t"
    "v_mul_f32 v76, v76, v240\n\t"
    "v_mul_f32 v77, v77, v241\n\t"
    "v_mul_f32 v78, v78, v242\n\t"
    "v_mul_f32 v79, v79, v243\n\t"
    "v_cvt_pk_bf16_f32 v74, v76, v77\n\t"
    "v_cvt_pk_bf16_f32 v75, v78, v79\n\t"
    "buffer_store_dwordx4 v[72:75], %[xl], %[srd], s53 offen sc1\n\t"
    "v_fmaak_f32 v240, v80, v80, 0x409d30fc\n\t"
    "v_fmaak_f32 v241, v81, v81, 0x409d30fc\n\t"
    "v_fmaak_f32 v242, v82, v82, 0x409d30fc\n\t"
    "v_fmaak_f32 v243, v83, v83, 0x409d30fc\n\t"
    "v_mul_f32 v240, v240, v80\n\t"
    "v_mul_f32 v241, v241, v81\n\t"
    "v_mul_f32 v242, v242, v82\n\t"
    "v_mul_f32 v243, v243, v83\n\t"
    "v_exp_f32 v240, v240\n\t"
    "v_exp_f32 v241, v241\n\t"
    "v_exp_f32 v242, v242\n\t"
    "v_exp_f32 v243, v243\n\t"
    "v_fma_f32 v240, v240, v244, v244\n\t"
    "v_fma_f32 v241, v241, v244, v244\n\t"
    "v_fma_f32 v242, v242, v244, v244\n\t"
    "v_fma_f32 v243, v243, v244, v244\n\t"
    "v_rcp_f32 v240, v240\n\t"
    "v_rcp_f32 v241, v241\n\t"
    "v_rcp_f32 v242, v242\n\t"
    "v_rcp_f32 v243, v243\n\t"
    "v_mul_f32 v80, v80, v240\n\t"
    "v_mul_f32 v81, v81, v241\n\t"
    "v_mul_f32 v82, v82, v242\n\t"
    "v_mul_f32 v83, v83, v243\n\t"
    "v_cvt_pk_bf16_f32 v80, v80, v81\n\t"
    "v_cvt_pk_bf16_f32 v81, v82, v83\n\t"
    "v_fmaak_f32 v240, v84, v84, 0x409d30fc\n\t"
    "v_fmaak_f32 v241, v85, v85, 0x409d30fc\n\t"
    "v_fmaak_f32 v242, v86, v86, 0x409d30fc\n\t"
    "v_fmaak_f32 v243, v87, v87, 0x409d30fc\n\t"
    "v_mul_f32 v240, v240, v84\n\t"
    "v_mul_f32 v241, v241, v85\n\t"
    "v_mul_f32 v242, v242, v86\n\t"
    "v_mul_f32 v243, v243, v87\n\t"
    "v_exp_f32 v240, v240\n\t"
    "v_exp_f32 v241, v241\n\t"
    "v_exp_f32 v242, v242\n\t"
    "v_exp_f32 v243, v243\n\t"
    "v_fma_f32 v240, v240, v244, v244\n\t"
    "v_fma_f32 v241, v241, v244, v244\n\t"
    "v_fma_f32 v242, v242, v244, v244\n\t"
    "v_fma_f32 v243, v243, v244, v244\n\t"
    "v_rcp_f32 v240, v240\n\t"
    "v_rcp_f32 v241, v241\n\t"
    "v_rcp_f32 v242, v242\n\t"
    "v_rcp_f32 v243, v243\n\t"
    "v_mul_f32 v84, v84, v240\n\t"
    "v_mul_f32 v85, v85, v241\n\t"
    "v_mul_f32 v86, v86, v242\n\t"
    "v_mul_f32 v87, v87, v243\n\t"
    "v_cvt_pk_bf16_f32 v82, v84, v85\n\t"
    "v_cvt_pk_bf16_f32 v83, v86, v87\n\t"
    "buffer_store_dwordx4 v[80:83], %[xl], %[srd], s54 offen sc1\n\t"
    "v_fmaak_f32 v240, v88, v88, 0x409d30fc\n\t"
    "v_fmaak_f32 v241, v89, v89, 0x409d30fc\n\t"
    "v_fmaak_f32 v242, v90, v90, 0x409d30fc\n\t"
    "v_fmaak_f32 v243, v91, v91, 0x409d30fc\n\t"
    "v_mul_f32 v240, v240, v88\n\t"
    "v_mul_f32 v241, v241, v89\n\t"
    "v_mul_f32 v242, v242, v90\n\t"
    "v_mul_f32 v243, v243, v91\n\t"
    "v_exp_f32 v240, v240\n\t"
    "v_exp_f32 v241, v241\n\t"
    "v_exp_f32 v242, v242\n\t"
    "v_exp_f32 v243, v243\n\t"
    "v_fma_f32 v240, v240, v244, v244\n\t"
    "v_fma_f32 v241, v241, v244, v244\n\t"
    "v_fma_f32 v242, v242, v244, v244\n\t"
    "v_fma_f32 v243, v243, v244, v244\n\t"
    "v_rcp_f32 v240, v240\n\t"
    "v_rcp_f32 v241, v241\n\t"
    "v_rcp_f32 v242, v242\n\t"
    "v_rcp_f32 v243, v243\n\t"
    "v_mul_f32 v88, v88, v240\n\t"
    "v_mul_f32 v89, v89, v241\n\t"
    "v_mul_f32 v90, v90, v242\n\t"
    "v_mul_f32 v91, v91, v243\n\t"
    "v_cvt_pk_bf16_f32 v88, v88, v89\n\t"
    "v_cvt_pk_bf16_f32 v89, v90, v91\n\t"
    "v_fmaak_f32 v240, v92, v92, 0x409d30fc\n\t"
    "v_fmaak_f32 v241, v93, v93, 0x409d30fc\n\t"
    "v_fmaak_f32 v242, v94, v94, 0x409d30fc\n\t"
    "v_fmaak_f32 v243, v95, v95, 0x409d30fc\n\t"
    "v_mul_f32 v240, v240, v92\n\t"
    "v_mul_f32 v241, v241, v93\n\t"
    "v_mul_f32 v242, v242, v94\n\t"
    "v_mul_f32 v243, v243, v95\n\t"
    "v_exp_f32 v240, v240\n\t"
    "v_exp_f32 v241, v241\n\t"
    "v_exp_f32 v242, v242\n\t"
    "v_exp_f32 v243, v243\n\t"
    "v_fma_f32 v240, v240, v244, v244\n\t"
    "v_fma_f32 v241, v241, v244, v244\n\t"
    "v_fma_f32 v242, v242, v244, v244\n\t"
    "v_fma_f32 v243, v243, v244, v244\n\t"
    "v_rcp_f32 v240, v240\n\t"
    "v_rcp_f32 v241, v241\n\t"
    "v_rcp_f32 v242, v242\n\t"
    "v_rcp_f32 v243, v243\n\t"
    "v_mul_f32 v92, v92, v240\n\t"
    "v_mul_f32 v93, v93, v241\n\t"
    "v_mul_f32 v94, v94, v242\n\t"
    "v_mul_f32 v95, v95, v243\n\t"
    "v_cvt_pk_bf16_f32 v90, v92, v93\n\t"
    "v_cvt_pk_bf16_f32 v91, v94, v95\n\t"
    "buffer_store_dwordx4 v[88:91], %[xl], %[srd], s55 offen sc1\n\t"
    "v_fmaak_f32 v240, v96, v96, 0x409d30fc\n\t"
    "v_fmaak_f32 v241, v97, v97, 0x409d30fc\n\t"
    "v_fmaak_f32 v242, v98, v98, 0x409d30fc\n\t"
    "v_fmaak_f32 v243, v99, v99, 0x409d30fc\n\t"
    "v_mul_f32 v240, v240, v96\n\t"
    "v_mul_f32 v241, v241, v97\n\t"
    "v_mul_f32 v242, v242, v98\n\t"
    "v_mul_f32 v243, v243, v99\n\t"
    "v_exp_f32 v240, v240\n\t"
    "v_exp_f32 v241, v241\n\t"
    "v_exp_f32 v242, v242\n\t"
    "v_exp_f32 v243, v243\n\t"
    "v_fma_f32 v240, v240, v244, v244\n\t"
    "v_fma_f32 v241, v241, v244, v244\n\t"
    "v_fma_f32 v242, v242, v244, v244\n\t"
    "v_fma_f32 v243, v243, v244, v244\n\t"
    "v_rcp_f32 v240, v240\n\t"
    "v_rcp_f32 v241, v241\n\t"
    "v_rcp_f32 v242, v242\n\t"
    "v_rcp_f32 v243, v243\n\t"
    "v_mul_f32 v96, v96, v240\n\t"
    "v_mul_f32 v97, v97, v241\n\t"
    "v_mul_f32 v98, v98, v242\n\t"
    "v_mul_f32 v99, v99, v243\n\t"
    "v_cvt_pk_bf16_f32 v96, v96, v97\n\t"
    "v_cvt_pk_bf16_f32 v97, v98, v99\n\t"
    "v_fmaak_f32 v240, v100, v100, 0x409d30fc\n\t"
    "v_fmaak_f32 v241, v101, v101, 0x409d30fc\n\t"
    "v_fmaak_f32 v242, v102, v102, 0x409d30fc\n\t"
    "v_fmaak_f32 v243, v103, v103, 0x409d30fc\n\t"
    "v_mul_f32 v240, v240, v100\n\t"
    "v_mul_f32 v241, v241, v101\n\t"
    "v_mul_f32 v242, v242, v102\n\t"
    "v_mul_f32 v243, v243, v103\n\t"
    "v_exp_f32 v240, v240\n\t"
    "v_exp_f32 v241, v241\n\t"
    "v_exp_f32 v242, v242\n\t"
    "v_exp_f32 v243, v243\n\t"
    "v_fma_f32 v240, v240, v244, v244\n\t"
    "v_fma_f32 v241, v241, v244, v244\n\t"
    "v_fma_f32 v242, v242, v244, v244\n\t"
    "v_fma_f32 v243, v243, v244, v244\n\t"
    "v_rcp_f32 v240, v240\n\t"
    "v_rcp_f32 v241, v241\n\t"
    "v_rcp_f32 v242, v242\n\t"
    "v_rcp_f32 v243, v243\n\t"
    "v_mul_f32 v100, v100, v240\n\t"
    "v_mul_f32 v101, v101, v241\n\t"
    "v_mul_f32 v102, v102, v242\n\t"
    "v_mul_f32 v103, v103, v243\n\t"
    "v_cvt_pk_bf16_f32 v98, v100, v101\n\t"
    "v_cvt_pk_bf16_f32 v99, v102, v103\n\t"
    "buffer_store_dwordx4 v[96:99], %[xl], %[srd], s52 offen offset:1024 sc1\n\t"
    "v_fmaak_f32 v240, v104, v104, 0x409d30fc\n\t"
    "v_fmaak_f32 v241, v105, v105, 0x409d30fc\n\t"
    "v_fmaak_f32 v242, v106, v106, 0x409d30fc\n\t"
    "v_fmaak_f32 v243, v107, v107, 0x409d30fc\n\t"
    "v_mul_f32 v240, v240, v104\n\t"
    "v_mul_f32 v241, v241, v105\n\t"
    "v_mul_f32 v242, v242, v106\n\t"
    "v_mul_f32 v243, v243, v107\n\t"
    "v_exp_f32 v240, v240\n\t"
    "v_exp_f32 v241, v241\n\t"
    "v_exp_f32 v242, v242\n\t"
    "v_exp_f32 v243, v243\n\t"
    "v_fma_f32 v240, v240, v244, v244\n\t"
    "v_fma_f32 v241, v241, v244, v244\n\t"
    "v_fma_f32 v242, v242, v244, v244\n\t"
    "v_fma_f32 v243, v243, v244, v244\n\t"
    "v_rcp_f32 v240, v240\n\t"
    "v_rcp_f32 v241, v241\n\t"
    "v_rcp_f32 v242, v242\n\t"
    "v_rcp_f32 v243, v243\n\t"
    "v_mul_f32 v104, v104, v240\n\t"
    "v_mul_f32 v105, v105, v241\n\t"
    "v_mul_f32 v106, v106, v242\n\t"
    "v_mul_f32 v107, v107, v243\n\t"
    "v_cvt_pk_bf16_f32 v104, v104, v105\n\t"
    "v_cvt_pk_bf16_f32 v105, v106, v107\n\t"
    "v_fmaak_f32 v240, v108, v108, 0x409d30fc\n\t"
    "v_fmaak_f32 v241, v109, v109, 0x409d30fc\n\t"
    "v_fmaak_f32 v242, v110, v110, 0x409d30fc\n\t"
    "v_fmaak_f32 v243, v111, v111, 0x409d30fc\n\t"
    "v_mul_f32 v240, v240, v108\n\t"
    "v_mul_f32 v241, v241, v109\n\t"
    "v_mul_f32 v242, v242, v110\n\t"
    "v_mul_f32 v243, v243, v111\n\t"
    "v_exp_f32 v240, v240\n\t"
    "v_exp_f32 v241, v241\n\t"
    "v_exp_f32 v242, v242\n\t"
    "v_exp_f32 v243, v243\n\t"
    "v_fma_f32 v240, v240, v244, v244\n\t"
    "v_fma_f32 v241, v241, v244, v244\n\t"
    "v_fma_f32 v242, v242, v244, v244\n\t"
    "v_fma_f32 v243, v243, v244, v244\n\t"
    "v_rcp_f32 v240, v240\n\t"
    "v_rcp_f32 v241, v241\n\t"
    "v_rcp_f32 v242, v242\n\t"
    "v_rcp_f32 v243, v243\n\t"
    "v_mul_f32 v108, v108, v240\n\t"
    "v_mul_f32 v109, v109, v241\n\t"
    "v_mul_f32 v110, v110, v242\n\t"
    "v_mul_f32 v111, v111, v243\n\t"
    "v_cvt_pk_bf16_f32 v106, v108, v109\n\t"
    "v_cvt_pk_bf16_f32 v107, v110, v111\n\t"
    "buffer_store_dwordx4 v[104:107], %[xl], %[srd], s53 offen offset:1024 sc1\n\t"
    "v_fmaak_f32 v240, v112, v112, 0x409d30fc\n\t"
    "v_fmaak_f32 v241, v113, v113, 0x409d30fc\n\t"
    "v_fmaak_f32 v242, v114, v114, 0x409d30fc\n\t"
    "v_fmaak_f32 v243, v115, v115, 0x409d30fc\n\t"
    "v_mul_f32 v240, v240, v112\n\t"
    "v_mul_f32 v241, v241, v113\n\t"
    "v_mul_f32 v242, v242, v114\n\t"
    "v_mul_f32 v243, v243, v115\n\t"
    "v_exp_f32 v240, v240\n\t"
    "v_exp_f32 v241, v241\n\t"
    "v_exp_f32 v242, v242\n\t"
    "v_exp_f32 v243, v243\n\t"
    "v_fma_f32 v240, v240, v244, v244\n\t"
    "v_fma_f32 v241, v241, v244, v244\n\t"
    "v_fma_f32 v242, v242, v244, v244\n\t"
    "v_fma_f32 v243, v243, v244, v244\n\t"
    "v_rcp_f32 v240, v240\n\t"
    "v_rcp_f32 v241, v241\n\t"
    "v_rcp_f32 v242, v242\n\t"
    "v_rcp_f32 v243, v243\n\t"
    "v_mul_f32 v112, v112, v240\n\t"
    "v_mul_f32 v113, v113, v241\n\t"
    "v_mul_f32 v114, v114, v242\n\t"
    "v_mul_f32 v115, v115, v243\n\t"
    "v_cvt_pk_bf16_f32 v112, v112, v113\n\t"
    "v_cvt_pk_bf16_f32 v113, v114, v115\n\t"
    "v_fmaak_f32 v240, v116, v116, 0x409d30fc\n\t"
    "v_fmaak_f32 v241, v117, v117, 0x409d30fc\n\t"
    "v_fmaak_f32 v242, v118, v118, 0x409d30fc\n\t"
    "v_fmaak_f32 v243, v119, v119, 0x409d30fc\n\t"
    "v_mul_f32 v240, v240, v116\n\t"
    "v_mul_f32 v241, v241, v117\n\t"
    "v_mul_f32 v242, v242, v118\n\t"
    "v_mul_f32 v243, v243, v119\n\t"
    "v_exp_f32 v240, v240\n\t"
    "v_exp_f32 v241, v241\n\t"
    "v_exp_f32 v242, v242\n\t"
    "v_exp_f32 v243, v243\n\t"
    "v_fma_f32 v240, v240, v244, v244\n\t"
    "v_fma_f32 v241, v241, v244, v244\n\t"
    "v_fma_f32 v242, v242, v244, v244\n\t"
    "v_fma_f32 v243, v243, v244, v244\n\t"
    "v_rcp_f32 v240, v240\n\t"
    "v_rcp_f32 v241, v241\n\t"
    "v_rcp_f32 v242, v242\n\t"
    "v_rcp_f32 v243, v243\n\t"
    "v_mul_f32 v116, v116, v240\n\t"
    "v_mul_f32 v117, v117, v241\n\t"
    "v_mul_f32 v118, v118, v242\n\t"
    "v_mul_f32 v119, v119, v243\n\t"
    "v_cvt_pk_bf16_f32 v114, v116, v117\n\t"
    "v_cvt_pk_bf16_f32 v115, v118, v119\n\t"
    "buffer_store_dwordx4 v[112:115], %[xl], %[srd], s54 offen offset:1024 sc1\n\t"
    "v_fmaak_f32 v240, v120, v120, 0x409d30fc\n\t"
    "v_fmaak_f32 v241, v121, v121, 0x409d30fc\n\t"
    "v_fmaak_f32 v242, v122, v122, 0x409d30fc\n\t"
    "v_fmaak_f32 v243, v123, v123, 0x409d30fc\n\t"
    "v_mul_f32 v240, v240, v120\n\t"
    "v_mul_f32 v241, v241, v121\n\t"
    "v_mul_f32 v242, v242, v122\n\t"
    "v_mul_f32 v243, v243, v123\n\t"
    "v_exp_f32 v240, v240\n\t"
    "v_exp_f32 v241, v241\n\t"
    "v_exp_f32 v242, v242\n\t"
    "v_exp_f32 v243, v243\n\t"
    "v_fma_f32 v240, v240, v244, v244\n\t"
    "v_fma_f32 v241, v241, v244, v244\n\t"
    "v_fma_f32 v242, v242, v244, v244\n\t"
    "v_fma_f32 v243, v243, v244, v244\n\t"
    "v_rcp_f32 v240, v240\n\t"
    "v_rcp_f32 v241, v241\n\t"
    "v_rcp_f32 v242, v242\n\t"
    "v_rcp_f32 v243, v243\n\t"
    "v_mul_f32 v120, v120, v240\n\t"
    "v_mul_f32 v121, v121, v241\n\t"
    "v_mul_f32 v122, v122, v242\n\t"
    "v_mul_f32 v123, v123, v243\n\t"
    "v_cvt_pk_bf16_f32 v120, v120, v121\n\t"
    "v_cvt_pk_bf16_f32 v121, v122, v123\n\t"
    "v_fmaak_f32 v240, v124, v124, 0x409d30fc\n\t"
    "v_fmaak_f32 v241, v125, v125, 0x409d30fc\n\t"
    "v_fmaak_f32 v242, v126, v126, 0x409d30fc\n\t"
    "v_fmaak_f32 v243, v127, v127, 0x409d30fc\n\t"
    "v_mul_f32 v240, v240, v124\n\t"
    "v_mul_f32 v241, v241, v125\n\t"
    "v_mul_f32 v242, v242, v126\n\t"
    "v_mul_f32 v243, v243, v127\n\t"
    "v_exp_f32 v240, v240\n\t"
    "v_exp_f32 v241, v241\n\t"
    "v_exp_f32 v242, v242\n\t"
    "v_exp_f32 v243, v243\n\t"
    "v_fma_f32 v240, v240, v244, v244\n\t"
    "v_fma_f32 v241, v241, v244, v244\n\t"
    "v_fma_f32 v242, v242, v244, v244\n\t"
    "v_fma_f32 v243, v243, v244, v244\n\t"
    "v_rcp_f32 v240, v240\n\t"
    "v_rcp_f32 v241, v241\n\t"
    "v_rcp_f32 v242, v242\n\t"
    "v_rcp_f32 v243, v243\n\t"
    "v_mul_f32 v124, v124, v240\n\t"
    "v_mul_f32 v125, v125, v241\n\t"
    "v_mul_f32 v126, v126, v242\n\t"
    "v_mul_f32 v127, v127, v243\n\t"
    "v_cvt_pk_bf16_f32 v122, v124, v125\n\t"
    "v_cvt_pk_bf16_f32 v123, v126, v127\n\t"
    "buffer_store_dwordx4 v[120:123], %[xl], %[srd], s55 offen offset:1024 sc1\n\t"
    "v_mov_b64 v[128:129], 0\n\t"
    "v_mov_b64 v[130:131], 0\n\t"
    "v_mov_b64 v[132:133], 0\n\t"
    "v_mov_b64 v[134:135], 0\n\t"
    "v_mov_b64 v[136:137], 0\n\t"
    "v_mov_b64 v[138:139], 0\n\t"
    "v_mov_b64 v[140:141], 0\n\t"
    "v_mov_b64 v[142:143], 0\n\t"
    "v_mov_b64 v[144:145], 0\n\t"
    "v_mov_b64 v[146:147], 0\n\t"
    "v_mov_b64 v[148:149], 0\n\t"
    "v_mov_b64 v[150:151], 0\n\t"
    "v_mov_b64 v[152:153], 0\n\t"
    "v_mov_b64 v[154:155], 0\n\t"
    "v_mov_b64 v[156:157], 0\n\t"
    "v_mov_b64 v[158:159], 0\n\t"
    "v_mov_b64 v[160:161], 0\n\t"
    "v_mov_b64 v[162:163], 0\n\t"
    "v_mov_b64 v[164:165], 0\n\t"
    "v_mov_b64 v[166:167], 0\n\t"
    "v_mov_b64 v[168:169], 0\n\t"
    "v_mov_b64 v[170:171], 0\n\t"
    "v_mov_b64 v[172:173], 0\n\t"
    "v_mov_b64 v[174:175], 0\n\t"
    "v_mov_b64 v[176:177], 0\n\t"
    "v_mov_b64 v[178:179], 0\n\t"
    "v_mov_b64 v[180:181], 0\n\t"
    "v_mov_b64 v[182:183], 0\n\t"
    "v_mov_b64 v[184:185], 0\n\t"
    "v_mov_b64 v[186:187], 0\n\t"
    "v_mov_b64 v[188:189], 0\n\t"
    "v_mov_b64 v[190:191], 0\n\t"
    "v_mov_b64 v[192:193], 0\n\t"
    "v_mov_b64 v[194:195], 0\n\t"
    "v_mov_b64 v[196:197], 0\n\t"
    "v_mov_b64 v[198:199], 0\n\t"
    "v_mov_b64 v[200:201], 0\n\t"
    "v_mov_b64 v[202:203], 0\n\t"
    "v_mov_b64 v[204:205], 0\n\t"
    "v_mov_b64 v[206:207], 0\n\t"
    "v_mov_b64 v[208:209], 0\n\t"
    "v_mov_b64 v[210:211], 0\n\t"
    "v_mov_b64 v[212:213], 0\n\t"
    "v_mov_b64 v[214:215], 0\n\t"
    "v_mov_b64 v[216:217], 0\n\t"
    "v_mov_b64 v[218:219], 0\n\t"
    "v_mov_b64 v[220:221], 0\n\t"
    "v_mov_b64 v[222:223], 0\n\t"
    "s_mov_b32 s55, -1\n\t"
    "s_waitcnt vmcnt(40)\n\t"
    "s_barrier\n\t"
    "ds_read_b64_tr_b16 a[0:1], %[rb00] offset:0\n\t"
    "ds_read_b64_tr_b16 a[2:3], %[rb00] offset:6144\n\t"
    "ds_read_b64_tr_b16 a[4:5], %[rb00] offset:12288\n\t"
    "ds_read_b64_tr_b16 a[6:7], %[rb00] offset:18432\n\t"
    "ds_read_b64_tr_b16 a[8:9], %[rb00] offset:24576\n\t"
    "ds_read_b64_tr_b16 a[10:11], %[rb00] offset:30720\n\t"
    "ds_read_b64_tr_b16 a[12:13], %[rb00] offset:36864\n\t"
    "ds_read_b64_tr_b16 a[14:15], %[rb00] offset:43008\n\t"
    "ds_read_b64_tr_b16 a[16:17], %[rb01] offset:0\n\t"
    "ds_read_b64_tr_b16 a[18:19], %[rb01] offset:6144\n\t"
    "ds_read_b64_tr_b16 a[20:21], %[rb01] offset:12288\n\t"
    "ds_read_b64_tr_b16 a[22:23], %[rb01] offset:18432\n\t"
    "ds_read_b64_tr_b16 a[24:25], %[rb01] offset:24576\n\t"
    "ds_read_b64_tr_b16 a[26:27], %[rb01] offset:30720\n\t"
    "ds_read_b64_tr_b16 a[28:29], %[rb01] offset:36864\n\t"
    "ds_read_b64_tr_b16 a[30:31], %[rb01] offset:43008\n\t"
    "s_waitcnt lgkmcnt(8)\n\t"
    "Lpc0_%=:\n\t"
    "v_mfma_f32_16x16x32_bf16 v[128:131], a[0:3], a[128:131], v[128:131]\n\t"
    "buffer_load_dword v245, v247, %[srd], 0 offen sc1\n\t"
    "ds_read_b64_tr_b16 a[32:33], %[rb00] offset:512\n\t"
    "s_add_u32 m0, %[m0b], 0\n\t"
    "v_mfma_f32_16x16x32_bf16 v[128:131], a[4:7], a[132:135], v[128:131]\n\t"
    "ds_read_b64_tr_b16 a[34:35], %[rb00] offset:6656\n\t"
    "s_add_u32 s46, s40, 0\n\t"
    "v_mfma_f32_16x16x32_bf16 v[128:131], a[8:11], a[136:139], v[128:131]\n\t"
    "ds_read_b64_tr_b16 a[36:37], %[rb00] offset:12800\n\t"
    "buffer_load_dwordx4 %[wv0], s[48:51], s46 offen lds\n\t"
    "v_mfma_f32_16x16x32_bf16 v[128:131], a[12:15], a[140:143], v[128:131]\n\t"
    "ds_read_b64_tr_b16 a[38:39], %[rb00] offset:18944\n\t"
    "v_mfma_f32_16x16x32_bf16 v[132:135], a[12:15], a[156:159], v[132:135]\n\t"
    "ds_read_b64_tr_b16 a[40:41], %[rb00] offset:25088\n\t"
    "v_mfma_f32_16x16x32_bf16 v[132:135], a[8:11], a[152:155], v[132:135]\n\t"
    "ds_read_b64_tr_b16 a[42:43], %[rb00] offset:31232\n\t"
    "v_mfma_f32_16x16x32_bf16 v[132:135], a[4:7], a[148:151], v[132:135]\n\t"
    "ds_read_b64_tr_b16 a[44:45], %[rb00] offset:37376\n\t"
    "buffer_load_dwordx4 %[wv1], s[48:51], s46 offen offset:1024 lds\n\t"
    "v_mfma_f32_16x16x32_bf16 v[132:135], a[0:3], a[144:147], v[132:135]\n\t"
    "ds_read_b64_tr_b16 a[46:47], %[rb00] offset:43520\n\t"
    "s_waitcnt lgkmcnt(8)\n\t"
    "v_mfma_f32_16x16x32_bf16 v[140:143], a[16:19], a[144:147], v[140:143]\n\t"
    "ds_read_b64_tr_b16 a[0:1], %[rb01] offset:512\n\t"
    "v_mfma_f32_16x16x32_bf16 v[140:143], a[20:23], a[148:151], v[140:143]\n\t"
    "ds_read_b64_tr_b16 a[2:3], %[rb01] offset:6656\n\t"
    "v_mfma_f32_16x16x32_bf16 v[140:143], a[24:27], a[152:155], v[140:143]\n\t"
    "ds_read_b64_tr_b16 a[4:5], %[rb01] offset:12800\n\t"
    "buffer_load_dwordx4 %[wv2], s[48:51], s46 offen offset:2048 lds\n\t"
    "v_mfma_f32_16x16x32_bf16 v[140:143], a[28:31], a[156:159], v[140:143]\n\t"
    "ds_read_b64_tr_b16 a[6:7], %[rb01] offset:18944\n\t"
    "v_mfma_f32_16x16x32_bf16 v[136:139], a[28:31], a[140:143], v[136:139]\n\t"
    "ds_read_b64_tr_b16 a[8:9], %[rb01] offset:25088\n\t"
    "v_mfma_f32_16x16x32_bf16 v[136:139], a[24:27], a[136:139], v[136:139]\n\t"
    "ds_read_b64_tr_b16 a[10:11], %[rb01] offset:31232\n\t"
    "v_mfma_f32_16x16x32_bf16 v[136:139], a[20:23], a[132:135], v[136:139]\n\t"
    "ds_read_b64_tr_b16 a[12:13], %[rb01] offset:37376\n\t"
    "buffer_load_dwordx4 a[192:195], %[xl], %[srdx0], s41 offen sc1\n\t"
    "v_mfma_f32_16x16x32_bf16 v[136:139], a[16:19], a[128:131], v[136:139]\n\t"
    "ds_read_b64_tr_b16 a[14:15], %[rb01] offset:43520\n\t"
    "s_waitcnt lgkmcnt(8)\n\t"
    "v_mfma_f32_16x16x32_bf16 v[144:147], a[32:35], a[128:131], v[144:147]\n\t"
    "ds_read_b64_tr_b16 a[16:17], %[rb00] offset:1024\n\t"
    "v_mfma_f32_16x16x32_bf16 v[144:147], a[36:39], a[132:135], v[144:147]\n\t"
    "ds_read_b64_tr_b16 a[18:19], %[rb00] offset:7168\n\t"
    "v_mfma_f32_16x16x32_bf16 v[144:147], a[40:43], a[136:139], v[144:147]\n\t"
    "ds_read_b64_tr_b16 a[20:21], %[rb00] offset:13312\n\t"
    "buffer_load_dwordx4 a[196:199], %[xl], %[srdx0], s41 offen offset:1024 sc1\n\t"
    "v_mfma_f32_16x16x32_bf16 v[144:147], a[44:47], a[140:143], v[144:147]\n\t"
    "ds_read_b64_tr_b16 a[22:23], %[rb00] offset:19456\n\t"
    "v_mfma_f32_16x16x32_bf16 v[148:151], a[44:47], a[156:159], v[148:151]\n\t"
    "ds_read_b64_tr_b16 a[24:25], %[rb00] offset:25600\n\t"
    "v_mfma_f32_16x16x32_bf16 v[148:151], a[40:43], a[152:155], v[148:151]\n\t"
    "ds_read_b64_tr_b16 a[26:27], %[rb00] offset:31744\n\t"
    "v_mfma_f32_16x16x32_bf16 v[148:151], a[36:39], a[148:151], v[148:151]\n\t"
    "ds_read_b64_tr_b16 a[28:29], %[rb00] offset:37888\n\t"
    "buffer_load_dwordx4 a[200:203], %[xl], %[srdx0], s41 offen offset:2048 sc1\n\t"
    "v_mfma_f32_16x16x32_bf16 v[148:151], a[32:35], a[144:147], v[148:151]\n\t"
    "ds_read_b64_tr_b16 a[30:31], %[rb00] offset:44032\n\t"
    "s_waitcnt lgkmcnt(8)\n\t"
    "v_mfma_f32_16x16x32_bf16 v[156:159], a[0:3], a[144:147], v[156:159]\n\t"
    "ds_read_b64_tr_b16 a[32:33], %[rb01] offset:1024\n\t"
    "v_mfma_f32_16x16x32_bf16 v[156:159], a[4:7], a[148:151], v[156:159]\n\t"
    "ds_read_b64_tr_b16 a[34:35], %[rb01] offset:7168\n\t"
    "v_mfma_f32_16x16x32_bf16 v[156:159], a[8:11], a[152:155], v[156:159]\n\t"
    "ds_read_b64_tr_b16 a[36:37], %[rb01] offset:13312\n\t"
    "buffer_load_dwordx4 a[204:207], %[xl], %[srdx0], s41 offen offset:3072 sc1\n\t"
    "v_mfma_f32_16x16x32_bf16 v[156:159], a[12:15], a[156:159], v[156:159]\n\t"
    "ds_read_b64_tr_b16 a[38:39], %[rb01] offset:19456\n\t"
    "v_mfma_f32_16x16x32_bf16 v[152:155], a[12:15], a[140:143], v[152:155]\n\t"
    "ds_read_b64_tr_b16 a[40:41], %[rb01] offset:25600\n\t"
    "s_add_u32 m0, %[m0b], 3072\n\t"
    "v_mfma_f32_16x16x32_bf16 v[152:155], a[8:11], a[136:139], v[152:155]\n\t"
    "ds_read_b64_tr_b16 a[42:43], %[rb01] offset:31744\n\t"
    "s_add_u32 s46, s40, 9216\n\t"
    "v_mfma_f32_16x16x32_bf16 v[152:155], a[4:7], a[132:135], v[152:155]\n\t"
    "ds_read_b64_tr_b16 a[44:45], %[rb01] offset:37888\n\t"
    "buffer_load_dwordx4 %[wv0], s[48:51], s46 offen lds\n\t"
    "v_mfma_f32_16x16x32_bf16 v[152:155], a[0:3], a[128:131], v[152:155]\n\t"
    "ds_read_b64_tr_b16 a[46:47], %[rb01] offset:44032\n\t"
    "s_waitcnt lgkmcnt(8)\n\t"
    "v_mfma_f32_16x16x32_bf16 v[160:163], a[16:19], a[128:131], v[160:163]\n\t"
    "ds_read_b64_tr_b16 a[0:1], %[rb00] offset:1536\n\t"
    "v_mfma_f32_16x16x32_bf16 v[160:163], a[20:23], a[132:135], v[160:163]\n\t"
    "ds_read_b64_tr_b16 a[2:3], %[rb00] offset:7680\n\t"
    "v_mfma_f32_16x16x32_bf16 v[160:163], a[24:27], a[136:139], v[160:163]\n\t"
    "ds_read_b64_tr_b16 a[4:5], %[rb00] offset:13824\n\t"
    "buffer_load_dwordx4 %[wv1], s[48:51], s46 offen offset:1024 lds\n\t"
    "v_mfma_f32_16x16x32_bf16 v[160:163], a[28:31], a[140:143], v[160:163]\n\t"
    "ds_read_b64_tr_b16 a[6:7], %[rb00] offset:19968\n\t"
    "v_mfma_f32_16x16x32_bf16 v[164:167], a[28:31], a[156:159], v[164:167]\n\t"
    "ds_read_b64_tr_b16 a[8:9], %[rb00] offset:26112\n\t"
    "v_mfma_f32_16x16x32_bf16 v[164:167], a[24:27], a[152:155], v[164:167]\n\t"
    "ds_read_b64_tr_b16 a[10:11], %[rb00] offset:32256\n\t"
    "v_mfma_f32_16x16x32_bf16 v[164:167], a[20:23], a[148:151], v[164:167]\n\t"
    "ds_read_b64_tr_b16 a[12:13], %[rb00] offset:38400\n\t"
    "buffer_load_dwordx4 %[wv2], s[48:51], s46 offen offset:2048 lds\n\t"
    "v_mfma_f32_16x16x32_bf16 v[164:167], a[16:19], a[144:147], v[164:167]\n\t"
    "ds_read_b64_tr_b16 a[14:15], %[rb00] offset:44544\n\t"
    "s_waitcnt lgkmcnt(8)\n\t"
    "v_mfma_f32_16x16x32_bf16 v[172:175], a[32:35], a[144:147], v[172:175]\n\t"
    "ds_read_b64_tr_b16 a[16:17], %[rb01] offset:1536\n\t"
    "s_add_u32 m0, %[m0b], 6144\n\t"
    "v_mfma_f32_16x16x32_bf16 v[172:175], a[36:39], a[148:151], v[172:175]\n\t"
    "ds_read_b64_tr_b16 a[18:19], %[rb01] offset:7680\n\t"
    "s_add_u32 s46, s40, 18432\n\t"
    "v_mfma_f32_16x16x32_bf16 v[172:175], a[40:43], a[152:155], v[172:175]\n\t"
    "ds_read_b64_tr_b16 a[20:21], %[rb01] offset:13824\n\t"
    "buffer_load_dwordx4 %[wv0], s[48:51], s46 offen lds\n\t"
    "v_mfma_f32_16x16x32_bf16 v[172:175], a[44:47], a[156:159], v[172:175]\n\t"
    "ds_read_b64_tr_b16 a[22:23], %[rb01] offset:19968\n\t"
    "v_mfma_f32_16x16x32_bf16 v[168:171], a[44:47], a[140:143], v[168:171]\n\t"
    "ds_read_b64_tr_b16 a[24:25], %[rb01] offset:26112\n\t"
    "v_mfma_f32_16x16x32_bf16 v[168:171], a[40:43], a[136:139], v[168:171]\n\t"
    "ds_read_b64_tr_b16 a[26:27], %[rb01] offset:32256\n\t"
    "v_mfma_f32_16x16x32_bf16 v[168:171], a[36:39], a[132:135], v[168:171]\n\t"
    "ds_read_b64_tr_b16 a[28:29], %[rb01] offset:38400\n\t"
    "buffer_load_dwordx4 %[wv1], s[48:51], s46 offen offset:1024 lds\n\t"
    "v_mfma_f32_16x16x32_bf16 v[168:171], a[32:35], a[128:131], v[168:171]\n\t"
    "ds_read_b64_tr_b16 a[30:31], %[rb01] offset:44544\n\t"
    "s_waitcnt lgkmcnt(8)\n\t"
    "v_mfma_f32_16x16x32_bf16 v[176:179], a[0:3], a[128:131], v[176:179]\n\t"
    "ds_read_b64_tr_b16 a[32:33], %[rb00] offset:2048\n\t"
    "v_mfma_f32_16x16x32_bf16 v[176:179], a[4:7], a[132:135], v[176:179]\n\t"
    "ds_read_b64_tr_b16 a[34:35], %[rb00] offset:8192\n\t"
    "v_mfma_f32_16x16x32_bf16 v[176:179], a[8:11], a[136:139], v[176:179]\n\t"
    "ds_read_b64_tr_b16 a[36:37], %[rb00] offset:14336\n\t"
    "buffer_load_dwordx4 %[wv2], s[48:51], s46 offen offset:2048 lds\n\t"
    "v_mfma_f32_16x16x32_bf16 v[176:179], a[12:15], a[140:143], v[176:179]\n\t"
    "ds_read_b64_tr_b16 a[38:39], %[rb00] offset:20480\n\t"
    "v_mfma_f32_16x16x32_bf16 v[180:183], a[12:15], a[156:159], v[180:183]\n\t"
    "ds_read_b64_tr_b16 a[40:41], %[rb00] offset:26624\n\t"
    "v_mfma_f32_16x16x32_bf16 v[180:183], a[8:11], a[152:155], v[180:183]\n\t"
    "ds_read_b64_tr_b16 a[42:43], %[rb00] offset:32768\n\t"
    "v_mfma_f32_16x16x32_bf16 v[180:183], a[4:7], a[148:151], v[180:183]\n\t"
    "ds_read_b64_tr_b16 a[44:45], %[rb00] offset:38912\n\t"
    "buffer_load_dwordx4 a[208:211], %[xl], %[srdx1], s41 offen sc1\n\t"
    "v_mfma_f32_16x16x32_bf16 v[180:183], a[0:3], a[144:147], v[180:183]\n\t"
    "ds_read_b64_tr_b16 a[46:47], %[rb00] offset:45056\n\t"
    "s_waitcnt lgkmcnt(8)\n\t"
    "v_mfma_f32_16x16x32_bf16 v[188:191], a[16:19], a[144:147], v[188:191]\n\t"
    "ds_read_b64_tr_b16 a[0:1], %[rb01] offset:2048\n\t"
    "v_mfma_f32_16x16x32_bf16 v[188:191], a[20:23], a[148:151], v[188:191]\n\t"
    "ds_read_b64_tr_b16 a[2:3], %[rb01] offset:8192\n\t"
    "v_mfma_f32_16x16x32_bf16 v[188:191], a[24:27], a[152:155], v[188:191]\n\t"
    "ds_read_b64_tr_b16 a[4:5], %[rb01] offset:14336\n\t"
    "buffer_load_dwordx4 a[212:215], %[xl], %[srdx1], s41 offen offset:1024 sc1\n\t"
    "v_mfma_f32_16x16x32_bf16 v[188:191], a[28:31], a[156:159], v[188:191]\n\t"
    "ds_read_b64_tr_b16 a[6:7], %[rb01] offset:20480\n\t"
    "v_mfma_f32_16x16x32_bf16 v[184:187], a[28:31], a[140:143], v[184:187]\n\t"
    "ds_read_b64_tr_b16 a[8:9], %[rb01] offset:26624\n\t"
    "v_mfma_f32_16x16x32_bf16 v[184:187], a[24:27], a[136:139], v[184:187]\n\t"
    "ds_read_b64_tr_b16 a[10:11], %[rb01] offset:32768\n\t"
    "v_mfma_f32_16x16x32_bf16 v[184:187], a[20:23], a[132:135], v[184:187]\n\t"
    "ds_read_b64_tr_b16 a[12:13], %[rb01] offset:38912\n\t"
    "buffer_load_dwordx4 a[216:219], %[xl], %[srdx1], s41 offen offset:2048 sc1\n\t"
    "v_mfma_f32_16x16x32_bf16 v[184:187], a[16:19], a[128:131], v[184:187]\n\t"
    "ds_read_b64_tr_b16 a[14:15], %[rb01] offset:45056\n\t"
    "s_waitcnt lgkmcnt(8)\n\t"
    "v_mfma_f32_16x16x32_bf16 v[192:195], a[32:35], a[128:131], v[192:195]\n\t"
    "ds_read_b64_tr_b16 a[16:17], %[rb00] offset:2560\n\t"
    "v_mfma_f32_16x16x32_bf16 v[192:195], a[36:39], a[132:135], v[192:195]\n\t"
    "ds_read_b64_tr_b16 a[18:19], %[rb00] offset:8704\n\t"
    "v_mfma_f32_16x16x32_bf16 v[192:195], a[40:43], a[136:139], v[192:195]\n\t"
    "ds_read_b64_tr_b16 a[20:21], %[rb00] offset:14848\n\t"
    "buffer_load_dwordx4 a[220:223], %[xl], %[srdx1], s41 offen offset:3072 sc1\n\t"
    "v_mfma_f32_16x16x32_bf16 v[192:195], a[44:47], a[140:143], v[192:195]\n\t"
    "ds_read_b64_tr_b16 a[22:23], %[rb00] offset:20992\n\t"
    "v_mfma_f32_16x16x32_bf16 v[196:199], a[44:47], a[156:159], v[196:199]\n\t"
    "ds_read_b64_tr_b16 a[24:25], %[rb00] offset:27136\n\t"
    "s_add_u32 m0, %[m0b], 9216\n\t"
    "v_mfma_f32_16x16x32_bf16 v[196:199], a[40:43], a[152:155], v[196:199]\n\t"
    "ds_read_b64_tr_b16 a[26:27], %[rb00] offset:33280\n\t"
    "s_add_u32 s46, s40, 27648\n\t"
    "v_mfma_f32_16x16x32_bf16 v[196:199], a[36:39], a[148:151], v[196:199]\n\t"
    "ds_read_b64_tr_b16 a[28:29], %[rb00] offset:39424\n\t"
    "buffer_load_dwordx4 %[wv0], s[48:51], s46 offen lds\n\t"
    "v_mfma_f32_16x16x32_bf16 v[196:199], a[32:35], a[144:147], v[196:199]\n\t"
    "ds_read_b64_tr_b16 a[30:31], %[rb00] offset:45568\n\t"
    "s_waitcnt lgkmcnt(8)\n\t"
    "v_mfma_f32_16x16x32_bf16 v[204:207], a[0:3], a[144:147], v[204:207]\n\t"
    "ds_read_b64_tr_b16 a[32:33], %[rb01] offset:2560\n\t"
    "ds_read_b64_tr_b16 a[34:35], %[rb01] offset:8704\n\t"
    "v_mfma_f32_16x16x32_bf16 v[204:207], a[4:7], a[148:151], v[204:207]\n\t"
    "ds_read_b64_tr_b16 a[36:37], %[rb01] offset:14848\n\t"
    "ds_read_b64_tr_b16 a[38:39], %[rb01] offset:20992\n\t"
    "v_mfma_f32_16x16x32_bf16 v[204:207], a[8:11], a[152:155], v[204:207]\n\t"
    "ds_read_b64_tr_b16 a[40:41], %[rb01] offset:27136\n\t"
    "ds_read_b64_tr_b16 a[42:43], %[rb01] offset:33280\n\t"
    "v_mfma_f32_16x16x32_bf16 v[204:207], a[12:15], a[156:159], v[204:207]\n\t"
    "ds_read_b64_tr_b16 a[44:45], %[rb01] offset:39424\n\t"
    "ds_read_b64_tr_b16 a[46:47], %[rb01] offset:45568\n\t"
    "v_mfma_f32_16x16x32_bf16 v[200:203], a[12:15], a[140:143], v[200:203]\n\t"
    "v_mfma_f32_16x16x32_bf16 v[200:203], a[8:11], a[136:139], v[200:203]\n\t"
    "v_mfma_f32_16x16x32_bf16 v[200:203], a[4:7], a[132:135], v[200:203]\n\t"
    "buffer_load_dwordx4 %[wv1], s[48:51], s46 offen offset:1024 lds\n\t"
    "v_mfma_f32_16x16x32_bf16 v[200:203], a[0:3], a[128:131], v[200:203]\n\t"
    "s_waitcnt vmcnt(20) lgkmcnt(0)\n\t"
    "s_barrier\n\t"
    "v_mfma_f32_16x16x32_bf16 v[208:211], a[16:19], a[128:131], v[208:211]\n\t"
    "ds_read_b64_tr_b16 a[0:1], %[rb10] offset:0\n\t"
    "v_mfma_f32_16x16x32_bf16 v[208:211], a[20:23], a[132:135], v[208:211]\n\t"
    "buffer_store_dword v246, %[xz], %[srd], %[fho] offen sc1\n\t"
    "ds_read_b64_tr_b16 a[2:3], %[rb10] offset:6144\n\t"
    "v_mfma_f32_16x16x32_bf16 v[208:211], a[24:27], a[136:139], v[208:211]\n\t"
    "ds_read_b64_tr_b16 a[4:5], %[rb10] offset:12288\n\t"
    "buffer_load_dwordx4 %[wv2], s[48:51], s46 offen offset:2048 lds\n\t"
    "v_mfma_f32_16x16x32_bf16 v[208:211], a[28:31], a[140:143], v[208:211]\n\t"
    "ds_read_b64_tr_b16 a[6:7], %[rb10] offset:18432\n\t"
    "v_mfma_f32_16x16x32_bf16 v[212:215], a[28:31], a[156:159], v[212:215]\n\t"
    "ds_read_b64_tr_b16 a[8:9], %[rb10] offset:24576\n\t"
    "v_mfma_f32_16x16x32_bf16 v[212:215], a[24:27], a[152:155], v[212:215]\n\t"
    "ds_read_b64_tr_b16 a[10:11], %[rb10] offset:30720\n\t"
    "v_mfma_f32_16x16x32_bf16 v[212:215], a[20:23], a[148:151], v[212:215]\n\t"
    "ds_read_b64_tr_b16 a[12:13], %[rb10] offset:36864\n\t"
    "v_mfma_f32_16x16x32_bf16 v[212:215], a[16:19], a[144:147], v[212:215]\n\t"
    "ds_read_b64_tr_b16 a[14:15], %[rb10] offset:43008\n\t"
    "s_waitcnt lgkmcnt(8)\n\t"
    "v_mfma_f32_16x16x32_bf16 v[220:223], a[32:35], a[144:147], v[220:223]\n\t"
    "ds_read_b64_tr_b16 a[16:17], %[rb11] offset:0\n\t"
    "v_mfma_f32_16x16x32_bf16 v[220:223], a[36:39], a[148:151], v[220:223]\n\t"
    "ds_read_b64_tr_b16 a[18:19], %[rb11] offset:6144\n\t"
    "v_mfma_f32_16x16x32_bf16 v[220:223], a[40:43], a[152:155], v[220:223]\n\t"
    "ds_read_b64_tr_b16 a[20:21], %[rb11] offset:12288\n\t"
    "s_add_u32 s40, s40, 147456\n\t"
    "v_mfma_f32_16x16x32_bf16 v[220:223], a[44:47], a[156:159], v[220:223]\n\t"
    "ds_read_b64_tr_b16 a[22:23], %[rb11] offset:18432\n\t"
    "s_add_u32 s41, s41, 4096\n\t"
    "v_mfma_f32_16x16x32_bf16 v[216:219], a[44:47], a[140:143], v[216:219]\n\t"
    "ds_read_b64_tr_b16 a[24:25], %[rb11] offset:24576\n\t"
    "s_add_u32 s58, s58, 4096\n\t"
    "v_mfma_f32_16x16x32_bf16 v[216:219], a[40:43], a[136:139], v[216:219]\n\t"
    "ds_read_b64_tr_b16 a[26:27], %[rb11] offset:30720\n\t"
    "s_cmp_eq_u32 s41, s59\n\t"
    "s_cselect_b32 s41, s60, s41\n\t"
    "s_cselect_b32 s40, 0, s40\n\t"
    "v_mfma_f32_16x16x32_bf16 v[216:219], a[36:39], a[132:135], v[216:219]\n\t"
    "ds_read_b64_tr_b16 a[28:29], %[rb11] offset:36864\n\t"
    "v_mfma_f32_16x16x32_bf16 v[216:219], a[32:35], a[128:131], v[216:219]\n\t"
    "ds_read_b64_tr_b16 a[30:31], %[rb11] offset:43008\n\t"
    "s_waitcnt lgkmcnt(8)\n\t"
    "Lpc1_%=:\n\t"
    "s_waitcnt vmcnt(21)\n\t"
    "v_cmp_ne_u32 vcc, 1, v245\n\t"
    "s_cmp_eq_u32 s58, s42\n\t"
    "s_cselect_b64 s[44:45], -1, 0\n\t"
    "s_cselect_b64 s[48:49], s[56:57], s[48:49]\n\t"
    "s_cselect_b32 s40, s61, s40\n\t"
    "s_cselect_b32 s41, s62, s41\n\t"
    "s_cselect_b32 s60, s59, s60\n\t"
    "s_cselect_b32 s59, s63, s59\n\t"
    "s_and_b64 vcc, vcc, s[44:45]\n\t"
    "s_cbranch_vccnz Lhs_%=\n\t"
    "Lhk_%=:\n\t"
    "v_mfma_f32_16x16x32_bf16 v[128:131], a[0:3], a[160:163], v[128:131]\n\t"
    "ds_read_b64_tr_b16 a[32:33], %[rb10] offset:512\n\t"
    "s_add_u32 m0, %[m0b], 49152\n\t"
    "v_mfma_f32_16x16x32_bf16 v[128:131], a[4:7], a[164:167], v[128:131]\n\t"
    "ds_read_b64_tr_b16 a[34:35], %[rb10] offset:6656\n\t"
    "s_add_u32 s46, s40, 0\n\t"
    "v_mfma_f32_16x16x32_bf16 v[128:131], a[8:11], a[168:171], v[128:131]\n\t"
    "ds_read_b64_tr_b16 a[36:37], %[rb10] offset:12800\n\t"
    "buffer_load_dwordx4 %[wv0], s[48:51], s46 offen lds\n\t"
    "v_mfma_f32_16x16x32_bf16 v[128:131], a[12:15], a[172:175], v[128:131]\n\t"
    "ds_read_b64_tr_b16 a[38:39], %[rb10] offset:18944\n\t"
    "v_mfma_f32_16x16x32_bf16 v[132:135], a[12:15], a[188:191], v[132:135]\n\t"
    "ds_read_b64_tr_b16 a[40:41], %[rb10] offset:25088\n\t"
    "v_mfma_f32_16x16x32_bf16 v[132:135], a[8:11], a[184:187], v[132:135]\n\t"
    "ds_read_b64_tr_b16 a[42:43], %[rb10] offset:31232\n\t"
    "v_mfma_f32_16x16x32_bf16 v[132:135], a[4:7], a[180:183], v[132:135]\n\t"
    "ds_read_b64_tr_b16 a[44:45], %[rb10] offset:37376\n\t"
    "buffer_load_dwordx4 %[wv1], s[48:51], s46 offen offset:1024 lds\n\t"
    "v_mfma_f32_16x16x32_bf16 v[132:135], a[0:3], a[176:179], v[132:135]\n\t"
    "ds_read_b64_tr_b16 a[46:47], %[rb10] offset:43520\n\t"
    "s_waitcnt lgkmcnt(8)\n\t"
    "v_mfma_f32_16x16x32_bf16 v[140:143], a[16:19], a[176:179], v[140:143]\n\t"
    "ds_read_b64_tr_b16 a[0:1], %[rb11] offset:512\n\t"
    "v_mfma_f32_16x16x32_bf16 v[140:143], a[20:23], a[180:183], v[140:143]\n\t"
    "ds_read_b64_tr_b16 a[2:3], %[rb11] offset:6656\n\t"
    "v_mfma_f32_16x16x32_bf16 v[140:143], a[24:27], a[184:187], v[140:143]\n\t"
    "ds_read_b64_tr_b16 a[4:5], %[rb11] offset:12800\n\t"
    "buffer_load_dwordx4 %[wv2], s[48:51], s46 offen offset:2048 lds\n\t"
    "v_mfma_f32_16x16x32_bf16 v[140:143], a[28:31], a[188:191], v[140:143]\n\t"
    "ds_read_b64_tr_b16 a[6:7], %[rb11] offset:18944\n\t"
    "v_mfma_f32_16x16x32_bf16 v[136:139], a[28:31], a[172:175], v[136:139]\n\t"
    "ds_read_b64_tr_b16 a[8:9], %[rb11] offset:25088\n\t"
    "v_mfma_f32_16x16x32_bf16 v[136:139], a[24:27], a[168:171], v[136:139]\n\t"
    "ds_read_b64_tr_b16 a[10:11], %[rb11] offset:31232\n\t"
    "v_mfma_f32_16x16x32_bf16 v[136:139], a[20:23], a[164:167], v[136:139]\n\t"
    "ds_read_b64_tr_b16 a[12:13], %[rb11] offset:37376\n\t"
    "buffer_load_dwordx4 a[128:131], %[xl], %[srdx0], s41 offen sc1\n\t"
    "v_mfma_f32_16x16x32_bf16 v[136:139], a[16:19], a[160:163], v[136:139]\n\t"
    "ds_read_b64_tr_b16 a[14:15], %[rb11] offset:43520\n\t"
    "s_waitcnt lgkmcnt(8)\n\t"
    "v_mfma_f32_16x16x32_bf16 v[144:147], a[32:35], a[160:163], v[144:147]\n\t"
    "ds_read_b64_tr_b16 a[16:17], %[rb10] offset:1024\n\t"
    "v_mfma_f32_16x16x32_bf16 v[144:147], a[36:39], a[164:167], v[144:147]\n\t"
    "ds_read_b64_tr_b16 a[18:19], %[rb10] offset:7168\n\t"
    "v_mfma_f32_16x16x32_bf16 v[144:147], a[40:43], a[168:171], v[144:147]\n\t"
    "ds_read_b64_tr_b16 a[20:21], %[rb10] offset:13312\n\t"
    "buffer_load_dwordx4 a[132:135], %[xl], %[srdx0], s41 offen offset:1024 sc1\n\t"
    "v_mfma_f32_16x16x32_bf16 v[144:147], a[44:47], a[172:175], v[144:147]\n\t"
    "ds_read_b64_tr_b16 a[22:23], %[rb10] offset:19456\n\t"
    "v_mfma_f32_16x16x32_bf16 v[148:151], a[44:47], a[188:191], v[148:151]\n\t"
    "ds_read_b64_tr_b16 a[24:25], %[rb10] offset:25600\n\t"
    "v_mfma_f32_16x16x32_bf16 v[148:151], a[40:43], a[184:187], v[148:151]\n\t"
    "ds_read_b64_tr_b16 a[26:27], %[rb10] offset:31744\n\t"
    "v_mfma_f32_16x16x32_bf16 v[148:151], a[36:39], a[180:183], v[148:151]\n\t"
    "ds_read_b64_tr_b16 a[28:29], %[rb10] offset:37888\n\t"
    "buffer_load_dwordx4 a[136:139], %[xl], %[srdx0], s41 offen offset:2048 sc1\n\t"
    "v_mfma_f32_16x16x32_bf16 v[148:151], a[32:35], a[176:179], v[148:151]\n\t"
    "ds_read_b64_tr_b16 a[30:31], %[rb10] offset:44032\n\t"
    "s_waitcnt lgkmcnt(8)\n\t"
    "v_mfma_f32_16x16x32_bf16 v[156:159], a[0:3], a[176:179], v[156:159]\n\t"
    "ds_read_b64_tr_b16 a[32:33], %[rb11] offset:1024\n\t"
    "v_mfma_f32_16x16x32_bf16 v[156:159], a[4:7], a[180:183], v[156:159]\n\t"
    "ds_read_b64_tr_b16 a[34:35], %[rb11] offset:7168\n\t"
    "v_mfma_f32_16x16x32_bf16 v[156:159], a[8:11], a[184:187], v[156:159]\n\t"
    "ds_read_b64_tr_b16 a[36:37], %[rb11] offset:13312\n\t"
    "buffer_load_dwordx4 a[140:143], %[xl], %[srdx0], s41 offen offset:3072 sc1\n\t"
    "v_mfma_f32_16x16x32_bf16 v[156:159], a[12:15], a[188:191], v[156:159]\n\t"
    "ds_read_b64_tr_b16 a[38:39], %[rb11] offset:19456\n\t"
    "v_mfma_f32_16x16x32_bf16 v[152:155], a[12:15], a[172:175], v[152:155]\n\t"
    "ds_read_b64_tr_b16 a[40:41], %[rb11] offset:25600\n\t"
    "s_add_u32 m0, %[m0b], 52224\n\t"
    "v_mfma_f32_16x16x32_bf16 v[152:155], a[8:11], a[168:171], v[152:155]\n\t"
    "ds_read_b64_tr_b16 a[42:43], %[rb11] offset:31744\n\t"
    "s_add_u32 s46, s40, 9216\n\t"
    "v_mfma_f32_16x16x32_bf16 v[152:155], a[4:7], a[164:167], v[152:155]\n\t"
    "ds_read_b64_tr_b16 a[44:45], %[rb11] offset:37888\n\t"
    "buffer_load_dwordx4 %[wv0], s[48:51], s46 offen lds\n\t"
    "v_mfma_f32_16x16x32_bf16 v[152:155], a[0:3], a[160:163], v[152:155]\n\t"
    "ds_read_b64_tr_b16 a[46:47], %[rb11] offset:44032\n\t"
    "s_waitcnt lgkmcnt(8)\n\t"
    "v_mfma_f32_16x16x32_bf16 v[160:163], a[16:19], a[160:163], v[160:163]\n\t"
    "ds_read_b64_tr_b16 a[0:1], %[rb10] offset:1536\n\t"
    "v_mfma_f32_16x16x32_bf16 v[160:163], a[20:23], a[164:167], v[160:163]\n\t"
    "ds_read_b64_tr_b16 a[2:3], %[rb10] offset:7680\n\t"
    "v_mfma_f32_16x16x32_bf16 v[160:163], a[24:27], a[168:171], v[160:163]\n\t"
    "ds_read_b64_tr_b16 a[4:5], %[rb10] offset:13824\n\t"
    "buffer_load_dwordx4 %[wv1], s[48:51], s46 offen offset:1024 lds\n\t"
    "v_mfma_f32_16x16x32_bf16 v[160:163], a[28:31], a[172:175], v[160:163]\n\t"
    "ds_read_b64_tr_b16 a[6:7], %[rb10] offset:19968\n\t"
    "v_mfma_f32_16x16x32_bf16 v[164:167], a[28:31], a[188:191], v[164:167]\n\t"
    "ds_read_b64_tr_b16 a[8:9], %[rb10] offset:26112\n\t"
    "v_mfma_f32_16x16x32_bf16 v[164:167], a[24:27], a[184:187], v[164:167]\n\t"
    "ds_read_b64_tr_b16 a[10:11], %[rb10] offset:32256\n\t"
    "v_mfma_f32_16x16x32_bf16 v[164:167], a[20:23], a[180:183], v[164:167]\n\t"
    "ds_read_b64_tr_b16 a[12:13], %[rb10] offset:38400\n\t"
    "buffer_load_dwordx4 %[wv2], s[48:51], s46 offen offset:2048 lds\n\t"
    "v_mfma_f32_16x16x32_bf16 v[164:167], a[16:19], a[176:179], v[164:167]\n\t"
    "ds_read_b64_tr_b16 a[14:15], %[rb10] offset:44544\n\t"
    "s_waitcnt lgkmcnt(8)\n\t"
    "v_mfma_f32_16x16x32_bf16 v[172:175], a[32:35], a[176:179], v[172:175]\n\t"
    "ds_read_b64_tr_b16 a[16:17], %[rb11] offset:1536\n\t"
    "s_add_u32 m0, %[m0b], 55296\n\t"
    "v_mfma_f32_16x16x32_bf16 v[172:175], a[36:39], a[180:183], v[172:175]\n\t"
    "ds_read_b64_tr_b16 a[18:19], %[rb11] offset:7680\n\t"
    "s_add_u32 s46, s40, 18432\n\t"
    "v_mfma_f32_16x16x32_bf16 v[172:175], a[40:43], a[184:187], v[172:175]\n\t"
    "ds_read_b64_tr_b16 a[20:21], %[rb11] offset:13824\n\t"
    "buffer_load_dwordx4 %[wv0], s[48:51], s46 offen lds\n\t"
    "v_mfma_f32_16x16x32_bf16 v[172:175], a[44:47], a[188:191], v[172:175]\n\t"
    "ds_read_b64_tr_b16 a[22:23], %[rb11] offset:19968\n\t"
    "v_mfma_f32_16x16x32_bf16 v[168:171], a[44:47], a[172:175], v[168:171]\n\t"
    "ds_read_b64_tr_b16 a[24:25], %[rb11] offset:26112\n\t"
    "v_mfma_f32_16x16x32_bf16 v[168:171], a[40:43], a[168:171], v[168:171]\n\t"
    "ds_read_b64_tr_b16 a[26:27], %[rb11] offset:32256\n\t"
    "v_mfma_f32_16x16x32_bf16 v[168:171], a[36:39], a[164:167], v[168:171]\n\t"
    "ds_read_b64_tr_b16 a[28:29], %[rb11] offset:38400\n\t"
    "buffer_load_dwordx4 %[wv1], s[48:51], s46 offen offset:1024 lds\n\t"
    "v_mfma_f32_16x16x32_bf16 v[168:171], a[32:35], a[160:163], v[168:171]\n\t"
    "ds_read_b64_tr_b16 a[30:31], %[rb11] offset:44544\n\t"
    "s_waitcnt lgkmcnt(8)\n\t"
    "v_mfma_f32_16x16x32_bf16 v[176:179], a[0:3], a[160:163], v[176:179]\n\t"
    "ds_read_b64_tr_b16 a[32:33], %[rb10] offset:2048\n\t"
    "v_mfma_f32_16x16x32_bf16 v[176:179], a[4:7], a[164:167], v[176:179]\n\t"
    "ds_read_b64_tr_b16 a[34:35], %[rb10] offset:8192\n\t"
    "v_mfma_f32_16x16x32_bf16 v[176:179], a[8:11], a[168:171], v[176:179]\n\t"
    "ds_read_b64_tr_b16 a[36:37], %[rb10] offset:14336\n\t"
    "buffer_load_dwordx4 %[wv2], s[48:51], s46 offen offset:2048 lds\n\t"
    "v_mfma_f32_16x16x32_bf16 v[176:179], a[12:15], a[172:175], v[176:179]\n\t"
    "ds_read_b64_tr_b16 a[38:39], %[rb10] offset:20480\n\t"
    "v_mfma_f32_16x16x32_bf16 v[180:183], a[12:15], a[188:191], v[180:183]\n\t"
    "ds_read_b64_tr_b16 a[40:41], %[rb10] offset:26624\n\t"
    "v_mfma_f32_16x16x32_bf16 v[180:183], a[8:11], a[184:187], v[180:183]\n\t"
    "ds_read_b64_tr_b16 a[42:43], %[rb10] offset:32768\n\t"
    "v_mfma_f32_16x16x32_bf16 v[180:183], a[4:7], a[180:183], v[180:183]\n\t"
    "ds_read_b64_tr_b16 a[44:45], %[rb10] offset:38912\n\t"
    "buffer_load_dwordx4 a[144:147], %[xl], %[srdx1], s41 offen sc1\n\t"
    "v_mfma_f32_16x16x32_bf16 v[180:183], a[0:3], a[176:179], v[180:183]\n\t"
    "ds_read_b64_tr_b16 a[46:47], %[rb10] offset:45056\n\t"
    "s_waitcnt lgkmcnt(8)\n\t"
    "v_mfma_f32_16x16x32_bf16 v[188:191], a[16:19], a[176:179], v[188:191]\n\t"
    "ds_read_b64_tr_b16 a[0:1], %[rb11] offset:2048\n\t"
    "v_mfma_f32_16x16x32_bf16 v[188:191], a[20:23], a[180:183], v[188:191]\n\t"
    "ds_read_b64_tr_b16 a[2:3], %[rb11] offset:8192\n\t"
    "v_mfma_f32_16x16x32_bf16 v[188:191], a[24:27], a[184:187], v[188:191]\n\t"
    "ds_read_b64_tr_b16 a[4:5], %[rb11] offset:14336\n\t"
    "buffer_load_dwordx4 a[148:151], %[xl], %[srdx1], s41 offen offset:1024 sc1\n\t"
    "v_mfma_f32_16x16x32_bf16 v[188:191], a[28:31], a[188:191], v[188:191]\n\t"
    "ds_read_b64_tr_b16 a[6:7], %[rb11] offset:20480\n\t"
    "v_mfma_f32_16x16x32_bf16 v[184:187], a[28:31], a[172:175], v[184:187]\n\t"
    "ds_read_b64_tr_b16 a[8:9], %[rb11] offset:26624\n\t"
    "v_mfma_f32_16x16x32_bf16 v[184:187], a[24:27], a[168:171], v[184:187]\n\t"
    "ds_read_b64_tr_b16 a[10:11], %[rb11] offset:32768\n\t"
    "v_mfma_f32_16x16x32_bf16 v[184:187], a[20:23], a[164:167], v[184:187]\n\t"
    "ds_read_b64_tr_b16 a[12:13], %[rb11] offset:38912\n\t"
    "buffer_load_dwordx4 a[152:155], %[xl], %[srdx1], s41 offen offset:2048 sc1\n\t"
    "v_mfma_f32_16x16x32_bf16 v[184:187], a[16:19], a[160:163], v[184:187]\n\t"
    "ds_read_b64_tr_b16 a[14:15], %[rb11] offset:45056\n\t"
    "s_waitcnt lgkmcnt(8)\n\t"
    "v_mfma_f32_16x16x32_bf16 v[192:195], a[32:35], a[160:163], v[192:195]\n\t"
    "ds_read_b64_tr_b16 a[16:17], %[rb10] offset:2560\n\t"
    "v_mfma_f32_16x16x32_bf16 v[192:195], a[36:39], a[164:167], v[192:195]\n\t"
    "ds_read_b64_tr_b16 a[18:19], %[rb10] offset:8704\n\t"
    "v_mfma_f32_16x16x32_bf16 v[192:195], a[40:43], a[168:171], v[192:195]\n\t"
    "ds_read_b64_tr_b16 a[20:21], %[rb10] offset:14848\n\t"
    "buffer_load_dwordx4 a[156:159], %[xl], %[srdx1], s41 offen offset:3072 sc1\n\t"
    "v_mfma_f32_16x16x32_bf16 v[192:195], a[44:47], a[172:175], v[192:195]\n\t"
    "ds_read_b64_tr_b16 a[22:23], %[rb10] offset:20992\n\t"
    "v_mfma_f32_16x16x32_bf16 v[196:199], a[44:47], a[188:191], v[196:199]\n\t"
    "ds_read_b64_tr_b16 a[24:25], %[rb10] offset:27136\n\t"
    "s_add_u32 m0, %[m0b], 58368\n\t"
    "v_mfma_f32_16x16x32_bf16 v[196:199], a[40:43], a[184:187], v[196:199]\n\t"
    "ds_read_b64_tr_b16 a[26:27], %[rb10] offset:33280\n\t"
    "s_add_u32 s46, s40, 27648\n\t"
    "v_mfma_f32_16x16x32_bf16 v[196:199], a[36:39], a[180:183], v[196:199]\n\t"
    "ds_read_b64_tr_b16 a[28:29], %[rb10] offset:39424\n\t"
    "buffer_load_dwordx4 %[wv0], s[48:51], s46 offen lds\n\t"
    "v_mfma_f32_16x16x32_bf16 v[196:199], a[32:35], a[176:179], v[196:199]\n\t"
    "ds_read_b64_tr_b16 a[30:31], %[rb10] offset:45568\n\t"
    "s_waitcnt lgkmcnt(8)\n\t"
    "v_mfma_f32_16x16x32_bf16 v[204:207], a[0:3], a[176:179], v[204:207]\n\t"
    "ds_read_b64_tr_b16 a[32:33], %[rb11] offset:2560\n\t"
    "ds_read_b64_tr_b16 a[34:35], %[rb11] offset:8704\n\t"
    "v_mfma_f32_16x16x32_bf16 v[204:207], a[4:7], a[180:183], v[204:207]\n\t"
    "ds_read_b64_tr_b16 a[36:37], %[rb11] offset:14848\n\t"
    "ds_read_b64_tr_b16 a[38:39], %[rb11] offset:20992\n\t"
    "v_mfma_f32_16x16x32_bf16 v[204:207], a[8:11], a[184:187], v[204:207]\n\t"
    "ds_read_b64_tr_b16 a[40:41], %[rb11] offset:27136\n\t"
    "ds_read_b64_tr_b16 a[42:43], %[rb11] offset:33280\n\t"
    "v_mfma_f32_16x16x32_bf16 v[204:207], a[12:15], a[188:191], v[204:207]\n\t"
    "ds_read_b64_tr_b16 a[44:45], %[rb11] offset:39424\n\t"
    "ds_read_b64_tr_b16 a[46:47], %[rb11] offset:45568\n\t"
    "v_mfma_f32_16x16x32_bf16 v[200:203], a[12:15], a[172:175], v[200:203]\n\t"
    "v_mfma_f32_16x16x32_bf16 v[200:203], a[8:11], a[168:171], v[200:203]\n\t"
    "v_mfma_f32_16x16x32_bf16 v[200:203], a[4:7], a[164:167], v[200:203]\n\t"
    "buffer_load_dwordx4 %[wv1], s[48:51], s46 offen offset:1024 lds\n\t"
    "v_mfma_f32_16x16x32_bf16 v[200:203], a[0:3], a[160:163], v[200:203]\n\t"
    "s_waitcnt vmcnt(19) lgkmcnt(0)\n\t"
    "s_barrier\n\t"
    "v_mfma_f32_16x16x32_bf16 v[208:211], a[16:19], a[160:163], v[208:211]\n\t"
    "ds_read_b64_tr_b16 a[0:1], %[rb20] offset:0\n\t"
    "v_mfma_f32_16x16x32_bf16 v[208:211], a[20:23], a[164:167], v[208:211]\n\t"
    "ds_read_b64_tr_b16 a[2:3], %[rb20] offset:6144\n\t"
    "v_mfma_f32_16x16x32_bf16 v[208:211], a[24:27], a[168:171], v[208:211]\n\t"
    "ds_read_b64_tr_b16 a[4:5], %[rb20] offset:12288\n\t"
    "buffer_load_dwordx4 %[wv2], s[48:51], s46 offen offset:2048 lds\n\t"
    "v_mfma_f32_16x16x32_bf16 v[208:211], a[28:31], a[172:175], v[208:211]\n\t"
    "ds_read_b64_tr_b16 a[6:7], %[rb20] offset:18432\n\t"
    "v_mfma_f32_16x16x32_bf16 v[212:215], a[28:31], a[188:191], v[212:215]\n\t"
    "ds_read_b64_tr_b16 a[8:9], %[rb20] offset:24576\n\t"
    "v_mfma_f32_16x16x32_bf16 v[212:215], a[24:27], a[184:187], v[212:215]\n\t"
    "ds_read_b64_tr_b16 a[10:11], %[rb20] offset:30720\n\t"
    "v_mfma_f32_16x16x32_bf16 v[212:215], a[20:23], a[180:183], v[212:215]\n\t"
    "ds_read_b64_tr_b16 a[12:13], %[rb20] offset:36864\n\t"
    "v_mfma_f32_16x16x32_bf16 v[212:215], a[16:19], a[176:179], v[212:215]\n\t"
    "ds_read_b64_tr_b16 a[14:15], %[rb20] offset:43008\n\t"
    "s_waitcnt lgkmcnt(8)\n\t"
    "v_mfma_f32_16x16x32_bf16 v[220:223], a[32:35], a[176:179], v[220:223]\n\t"
    "ds_read_b64_tr_b16 a[16:17], %[rb21] offset:0\n\t"
    "v_mfma_f32_16x16x32_bf16 v[220:223], a[36:39], a[180:183], v[220:223]\n\t"
    "ds_read_b64_tr_b16 a[18:19], %[rb21] offset:6144\n\t"
    "v_mfma_f32_16x16x32_bf16 v[220:223], a[40:43], a[184:187], v[220:223]\n\t"
    "ds_read_b64_tr_b16 a[20:21], %[rb21] offset:12288\n\t"
    "s_add_u32 s40, s40, 147456\n\t"
    "v_mfma_f32_16x16x32_bf16 v[220:223], a[44:47], a[188:191], v[220:223]\n\t"
    "ds_read_b64_tr_b16 a[22:23], %[rb21] offset:18432\n\t"
    "s_add_u32 s41, s41, 4096\n\t"
    "v_mfma_f32_16x16x32_bf16 v[216:219], a[44:47], a[172:175], v[216:219]\n\t"
    "ds_read_b64_tr_b16 a[24:25], %[rb21] offset:24576\n\t"
    "s_add_u32 s58, s58, 4096\n\t"
    "v_mfma_f32_16x16x32_bf16 v[216:219], a[40:43], a[168:171], v[216:219]\n\t"
    "ds_read_b64_tr_b16 a[26:27], %[rb21] offset:30720\n\t"
    "s_cmp_eq_u32 s41, s59\n\t"
    "s_cselect_b32 s41, s60, s41\n\t"
    "s_cselect_b32 s40, 0, s40\n\t"
    "v_mfma_f32_16x16x32_bf16 v[216:219], a[36:39], a[164:167], v[216:219]\n\t"
    "ds_read_b64_tr_b16 a[28:29], %[rb21] offset:36864\n\t"
    "v_mfma_f32_16x16x32_bf16 v[216:219], a[32:35], a[160:163], v[216:219]\n\t"
    "ds_read_b64_tr_b16 a[30:31], %[rb21] offset:43008\n\t"
    "s_waitcnt lgkmcnt(8)\n\t"
    "Lpc2_%=:\n\t"
    "v_mfma_f32_16x16x32_bf16 v[128:131], a[0:3], a[192:195], v[128:131]\n\t"
    "ds_read_b64_tr_b16 a[32:33], %[rb20] offset:512\n\t"
    "s_add_u32 m0, %[m0b], 98304\n\t"
    "v_mfma_f32_16x16x32_bf16 v[128:131], a[4:7], a[196:199], v[128:131]\n\t"
    "ds_read_b64_tr_b16 a[34:35], %[rb20] offset:6656\n\t"
    "s_add_u32 s46, s40, 0\n\t"
    "v_mfma_f32_16x16x32_bf16 v[128:131], a[8:11], a[200:203], v[128:131]\n\t"
    "ds_read_b64_tr_b16 a[36:37], %[rb20] offset:12800\n\t"
    "buffer_load_dwordx4 %[wv0], s[48:51], s46 offen lds\n\t"
    "v_mfma_f32_16x16x32_bf16 v[128:131], a[12:15], a[204:207], v[128:131]\n\t"
    "ds_read_b64_tr_b16 a[38:39], %[rb20] offset:18944\n\t"
    "v_mfma_f32_16x16x32_bf16 v[132:135], a[12:15], a[220:223], v[132:135]\n\t"
    "ds_read_b64_tr_b16 a[40:41], %[rb20] offset:25088\n\t"
    "v_mfma_f32_16x16x32_bf16 v[132:135], a[8:11], a[216:219], v[132:135]\n\t"
    "ds_read_b64_tr_b16 a[42:43], %[rb20] offset:31232\n\t"
    "v_mfma_f32_16x16x32_bf16 v[132:135], a[4:7], a[212:215], v[132:135]\n\t"
    "ds_read_b64_tr_b16 a[44:45], %[rb20] offset:37376\n\t"
    "buffer_load_dwordx4 %[wv1], s[48:51], s46 offen offset:1024 lds\n\t"
    "v_mfma_f32_16x16x32_bf16 v[132:135], a[0:3], a[208:211], v[132:135]\n\t"
    "ds_read_b64_tr_b16 a[46:47], %[rb20] offset:43520\n\t"
    "s_waitcnt lgkmcnt(8)\n\t"
    "v_mfma_f32_16x16x32_bf16 v[140:143], a[16:19], a[208:211], v[140:143]\n\t"
    "ds_read_b64_tr_b16 a[0:1], %[rb21] offset:512\n\t"
    "v_mfma_f32_16x16x32_bf16 v[140:143], a[20:23], a[212:215], v[140:143]\n\t"
    "ds_read_b64_tr_b16 a[2:3], %[rb21] offset:6656\n\t"
    "v_mfma_f32_16x16x32_bf16 v[140:143], a[24:27], a[216:219], v[140:143]\n\t"
    "ds_read_b64_tr_b16 a[4:5], %[rb21] offset:12800\n\t"
    "buffer_load_dwordx4 %[wv2], s[48:51], s46 offen offset:2048 lds\n\t"
    "v_mfma_f32_16x16x32_bf16 v[140:143], a[28:31], a[220:223], v[140:143]\n\t"
    "ds_read_b64_tr_b16 a[6:7], %[rb21] offset:18944\n\t"
    "v_mfma_f32_16x16x32_bf16 v[136:139], a[28:31], a[204:207], v[136:139]\n\t"
    "ds_read_b64_tr_b16 a[8:9], %[rb21] offset:25088\n\t"
    "v_mfma_f32_16x16x32_bf16 v[136:139], a[24:27], a[200:203], v[136:139]\n\t"
    "ds_read_b64_tr_b16 a[10:11], %[rb21] offset:31232\n\t"
    "v_mfma_f32_16x16x32_bf16 v[136:139], a[20:23], a[196:199], v[136:139]\n\t"
    "ds_read_b64_tr_b16 a[12:13], %[rb21] offset:37376\n\t"
    "buffer_load_dwordx4 a[160:163], %[xl], %[srdx0], s41 offen sc1\n\t"
    "v_mfma_f32_16x16x32_bf16 v[136:139], a[16:19], a[192:195], v[136:139]\n\t"
    "ds_read_b64_tr_b16 a[14:15], %[rb21] offset:43520\n\t"
    "s_waitcnt lgkmcnt(8)\n\t"
    "v_mfma_f32_16x16x32_bf16 v[144:147], a[32:35], a[192:195], v[144:147]\n\t"
    "ds_read_b64_tr_b16 a[16:17], %[rb20] offset:1024\n\t"
    "v_mfma_f32_16x16x32_bf16 v[144:147], a[36:39], a[196:199], v[144:147]\n\t"
    "ds_read_b64_tr_b16 a[18:19], %[rb20] offset:7168\n\t"
    "v_mfma_f32_16x16x32_bf16 v[144:147], a[40:43], a[200:203], v[144:147]\n\t"
    "ds_read_b64_tr_b16 a[20:21], %[rb20] offset:13312\n\t"
    "buffer_load_dwordx4 a[164:167], %[xl], %[srdx0], s41 offen offset:1024 sc1\n\t"
    "v_mfma_f32_16x16x32_bf16 v[144:147], a[44:47], a[204:207], v[144:147]\n\t"
    "ds_read_b64_tr_b16 a[22:23], %[rb20] offset:19456\n\t"
    "v_mfma_f32_16x16x32_bf16 v[148:151], a[44:47], a[220:223], v[148:151]\n\t"
    "ds_read_b64_tr_b16 a[24:25], %[rb20] offset:25600\n\t"
    "v_mfma_f32_16x16x32_bf16 v[148:151], a[40:43], a[216:219], v[148:151]\n\t"
    "ds_read_b64_tr_b16 a[26:27], %[rb20] offset:31744\n\t"
    "v_mfma_f32_16x16x32_bf16 v[148:151], a[36:39], a[212:215], v[148:151]\n\t"
    "ds_read_b64_tr_b16 a[28:29], %[rb20] offset:37888\n\t"
    "buffer_load_dwordx4 a[168:171], %[xl], %[srdx0], s41 offen offset:2048 sc1\n\t"
    "v_mfma_f32_16x16x32_bf16 v[148:151], a[32:35], a[208:211], v[148:151]\n\t"
    "ds_read_b64_tr_b16 a[30:31], %[rb20] offset:44032\n\t"
    "s_waitcnt lgkmcnt(8)\n\t"
    "v_mfma_f32_16x16x32_bf16 v[156:159], a[0:3], a[208:211], v[156:159]\n\t"
    "ds_read_b64_tr_b16 a[32:33], %[rb21] offset:1024\n\t"
    "v_mfma_f32_16x16x32_bf16 v[156:159], a[4:7], a[212:215], v[156:159]\n\t"
    "ds_read_b64_tr_b16 a[34:35], %[rb21] offset:7168\n\t"
    "v_mfma_f32_16x16x32_bf16 v[156:159], a[8:11], a[216:219], v[156:159]\n\t"
    "ds_read_b64_tr_b16 a[36:37], %[rb21] offset:13312\n\t"
    "buffer_load_dwordx4 a[172:175], %[xl], %[srdx0], s41 offen offset:3072 sc1\n\t"
    "v_mfma_f32_16x16x32_bf16 v[156:159], a[12:15], a[220:223], v[156:159]\n\t"
    "ds_read_b64_tr_b16 a[38:39], %[rb21] offset:19456\n\t"
    "v_mfma_f32_16x16x32_bf16 v[152:155], a[12:15], a[204:207], v[152:155]\n\t"
    "ds_read_b64_tr_b16 a[40:41], %[rb21] offset:25600\n\t"
    "s_add_u32 m0, %[m0b], 101376\n\t"
    "v_mfma_f32_16x16x32_bf16 v[152:155], a[8:11], a[200:203], v[152:155]\n\t"
    "ds_read_b64_tr_b16 a[42:43], %[rb21] offset:31744\n\t"
    "s_add_u32 s46, s40, 9216\n\t"
    "v_mfma_f32_16x16x32_bf16 v[152:155], a[4:7], a[196:199], v[152:155]\n\t"
    "ds_read_b64_tr_b16 a[44:45], %[rb21] offset:37888\n\t"
    "buffer_load_dwordx4 %[wv0], s[48:51], s46 offen lds\n\t"
    "v_mfma_f32_16x16x32_bf16 v[152:155], a[0:3], a[192:195], v[152:155]\n\t"
    "ds_read_b64_tr_b16 a[46:47], %[rb21] offset:44032\n\t"
    "s_waitcnt lgkmcnt(8)\n\t"
    "v_mfma_f32_16x16x32_bf16 v[160:163], a[16:19], a[192:195], v[160:163]\n\t"
    "ds_read_b64_tr_b16 a[0:1], %[rb20] offset:1536\n\t"
    "v_mfma_f32_16x16x32_bf16 v[160:163], a[20:23], a[196:199], v[160:163]\n\t"
    "ds_read_b64_tr_b16 a[2:3], %[rb20] offset:7680\n\t"
    "v_mfma_f32_16x16x32_bf16 v[160:163], a[24:27], a[200:203], v[160:163]\n\t"
    "ds_read_b64_tr_b16 a[4:5], %[rb20] offset:13824\n\t"
    "buffer_load_dwordx4 %[wv1], s[48:51], s46 offen offset:1024 lds\n\t"
    "v_mfma_f32_16x16x32_bf16 v[160:163], a[28:31], a[204:207], v[160:163]\n\t"
    "ds_read_b64_tr_b16 a[6:7], %[rb20] offset:19968\n\t"
    "v_mfma_f32_16x16x32_bf16 v[164:167], a[28:31], a[220:223], v[164:167]\n\t"
    "ds_read_b64_tr_b16 a[8:9], %[rb20] offset:26112\n\t"
    "v_mfma_f32_16x16x32_bf16 v[164:167], a[24:27], a[216:219], v[164:167]\n\t"
    "ds_read_b64_tr_b16 a[10:11], %[rb20] offset:32256\n\t"
    "v_mfma_f32_16x16x32_bf16 v[164:167], a[20:23], a[212:215], v[164:167]\n\t"
    "ds_read_b64_tr_b16 a[12:13], %[rb20] offset:38400\n\t"
    "buffer_load_dwordx4 %[wv2], s[48:51], s46 offen offset:2048 lds\n\t"
    "v_mfma_f32_16x16x32_bf16 v[164:167], a[16:19], a[208:211], v[164:167]\n\t"
    "ds_read_b64_tr_b16 a[14:15], %[rb20] offset:44544\n\t"
    "s_waitcnt lgkmcnt(8)\n\t"
    "v_mfma_f32_16x16x32_bf16 v[172:175], a[32:35], a[208:211], v[172:175]\n\t"
    "ds_read_b64_tr_b16 a[16:17], %[rb21] offset:1536\n\t"
    "s_add_u32 m0, %[m0b], 104448\n\t"
    "v_mfma_f32_16x16x32_bf16 v[172:175], a[36:39], a[212:215], v[172:175]\n\t"
    "ds_read_b64_tr_b16 a[18:19], %[rb21] offset:7680\n\t"
    "s_add_u32 s46, s40, 18432\n\t"
    "v_mfma_f32_16x16x32_bf16 v[172:175], a[40:43], a[216:219], v[172:175]\n\t"
    "ds_read_b64_tr_b16 a[20:21], %[rb21] offset:13824\n\t"
    "buffer_load_dwordx4 %[wv0], s[48:51], s46 offen lds\n\t"
    "v_mfma_f32_16x16x32_bf16 v[172:175], a[44:47], a[220:223], v[172:175]\n\t"
    "ds_read_b64_tr_b16 a[22:23], %[rb21] offset:19968\n\t"
    "v_mfma_f32_16x16x32_bf16 v[168:171], a[44:47], a[204:207], v[168:171]\n\t"
    "ds_read_b64_tr_b16 a[24:25], %[rb21] offset:26112\n\t"
    "v_mfma_f32_16x16x32_bf16 v[168:171], a[40:43], a[200:203], v[168:171]\n\t"
    "ds_read_b64_tr_b16 a[26:27], %[rb21] offset:32256\n\t"
    "v_mfma_f32_16x16x32_bf16 v[168:171], a[36:39], a[196:199], v[168:171]\n\t"
    "ds_read_b64_tr_b16 a[28:29], %[rb21] offset:38400\n\t"
    "buffer_load_dwordx4 %[wv1], s[48:51], s46 offen offset:1024 lds\n\t"
    "v_mfma_f32_16x16x32_bf16 v[168:171], a[32:35], a[192:195], v[168:171]\n\t"
    "ds_read_b64_tr_b16 a[30:31], %[rb21] offset:44544\n\t"
    "s_waitcnt lgkmcnt(8)\n\t"
    "v_mfma_f32_16x16x32_bf16 v[176:179], a[0:3], a[192:195], v[176:179]\n\t"
    "ds_read_b64_tr_b16 a[32:33], %[rb20] offset:2048\n\t"
    "v_mfma_f32_16x16x32_bf16 v[176:179], a[4:7], a[196:199], v[176:179]\n\t"
    "ds_read_b64_tr_b16 a[34:35], %[rb20] offset:8192\n\t"
    "v_mfma_f32_16x16x32_bf16 v[176:179], a[8:11], a[200:203], v[176:179]\n\t"
    "ds_read_b64_tr_b16 a[36:37], %[rb20] offset:14336\n\t"
    "buffer_load_dwordx4 %[wv2], s[48:51], s46 offen offset:2048 lds\n\t"
    "v_mfma_f32_16x16x32_bf16 v[176:179], a[12:15], a[204:207], v[176:179]\n\t"
    "ds_read_b64_tr_b16 a[38:39], %[rb20] offset:20480\n\t"
    "v_mfma_f32_16x16x32_bf16 v[180:183], a[12:15], a[220:223], v[180:183]\n\t"
    "ds_read_b64_tr_b16 a[40:41], %[rb20] offset:26624\n\t"
    "v_mfma_f32_16x16x32_bf16 v[180:183], a[8:11], a[216:219], v[180:183]\n\t"
    "ds_read_b64_tr_b16 a[42:43], %[rb20] offset:32768\n\t"
    "v_mfma_f32_16x16x32_bf16 v[180:183], a[4:7], a[212:215], v[180:183]\n\t"
    "ds_read_b64_tr_b16 a[44:45], %[rb20] offset:38912\n\t"
    "buffer_load_dwordx4 a[176:179], %[xl], %[srdx1], s41 offen sc1\n\t"
    "v_mfma_f32_16x16x32_bf16 v[180:183], a[0:3], a[208:211], v[180:183]\n\t"
    "ds_read_b64_tr_b16 a[46:47], %[rb20] offset:45056\n\t"
    "s_waitcnt lgkmcnt(8)\n\t"
    "v_mfma_f32_16x16x32_bf16 v[188:191], a[16:19], a[208:211], v[188:191]\n\t"
    "ds_read_b64_tr_b16 a[0:1], %[rb21] offset:2048\n\t"
    "v_mfma_f32_16x16x32_bf16 v[188:191], a[20:23], a[212:215], v[188:191]\n\t"
    "ds_read_b64_tr_b16 a[2:3], %[rb21] offset:8192\n\t"
    "v_mfma_f32_16x16x32_bf16 v[188:191], a[24:27], a[216:219], v[188:191]\n\t"
    "ds_read_b64_tr_b16 a[4:5], %[rb21] offset:14336\n\t"
    "buffer_load_dwordx4 a[180:183], %[xl], %[srdx1], s41 offen offset:1024 sc1\n\t"
    "v_mfma_f32_16x16x32_bf16 v[188:191], a[28:31], a[220:223], v[188:191]\n\t"
    "ds_read_b64_tr_b16 a[6:7], %[rb21] offset:20480\n\t"
    "v_mfma_f32_16x16x32_bf16 v[184:187], a[28:31], a[204:207], v[184:187]\n\t"
    "ds_read_b64_tr_b16 a[8:9], %[rb21] offset:26624\n\t"
    "v_mfma_f32_16x16x32_bf16 v[184:187], a[24:27], a[200:203], v[184:187]\n\t"
    "ds_read_b64_tr_b16 a[10:11], %[rb21] offset:32768\n\t"
    "v_mfma_f32_16x16x32_bf16 v[184:187], a[20:23], a[196:199], v[184:187]\n\t"
    "ds_read_b64_tr_b16 a[12:13], %[rb21] offset:38912\n\t"
    "buffer_load_dwordx4 a[184:187], %[xl], %[srdx1], s41 offen offset:2048 sc1\n\t"
    "v_mfma_f32_16x16x32_bf16 v[184:187], a[16:19], a[192:195], v[184:187]\n\t"
    "ds_read_b64_tr_b16 a[14:15], %[rb21] offset:45056\n\t"
    "s_waitcnt lgkmcnt(8)\n\t"
    "v_mfma_f32_16x16x32_bf16 v[192:195], a[32:35], a[192:195], v[192:195]\n\t"
    "ds_read_b64_tr_b16 a[16:17], %[rb20] offset:2560\n\t"
    "v_mfma_f32_16x16x32_bf16 v[192:195], a[36:39], a[196:199], v[192:195]\n\t"
    "ds_read_b64_tr_b16 a[18:19], %[rb20] offset:8704\n\t"
    "v_mfma_f32_16x16x32_bf16 v[192:195], a[40:43], a[200:203], v[192:195]\n\t"
    "ds_read_b64_tr_b16 a[20:21], %[rb20] offset:14848\n\t"
    "buffer_load_dwordx4 a[188:191], %[xl], %[srdx1], s41 offen offset:3072 sc1\n\t"
    "v_mfma_f32_16x16x32_bf16 v[192:195], a[44:47], a[204:207], v[192:195]\n\t"
    "ds_read_b64_tr_b16 a[22:23], %[rb20] offset:20992\n\t"
    "v_mfma_f32_16x16x32_bf16 v[196:199], a[44:47], a[220:223], v[196:199]\n\t"
    "ds_read_b64_tr_b16 a[24:25], %[rb20] offset:27136\n\t"
    "s_add_u32 m0, %[m0b], 107520\n\t"
    "v_mfma_f32_16x16x32_bf16 v[196:199], a[40:43], a[216:219], v[196:199]\n\t"
    "ds_read_b64_tr_b16 a[26:27], %[rb20] offset:33280\n\t"
    "s_add_u32 s46, s40, 27648\n\t"
    "v_mfma_f32_16x16x32_bf16 v[196:199], a[36:39], a[212:215], v[196:199]\n\t"
    "ds_read_b64_tr_b16 a[28:29], %[rb20] offset:39424\n\t"
    "buffer_load_dwordx4 %[wv0], s[48:51], s46 offen lds\n\t"
    "v_mfma_f32_16x16x32_bf16 v[196:199], a[32:35], a[208:211], v[196:199]\n\t"
    "ds_read_b64_tr_b16 a[30:31], %[rb20] offset:45568\n\t"
    "s_waitcnt lgkmcnt(8)\n\t"
    "v_mfma_f32_16x16x32_bf16 v[204:207], a[0:3], a[208:211], v[204:207]\n\t"
    "ds_read_b64_tr_b16 a[32:33], %[rb21] offset:2560\n\t"
    "ds_read_b64_tr_b16 a[34:35], %[rb21] offset:8704\n\t"
    "v_mfma_f32_16x16x32_bf16 v[204:207], a[4:7], a[212:215], v[204:207]\n\t"
    "ds_read_b64_tr_b16 a[36:37], %[rb21] offset:14848\n\t"
    "ds_read_b64_tr_b16 a[38:39], %[rb21] offset:20992\n\t"
    "v_mfma_f32_16x16x32_bf16 v[204:207], a[8:11], a[216:219], v[204:207]\n\t"
    "ds_read_b64_tr_b16 a[40:41], %[rb21] offset:27136\n\t"
    "ds_read_b64_tr_b16 a[42:43], %[rb21] offset:33280\n\t"
    "v_mfma_f32_16x16x32_bf16 v[204:207], a[12:15], a[220:223], v[204:207]\n\t"
    "ds_read_b64_tr_b16 a[44:45], %[rb21] offset:39424\n\t"
    "ds_read_b64_tr_b16 a[46:47], %[rb21] offset:45568\n\t"
    "v_mfma_f32_16x16x32_bf16 v[200:203], a[12:15], a[204:207], v[200:203]\n\t"
    "v_mfma_f32_16x16x32_bf16 v[200:203], a[8:11], a[200:203], v[200:203]\n\t"
    "v_mfma_f32_16x16x32_bf16 v[200:203], a[4:7], a[196:199], v[200:203]\n\t"
    "buffer_load_dwordx4 %[wv1], s[48:51], s46 offen offset:1024 lds\n\t"
    "v_mfma_f32_16x16x32_bf16 v[200:203], a[0:3], a[192:195], v[200:203]\n\t"
    "s_waitcnt vmcnt(19) lgkmcnt(0)\n\t"
    "s_barrier\n\t"
    "v_mfma_f32_16x16x32_bf16 v[208:211], a[16:19], a[192:195], v[208:211]\n\t"
    "ds_read_b64_tr_b16 a[0:1], %[rb00] offset:0\n\t"
    "v_mfma_f32_16x16x32_bf16 v[208:211], a[20:23], a[196:199], v[208:211]\n\t"
    "ds_read_b64_tr_b16 a[2:3], %[rb00] offset:6144\n\t"
    "v_mfma_f32_16x16x32_bf16 v[208:211], a[24:27], a[200:203], v[208:211]\n\t"
    "ds_read_b64_tr_b16 a[4:5], %[rb00] offset:12288\n\t"
    "buffer_load_dwordx4 %[wv2], s[48:51], s46 offen offset:2048 lds\n\t"
    "v_mfma_f32_16x16x32_bf16 v[208:211], a[28:31], a[204:207], v[208:211]\n\t"
    "ds_read_b64_tr_b16 a[6:7], %[rb00] offset:18432\n\t"
    "v_mfma_f32_16x16x32_bf16 v[212:215], a[28:31], a[220:223], v[212:215]\n\t"
    "ds_read_b64_tr_b16 a[8:9], %[rb00] offset:24576\n\t"
    "v_mfma_f32_16x16x32_bf16 v[212:215], a[24:27], a[216:219], v[212:215]\n\t"
    "ds_read_b64_tr_b16 a[10:11], %[rb00] offset:30720\n\t"
    "v_mfma_f32_16x16x32_bf16 v[212:215], a[20:23], a[212:215], v[212:215]\n\t"
    "ds_read_b64_tr_b16 a[12:13], %[rb00] offset:36864\n\t"
    "v_mfma_f32_16x16x32_bf16 v[212:215], a[16:19], a[208:211], v[212:215]\n\t"
    "ds_read_b64_tr_b16 a[14:15], %[rb00] offset:43008\n\t"
    "s_waitcnt lgkmcnt(8)\n\t"
    "v_mfma_f32_16x16x32_bf16 v[220:223], a[32:35], a[208:211], v[220:223]\n\t"
    "ds_read_b64_tr_b16 a[16:17], %[rb01] offset:0\n\t"
    "v_mfma_f32_16x16x32_bf16 v[220:223], a[36:39], a[212:215], v[220:223]\n\t"
    "ds_read_b64_tr_b16 a[18:19], %[rb01] offset:6144\n\t"
    "v_mfma_f32_16x16x32_bf16 v[220:223], a[40:43], a[216:219], v[220:223]\n\t"
    "ds_read_b64_tr_b16 a[20:21], %[rb01] offset:12288\n\t"
    "s_add_u32 s40, s40, 147456\n\t"
    "v_mfma_f32_16x16x32_bf16 v[220:223], a[44:47], a[220:223], v[220:223]\n\t"
    "ds_read_b64_tr_b16 a[22:23], %[rb01] offset:18432\n\t"
    "s_add_u32 s41, s41, 4096\n\t"
    "v_mfma_f32_16x16x32_bf16 v[216:219], a[44:47], a[204:207], v[216:219]\n\t"
    "ds_read_b64_tr_b16 a[24:25], %[rb01] offset:24576\n\t"
    "s_add_u32 s58, s58, 4096\n\t"
    "v_mfma_f32_16x16x32_bf16 v[216:219], a[40:43], a[200:203], v[216:219]\n\t"
    "ds_read_b64_tr_b16 a[26:27], %[rb01] offset:30720\n\t"
    "s_cmp_eq_u32 s41, s59\n\t"
    "s_cselect_b32 s41, s60, s41\n\t"
    "s_cselect_b32 s40, 0, s40\n\t"
    "v_mfma_f32_16x16x32_bf16 v[216:219], a[36:39], a[196:199], v[216:219]\n\t"
    "ds_read_b64_tr_b16 a[28:29], %[rb01] offset:36864\n\t"
    "v_mfma_f32_16x16x32_bf16 v[216:219], a[32:35], a[192:195], v[216:219]\n\t"
    "ds_read_b64_tr_b16 a[30:31], %[rb01] offset:43008\n\t"
    "s_waitcnt lgkmcnt(8)\n\t"
    "s_cmp_eq_u32 s58, s47\n\t"
    "s_cbranch_scc0 Lpc0_%=\n\t"
    "s_cmp_eq_u32 s47, %[xmid]\n\t"
    "s_cbranch_scc0 Lpctail_%=\n\t"
    "s_nop 4\n\t"
    "v_mov_b64 v[32:33], v[128:129]\n\t"
    "v_mov_b64 v[34:35], v[130:131]\n\t"
    "v_mov_b64 v[36:37], v[132:133]\n\t"
    "v_mov_b64 v[38:39], v[134:135]\n\t"
    "v_mov_b64 v[40:41], v[136:137]\n\t"
    "v_mov_b64 v[42:43], v[138:139]\n\t"
    "v_mov_b64 v[44:45], v[140:141]\n\t"
    "v_mov_b64 v[46:47], v[142:143]\n\t"
    "v_mov_b64 v[48:49], v[144:145]\n\t"
    "v_mov_b64 v[50:51], v[146:147]\n\t"
    "v_mov_b64 v[52:53], v[148:149]\n\t"
    "v_mov_b64 v[54:55], v[150:151]\n\t"
    "v_mov_b64 v[56:57], v[152:153]\n\t"
    "v_mov_b64 v[58:59], v[154:155]\n\t"
    "v_mov_b64 v[60:61], v[156:157]\n\t"
    "v_mov_b64 v[62:63], v[158:159]\n\t"
    "v_mov_b64 v[64:65], v[160:161]\n\t"
    "v_mov_b64 v[66:67], v[162:163]\n\t"
    "v_mov_b64 v[68:69], v[164:165]\n\t"
    "v_mov_b64 v[70:71], v[166:167]\n\t"
    "v_mov_b64 v[72:73], v[168:169]\n\t"
    "v_mov_b64 v[74:75], v[170:171]\n\t"
    "v_mov_b64 v[76:77], v[172:173]\n\t"
    "v_mov_b64 v[78:79], v[174:175]\n\t"
    "v_mov_b64 v[80:81], v[176:177]\n\t"
    "v_mov_b64 v[82:83], v[178:179]\n\t"
    "v_mov_b64 v[84:85], v[180:181]\n\t"
    "v_mov_b64 v[86:87], v[182:183]\n\t"
    "v_mov_b64 v[88:89], v[184:185]\n\t"
    "v_mov_b64 v[90:91], v[186:187]\n\t"
    "v_mov_b64 v[92:93], v[188:189]\n\t"
    "v_mov_b64 v[94:95], v[190:191]\n\t"
    "v_mov_b64 v[96:97], v[192:193]\n\t"
    "v_mov_b64 v[98:99], v[194:195]\n\t"
    "v_mov_b64 v[100:101], v[196:197]\n\t"
    "v_mov_b64 v[102:103], v[198:199]\n\t"
    "v_mov_b64 v[104:105], v[200:201]\n\t"
    "v_mov_b64 v[106:107], v[202:203]\n\t"
    "v_mov_b64 v[108:109], v[204:205]\n\t"
    "v_mov_b64 v[110:111], v[206:207]\n\t"
    "v_mov_b64 v[112:113], v[208:209]\n\t"
    "v_mov_b64 v[114:115], v[210:211]\n\t"
    "v_mov_b64 v[116:117], v[212:213]\n\t"
    "v_mov_b64 v[118:119], v[214:215]\n\t"
    "v_mov_b64 v[120:121], v[216:217]\n\t"
    "v_mov_b64 v[122:123], v[218:219]\n\t"
    "v_mov_b64 v[124:125], v[220:221]\n\t"
    "v_mov_b64 v[126:127], v[222:223]\n\t"
    "v_mov_b64 v[128:129], 0\n\t"
    "v_mov_b64 v[130:131], 0\n\t"
    "v_mov_b64 v[132:133], 0\n\t"
    "v_mov_b64 v[134:135], 0\n\t"
    "v_mov_b64 v[136:137], 0\n\t"
    "v_mov_b64 v[138:139], 0\n\t"
    "v_mov_b64 v[140:141], 0\n\t"
    "v_mov_b64 v[142:143], 0\n\t"
    "v_mov_b64 v[144:145], 0\n\t"
    "v_mov_b64 v[146:147], 0\n\t"
    "v_mov_b64 v[148:149], 0\n\t"
    "v_mov_b64 v[150:151], 0\n\t"
    "v_mov_b64 v[152:153], 0\n\t"
    "v_mov_b64 v[154:155], 0\n\t"
    "v_mov_b64 v[156:157], 0\n\t"
    "v_mov_b64 v[158:159], 0\n\t"
    "v_mov_b64 v[160:161], 0\n\t"
    "v_mov_b64 v[162:163], 0\n\t"
    "v_mov_b64 v[164:165], 0\n\t"
    "v_mov_b64 v[166:167], 0\n\t"
    "v_mov_b64 v[168:169], 0\n\t"
    "v_mov_b64 v[170:171], 0\n\t"
    "v_mov_b64 v[172:173], 0\n\t"
    "v_mov_b64 v[174:175], 0\n\t"
    "v_mov_b64 v[176:177], 0\n\t"
    "v_mov_b64 v[178:179], 0\n\t"
    "v_mov_b64 v[180:181], 0\n\t"
    "v_mov_b64 v[182:183], 0\n\t"
    "v_mov_b64 v[184:185], 0\n\t"
    "v_mov_b64 v[186:187], 0\n\t"
    "v_mov_b64 v[188:189], 0\n\t"
    "v_mov_b64 v[190:191], 0\n\t"
    "v_mov_b64 v[192:193], 0\n\t"
    "v_mov_b64 v[194:195], 0\n\t"
    "v_mov_b64 v[196:197], 0\n\t"
    "v_mov_b64 v[198:199], 0\n\t"
    "v_mov_b64 v[200:201], 0\n\t"
    "v_mov_b64 v[202:203], 0\n\t"
    "v_mov_b64 v[204:205], 0\n\t"
    "v_mov_b64 v[206:207], 0\n\t"
    "v_mov_b64 v[208:209], 0\n\t"
    "v_mov_b64 v[210:211], 0\n\t"
    "v_mov_b64 v[212:213], 0\n\t"
    "v_mov_b64 v[214:215], 0\n\t"
    "v_mov_b64 v[216:217], 0\n\t"
    "v_mov_b64 v[218:219], 0\n\t"
    "v_mov_b64 v[220:221], 0\n\t"
    "v_mov_b64 v[222:223], 0\n\t"
    "v_mov_b32 v247, %[fv2]\n\t"
    "s_mov_b64 s[56:57], %[wc]\n\t"
    "s_sub_u32 s42, %[xend], 8192\n\t"
    "s_mov_b32 s47, %[xend]\n\t"
    "s_mov_b32 s61, 0\n\t"
    "s_mov_b32 s62, s42\n\t"
    "s_mov_b32 s63, -1\n\t"
    "s_cmp_eq_u32 s58, %[xend]\n\t"
    "s_cbranch_scc0 Lpc0_%=\n\t"
    "s_branch Lpctail_%=\n\t"
    "Lhs_%=:\n\t"
    "s_mov_b32 s43, 0x40000\n\t"
    "Lhp_%=:\n\t"
    "s_sleep 4\n\t"
    "buffer_load_dword v245, v247, %[srd], 0 offen sc1\n\t"
    "s_waitcnt vmcnt(0)\n\t"
    "v_cmp_ne_u32 vcc, 1, v245\n\t"
    "s_sub_u32 s43, s43, 1\n\t"
    "s_cbranch_scc1 Lhk_%=\n\t"
    "s_cbranch_vccnz Lhp_%=\n\t"
    "s_branch Lhk_%=\n\t"
    "Los_%=:\n\t"
    "s_mov_b32 s43, 0x40000\n\t"
    "Lop_%=:\n\t"
    "s_sleep 4\n\t"
    "buffer_load_dword v245, %[fa], %[srd], 0 offen sc1\n\t"
    "s_waitcnt vmcnt(0)\n\t"
    "v_cmp_ne_u32 vcc, 1, v245\n\t"
    "s_sub_u32 s43, s43, 1\n\t"
    "s_cbranch_scc1 Lok_%=\n\t"
    "s_cbranch_vccnz Lop_%=\n\t"
    "s_branch Lok_%=\n\t"
    "Lpctail_%=:\n\t"
    "v_mfma_f32_16x16x32_bf16 v[32:35], a[0:3], a[128:131], v[32:35]\n\t"
    "ds_read_b64_tr_b16 a[32:33], %[rb00] offset:512\n\t"
    "v_mfma_f32_16x16x32_bf16 v[32:35], a[4:7], a[132:135], v[32:35]\n\t"
    "ds_read_b64_tr_b16 a[34:35], %[rb00] offset:6656\n\t"
    "v_cvt_pk_bf16_f32 v224, v132, v133\n\t"
    "v_cvt_pk_bf16_f32 v225, v134, v135\n\t"
    "v_mfma_f32_16x16x32_bf16 v[32:35], a[8:11], a[136:139], v[32:35]\n\t"
    "ds_read_b64_tr_b16 a[36:37], %[rb00] offset:12800\n\t"
    "v_cvt_pk_bf16_f32 v226, v140, v141\n\t"
    "v_cvt_pk_bf16_f32 v227, v142, v143\n\t"
    "v_mfma_f32_16x16x32_bf16 v[32:35], a[12:15], a[140:143], v[32:35]\n\t"
    "ds_read_b64_tr_b16 a[38:39], %[rb00] offset:18944\n\t"
    "v_min_u32 v224, -2, v224\n\t"
    "v_min_u32 v225, -2, v225\n\t"
    "v_mfma_f32_16x16x32_bf16 v[36:39], a[12:15], a[156:159], v[36:39]\n\t"
    "ds_read_b64_tr_b16 a[40:41], %[rb00] offset:25088\n\t"
    "v_min_u32 v226, -2, v226\n\t"
    "v_min_u32 v227, -2, v227\n\t"
    "v_mfma_f32_16x16x32_bf16 v[36:39], a[8:11], a[152:155], v[36:39]\n\t"
    "ds_read_b64_tr_b16 a[42:43], %[rb00] offset:31232\n\t"
    "buffer_store_dwordx4 v[224:227], %[xl], %[srd], %[eo2] offen sc1\n\t"
    "v_mfma_f32_16x16x32_bf16 v[36:39], a[4:7], a[148:151], v[36:39]\n\t"
    "ds_read_b64_tr_b16 a[44:45], %[rb00] offset:37376\n\t"
    "v_mfma_f32_16x16x32_bf16 v[36:39], a[0:3], a[144:147], v[36:39]\n\t"
    "ds_read_b64_tr_b16 a[46:47], %[rb00] offset:43520\n\t"
    "s_waitcnt lgkmcnt(8)\n\t"
    "v_mfma_f32_16x16x32_bf16 v[44:47], a[16:19], a[144:147], v[44:47]\n\t"
    "ds_read_b64_tr_b16 a[0:1], %[rb01] offset:512\n\t"
    "v_cvt_pk_bf16_f32 v228, v128, v129\n\t"
    "v_cvt_pk_bf16_f32 v229, v130, v131\n\t"
    "v_mfma_f32_16x16x32_bf16 v[44:47], a[20:23], a[148:151], v[44:47]\n\t"
    "ds_read_b64_tr_b16 a[2:3], %[rb01] offset:6656\n\t"
    "v_cvt_pk_bf16_f32 v230, v136, v137\n\t"
    "v_cvt_pk_bf16_f32 v231, v138, v139\n\t"
    "v_mfma_f32_16x16x32_bf16 v[44:47], a[24:27], a[152:155], v[44:47]\n\t"
    "ds_read_b64_tr_b16 a[4:5], %[rb01] offset:12800\n\t"
    "v_min_u32 v228, -2, v228\n\t"
    "v_min_u32 v229, -2, v229\n\t"
    "v_mfma_f32_16x16x32_bf16 v[44:47], a[28:31], a[156:159], v[44:47]\n\t"
    "ds_read_b64_tr_b16 a[6:7], %[rb01] offset:18944\n\t"
    "v_min_u32 v230, -2, v230\n\t"
    "v_min_u32 v231, -2, v231\n\t"
    "v_mfma_f32_16x16x32_bf16 v[40:43], a[28:31], a[140:143], v[40:43]\n\t"
    "ds_read_b64_tr_b16 a[8:9], %[rb01] offset:25088\n\t"
    "buffer_store_dwordx4 v[228:231], %[xl], %[srd], %[eo3] offen sc1\n\t"
    "v_mfma_f32_16x16x32_bf16 v[40:43], a[24:27], a[136:139], v[40:43]\n\t"
    "ds_read_b64_tr_b16 a[10:11], %[rb01] offset:31232\n\t"
    "v_mfma_f32_16x16x32_bf16 v[40:43], a[20:23], a[132:135], v[40:43]\n\t"
    "ds_read_b64_tr_b16 a[12:13], %[rb01] offset:37376\n\t"
    "v_mfma_f32_16x16x32_bf16 v[40:43], a[16:19], a[128:131], v[40:43]\n\t"
    "ds_read_b64_tr_b16 a[14:15], %[rb01] offset:43520\n\t"
    "v_cvt_pk_bf16_f32 v232, v148, v149\n\t"
    "v_cvt_pk_bf16_f32 v233, v150, v151\n\t"
    "s_waitcnt lgkmcnt(8)\n\t"
    "v_mfma_f32_16x16x32_bf16 v[48:51], a[32:35], a[128:131], v[48:51]\n\t"
    "ds_read_b64_tr_b16 a[16:17], %[rb00] offset:1024\n\t"
    "v_cvt_pk_bf16_f32 v234, v156, v157\n\t"
    "v_cvt_pk_bf16_f32 v235, v158, v159\n\t"
    "v_mfma_f32_16x16x32_bf16 v[48:51], a[36:39], a[132:135], v[48:51]\n\t"
    "ds_read_b64_tr_b16 a[18:19], %[rb00] offset:7168\n\t"
    "v_min_u32 v232, -2, v232\n\t"
    "v_min_u32 v233, -2, v233\n\t"
    "v_mfma_f32_16x16x32_bf16 v[48:51], a[40:43], a[136:139], v[48:51]\n\t"
    "ds_read_b64_tr_b16 a[20:21], %[rb00] offset:13312\n\t"
    "v_min_u32 v234, -2, v234\n\t"
    "v_min_u32 v235, -2, v235\n\t"
    "v_mfma_f32_16x16x32_bf16 v[48:51], a[44:47], a[140:143], v[48:51]\n\t"
    "ds_read_b64_tr_b16 a[22:23], %[rb00] offset:19456\n\t"
    "buffer_store_dwordx4 v[232:235], %[xl], %[srd], %[eo2] offen offset:1024 sc1\n\t"
    "v_mfma_f32_16x16x32_bf16 v[52:55], a[44:47], a[156:159], v[52:55]\n\t"
    "ds_read_b64_tr_b16 a[24:25], %[rb00] offset:25600\n\t"
    "v_mfma_f32_16x16x32_bf16 v[52:55], a[40:43], a[152:155], v[52:55]\n\t"
    "ds_read_b64_tr_b16 a[26:27], %[rb00] offset:31744\n\t"
    "v_mfma_f32_16x16x32_bf16 v[52:55], a[36:39], a[148:151], v[52:55]\n\t"
    "ds_read_b64_tr_b16 a[28:29], %[rb00] offset:37888\n\t"
    "v_cvt_pk_bf16_f32 v224, v144, v145\n\t"
    "v_cvt_pk_bf16_f32 v225, v146, v147\n\t"
    "v_mfma_f32_16x16x32_bf16 v[52:55], a[32:35], a[144:147], v[52:55]\n\t"
    "ds_read_b64_tr_b16 a[30:31], %[rb00] offset:44032\n\t"
    "v_cvt_pk_bf16_f32 v226, v152, v153\n\t"
    "v_cvt_pk_bf16_f32 v227, v154, v155\n\t"
    "s_waitcnt lgkmcnt(8)\n\t"
    "v_mfma_f32_16x16x32_bf16 v[60:63], a[0:3], a[144:147], v[60:63]\n\t"
    "ds_read_b64_tr_b16 a[32:33], %[rb01] offset:1024\n\t"
    "v_min_u32 v224, -2, v224\n\t"
    "v_min_u32 v225, -2, v225\n\t"
    "v_mfma_f32_16x16x32_bf16 v[60:63], a[4:7], a[148:151], v[60:63]\n\t"
    "ds_read_b64_tr_b16 a[34:35], %[rb01] offset:7168\n\t"
    "v_min_u32 v226, -2, v226\n\t"
    "v_min_u32 v227, -2, v227\n\t"
    "v_mfma_f32_16x16x32_bf16 v[60:63], a[8:11], a[152:155], v[60:63]\n\t"
    "ds_read_b64_tr_b16 a[36:37], %[rb01] offset:13312\n\t"
    "buffer_store_dwordx4 v[224:227], %[xl], %[srd], %[eo3] offen offset:1024 sc1\n\t"
    "v_mfma_f32_16x16x32_bf16 v[60:63], a[12:15], a[156:159], v[60:63]\n\t"
    "ds_read_b64_tr_b16 a[38:39], %[rb01] offset:19456\n\t"
    "v_mfma_f32_16x16x32_bf16 v[56:59], a[12:15], a[140:143], v[56:59]\n\t"
    "ds_read_b64_tr_b16 a[40:41], %[rb01] offset:25600\n\t"
    "v_mfma_f32_16x16x32_bf16 v[56:59], a[8:11], a[136:139], v[56:59]\n\t"
    "ds_read_b64_tr_b16 a[42:43], %[rb01] offset:31744\n\t"
    "v_cvt_pk_bf16_f32 v228, v164, v165\n\t"
    "v_cvt_pk_bf16_f32 v229, v166, v167\n\t"
    "v_mfma_f32_16x16x32_bf16 v[56:59], a[4:7], a[132:135], v[56:59]\n\t"
    "ds_read_b64_tr_b16 a[44:45], %[rb01] offset:37888\n\t"
    "v_cvt_pk_bf16_f32 v230, v172, v173\n\t"
    "v_cvt_pk_bf16_f32 v231, v174, v175\n\t"
    "v_mfma_f32_16x16x32_bf16 v[56:59], a[0:3], a[128:131], v[56:59]\n\t"
    "ds_read_b64_tr_b16 a[46:47], %[rb01] offset:44032\n\t"
    "v_min_u32 v228, -2, v228\n\t"
    "v_min_u32 v229, -2, v229\n\t"
    "s_waitcnt lgkmcnt(8)\n\t"
    "v_mfma_f32_16x16x32_bf16 v[64:67], a[16:19], a[128:131], v[64:67]\n\t"
    "ds_read_b64_tr_b16 a[0:1], %[rb00] offset:1536\n\t"
    "v_min_u32 v230, -2, v230\n\t"
    "v_min_u32 v231, -2, v231\n\t"
    "v_mfma_f32_16x16x32_bf16 v[64:67], a[20:23], a[132:135], v[64:67]\n\t"
    "ds_read_b64_tr_b16 a[2:3], %[rb00] offset:7680\n\t"
    "buffer_store_dwordx4 v[228:231], %[xl], %[srd], %[eo2] offen offset:2048 sc1\n\t"
    "v_mfma_f32_16x16x32_bf16 v[64:67], a[24:27], a[136:139], v[64:67]\n\t"
    "ds_read_b64_tr_b16 a[4:5], %[rb00] offset:13824\n\t"
    "v_mfma_f32_16x16x32_bf16 v[64:67], a[28:31], a[140:143], v[64:67]\n\t"
    "ds_read_b64_tr_b16 a[6:7], %[rb00] offset:19968\n\t"
    "v_mfma_f32_16x16x32_bf16 v[68:71], a[28:31], a[156:159], v[68:71]\n\t"
    "ds_read_b64_tr_b16 a[8:9], %[rb00] offset:26112\n\t"
    "v_cvt_pk_bf16_f32 v232, v160, v161\n\t"
    "v_cvt_pk_bf16_f32 v233, v162, v163\n\t"
    "v_mfma_f32_16x16x32_bf16 v[68:71], a[24:27], a[152:155], v[68:71]\n\t"
    "ds_read_b64_tr_b16 a[10:11], %[rb00] offset:32256\n\t"
    "v_cvt_pk_bf16_f32 v234, v168, v169\n\t"
    "v_cvt_pk_bf16_f32 v235, v170, v171\n\t"
    "v_mfma_f32_16x16x32_bf16 v[68:71], a[20:23], a[148:151], v[68:71]\n\t"
    "ds_read_b64_tr_b16 a[12:13], %[rb00] offset:38400\n\t"
    "v_min_u32 v232, -2, v232\n\t"
    "v_min_u32 v233, -2, v233\n\t"
    "v_mfma_f32_16x16x32_bf16 v[68:71], a[16:19], a[144:147], v[68:71]\n\t"
    "ds_read_b64_tr_b16 a[14:15], %[rb00] offset:44544\n\t"
    "v_min_u32 v234, -2, v234\n\t"
    "v_min_u32 v235, -2, v235\n\t"
    "s_waitcnt lgkmcnt(8)\n\t"
    "v_mfma_f32_16x16x32_bf16 v[76:79], a[32:35], a[144:147], v[76:79]\n\t"
    "ds_read_b64_tr_b16 a[16:17], %[rb01] offset:1536\n\t"
    "buffer_store_dwordx4 v[232:235], %[xl], %[srd], %[eo3] offen offset:2048 sc1\n\t"
    "v_mfma_f32_16x16x32_bf16 v[76:79], a[36:39], a[148:151], v[76:79]\n\t"
    "ds_read_b64_tr_b16 a[18:19], %[rb01] offset:7680\n\t"
    "v_mfma_f32_16x16x32_bf16 v[76:79], a[40:43], a[152:155], v[76:79]\n\t"
    "ds_read_b64_tr_b16 a[20:21], %[rb01] offset:13824\n\t"
    "v_mfma_f32_16x16x32_bf16 v[76:79], a[44:47], a[156:159], v[76:79]\n\t"
    "ds_read_b64_tr_b16 a[22:23], %[rb01] offset:19968\n\t"
    "v_cvt_pk_bf16_f32 v224, v176, v177\n\t"
    "v_cvt_pk_bf16_f32 v225, v178, v179\n\t"
    "v_mfma_f32_16x16x32_bf16 v[72:75], a[44:47], a[140:143], v[72:75]\n\t"
    "ds_read_b64_tr_b16 a[24:25], %[rb01] offset:26112\n\t"
    "v_cvt_pk_bf16_f32 v226, v184, v185\n\t"
    "v_cvt_pk_bf16_f32 v227, v186, v187\n\t"
    "v_mfma_f32_16x16x32_bf16 v[72:75], a[40:43], a[136:139], v[72:75]\n\t"
    "ds_read_b64_tr_b16 a[26:27], %[rb01] offset:32256\n\t"
    "v_min_u32 v224, -2, v224\n\t"
    "v_min_u32 v225, -2, v225\n\t"
    "v_mfma_f32_16x16x32_bf16 v[72:75], a[36:39], a[132:135], v[72:75]\n\t"
    "ds_read_b64_tr_b16 a[28:29], %[rb01] offset:38400\n\t"
    "v_min_u32 v226, -2, v226\n\t"
    "v_min_u32 v227, -2, v227\n\t"
    "v_mfma_f32_16x16x32_bf16 v[72:75], a[32:35], a[128:131], v[72:75]\n\t"
    "ds_read_b64_tr_b16 a[30:31], %[rb01] offset:44544\n\t"
    "buffer_store_dwordx4 v[224:227], %[xl], %[srd], %[eo1] offen sc1\n\t"
    "s_waitcnt lgkmcnt(8)\n\t"
    "v_mfma_f32_16x16x32_bf16 v[80:83], a[0:3], a[128:131], v[80:83]\n\t"
    "ds_read_b64_tr_b16 a[32:33], %[rb00] offset:2048\n\t"
    "v_mfma_f32_16x16x32_bf16 v[80:83], a[4:7], a[132:135], v[80:83]\n\t"
    "ds_read_b64_tr_b16 a[34:35], %[rb00] offset:8192\n\t"
    "v_mfma_f32_16x16x32_bf16 v[80:83], a[8:11], a[136:139], v[80:83]\n\t"
    "ds_read_b64_tr_b16 a[36:37], %[rb00] offset:14336\n\t"
    "v_cvt_pk_bf16_f32 v228, v192, v193\n\t"
    "v_cvt_pk_bf16_f32 v229, v194, v195\n\t"
    "v_mfma_f32_16x16x32_bf16 v[80:83], a[12:15], a[140:143], v[80:83]\n\t"
    "ds_read_b64_tr_b16 a[38:39], %[rb00] offset:20480\n\t"
    "v_cvt_pk_bf16_f32 v230, v200, v201\n\t"
    "v_cvt_pk_bf16_f32 v231, v202, v203\n\t"
    "v_mfma_f32_16x16x32_bf16 v[84:87], a[12:15], a[156:159], v[84:87]\n\t"
    "ds_read_b64_tr_b16 a[40:41], %[rb00] offset:26624\n\t"
    "v_min_u32 v228, -2, v228\n\t"
    "v_min_u32 v229, -2, v229\n\t"
    "v_mfma_f32_16x16x32_bf16 v[84:87], a[8:11], a[152:155], v[84:87]\n\t"
    "ds_read_b64_tr_b16 a[42:43], %[rb00] offset:32768\n\t"
    "v_min_u32 v230, -2, v230\n\t"
    "v_min_u32 v231, -2, v231\n\t"
    "v_mfma_f32_16x16x32_bf16 v[84:87], a[4:7], a[148:151], v[84:87]\n\t"
    "ds_read_b64_tr_b16 a[44:45], %[rb00] offset:38912\n\t"
    "buffer_store_dwordx4 v[228:231], %[xl], %[srd], %[eo1] offen offset:1024 sc1\n\t"
    "v_mfma_f32_16x16x32_bf16 v[84:87], a[0:3], a[144:147], v[84:87]\n\t"
    "ds_read_b64_tr_b16 a[46:47], %[rb00] offset:45056\n\t"
    "s_waitcnt lgkmcnt(8)\n\t"
    "v_mfma_f32_16x16x32_bf16 v[92:95], a[16:19], a[144:147], v[92:95]\n\t"
    "ds_read_b64_tr_b16 a[0:1], %[rb01] offset:2048\n\t"
    "v_mfma_f32_16x16x32_bf16 v[92:95], a[20:23], a[148:151], v[92:95]\n\t"
    "ds_read_b64_tr_b16 a[2:3], %[rb01] offset:8192\n\t"
    "v_cvt_pk_bf16_f32 v232, v208, v209\n\t"
    "v_cvt_pk_bf16_f32 v233, v210, v211\n\t"
    "v_mfma_f32_16x16x32_bf16 v[92:95], a[24:27], a[152:155], v[92:95]\n\t"
    "ds_read_b64_tr_b16 a[4:5], %[rb01] offset:14336\n\t"
    "v_cvt_pk_bf16_f32 v234, v216, v217\n\t"
    "v_cvt_pk_bf16_f32 v235, v218, v219\n\t"
    "v_mfma_f32_16x16x32_bf16 v[92:95], a[28:31], a[156:159], v[92:95]\n\t"
    "ds_read_b64_tr_b16 a[6:7], %[rb01] offset:20480\n\t"
    "v_min_u32 v232, -2, v232\n\t"
    "v_min_u32 v233, -2, v233\n\t"
    "v_mfma_f32_16x16x32_bf16 v[88:91], a[28:31], a[140:143], v[88:91]\n\t"
    "ds_read_b64_tr_b16 a[8:9], %[rb01] offset:26624\n\t"
    "v_min_u32 v234, -2, v234\n\t"
    "v_min_u32 v235, -2, v235\n\t"
    "v_mfma_f32_16x16x32_bf16 v[88:91], a[24:27], a[136:139], v[88:91]\n\t"
    "ds_read_b64_tr_b16 a[10:11], %[rb01] offset:32768\n\t"
    "buffer_store_dwordx4 v[232:235], %[xl], %[srd], %[eo1] offen offset:2048 sc1\n\t"
    "v_mfma_f32_16x16x32_bf16 v[88:91], a[20:23], a[132:135], v[88:91]\n\t"
    "ds_read_b64_tr_b16 a[12:13], %[rb01] offset:38912\n\t"
    "v_mfma_f32_16x16x32_bf16 v[88:91], a[16:19], a[128:131], v[88:91]\n\t"
    "ds_read_b64_tr_b16 a[14:15], %[rb01] offset:45056\n\t"
    "s_waitcnt lgkmcnt(8)\n\t"
    "v_mfma_f32_16x16x32_bf16 v[96:99], a[32:35], a[128:131], v[96:99]\n\t"
    "ds_read_b64_tr_b16 a[16:17], %[rb00] offset:2560\n\t"
    "v_mfma_f32_16x16x32_bf16 v[96:99], a[36:39], a[132:135], v[96:99]\n\t"
    "ds_read_b64_tr_b16 a[18:19], %[rb00] offset:8704\n\t"
    "v_mfma_f32_16x16x32_bf16 v[96:99], a[40:43], a[136:139], v[96:99]\n\t"
    "ds_read_b64_tr_b16 a[20:21], %[rb00] offset:14848\n\t"
    "v_mfma_f32_16x16x32_bf16 v[96:99], a[44:47], a[140:143], v[96:99]\n\t"
    "ds_read_b64_tr_b16 a[22:23], %[rb00] offset:20992\n\t"
    "v_mfma_f32_16x16x32_bf16 v[100:103], a[44:47], a[156:159], v[100:103]\n\t"
    "ds_read_b64_tr_b16 a[24:25], %[rb00] offset:27136\n\t"
    "v_mfma_f32_16x16x32_bf16 v[100:103], a[40:43], a[152:155], v[100:103]\n\t"
    "ds_read_b64_tr_b16 a[26:27], %[rb00] offset:33280\n\t"
    "v_mfma_f32_16x16x32_bf16 v[100:103], a[36:39], a[148:151], v[100:103]\n\t"
    "ds_read_b64_tr_b16 a[28:29], %[rb00] offset:39424\n\t"
    "v_mfma_f32_16x16x32_bf16 v[100:103], a[32:35], a[144:147], v[100:103]\n\t"
    "ds_read_b64_tr_b16 a[30:31], %[rb00] offset:45568\n\t"
    "s_waitcnt lgkmcnt(8)\n\t"
    "v_mfma_f32_16x16x32_bf16 v[108:111], a[0:3], a[144:147], v[108:111]\n\t"
    "ds_read_b64_tr_b16 a[32:33], %[rb01] offset:2560\n\t"
    "ds_read_b64_tr_b16 a[34:35], %[rb01] offset:8704\n\t"
    "v_mfma_f32_16x16x32_bf16 v[108:111], a[4:7], a[148:151], v[108:111]\n\t"
    "ds_read_b64_tr_b16 a[36:37], %[rb01] offset:14848\n\t"
    "ds_read_b64_tr_b16 a[38:39], %[rb01] offset:20992\n\t"
    "v_mfma_f32_16x16x32_bf16 v[108:111], a[8:11], a[152:155], v[108:111]\n\t"
    "ds_read_b64_tr_b16 a[40:41], %[rb01] offset:27136\n\t"
    "ds_read_b64_tr_b16 a[42:43], %[rb01] offset:33280\n\t"
    "v_mfma_f32_16x16x32_bf16 v[108:111], a[12:15], a[156:159], v[108:111]\n\t"
    "ds_read_b64_tr_b16 a[44:45], %[rb01] offset:39424\n\t"
    "ds_read_b64_tr_b16 a[46:47], %[rb01] offset:45568\n\t"
    "v_mfma_f32_16x16x32_bf16 v[104:107], a[12:15], a[140:143], v[104:107]\n\t"
    "v_mfma_f32_16x16x32_bf16 v[104:107], a[8:11], a[136:139], v[104:107]\n\t"
    "v_mfma_f32_16x16x32_bf16 v[104:107], a[4:7], a[132:135], v[104:107]\n\t"
    "v_mfma_f32_16x16x32_bf16 v[104:107], a[0:3], a[128:131], v[104:107]\n\t"
    "s_waitcnt vmcnt(9) lgkmcnt(0)\n\t"
    "s_barrier\n\t"
    "v_mfma_f32_16x16x32_bf16 v[112:115], a[16:19], a[128:131], v[112:115]\n\t"
    "ds_read_b64_tr_b16 a[0:1], %[rb10] offset:0\n\t"
    "v_mfma_f32_16x16x32_bf16 v[112:115], a[20:23], a[132:135], v[112:115]\n\t"
    "ds_read_b64_tr_b16 a[2:3], %[rb10] offset:6144\n\t"
    "v_mfma_f32_16x16x32_bf16 v[112:115], a[24:27], a[136:139], v[112:115]\n\t"
    "ds_read_b64_tr_b16 a[4:5], %[rb10] offset:12288\n\t"
    "v_mfma_f32_16x16x32_bf16 v[112:115], a[28:31], a[140:143], v[112:115]\n\t"
    "ds_read_b64_tr_b16 a[6:7], %[rb10] offset:18432\n\t"
    "v_mfma_f32_16x16x32_bf16 v[116:119], a[28:31], a[156:159], v[116:119]\n\t"
    "ds_read_b64_tr_b16 a[8:9], %[rb10] offset:24576\n\t"
    "v_mfma_f32_16x16x32_bf16 v[116:119], a[24:27], a[152:155], v[116:119]\n\t"
    "ds_read_b64_tr_b16 a[10:11], %[rb10] offset:30720\n\t"
    "v_mfma_f32_16x16x32_bf16 v[116:119], a[20:23], a[148:151], v[116:119]\n\t"
    "ds_read_b64_tr_b16 a[12:13], %[rb10] offset:36864\n\t"
    "v_mfma_f32_16x16x32_bf16 v[116:119], a[16:19], a[144:147], v[116:119]\n\t"
    "ds_read_b64_tr_b16 a[14:15], %[rb10] offset:43008\n\t"
    "s_waitcnt lgkmcnt(8)\n\t"
    "v_mfma_f32_16x16x32_bf16 v[124:127], a[32:35], a[144:147], v[124:127]\n\t"
    "ds_read_b64_tr_b16 a[16:17], %[rb11] offset:0\n\t"
    "v_mfma_f32_16x16x32_bf16 v[124:127], a[36:39], a[148:151], v[124:127]\n\t"
    "ds_read_b64_tr_b16 a[18:19], %[rb11] offset:6144\n\t"
    "v_mfma_f32_16x16x32_bf16 v[124:127], a[40:43], a[152:155], v[124:127]\n\t"
    "ds_read_b64_tr_b16 a[20:21], %[rb11] offset:12288\n\t"
    "v_mfma_f32_16x16x32_bf16 v[124:127], a[44:47], a[156:159], v[124:127]\n\t"
    "ds_read_b64_tr_b16 a[22:23], %[rb11] offset:18432\n\t"
    "v_mfma_f32_16x16x32_bf16 v[120:123], a[44:47], a[140:143], v[120:123]\n\t"
    "ds_read_b64_tr_b16 a[24:25], %[rb11] offset:24576\n\t"
    "v_mfma_f32_16x16x32_bf16 v[120:123], a[40:43], a[136:139], v[120:123]\n\t"
    "ds_read_b64_tr_b16 a[26:27], %[rb11] offset:30720\n\t"
    "v_mfma_f32_16x16x32_bf16 v[120:123], a[36:39], a[132:135], v[120:123]\n\t"
    "ds_read_b64_tr_b16 a[28:29], %[rb11] offset:36864\n\t"
    "v_mfma_f32_16x16x32_bf16 v[120:123], a[32:35], a[128:131], v[120:123]\n\t"
    "ds_read_b64_tr_b16 a[30:31], %[rb11] offset:43008\n\t"
    "s_waitcnt lgkmcnt(8)\n\t"
    "v_mfma_f32_16x16x32_bf16 v[32:35], a[0:3], a[160:163], v[32:35]\n\t"
    "ds_read_b64_tr_b16 a[32:33], %[rb10] offset:512\n\t"
    "v_mfma_f32_16x16x32_bf16 v[32:35], a[4:7], a[164:167], v[32:35]\n\t"
    "ds_read_b64_tr_b16 a[34:35], %[rb10] offset:6656\n\t"
    "v_mfma_f32_16x16x32_bf16 v[32:35], a[8:11], a[168:171], v[32:35]\n\t"
    "ds_read_b64_tr_b16 a[36:37], %[rb10] offset:12800\n\t"
    "v_mfma_f32_16x16x32_bf16 v[32:35], a[12:15], a[172:175], v[32:35]\n\t"
    "ds_read_b64_tr_b16 a[38:39], %[rb10] offset:18944\n\t"
    "v_mfma_f32_16x16x32_bf16 v[36:39], a[12:15], a[188:191], v[36:39]\n\t"
    "ds_read_b64_tr_b16 a[40:41], %[rb10] offset:25088\n\t"
    "v_mfma_f32_16x16x32_bf16 v[36:39], a[8:11], a[184:187], v[36:39]\n\t"
    "ds_read_b64_tr_b16 a[42:43], %[rb10] offset:31232\n\t"
    "v_mfma_f32_16x16x32_bf16 v[36:39], a[4:7], a[180:183], v[36:39]\n\t"
    "ds_read_b64_tr_b16 a[44:45], %[rb10] offset:37376\n\t"
    "v_mfma_f32_16x16x32_bf16 v[36:39], a[0:3], a[176:179], v[36:39]\n\t"
    "ds_read_b64_tr_b16 a[46:47], %[rb10] offset:43520\n\t"
    "s_waitcnt lgkmcnt(8)\n\t"
    "v_mfma_f32_16x16x32_bf16 v[40:43], a[16:19], a[160:163], v[40:43]\n\t"
    "ds_read_b64_tr_b16 a[0:1], %[rb11] offset:512\n\t"
    "v_mfma_f32_16x16x32_bf16 v[40:43], a[20:23], a[164:167], v[40:43]\n\t"
    "ds_read_b64_tr_b16 a[2:3], %[rb11] offset:6656\n\t"
    "v_mfma_f32_16x16x32_bf16 v[40:43], a[24:27], a[168:171], v[40:43]\n\t"
    "ds_read_b64_tr_b16 a[4:5], %[rb11] offset:12800\n\t"
    "v_mfma_f32_16x16x32_bf16 v[40:43], a[28:31], a[172:175], v[40:43]\n\t"
    "ds_read_b64_tr_b16 a[6:7], %[rb11] offset:18944\n\t"
    "v_mfma_f32_16x16x32_bf16 v[44:47], a[28:31], a[188:191], v[44:47]\n\t"
    "ds_read_b64_tr_b16 a[8:9], %[rb11] offset:25088\n\t"
    "v_mfma_f32_16x16x32_bf16 v[44:47], a[24:27], a[184:187], v[44:47]\n\t"
    "ds_read_b64_tr_b16 a[10:11], %[rb11] offset:31232\n\t"
    "v_mfma_f32_16x16x32_bf16 v[44:47], a[20:23], a[180:183], v[44:47]\n\t"
    "ds_read_b64_tr_b16 a[12:13], %[rb11] offset:37376\n\t"
    "v_mfma_f32_16x16x32_bf16 v[44:47], a[16:19], a[176:179], v[44:47]\n\t"
    "ds_read_b64_tr_b16 a[14:15], %[rb11] offset:43520\n\t"
    "s_waitcnt lgkmcnt(8)\n\t"
    "v_mfma_f32_16x16x32_bf16 v[48:51], a[32:35], a[160:163], v[48:51]\n\t"
    "ds_read_b64_tr_b16 a[16:17], %[rb10] offset:1024\n\t"
    "v_mfma_f32_16x16x32_bf16 v[48:51], a[36:39], a[164:167], v[48:51]\n\t"
    "ds_read_b64_tr_b16 a[18:19], %[rb10] offset:7168\n\t"
    "v_cvt_pk_bf16_f32 v224, v32, v33\n\t"
    "v_cvt_pk_bf16_f32 v225, v34, v35\n\t"
    "v_mfma_f32_16x16x32_bf16 v[48:51], a[40:43], a[168:171], v[48:51]\n\t"
    "ds_read_b64_tr_b16 a[20:21], %[rb10] offset:13312\n\t"
    "v_cvt_pk_bf16_f32 v226, v40, v41\n\t"
    "v_cvt_pk_bf16_f32 v227, v42, v43\n\t"
    "v_mfma_f32_16x16x32_bf16 v[48:51], a[44:47], a[172:175], v[48:51]\n\t"
    "ds_read_b64_tr_b16 a[22:23], %[rb10] offset:19456\n\t"
    "v_min_u32 v224, -2, v224\n\t"
    "v_min_u32 v225, -2, v225\n\t"
    "v_mfma_f32_16x16x32_bf16 v[52:55], a[44:47], a[188:191], v[52:55]\n\t"
    "ds_read_b64_tr_b16 a[24:25], %[rb10] offset:25600\n\t"
    "v_min_u32 v226, -2, v226\n\t"
    "v_min_u32 v227, -2, v227\n\t"
    "v_mfma_f32_16x16x32_bf16 v[52:55], a[40:43], a[184:187], v[52:55]\n\t"
    "ds_read_b64_tr_b16 a[26:27], %[rb10] offset:31744\n\t"
    "buffer_store_dwordx4 v[224:227], %[xl], %[srd], %[lo3] offen sc1\n\t"
    "v_mfma_f32_16x16x32_bf16 v[52:55], a[36:39], a[180:183], v[52:55]\n\t"
    "ds_read_b64_tr_b16 a[28:29], %[rb10] offset:37888\n\t"
    "v_cvt_pk_bf16_f32 v228, v36, v37\n\t"
    "v_cvt_pk_bf16_f32 v229, v38, v39\n\t"
    "v_mfma_f32_16x16x32_bf16 v[52:55], a[32:35], a[176:179], v[52:55]\n\t"
    "ds_read_b64_tr_b16 a[30:31], %[rb10] offset:44032\n\t"
    "v_cvt_pk_bf16_f32 v230, v44, v45\n\t"
    "v_cvt_pk_bf16_f32 v231, v46, v47\n\t"
    "s_waitcnt lgkmcnt(8)\n\t"
    "v_mfma_f32_16x16x32_bf16 v[56:59], a[0:3], a[160:163], v[56:59]\n\t"
    "ds_read_b64_tr_b16 a[32:33], %[rb11] offset:1024\n\t"
    "v_min_u32 v228, -2, v228\n\t"
    "v_min_u32 v229, -2, v229\n\t"
    "v_mfma_f32_16x16x32_bf16 v[56:59], a[4:7], a[164:167], v[56:59]\n\t"
    "ds_read_b64_tr_b16 a[34:35], %[rb11] offset:7168\n\t"
    "v_min_u32 v230, -2, v230\n\t"
    "v_min_u32 v231, -2, v231\n\t"
    "v_mfma_f32_16x16x32_bf16 v[56:59], a[8:11], a[168:171], v[56:59]\n\t"
    "ds_read_b64_tr_b16 a[36:37], %[rb11] offset:13312\n\t"
    "buffer_store_dwordx4 v[228:231], %[xl], %[srd], %[lo2] offen sc1\n\t"
    "v_mfma_f32_16x16x32_bf16 v[56:59], a[12:15], a[172:175], v[56:59]\n\t"
    "ds_read_b64_tr_b16 a[38:39], %[rb11] offset:19456\n\t"
    "v_mfma_f32_16x16x32_bf16 v[60:63], a[12:15], a[188:191], v[60:63]\n\t"
    "ds_read_b64_tr_b16 a[40:41], %[rb11] offset:25600\n\t"
    "v_mfma_f32_16x16x32_bf16 v[60:63], a[8:11], a[184:187], v[60:63]\n\t"
    "ds_read_b64_tr_b16 a[42:43], %[rb11] offset:31744\n\t"
    "v_mfma_f32_16x16x32_bf16 v[60:63], a[4:7], a[180:183], v[60:63]\n\t"
    "ds_read_b64_tr_b16 a[44:45], %[rb11] offset:37888\n\t"
    "v_mfma_f32_16x16x32_bf16 v[60:63], a[0:3], a[176:179], v[60:63]\n\t"
    "ds_read_b64_tr_b16 a[46:47], %[rb11] offset:44032\n\t"
    "v_cvt_pk_bf16_f32 v232, v48, v49\n\t"
    "v_cvt_pk_bf16_f32 v233, v50, v51\n\t"
    "s_waitcnt lgkmcnt(8)\n\t"
    "v_mfma_f32_16x16x32_bf16 v[64:67], a[16:19], a[160:163], v[64:67]\n\t"
    "ds_read_b64_tr_b16 a[0:1], %[rb10] offset:1536\n\t"
    "v_cvt_pk_bf16_f32 v234, v56, v57\n\t"
    "v_cvt_pk_bf16_f32 v235, v58, v59\n\t"
    "v_mfma_f32_16x16x32_bf16 v[64:67], a[20:23], a[164:167], v[64:67]\n\t"
    "ds_read_b64_tr_b16 a[2:3], %[rb10] offset:7680\n\t"
    "v_min_u32 v232, -2, v232\n\t"
    "v_min_u32 v233, -2, v233\n\t"
    "v_mfma_f32_16x16x32_bf16 v[64:67], a[24:27], a[168:171], v[64:67]\n\t"
    "ds_read_b64_tr_b16 a[4:5], %[rb10] offset:13824\n\t"
    "v_min_u32 v234, -2, v234\n\t"
    "v_min_u32 v235, -2, v235\n\t"
    "v_mfma_f32_16x16x32_bf16 v[64:67], a[28:31], a[172:175], v[64:67]\n\t"
    "ds_read_b64_tr_b16 a[6:7], %[rb10] offset:19968\n\t"
    "buffer_store_dwordx4 v[232:235], %[xl], %[srd], %[lo3] offen offset:1024 sc1\n\t"
    "v_mfma_f32_16x16x32_bf16 v[68:71], a[28:31], a[188:191], v[68:71]\n\t"
    "ds_read_b64_tr_b16 a[8:9], %[rb10] offset:26112\n\t"
    "v_cvt_pk_bf16_f32 v224, v52, v53\n\t"
    "v_cvt_pk_bf16_f32 v225, v54, v55\n\t"
    "v_mfma_f32_16x16x32_bf16 v[68:71], a[24:27], a[184:187], v[68:71]\n\t"
    "ds_read_b64_tr_b16 a[10:11], %[rb10] offset:32256\n\t"
    "v_cvt_pk_bf16_f32 v226, v60, v61\n\t"
    "v_cvt_pk_bf16_f32 v227, v62, v63\n\t"
    "v_mfma_f32_16x16x32_bf16 v[68:71], a[20:23], a[180:183], v[68:71]\n\t"
    "ds_read_b64_tr_b16 a[12:13], %[rb10] offset:38400\n\t"
    "v_min_u32 v224, -2, v224\n\t"
    "v_min_u32 v225, -2, v225\n\t"
    "v_mfma_f32_16x16x32_bf16 v[68:71], a[16:19], a[176:179], v[68:71]\n\t"
    "ds_read_b64_tr_b16 a[14:15], %[rb10] offset:44544\n\t"
    "v_min_u32 v226, -2, v226\n\t"
    "v_min_u32 v227, -2, v227\n\t"
    "s_waitcnt lgkmcnt(8)\n\t"
    "v_mfma_f32_16x16x32_bf16 v[72:75], a[32:35], a[160:163], v[72:75]\n\t"
    "ds_read_b64_tr_b16 a[16:17], %[rb11] offset:1536\n\t"
    "buffer_store_dwordx4 v[224:227], %[xl], %[srd], %[lo2] offen offset:1024 sc1\n\t"
    "s_add_u32 s46, %[ib], 0\n\t"
    "v_mfma_f32_16x16x32_bf16 v[72:75], a[36:39], a[164:167], v[72:75]\n\t"
    "ds_read_b64_tr_b16 a[18:19], %[rb11] offset:7680\n\t"
    "buffer_load_dwordx4 v[152:155], %[xl], %[srd], s46 offen sc1\n\t"
    "v_mfma_f32_16x16x32_bf16 v[72:75], a[40:43], a[168:171], v[72:75]\n\t"
    "ds_read_b64_tr_b16 a[20:21], %[rb11] offset:13824\n\t"
    "v_mfma_f32_16x16x32_bf16 v[72:75], a[44:47], a[172:175], v[72:75]\n\t"
    "ds_read_b64_tr_b16 a[22:23], %[rb11] offset:19968\n\t"
    "buffer_load_dwordx4 v[164:167], %[xl], %[srd], s46 offen offset:1024 sc1\n\t"
    "v_mfma_f32_16x16x32_bf16 v[76:79], a[44:47], a[188:191], v[76:79]\n\t"
    "ds_read_b64_tr_b16 a[24:25], %[rb11] offset:26112\n\t"
    "v_mfma_f32_16x16x32_bf16 v[76:79], a[40:43], a[184:187], v[76:79]\n\t"
    "ds_read_b64_tr_b16 a[26:27], %[rb11] offset:32256\n\t"
    "buffer_load_dwordx4 v[176:179], %[xl], %[srd], s46 offen offset:2048 sc1\n\t"
    "v_mfma_f32_16x16x32_bf16 v[76:79], a[36:39], a[180:183], v[76:79]\n\t"
    "ds_read_b64_tr_b16 a[28:29], %[rb11] offset:38400\n\t"
    "s_add_u32 s46, %[ib], 12288\n\t"
    "v_mfma_f32_16x16x32_bf16 v[76:79], a[32:35], a[176:179], v[76:79]\n\t"
    "ds_read_b64_tr_b16 a[30:31], %[rb11] offset:44544\n\t"
    "buffer_load_dwordx4 v[156:159], %[xl], %[srd], s46 offen sc1\n\t"
    "v_cvt_pk_bf16_f32 v228, v64, v65\n\t"
    "v_cvt_pk_bf16_f32 v229, v66, v67\n\t"
    "s_waitcnt lgkmcnt(8)\n\t"
    "v_mfma_f32_16x16x32_bf16 v[80:83], a[0:3], a[160:163], v[80:83]\n\t"
    "ds_read_b64_tr_b16 a[32:33], %[rb10] offset:2048\n\t"
    "v_cvt_pk_bf16_f32 v230, v72, v73\n\t"
    "v_cvt_pk_bf16_f32 v231, v74, v75\n\t"
    "v_mfma_f32_16x16x32_bf16 v[80:83], a[4:7], a[164:167], v[80:83]\n\t"
    "ds_read_b64_tr_b16 a[34:35], %[rb10] offset:8192\n\t"
    "buffer_load_dwordx4 v[168:171], %[xl], %[srd], s46 offen offset:1024 sc1\n\t"
    "v_min_u32 v228, -2, v228\n\t"
    "v_min_u32 v229, -2, v229\n\t"
    "v_mfma_f32_16x16x32_bf16 v[80:83], a[8:11], a[168:171], v[80:83]\n\t"
    "ds_read_b64_tr_b16 a[36:37], %[rb10] offset:14336\n\t"
    "v_min_u32 v230, -2, v230\n\t"
    "v_min_u32 v231, -2, v231\n\t"
    "v_mfma_f32_16x16x32_bf16 v[80:83], a[12:15], a[172:175], v[80:83]\n\t"
    "ds_read_b64_tr_b16 a[38:39], %[rb10] offset:20480\n\t"
    "buffer_load_dwordx4 v[184:187], %[xl], %[srd], s46 offen offset:2048 sc1\n\t"
    "buffer_store_dwordx4 v[228:231], %[xl], %[srd], %[lo3] offen offset:2048 sc1\n\t"
    "v_mfma_f32_16x16x32_bf16 v[84:87], a[12:15], a[188:191], v[84:87]\n\t"
    "ds_read_b64_tr_b16 a[40:41], %[rb10] offset:26624\n\t"
    "s_add_u32 s46, %[ib], 24576\n\t"
    "v_cvt_pk_bf16_f32 v232, v68, v69\n\t"
    "v_cvt_pk_bf16_f32 v233, v70, v71\n\t"
    "v_mfma_f32_16x16x32_bf16 v[84:87], a[8:11], a[184:187], v[84:87]\n\t"
    "ds_read_b64_tr_b16 a[42:43], %[rb10] offset:32768\n\t"
    "buffer_load_dwordx4 v[160:163], %[xl], %[srd], s46 offen sc1\n\t"
    "v_cvt_pk_bf16_f32 v234, v76, v77\n\t"
    "v_cvt_pk_bf16_f32 v235, v78, v79\n\t"
    "v_mfma_f32_16x16x32_bf16 v[84:87], a[4:7], a[180:183], v[84:87]\n\t"
    "ds_read_b64_tr_b16 a[44:45], %[rb10] offset:38912\n\t"
    "v_min_u32 v232, -2, v232\n\t"
    "v_min_u32 v233, -2, v233\n\t"
    "v_mfma_f32_16x16x32_bf16 v[84:87], a[0:3], a[176:179], v[84:87]\n\t"
    "ds_read_b64_tr_b16 a[46:47], %[rb10] offset:45056\n\t"
    "buffer_load_dwordx4 v[172:175], %[xl], %[srd], s46 offen offset:1024 sc1\n\t"
    "v_min_u32 v234, -2, v234\n\t"
    "v_min_u32 v235, -2, v235\n\t"
    "s_waitcnt lgkmcnt(8)\n\t"
    "v_mfma_f32_16x16x32_bf16 v[88:91], a[16:19], a[160:163], v[88:91]\n\t"
    "ds_read_b64_tr_b16 a[0:1], %[rb11] offset:2048\n\t"
    "buffer_store_dwordx4 v[232:235], %[xl], %[srd], %[lo2] offen offset:2048 sc1\n\t"
    "v_mfma_f32_16x16x32_bf16 v[88:91], a[20:23], a[164:167], v[88:91]\n\t"
    "ds_read_b64_tr_b16 a[2:3], %[rb11] offset:8192\n\t"
    "buffer_load_dwordx4 v[192:195], %[xl], %[srd], s46 offen offset:2048 sc1\n\t"
    "v_mfma_f32_16x16x32_bf16 v[88:91], a[24:27], a[168:171], v[88:91]\n\t"
    "ds_read_b64_tr_b16 a[4:5], %[rb11] offset:14336\n\t"
    "v_mfma_f32_16x16x32_bf16 v[88:91], a[28:31], a[172:175], v[88:91]\n\t"
    "ds_read_b64_tr_b16 a[6:7], %[rb11] offset:20480\n\t"
    "v_mfma_f32_16x16x32_bf16 v[92:95], a[28:31], a[188:191], v[92:95]\n\t"
    "ds_read_b64_tr_b16 a[8:9], %[rb11] offset:26624\n\t"
    "v_mfma_f32_16x16x32_bf16 v[92:95], a[24:27], a[184:187], v[92:95]\n\t"
    "ds_read_b64_tr_b16 a[10:11], %[rb11] offset:32768\n\t"
    "v_mfma_f32_16x16x32_bf16 v[92:95], a[20:23], a[180:183], v[92:95]\n\t"
    "ds_read_b64_tr_b16 a[12:13], %[rb11] offset:38912\n\t"
    "v_mfma_f32_16x16x32_bf16 v[92:95], a[16:19], a[176:179], v[92:95]\n\t"
    "ds_read_b64_tr_b16 a[14:15], %[rb11] offset:45056\n\t"
    "v_cvt_pk_bf16_f32 v224, v80, v81\n\t"
    "v_cvt_pk_bf16_f32 v225, v82, v83\n\t"
    "s_waitcnt lgkmcnt(8)\n\t"
    "v_mfma_f32_16x16x32_bf16 v[96:99], a[32:35], a[160:163], v[96:99]\n\t"
    "ds_read_b64_tr_b16 a[16:17], %[rb10] offset:2560\n\t"
    "v_cvt_pk_bf16_f32 v226, v88, v89\n\t"
    "v_cvt_pk_bf16_f32 v227, v90, v91\n\t"
    "v_mfma_f32_16x16x32_bf16 v[96:99], a[36:39], a[164:167], v[96:99]\n\t"
    "ds_read_b64_tr_b16 a[18:19], %[rb10] offset:8704\n\t"
    "v_min_u32 v224, -2, v224\n\t"
    "v_min_u32 v225, -2, v225\n\t"
    "v_mfma_f32_16x16x32_bf16 v[96:99], a[40:43], a[168:171], v[96:99]\n\t"
    "ds_read_b64_tr_b16 a[20:21], %[rb10] offset:14848\n\t"
    "v_min_u32 v226, -2, v226\n\t"
    "v_min_u32 v227, -2, v227\n\t"
    "v_mfma_f32_16x16x32_bf16 v[96:99], a[44:47], a[172:175], v[96:99]\n\t"
    "ds_read_b64_tr_b16 a[22:23], %[rb10] offset:20992\n\t"
    "buffer_store_dwordx4 v[224:227], %[xl], %[srd], %[lo1] offen sc1\n\t"
    "v_mfma_f32_16x16x32_bf16 v[100:103], a[44:47], a[188:191], v[100:103]\n\t"
    "ds_read_b64_tr_b16 a[24:25], %[rb10] offset:27136\n\t"
    "v_cvt_pk_bf16_f32 v228, v84, v85\n\t"
    "v_cvt_pk_bf16_f32 v229, v86, v87\n\t"
    "v_mfma_f32_16x16x32_bf16 v[100:103], a[40:43], a[184:187], v[100:103]\n\t"
    "ds_read_b64_tr_b16 a[26:27], %[rb10] offset:33280\n\t"
    "v_cvt_pk_bf16_f32 v230, v92, v93\n\t"
    "v_cvt_pk_bf16_f32 v231, v94, v95\n\t"
    "v_mfma_f32_16x16x32_bf16 v[100:103], a[36:39], a[180:183], v[100:103]\n\t"
    "ds_read_b64_tr_b16 a[28:29], %[rb10] offset:39424\n\t"
    "v_min_u32 v228, -2, v228\n\t"
    "v_min_u32 v229, -2, v229\n\t"
    "v_mfma_f32_16x16x32_bf16 v[100:103], a[32:35], a[176:179], v[100:103]\n\t"
    "ds_read_b64_tr_b16 a[30:31], %[rb10] offset:45568\n\t"
    "v_min_u32 v230, -2, v230\n\t"
    "v_min_u32 v231, -2, v231\n\t"
    "s_waitcnt lgkmcnt(8)\n\t"
    "v_mfma_f32_16x16x32_bf16 v[104:107], a[0:3], a[160:163], v[104:107]\n\t"
    "ds_read_b64_tr_b16 a[32:33], %[rb11] offset:2560\n\t"
    "ds_read_b64_tr_b16 a[34:35], %[rb11] offset:8704\n\t"
    "buffer_store_dwordx4 v[228:231], %[xl], %[srd], %[lo0] offen sc1\n\t"
    "v_mfma_f32_16x16x32_bf16 v[104:107], a[4:7], a[164:167], v[104:107]\n\t"
    "ds_read_b64_tr_b16 a[36:37], %[rb11] offset:14848\n\t"
    "ds_read_b64_tr_b16 a[38:39], %[rb11] offset:20992\n\t"
    "v_mfma_f32_16x16x32_bf16 v[104:107], a[8:11], a[168:171], v[104:107]\n\t"
    "ds_read_b64_tr_b16 a[40:41], %[rb11] offset:27136\n\t"
    "ds_read_b64_tr_b16 a[42:43], %[rb11] offset:33280\n\t"
    "v_mfma_f32_16x16x32_bf16 v[104:107], a[12:15], a[172:175], v[104:107]\n\t"
    "ds_read_b64_tr_b16 a[44:45], %[rb11] offset:39424\n\t"
    "ds_read_b64_tr_b16 a[46:47], %[rb11] offset:45568\n\t"
    "v_mfma_f32_16x16x32_bf16 v[108:111], a[12:15], a[188:191], v[108:111]\n\t"
    "v_mfma_f32_16x16x32_bf16 v[108:111], a[8:11], a[184:187], v[108:111]\n\t"
    "v_mfma_f32_16x16x32_bf16 v[108:111], a[4:7], a[180:183], v[108:111]\n\t"
    "v_mfma_f32_16x16x32_bf16 v[108:111], a[0:3], a[176:179], v[108:111]\n\t"
    "v_cvt_pk_bf16_f32 v232, v96, v97\n\t"
    "v_cvt_pk_bf16_f32 v233, v98, v99\n\t"
    "s_waitcnt lgkmcnt(8)\n\t"
    "v_mfma_f32_16x16x32_bf16 v[112:115], a[16:19], a[160:163], v[112:115]\n\t"
    "v_cvt_pk_bf16_f32 v234, v104, v105\n\t"
    "v_cvt_pk_bf16_f32 v235, v106, v107\n\t"
    "v_mfma_f32_16x16x32_bf16 v[112:115], a[20:23], a[164:167], v[112:115]\n\t"
    "v_min_u32 v232, -2, v232\n\t"
    "v_min_u32 v233, -2, v233\n\t"
    "v_mfma_f32_16x16x32_bf16 v[112:115], a[24:27], a[168:171], v[112:115]\n\t"
    "v_min_u32 v234, -2, v234\n\t"
    "v_min_u32 v235, -2, v235\n\t"
    "v_mfma_f32_16x16x32_bf16 v[112:115], a[28:31], a[172:175], v[112:115]\n\t"
    "buffer_store_dwordx4 v[232:235], %[xl], %[srd], %[lo1] offen offset:1024 sc1\n\t"
    "v_mfma_f32_16x16x32_bf16 v[116:119], a[28:31], a[188:191], v[116:119]\n\t"
    "v_cvt_pk_bf16_f32 v224, v100, v101\n\t"
    "v_cvt_pk_bf16_f32 v225, v102, v103\n\t"
    "v_mfma_f32_16x16x32_bf16 v[116:119], a[24:27], a[184:187], v[116:119]\n\t"
    "v_cvt_pk_bf16_f32 v226, v108, v109\n\t"
    "v_cvt_pk_bf16_f32 v227, v110, v111\n\t"
    "v_mfma_f32_16x16x32_bf16 v[116:119], a[20:23], a[180:183], v[116:119]\n\t"
    "v_min_u32 v224, -2, v224\n\t"
    "v_min_u32 v225, -2, v225\n\t"
    "v_mfma_f32_16x16x32_bf16 v[116:119], a[16:19], a[176:179], v[116:119]\n\t"
    "v_min_u32 v226, -2, v226\n\t"
    "v_min_u32 v227, -2, v227\n\t"
    "s_waitcnt lgkmcnt(0)\n\t"
    "v_mfma_f32_16x16x32_bf16 v[120:123], a[32:35], a[160:163], v[120:123]\n\t"
    "buffer_store_dwordx4 v[224:227], %[xl], %[srd], %[lo0] offen offset:1024 sc1\n\t"
    "v_mfma_f32_16x16x32_bf16 v[120:123], a[36:39], a[164:167], v[120:123]\n\t"
    "v_mfma_f32_16x16x32_bf16 v[120:123], a[40:43], a[168:171], v[120:123]\n\t"
    "v_mfma_f32_16x16x32_bf16 v[120:123], a[44:47], a[172:175], v[120:123]\n\t"
    "v_mfma_f32_16x16x32_bf16 v[124:127], a[44:47], a[188:191], v[124:127]\n\t"
    "v_mfma_f32_16x16x32_bf16 v[124:127], a[40:43], a[184:187], v[124:127]\n\t"
    "v_mfma_f32_16x16x32_bf16 v[124:127], a[36:39], a[180:183], v[124:127]\n\t"
    "v_mfma_f32_16x16x32_bf16 v[124:127], a[32:35], a[176:179], v[124:127]\n\t"
    "v_cvt_pk_bf16_f32 v228, v112, v113\n\t"
    "v_cvt_pk_bf16_f32 v229, v114, v115\n\t"
    "s_nop 1\n\t"
    "v_cvt_pk_bf16_f32 v230, v120, v121\n\t"
    "v_cvt_pk_bf16_f32 v231, v122, v123\n\t"
    "v_min_u32 v228, -2, v228\n\t"
    "v_min_u32 v229, -2, v229\n\t"
    "v_min_u32 v230, -2, v230\n\t"
    "v_min_u32 v231, -2, v231\n\t"
    "buffer_store_dwordx4 v[228:231], %[xl], %[srd], %[lo1] offen offset:2048 sc1\n\t"
    "v_cvt_pk_bf16_f32 v232, v116, v117\n\t"
    "v_cvt_pk_bf16_f32 v233, v118, v119\n\t"
    "v_cvt_pk_bf16_f32 v234, v124, v125\n\t"
    "v_cvt_pk_bf16_f32 v235, v126, v127\n\t"
    "v_min_u32 v232, -2, v232\n\t"
    "v_min_u32 v233, -2, v233\n\t"
    "v_min_u32 v234, -2, v234\n\t"
    "v_min_u32 v235, -2, v235\n\t"
    "buffer_store_dwordx4 v[232:235], %[xl], %[srd], %[lo0] offen offset:2048 sc1\n\t"
    "s_waitcnt vmcnt(6)\n\t"
    "s_nop 7\n\t"
    : "=&{v[180:183]}"(oh[0]), "=&{v[188:191]}"(oh[1]), "=&{v[196:199]}"(oh[2]), "=&{v[204:207]}"(oh[3]), "=&{v[212:215]}"(oh[4]), "=&{v[220:223]}"(oh[5]),
      "=&{v[84:87]}"(os[0]), "=&{v[92:95]}"(os[1]), "=&{v[100:103]}"(os[2]), "=&{v[108:111]}"(os[3]), "=&{v[116:119]}"(os[4]), "=&{v[124:127]}"(os[5]),
      "=&{a[64:67]}"(xv[0]), "=&{a[68:71]}"(xv[1]), "=&{a[72:75]}"(xv[2]), "=&{a[76:79]}"(x1v[0]), "=&{a[80:83]}"(x1v[1]), "=&{a[84:87]}"(x1v[2]),
      "=&{v[152:155]}"(pr[0][0]), "=&{v[156:159]}"(pr[0][1]), "=&{v[160:163]}"(pr[0][2]), "=&{v[164:167]}"(pr[1][0]), "=&{v[168:171]}"(pr[1][1]),
      "=&{v[172:175]}"(pr[1][2]), "=&{v[176:179]}"(pr[2][0]), "=&{v[184:187]}"(pr[2][1]), "=&{v[192:195]}"(pr[2][2])
    : [xl] "v"(xl), [wv0] "v"(wv[0]), [wv1] "v"(wv[1]), [wv2] "v"(wv[2]), [rb20] "v"(r0), [rb21] "v"(r1), [rb00] "v"(r0 + 49152u), [rb01] "v"(r1 + 49152u),
      [rb10] "v"(r0 + 98304u), [rb11] "v"(r1 + 98304u), [ur00] "v"(u0), [ur01] "v"(u1), [ur10] "v"(u0 + 65536u), [ur11] "v"(u1 + 65536u),
      [ur20] "v"(u0 + 131072u), [ur21] "v"(u1 + 131072u), [uwv] "v"(9216u * r8 + 64u * (l >> 5) + cs), [xz] "v"(0u),
      [fv] "v"(FLAG(FL_H + qb * 24u + 6u * mo + l % 6u)), [fa] "v"(FLAG(fa)), [fv2] "v"(FLAG(FL_O + (24u + 2u * mo + (l & 1u)) * 8u + qb)),
      [fov] "v"(FLAG((l & 1u) ? FL_O + u.h * 8u + u.qblk : FL_R + tile * 4u + mo)), [xo] "v"(((16u * mblk + i16) * D + col + 4u * g) * 2u),
      [srdx0] "s"(srdx0), [srdx1] "s"(srdx1), [srd] "s"(srd), [usrdw] "s"(usrdw),
      [srdi] "s"(srdi), [wa] "s"(uni64(wa)), [wb] "s"(uni64(wb)), [wc] "s"(uni64(wc)), [wlen] "s"(9u * 147456u + 1280u),
      [rot1] "s"(0u), [rot2] "s"(0u),
      [m0b] "s"(rfl(ldsb) + 12288u * w), [xend] "s"(nch * 4096u), [xmid] "s"((NYO + 2u) * 4096u), [chk] "s"(NYO * 4096u),
      [fho] "s"(FLAG(FL_H + u.rb * 24u + u.cb)),
      [eo1] "s"(eo[1]), [eo2] "s"(eo[2]), [eo3] "s"(eo[3]), [lo0] "s"(lo[0]), [lo1] "s"(lo[1]), [lo2] "s"(lo[2]), [lo3] "s"(lo[3]),
      [ib] "s"(ib), [x1o] "s"(WS_X1 + (mblk * 18u + (col >> 5)) * 1024u), [um0] "s"(rfl(ldsb) + 3072u * w), [uws] "s"(24u * 9216u * w),
      [uxs] "s"(WS_X1N + (16u * u.rb + 4u * w) * 18432u), [hs] "s"(WS_A + ((16u * u.rb + 4u * w) * 272u + h3_ks(6u * u.cb)) * 1024u)
    : "v32", "v33", "v34", "v35", "v36", "v37", "v38", "v39", "v40", "v41", "v42", "v43", "v44", "v45", "v46", "v47", "v48", "v49", "v50", "v51", "v52", "v53", "v54", "v55", "v56", "v57", "v58", "v59", "v60", "v61", "v62", "v63", "v64", "v65", "v66", "v67", "v68", "v69", "v70", "v71", "v72", "v73", "v74", "v75", "v76", "v77", "v78", "v79", "v80", "v81", "v82", "v83", "v88", "v89", "v90", "v91", "v96", "v97", "v98", "v99", "v104", "v105", "v106", "v107", "v112", "v113", "v114", "v115", "v120", "v121", "v122", "v123", "v128", "v129", "v130", "v131", "v132", "v133", "v134", "v135", "v136", "v137", "v138", "v139", "v140", "v141", "v142", "v143", "v144", "v145", "v146", "v147", "v148", "v149", "v150", "v151", "v200", "v201", "v202", "v203", "v208", "v209", "v210", "v211", "v216", "v217", "v218", "v219", "v224", "v225", "v226", "v227", "v228", "v229", "v230", "v231", "v232", "v233", "v234", "v235", "v236", "v237", "v238", "v239", "v240", "v241", "v242", "v243", "v244", "v245", "v246", "v247", "a0", "a1", "a2", "a3", "a4", "a5", "a6", "a7", "a8", "a9", "a10", "a11", "a12", "a13", "a14", "a15", "a16", "a17", "a18", "a19", "a20", "a21", "a22", "a23", "a24", "a25", "a26", "a27", "a28", "a29", "a30", "a31", "a32", "a33", "a34", "a35", "a36", "a37", "a38", "a39", "a40", "a41", "a42", "a43", "a44", "a45", "a46", "a47", "a48", "a49", "a50", "a51", "a52", "a53", "a54", "a55", "a56", "a57", "a58", "a59", "a60", "a61", "a62", "a63", "a88", "a89", "a90", "a91", "a92", "a93", "a94", "a95", "a96", "a97", "a98", "a99", "a100", "a101", "a102", "a103", "a104", "a105", "a106", "a107", "a108", "a109", "a110", "a111", "a112", "a113", "a114", "a115", "a116", "a117", "a118", "a119", "a120", "a121", "a122", "a123", "a124", "a125", "a126", "a127", "a128", "a129", "a130", "a131", "a132", "a133", "a134", "a135", "a136", "a137", "a138", "a139", "a140", "a141", "a142", "a143", "a144", "a145", "a146", "a147", "a148", "a149", "a150", "a151", "a152", "a153", "a154", "a155", "a156", "a157", "a158", "a159", "a160", "a161", "a162", "a163", "a164", "a165", "a166", "a167", "a168", "a169", "a170", "a171", "a172", "a173", "a174", "a175", "a176", "a177", "a178", "a179", "a180", "a181", "a182", "a183", "a184", "a185", "a186", "a187", "a188", "a189", "a190", "a191", "a192", "a193", "a194", "a195", "a196", "a197", "a198", "a199", "a200", "a201", "a202", "a203", "a204", "a205", "a206", "a207", "a208", "a209", "a210", "a211", "a212", "a213", "a214", "a215", "a216", "a217", "a218", "a219", "a220", "a221", "a222", "a223", "a224", "a225", "a226", "a227", "a228", "a229", "a230", "a231", "a232", "a233", "a234", "a235", "a236", "a237", "a238", "a239", "a240", "a241", "a242", "a243", "a244", "a245", "a246", "a247", "a248", "a249", "a250", "a251", "a252", "a253", "a254", "a255", "s40", "s41", "s42", "s43", "s44", "s45", "s46", "s47", "s48", "s49", "s50", "s51", "s52", "s53", "s54", "s55", "s56", "s57", "s58", "s59", "s60", "s61", "s62", "s63", "vcc", "scc", "memory");
  auto fetch = [&](const unsigned k0, const unsigned k1) {
#pragma unroll
    for (unsigned k = k0; k < k1; ++k)
#pragma unroll
      for (unsigned i = 0; i < 3; ++i) pr[i][k] = __builtin_amdgcn_raw_buffer_load_b128(srd, xl, ib + k * 12288u + i * 1024u, 16);
  };
  auto missing = [&](const unsigned k0, const unsigned k1) {
    unsigned mx = 0u;
#pragma unroll
    for (unsigned k = k0; k < k1; ++k)
#pragma unroll
      for (unsigned i = 0; i < 3; ++i) mx = max(max(mx, max(pr[i][k][0], pr[i][k][1])), max(pr[i][k][2], pr[i][k][3]));
    return __any(mx == 0xffffffffu) != 0;
  };
  auto settle = [&](const unsigned k0, const unsigned k1) {
    if (missing(k0, k1)) {
#pragma nounroll
      for (unsigned spin = 0; spin < (1u << 18); ++spin) {
        asm volatile("" ::: "memory");
        fetch(k0, k1);
        if (!missing(k0, k1)) break;
      }
    }
  };
  auto un = [](const unsigned d) { return (f32x2){bflo(d), bfhi(d)}; };
  fetch(3, NS);
  settle(0, 3);
  f32x2 gt[3][4];
#pragma unroll
  for (int i = 0; i < 3; ++i)
#pragma unroll
    for (int e = 0; e < 4; ++e) {
      const f32x2 hv = ((f32x2){oh[2 * i + (e >> 1)][2 * (e & 1)], oh[2 * i + (e >> 1)][2 * (e & 1) + 1]} + un(x1v[i][e])) + (un(pr[i][0][e]) + un(pr[i][1][e])) + un(pr[i][2][e]);
      gt[i][e] = (f32x2){silu(hv.x), silu(hv.y)};
      asm volatile("" : "+v"(gt[i][e]));
    }
  settle(3, NS);
#pragma unroll
  for (unsigned i = 0; i < 3; ++i) {
    unsigned o[4];
#pragma unroll
    for (int e = 0; e < 4; ++e) {
      const f32x2 sv = ((f32x2){os[2 * i + (e >> 1)][2 * (e & 1)], os[2 * i + (e >> 1)][2 * (e & 1) + 1]} + un(pr[i][3][e])) + (un(pr[i][4][e]) + un(pr[i][5][e]));
      const f32x2 r = un(xv[i][e]) + sv * gt[i][e];
      o[e] = cvtpk(r.x, r.y);
    }
    const auto e0 = __builtin_amdgcn_permlane16_swap(o[0], o[2], false, false), e1 = __builtin_amdgcn_permlane16_swap(o[1], o[3], false, false);
    __builtin_amdgcn_raw_buffer_store_b128((u32x4){e0[0], e1[0], e0[1], e1[1]}, mk_srd(out, S * D * 2),
                                           ((16u * mblk + i16) * D + col + 32u * i + 16u * (g & 1u) + 8u * (g >> 1)) * 2u, 0, 16);
  }
}

extern "C" __global__ __launch_bounds__(256, 1) void k1_kernel(const u16* __restrict__ x, const u16* __restrict__ Kc, const u16* __restrict__ Vc, const u16* __restrict__ wq,
                                                               const u16* __restrict__ wo, const u16* __restrict__ w1, const u16* __restrict__ w2, const u16* __restrict__ w3,
                                                               const u16* __restrict__ w4, char* __restrict__ ws, u16* __restrict__ out) {
  extern __shared__ __attribute__((aligned(16))) char lds[];
  const unsigned bid = blockIdx.x, xcd = bid & 7u, idx = bid >> 3, hl = idx & 3u, qblk = idx >> 2;
  start_unit(ws, x, wq, w1, hl != 0, 3u * xcd + hl - 1u, qblk, lds);
  Unit u;
  u.y = hl != 0; u.qblk = qblk; u.h = hl ? 3u * xcd + hl - 1u : 24u + xcd;
  u.tile = hl ? 24u * (xcd & 1u) + 3u * qblk + hl - 1u : 0u; u.mi = xcd >> 1;
  u.rb = 4u * (xcd & 1u) + (qblk >> 1); u.cb = hl ? 6u * (xcd >> 1) + 3u * (qblk & 1u) + hl - 1u : 0u;
  if (!u.y) mix_unit(ws, w1, w2, qblk * 8u + xcd, 2u * qblk, wq, lds);
  attn_unit(ws, wq, Kc, Vc, w3, wo, w4, u, lds);
  if (!u.y && threadIdx.x == 0) set_flag(ws, FL_O + u.h * 8u + qblk);
  if (u.y) tail_unit(ws, x, w3, wo, w4, out, u, lds);
  asm volatile("k1_code_end:");
}

extern "C" void kernel_launch(void* const* d_in, const int* in_sizes, int n_in, void* d_out, int out_size, void* d_ws, size_t ws_size, hipStream_t stream) {
  const u16* x = (const u16*)d_in[0];
  char* ws = (char*)d_ws;
  (void)hipFuncSetAttribute((const void*)k1_kernel, hipFuncAttributeMaxDynamicSharedMemorySize, 163840);
  k1_kernel<<<256, 256, 163840, stream>>>(x, (const u16*)d_in[1], (const u16*)d_in[2], (const u16*)d_in[3], (const u16*)d_in[4], (const u16*)d_in[6],
                                          (const u16*)d_in[7], (const u16*)d_in[8], (const u16*)d_in[9], ws, (u16*)d_out);
}
```
